```python
import torch

def _softmax(x, axis=-1):
    return torch.softmax(x, dim=axis)
def _gelu(x):
    return torch.nn.functional.gelu(x, approximate="tanh")
def _silu(x):
    return torch.nn.functional.silu(x)
def _sigmoid(x):
    return torch.sigmoid(x)
def _layernorm(x, axis=-1, eps=1e-5):
    m = x.mean(dim=axis, keepdim=True)
    v = x.var(dim=axis, keepdim=True, unbiased=False)
    return (x - m) / torch.sqrt(v + eps)
def _scatter_add(tgt, idx, src, axis=0):
    return tgt.clone().index_add_(axis, idx, src)
def _segment_sum(src, seg, n):
    out = torch.zeros((n,) + src.shape[1:], dtype=src.dtype)
    return out.index_add_(0, seg, src)
def _pad(x, pw):
    flat = tuple(v for pair in reversed(pw) for v in pair)
    return torch.nn.functional.pad(x, flat)

kernel_name = 'concat_heads__linear_kernel_hedgehog__compressed_kv_cla__prompt'

def setup_inputs():

    _w = (lambda a: (lambda t: t.to(torch.bfloat16) if t.is_floating_point() else t)(torch.as_tensor(a)))
    def _A_setup():
        B, S, D, H = 1, 2048, 640, 32
        Dh = D // H
        return {
            "x": _w((lambda *s: torch.randn(*s, dtype=torch.bfloat16) * 0.4)(B, S, D)),
            "wqkv": _w((lambda *s: torch.randn(*s, dtype=torch.bfloat16) * 0.4)(D, 3 * D)),
            "w_phi": _w((lambda *s: torch.randn(*s, dtype=torch.bfloat16) * 0.4)(H, Dh, Dh) * (Dh ** -0.5)),
            "wo": _w((lambda *s: torch.randn(*s, dtype=torch.bfloat16) * 0.05)(D, D)),
        }
    _A_in = _A_setup()

    def _B_setup():
        B, H, Sq, Sk, D, M = 1, 32, 2048, 2048, 128, 640
        Dc = 4 * D
        return {
            "x": _w((lambda *s: torch.randn(*s, dtype=torch.bfloat16) * 0.8)(B, Sq, M)),
            "c_kv": _w((lambda *s: torch.randn(*s, dtype=torch.bfloat16) * 0.4)(B, Sk, Dc)),
            "w_q": _w((lambda *s: torch.randn(*s, dtype=torch.bfloat16) * 6.4)(M, H, D) * (M ** -0.5)),
            "w_uk": _w((lambda *s: torch.randn(*s, dtype=torch.bfloat16) * 6.4)(Dc, H, D) * (Dc ** -0.5)),
            "w_uv": _w((lambda *s: torch.randn(*s, dtype=torch.bfloat16) * 0.4)(Dc, H, D) * (Dc ** -0.5)),
            "w_o": _w((lambda *s: torch.randn(*s, dtype=torch.bfloat16) * 0.4)(H, D, M) * ((H * D) ** -0.5)),
        }
    _B_in = _B_setup()

    return {
        "A_x": _A_in["x"],
        "A_wqkv": _A_in["wqkv"],
        "A_w_phi": _A_in["w_phi"],
        "A_wo": _A_in["wo"],
        "B_x": _B_in["x"],
        "B_c_kv": _B_in["c_kv"],
        "B_w_q": _B_in["w_q"],
        "B_w_uk": _B_in["w_uk"],
        "B_w_uv": _B_in["w_uv"],
        "B_w_o": _B_in["w_o"],
    }

def reference(A_x, A_wqkv, A_w_phi, A_wo, B_x, B_c_kv, B_w_q, B_w_uk, B_w_uv, B_w_o):
    def _A_ref(x, wqkv, w_phi, wo):
        B, S, D = x.shape
        H = 32; Dh = D // H
        qkv = (lambda x, *d: x.permute(*d))((x @ wqkv).reshape(B, S, 3, H, Dh), 2, 0, 3, 1, 4)
        q, k, v = qkv[0], qkv[1], qkv[2]
        pq = torch.einsum("bhsd,hdf->bhsf", q, w_phi)
        pk = torch.einsum("bhsd,hdf->bhsf", k, w_phi)
        phi_q = torch.concatenate([_softmax(pq, axis=-1), _softmax(-pq, axis=-1)], axis=-1)
        phi_k = torch.concatenate([_softmax(pk, axis=-1), _softmax(-pk, axis=-1)], axis=-1)
        kv = torch.einsum("bhsf,bhsd->bhfd", phi_k, v)
        z  = torch.einsum("bhsf->bhf", phi_k)
        num = torch.einsum("bhqf,bhfd->bhqd", phi_q, kv)
        den = torch.einsum("bhqf,bhf->bhq", phi_q, z)[..., None] + 1e-6
        o = num / den
        return (lambda x, *d: x.permute(*d))(o, 0, 2, 1, 3).reshape(B, S, D) @ wo

    def _B_ref(x, c_kv, w_q, w_uk, w_uv, w_o):
        B, Sq, M = x.shape[0], x.shape[1], x.shape[2]
        Sk = c_kv.shape[1]
        H, D = w_q.shape[1], w_q.shape[2]
        q = torch.einsum("bqm,mhd->bhqd", x, w_q)
        k = torch.einsum("bkc,chd->bhkd", c_kv, w_uk)
        v = torch.einsum("bkc,chd->bhkd", c_kv, w_uv)
        scores = torch.einsum("bhqd,bhkd->bhqk", q, k) * (D ** -0.5)
        off = Sk - Sq
        qi = torch.arange(Sq)[:, None]
        kj = torch.arange(Sk)[None, :]
        cmask = kj <= (qi + off)
        scores = torch.where(cmask[None, None], scores, -1e9)
        p = _softmax(scores, axis=-1)
        ctx = torch.einsum("bhqk,bhkd->bhqd", p, v)
        return torch.einsum("bhqd,hdm->bqm", ctx, w_o)

    a = _A_ref(A_x, A_wqkv, A_w_phi, A_wo)
    b = _B_ref(B_x, B_c_kv, B_w_q, B_w_uk, B_w_uv, B_w_o)
    return torch.concatenate([a, b], axis=-1)
```

```cpp
#include <hip/hip_runtime.h>
#include <hip/hip_bf16.h>
typedef unsigned short u16;
__device__ __forceinline__ float b2f(u16 x){ return __uint_as_float(((unsigned)x)<<16); }
__device__ __forceinline__ u16 f2b(float x){ __hip_bfloat16 h = __float2bfloat16(x); return *(u16*)&h; }

__global__ void gemm_naive(const u16* A, const u16* B, u16* C, int M, int N, int K, int ldc, int coff){
  __shared__ float As[16][65], Bs[16][65];
  int tx = threadIdx.x & 15, ty = threadIdx.x >> 4;
  int m0 = blockIdx.y*64, n0 = blockIdx.x*64;
  float acc[4][4] = {};
  for (int k0=0;k0<K;k0+=16){
    for (int i=threadIdx.x;i<1024;i+=256){ int r=i>>4,c=i&15; As[c][r]=b2f(A[(size_t)(m0+r)*K+k0+c]); }
    for (int i=threadIdx.x;i<1024;i+=256){ int r=i>>6,c=i&63; Bs[r][c]=b2f(B[(size_t)(k0+r)*N+n0+c]); }
    __syncthreads();
    for (int k=0;k<16;k++){
      float a[4],b[4];
      for(int i=0;i<4;i++){a[i]=As[k][ty*4+i]; b[i]=Bs[k][tx*4+i];}
      for(int i=0;i<4;i++)for(int j=0;j<4;j++)acc[i][j]+=a[i]*b[j];
    }
    __syncthreads();
  }
  for(int i=0;i<4;i++)for(int j=0;j<4;j++) C[(size_t)(m0+ty*4+i)*ldc+coff+n0+tx*4+j]=f2b(acc[i][j]);
}
__global__ void attn_naive(const u16* Q, const u16* Kk, const u16* V, u16* O){
  int row = blockIdx.x, h = blockIdx.y, lane = threadIdx.x;
  __shared__ float q[128]; __shared__ float p[2048];
  q[lane]=b2f(Q[(size_t)row*4096+h*128+lane]); q[lane+64]=b2f(Q[(size_t)row*4096+h*128+lane+64]);
  __syncthreads();
  float mx=-1e30f;
  for(int j=lane;j<=row;j+=64){ float s=0; const u16* kr=Kk+(size_t)j*4096+h*128; for(int d=0;d<128;d++) s+=q[d]*b2f(kr[d]); s*=0.08838834764831845f; p[j]=s; mx=fmaxf(mx,s);}
  for(int o=32;o;o>>=1) mx=fmaxf(mx,__shfl_xor(mx,o));
  float l=0; for(int j=lane;j<=row;j+=64){ float e=__expf(p[j]-mx); p[j]=e; l+=e;}
  for(int o=32;o;o>>=1) l+=__shfl_xor(l,o);
  __syncthreads();
  float a0=0,a1=0; for(int j=0;j<=row;j++){ float e=p[j]; a0+=e*b2f(V[(size_t)j*4096+h*128+lane]); a1+=e*b2f(V[(size_t)j*4096+h*128+lane+64]); }
  O[(size_t)row*4096+h*128+lane]=f2b(a0/l); O[(size_t)row*4096+h*128+lane+64]=f2b(a1/l);
}
__global__ void phi_naive(const u16* qkv, const u16* wphi, float* phi){
  int idx = blockIdx.x*256+threadIdx.x;
  int which = idx/(2048*32), r = idx%(2048*32), row=r/32, h=r%32;
  float x[20], p[20];
  for(int d=0;d<20;d++) x[d]=b2f(qkv[(size_t)row*1920+which*640+h*20+d]);
  float mx=-1e30f, mn=1e30f;
  for(int f=0;f<20;f++){ float s=0; for(int d=0;d<20;d++) s+=x[d]*b2f(wphi[h*400+d*20+f]); s=b2f(f2b(s)); p[f]=s; mx=fmaxf(mx,s); mn=fminf(mn,s);}
  float s1=0,s2=0; for(int f=0;f<20;f++){ s1+=__expf(p[f]-mx); s2+=__expf(mn-p[f]); }
  float* o = phi+(size_t)idx*40;
  for(int f=0;f<20;f++){ o[f]=b2f(f2b(__expf(p[f]-mx)/s1)); o[20+f]=b2f(f2b(__expf(mn-p[f])/s2)); }
}
__global__ void kv_naive(const float* phi, const u16* qkv, float* kv){
  int h=blockIdx.x, t=threadIdx.x; if(t>=840) return; int f=t/21,d=t%21;
  const float* pk = phi + (size_t)2048*32*40;
  float s=0; for(int r=0;r<2048;r++){ float v = d<20? b2f(qkv[(size_t)r*1920+1280+h*20+d]) : 1.f; s+=pk[((size_t)r*32+h)*40+f]*v; }
  kv[h*840+t]=b2f(f2b(s));
}
__global__ void o_naive(const float* phi, const float* kv, u16* o){
  int idx = blockIdx.x*256+threadIdx.x; int row=idx/32,h=idx%32;
  const float* pq = phi+(size_t)idx*40; const float* k=kv+h*840;
  float den=0; for(int f=0;f<40;f++) den+=pq[f]*k[f*21+20]; den=b2f(f2b(den)); den=b2f(f2b(den+1e-6f));
  for(int d=0;d<20;d++){ float n=0; for(int f=0;f<40;f++) n+=pq[f]*k[f*21+d]; n=b2f(f2b(n)); o[(size_t)row*640+h*20+d]=f2b(n/den);}
}
extern "C" void kernel_launch(void* const* d_in, const int* in_sizes, int n_in, void* d_out, int out_size, void* d_ws, size_t ws_size, hipStream_t stream){
  const u16* Ax=(const u16*)d_in[0]; const u16* wqkv=(const u16*)d_in[1]; const u16* wphi=(const u16*)d_in[2]; const u16* wo=(const u16*)d_in[3];
  const u16* Bx=(const u16*)d_in[4]; const u16* ckv=(const u16*)d_in[5]; const u16* wq=(const u16*)d_in[6]; const u16* wuk=(const u16*)d_in[7]; const u16* wuv=(const u16*)d_in[8]; const u16* wob=(const u16*)d_in[9];
  u16* out=(u16*)d_out; char* ws=(char*)d_ws;
  u16* Q=(u16*)ws; u16* K=Q+2048*4096; u16* V=K+2048*4096; u16* C=V+2048*4096; u16* qkv=C+2048*4096; u16* oa=qkv+2048*1920;
  float* phi=(float*)(oa+2048*640); float* kv=phi+2*2048*32*40;
  gemm_naive<<<dim3(64,32),256,0,stream>>>(Bx,wq,Q,2048,4096,640,4096,0);
  gemm_naive<<<dim3(64,32),256,0,stream>>>(ckv,wuk,K,2048,4096,512,4096,0);
  gemm_naive<<<dim3(64,32),256,0,stream>>>(ckv,wuv,V,2048,4096,512,4096,0);
  gemm_naive<<<dim3(30,32),256,0,stream>>>(Ax,wqkv,qkv,2048,1920,640,1920,0);
  attn_naive<<<dim3(2048,32),64,0,stream>>>(Q,K,V,C);
  gemm_naive<<<dim3(10,32),256,0,stream>>>(C,wob,out,2048,640,4096,1280,640);
  phi_naive<<<512,256,0,stream>>>(qkv,wphi,phi);
  kv_naive<<<32,896,0,stream>>>(phi,qkv,kv);
  o_naive<<<256,256,0,stream>>>(phi,kv,oa);
  gemm_naive<<<dim3(10,32),256,0,stream>>>(oa,wo,out,2048,640,640,1280,0);
}
```

```cpp
#include <hip/hip_runtime.h>
#include <hip/hip_bf16.h>
struct Params { const void* in[10]; void* out; void* ws; };

__global__ __attribute__((aligned(4096))) __launch_bounds__(256, 1) void k_main(Params p) {
  __shared__ __attribute__((aligned(16))) char lds[163840];
  unsigned wid = __builtin_amdgcn_readfirstlane(threadIdx.x >> 6);
  asm volatile(
R"(
.macro mm x:vararg
v_mfma_f32_16x16x32_bf16 \x
.endm
.macro dr x:vararg
ds_read_b128 \x
.endm
.macro dt x:vararg
ds_read_b64_tr_b16 \x
.endm
.macro cv x:vararg
v_cvt_pk_bf16_f32 \x
.endm
.macro bl x:vararg
buffer_load_dwordx4 \x
.endm
.macro bs x:vararg
buffer_store_dwordx4 \x
.endm
.macro ex2 x:vararg
v_exp_f32 \x
.endm
.macro aw x:vararg
v_accvgpr_write_b32 \x
.endm
.macro ard x:vararg
v_accvgpr_read_b32 \x
.endm
s_load_dwordx16 s[64:79],%0,0x0
s_load_dwordx8 s[80:87],%0,0x40
s_mov_b32 s24,%2
s_mov_b32 s29,%1
s_getpc_b64 s[40:41]
PF0:
s_and_b32 s41,s41,0xffff
s_mov_b32 s42,THEEND-PF0
s_mov_b32 s43,0x20000
s_lshr_b32 s44,s29,3
s_lshl_b32 s44,s44,2
s_add_u32 s44,s44,s24
s_lshl_b32 s44,s44,7
v_mbcnt_lo_u32_b32 v0,-1,0
v_mbcnt_hi_u32_b32 v0,-1,v0
v_min_u32 v0,8,v0
v_lshlrev_b32 v0,14,v0
buffer_load_dword v255,v0,s[40:43],s44 offen
s_and_b32 s25,%1,7
s_lshl_b32 s25,s25,2
s_lshr_b32 s26,%1,6
s_add_u32 s25,s25,s26
s_lshr_b32 s26,%1,3
s_and_b32 s26,s26,7
s_lshr_b32 s27,s24,1
s_and_b32 s28,s24,1
v_mbcnt_lo_u32_b32 v220,-1,0
v_mbcnt_hi_u32_b32 v220,-1,v220
v_lshrrev_b32 v221,3,v220
v_lshlrev_b32 v221,10,v221
v_and_b32 v222,7,v220
v_lshrrev_b32 v223,4,v220
v_xor_b32 v222,v222,v223
v_lshl_add_u32 v200,v222,4,v221
v_xor_b32 v221,64,v200
v_add_u32 v201,0x1c00,v221
v_add_u32 v202,0x3800,v200
v_add_u32 v203,0x5400,v221
s_mul_i32 s32,s26,-131072
s_add_u32 s32,s32,0xf0000
v_add_u32 v204,s32,v200
v_add_u32 v205,s32,v201
v_add_u32 v206,s32,v202
v_add_u32 v207,s32,v203
s_lshl_b32 s32,s26,16
s_lshl_b32 s35,s28,20
s_add_u32 s32,s32,s35
s_lshl_b32 s35,s27,15
s_add_u32 s32,s32,s35
s_lshl_b32 s34,s24,13
s_add_u32 s35,s34,65536
v_lshlrev_b32 v221,13,v223
v_bfe_u32 v222,v220,1,3
v_xor_b32 v222,v222,v223
v_lshl_add_u32 v221,v222,5,v221
v_and_b32 v222,1,v220
v_lshl_add_u32 v208,v222,4,v221
v_xor_b32 v221,128,v208
v_add_u32 v209,0x7c00,v208
v_add_u32 v210,0xf800,v221
v_add_u32 v211,0x17400,v221
v_add_u32 v212,0x20000,v208
v_add_u32 v213,0x27c00,v208
v_add_u32 v214,0x2f800,v221
)" R"(v_add_u32 v215,0x37400,v221
s_lshl_b32 s33,s28,18
s_lshl_b32 s36,s25,8
s_add_u32 s33,s33,s36
s_mul_i32 s37,s26,2
s_add_u32 s37,s37,s29
s_and_b32 s37,s37,7
s_lshl_b32 s36,s37,19
s_add_u32 s33,s33,s36
s_lshl_b32 s36,s37,7
s_add_u32 s32,s32,s36
s_waitcnt lgkmcnt(0)
s_mov_b32 s12,s74
s_and_b32 s13,s75,0xffff
s_mov_b32 s14,0x200000
s_mov_b32 s15,0x20000
s_cmp_eq_u32 s27,0
s_cselect_b32 s16,s78,s80
s_cselect_b32 s17,s79,s81
s_and_b32 s17,s17,0xffff
s_mov_b32 s18,0x400000
s_mov_b32 s19,0x20000
s_add_u32 m0,s35,0
s_nop 0
bl v200,s[12:15],s32 offen offset:0 lds
bl v201,s[12:15],s32 offen offset:1024 lds
bl v202,s[12:15],s32 offen offset:2048 lds
bl v203,s[12:15],s32 offen offset:3072 lds
s_add_u32 m0,s35,4096
s_nop 0
bl v204,s[12:15],s32 offen offset:0 lds
bl v205,s[12:15],s32 offen offset:1024 lds
bl v206,s[12:15],s32 offen offset:2048 lds
bl v207,s[12:15],s32 offen offset:3072 lds
s_add_u32 s32,s32,128
s_bitset0_b32 s32,10
s_add_u32 m0,s34,0
s_nop 0
bl v208,s[16:19],s33 offen offset:0 lds
bl v209,s[16:19],s33 offen offset:1024 lds
bl v210,s[16:19],s33 offen offset:2048 lds
bl v211,s[16:19],s33 offen offset:3072 lds
s_add_u32 m0,s34,4096
s_nop 0
bl v212,s[16:19],s33 offen offset:0 lds
bl v213,s[16:19],s33 offen offset:1024 lds
bl v214,s[16:19],s33 offen offset:2048 lds
bl v215,s[16:19],s33 offen offset:3072 lds
s_add_u32 s33,s33,0x80000
s_bitset0_b32 s33,22
s_lshl_b32 s2,s26,5
s_lshl_b32 s0,s24,3
s_add_u32 s2,s2,s0
s_add_u32 s2,s2,WTAB-PF0+4
s_load_dword s2,s[40:41],s2
s_mov_b32 s100,0x20000
s_add_u32 s54,s86,0x4000000
s_addc_u32 s55,s87,0
s_load_dwordx2 s[54:55],s[54:55],0x0
s_mov_b32 s8,s86
s_and_b32 s9,s87,0xffff
s_mov_b32 s10,0x5a00000
s_mov_b32 s11,0x20000
v_lshrrev_b32 v223,4,v220
)" R"(v_bfe_u32 v222,v220,2,2
v_lshl_add_u32 v221,v223,3,v222
v_lshlrev_b32 v221,8,v221
v_and_b32 v223,1,v223
v_xor_b32 v223,s27,v223
v_lshl_add_u32 v222,v223,2,v222
v_lshl_add_u32 v221,v222,5,v221
v_and_b32 v222,3,v220
v_lshl_add_u32 v192,v222,3,v221
v_xor_b32 v193,32,v192
v_xor_b32 v194,64,v192
v_xor_b32 v195,96,v192
v_and_b32 v221,15,v220
v_lshrrev_b32 v222,1,v221
v_lshrrev_b32 v223,4,v220
v_xor_b32 v222,v222,v223
v_lshlrev_b32 v221,7,v221
v_lshl_add_u32 v221,v222,4,v221
s_lshl_b32 s36,s28,14
s_add_u32 s36,s36,65536
v_add_u32 v196,s36,v221
v_xor_b32 v197,64,v196
v_add_u32 v198,0x10000,v196
v_add_u32 v199,0x10000,v197
v_lshlrev_b32 v221,4,v220
s_lshl_b32 s36,s27,11
v_add_u32 v216,s36,v221
v_add_u32 v217,0x1000,v216
s_add_u32 s36,s36,0x2000
v_add_u32 v218,s36,v216
s_lshl_b32 s36,s25,20
s_lshl_b32 s37,s28,14
s_add_u32 s36,s36,s37
s_lshl_b32 s37,s26,15
s_add_u32 s36,s36,s37
s_mul_i32 s37,s26,-65536
s_add_u32 s37,s37,s36
s_add_u32 s37,s37,0x78000
s_add_u32 s38,s36,524288
s_add_u32 s39,s37,524288
v_mbcnt_lo_u32_b32 v220,-1,0
v_mbcnt_hi_u32_b32 v220,-1,v220
v_and_b32 v221,15,v220
v_lshrrev_b32 v222,4,v220
v_lshlrev_b32 v253,4,v220
s_and_b32 s51,s25,28
s_add_u32 s51,s51,s24
s_add_u32 s60,s25,2
s_and_b32 s60,s60,3
s_lshl_b32 s60,s60,3
s_add_u32 s60,s60,s26
s_mul_i32 s61,s60,26
s_lshr_b32 s61,s61,8
s_mul_i32 s62,s61,10
s_sub_u32 s60,s60,s62
s_mul_i32 s62,s51,800
s_add_u32 s56,s68,s62
s_addc_u32 s57,s69,0
s_and_b32 s57,s57,0xffff
s_cmp_lt_u32 s61,2
s_cselect_b32 s58,800,0
s_cselect_b32 s42,0x3fb8aa3b,1.0
s_mov_b32 s59,0x20000
v_mul_u32_u24 v223,320,v222
)" R"(v_lshl_add_u32 v249,v221,1,v223
v_mov_b32 v223,0x40000000
v_cmp_gt_u32 vcc,4,v221
s_nop 1
v_cndmask_b32 v250,v223,v249,vcc
s_mov_b32 s20,s66
s_and_b32 s21,s67,0xffff
s_mov_b32 s22,0x258000
s_mov_b32 s23,0x20000
v_lshrrev_b32 v223,2,v221
v_lshlrev_b32 v223,2,v223
v_add_u32 v223,v223,v221
v_mul_u32_u24 v223,3840,v223
v_min_u32 v248,2,v222
v_lshl_add_u32 v248,v248,4,v223
s_mul_i32 s40,s60,245760
s_mul_i32 s62,s61,1280
s_add_u32 s40,s40,s62
s_mul_i32 s62,s51,40
s_add_u32 s40,s40,s62
s_lshr_b32 s62,s51,1
s_mul_i32 s62,s62,10
s_add_u32 s62,s62,s60
s_lshl_b32 s62,s62,14
s_add_u32 s41,s62,0x4200000
s_cmp_lt_u32 s61,3
s_cselect_b32 s40,s40,0x80000000
s_cselect_b32 s41,s41,0x80000000
s_waitcnt lgkmcnt(0)
s_add_u32 s54,s54,1
s_addc_u32 s55,s55,0
s_add_u32 m0,s34,32768
s_nop 0
bl v208,s[16:19],s33 offen offset:0 lds
bl v209,s[16:19],s33 offen offset:1024 lds
bl v210,s[16:19],s33 offen offset:2048 lds
bl v211,s[16:19],s33 offen offset:3072 lds
s_add_u32 m0,s34,36864
s_nop 0
bl v212,s[16:19],s33 offen offset:0 lds
bl v213,s[16:19],s33 offen offset:1024 lds
bl v214,s[16:19],s33 offen offset:2048 lds
bl v215,s[16:19],s33 offen offset:3072 lds
s_add_u32 s33,s33,0x80000
s_bitset0_b32 s33,22
s_mov_b64 exec,0
s_cmp_eq_u32 s24,0
s_cbranch_scc1 E0_0
s_cmp_eq_u32 s24,1
s_cbranch_scc1 E0_1
s_cmp_eq_u32 s24,2
s_cbranch_scc1 E0_2
s_cmp_eq_u32 s24,3
s_cbranch_scc1 E0_3
E0_R:
s_and_b32 s101,s2,0xffff
s_lshl_b32 s101,s101,2
s_lshr_b32 s2,s2,14
s_and_b32 s2,s2,-4
s_getpc_b64 s[0:1]
EA_G:
s_add_u32 s0,s0,s101
s_addc_u32 s1,s1,0
s_setpc_b64 s[0:1]
)" R"(EA_R:
s_mov_b64 exec,-1
s_waitcnt vmcnt(8)
s_barrier
dr a[64:67],v196 offset:0
dr a[96:99],v197 offset:0
dt a[128:129],v192 offset:0
dt a[130:131],v192 offset:1024
dt a[160:161],v192 offset:8192
dt a[162:163],v192 offset:9216
dt a[132:133],v193 offset:0
dt a[134:135],v193 offset:1024
dt a[164:165],v193 offset:8192
dt a[166:167],v193 offset:9216
dt a[136:137],v194 offset:0
dt a[138:139],v194 offset:1024
dt a[168:169],v194 offset:8192
dt a[170:171],v194 offset:9216
dt a[140:141],v195 offset:0
dt a[142:143],v195 offset:1024
dt a[172:173],v195 offset:8192
dt a[174:175],v195 offset:9216
dr a[68:71],v196 offset:2048
dr a[100:103],v197 offset:2048
dr a[72:75],v196 offset:4096
dr a[104:107],v197 offset:4096
dr a[76:79],v196 offset:6144
dr a[108:111],v197 offset:6144
dr a[80:83],v196 offset:8192
dr a[112:115],v197 offset:8192
dr a[84:87],v196 offset:10240
dr a[116:119],v197 offset:10240
dr a[88:91],v196 offset:12288
dr a[120:123],v197 offset:12288
s_waitcnt lgkmcnt(15)
mm v[0:3],a[128:131],a[64:67],0
s_add_u32 m0,s35,32768
dr a[92:95],v196 offset:14336
bl v200,s[12:15],s32 offen offset:0 lds
mm v[0:3],a[160:163],a[96:99],v[0:3]
v_and_b32 v223,1,v221
v_lshlrev_b32 v223,4,v223
mm v[4:7],a[164:167],a[96:99],0
dr a[124:127],v197 offset:14336
v_mov_b32 v251,0x3f80
mm v[4:7],a[132:135],a[64:67],v[4:7]
mm v[8:11],a[136:139],a[64:67],0
bl v201,s[12:15],s32 offen offset:1024 lds
v_lshlrev_b32 v251,v223,v251
mm v[8:11],a[168:171],a[96:99],v[8:11]
s_waitcnt lgkmcnt(14)
dt a[144:145],v192 offset:16384
v_lshrrev_b32 v223,1,v221
mm v[12:15],a[172:175],a[96:99],0
mm v[12:15],a[140:143],a[64:67],v[12:15]
s_waitcnt lgkmcnt(2)
v_lshlrev_b32 v252,2,v222
mm v[28:31],a[140:143],a[68:71],0
dt a[146:147],v192 offset:17408
bl v202,s[12:15],s32 offen offset:2048 lds
mm v[28:31],a[172:175],a[100:103],v[28:31]
v_sub_u32 v223,v223,v252
v_cmp_eq_u32 vcc,0,v223
mm v[24:27],a[168:171],a[100:103],0
)" R"(mm v[24:27],a[136:139],a[68:71],v[24:27]
dt a[176:177],v192 offset:24576
v_cndmask_b32 v240,0,v251,vcc
mm v[20:23],a[132:135],a[68:71],0
bl v203,s[12:15],s32 offen offset:3072 lds
v_cmp_eq_u32 vcc,1,v223
mm v[20:23],a[164:167],a[100:103],v[20:23]
mm v[16:19],a[160:163],a[100:103],0
s_add_u32 m0,s35,36864
dt a[178:179],v192 offset:25600
v_cndmask_b32 v241,0,v251,vcc
mm v[16:19],a[128:131],a[68:71],v[16:19]
v_cmp_eq_u32 vcc,2,v223
mm v[32:35],a[128:131],a[72:75],0
bl v204,s[12:15],s32 offen offset:0 lds
mm v[32:35],a[160:163],a[104:107],v[32:35]
dt a[148:149],v193 offset:16384
v_cndmask_b32 v242,0,v251,vcc
mm v[36:39],a[164:167],a[104:107],0
v_cmp_eq_u32 vcc,3,v223
mm v[36:39],a[132:135],a[72:75],v[36:39]
dt a[150:151],v193 offset:17408
v_cndmask_b32 v243,0,v251,vcc
mm v[40:43],a[136:139],a[72:75],0
bl v205,s[12:15],s32 offen offset:1024 lds
mm v[40:43],a[168:171],a[104:107],v[40:43]
v_cmp_eq_u32 vcc,-8,v223
mm v[44:47],a[172:175],a[104:107],0
dt a[180:181],v193 offset:24576
v_cndmask_b32 v244,0,v251,vcc
mm v[44:47],a[140:143],a[72:75],v[44:47]
mm v[60:63],a[140:143],a[76:79],0
bl v206,s[12:15],s32 offen offset:2048 lds
v_cmp_eq_u32 vcc,-7,v223
mm v[60:63],a[172:175],a[108:111],v[60:63]
dt a[182:183],v193 offset:25600
v_cndmask_b32 v245,0,v251,vcc
mm v[56:59],a[168:171],a[108:111],0
v_cmp_eq_u32 vcc,-6,v223
mm v[56:59],a[136:139],a[76:79],v[56:59]
mm v[52:55],a[132:135],a[76:79],0
dt a[152:153],v194 offset:16384
bl v207,s[12:15],s32 offen offset:3072 lds
mm v[52:55],a[164:167],a[108:111],v[52:55]
s_add_u32 s32,s32,128
s_bitset0_b32 s32,10
v_cndmask_b32 v246,0,v251,vcc
mm v[48:51],a[160:163],a[108:111],0
v_cmp_eq_u32 vcc,-5,v223
mm v[48:51],a[128:131],a[76:79],v[48:51]
dt a[154:155],v194 offset:17408
v_cndmask_b32 v247,0,v251,vcc
mm v[64:67],a[128:131],a[80:83],0
buffer_load_ushort v224,v249,s[56:59],0 offen offset:0
aw a192,v240
mm v[64:67],a[160:163],a[112:115],v[64:67]
buffer_load_ushort v225,v249,s[56:59],0 offen offset:40
mm v[68:71],a[164:167],a[112:115],0
dt a[184:185],v194 offset:24576
buffer_load_ushort v226,v249,s[56:59],0 offen offset:80
mm v[68:71],a[132:135],a[80:83],v[68:71]
buffer_load_ushort v227,v249,s[56:59],0 offen offset:120
aw a193,v241
mm v[72:75],a[136:139],a[80:83],0
)" R"(buffer_load_ushort v228,v249,s[56:59],0 offen offset:160
aw a194,v242
mm v[72:75],a[168:171],a[112:115],v[72:75]
dt a[186:187],v194 offset:25600
aw a195,v243
mm v[76:79],a[172:175],a[112:115],0
buffer_load_ushort v229,v249,s[56:59],0 offen offset:200
buffer_load_ushort v230,v249,s[56:59],0 offen offset:240
mm v[76:79],a[140:143],a[80:83],v[76:79]
buffer_load_ushort v231,v249,s[56:59],0 offen offset:280
aw a196,v244
mm v[92:95],a[140:143],a[84:87],0
dt a[156:157],v195 offset:16384
aw a197,v245
mm v[92:95],a[172:175],a[116:119],v[92:95]
buffer_load_ushort v232,v250,s[56:59],0 offen offset:32
buffer_load_ushort v233,v250,s[56:59],0 offen offset:72
mm v[88:91],a[168:171],a[116:119],0
buffer_load_ushort v234,v250,s[56:59],0 offen offset:112
aw a198,v246
mm v[88:91],a[136:139],a[84:87],v[88:91]
dt a[158:159],v195 offset:17408
aw a199,v247
mm v[84:87],a[132:135],a[84:87],0
s_cmp_eq_u32 s61,2
s_cselect_b32 s62,-1,0
buffer_load_ushort v235,v250,s[56:59],0 offen offset:152
mm v[84:87],a[164:167],a[116:119],v[84:87]
buffer_load_ushort v236,v250,s[56:59],0 offen offset:192
buffer_load_ushort v237,v250,s[56:59],0 offen offset:232
mm v[80:83],a[160:163],a[116:119],0
dt a[188:189],v195 offset:24576
v_mov_b32 v251,s62
mm v[80:83],a[128:131],a[84:87],v[80:83]
buffer_load_ushort v238,v250,s[56:59],0 offen offset:272
v_cmp_gt_u32 vcc,4,v221
mm v[96:99],a[128:131],a[88:91],0
buffer_load_ushort v239,v250,s[56:59],0 offen offset:312
bl a[200:203],v248,s[20:23],s40 offen
mm v[96:99],a[160:163],a[120:123],v[96:99]
dt a[190:191],v195 offset:25600
v_cndmask_b32 v252,0,v251,vcc
mm v[100:103],a[164:167],a[120:123],0
s_add_u32 s40,s40,15360
v_and_b32 v240,v251,v240
mm v[100:103],a[132:135],a[88:91],v[100:103]
v_and_b32 v241,v251,v241
mm v[104:107],a[136:139],a[88:91],0
bl a[204:207],v248,s[20:23],s40 offen
v_and_b32 v242,v251,v242
mm v[104:107],a[168:171],a[120:123],v[104:107]
v_and_b32 v243,v251,v243
mm v[108:111],a[172:175],a[120:123],0
s_add_u32 s40,s40,107520
mm v[108:111],a[140:143],a[88:91],v[108:111]
v_and_b32 v244,v252,v244
mm v[124:127],a[140:143],a[92:95],0
s_waitcnt lgkmcnt(0)
bl a[208:211],v248,s[20:23],s40 offen
v_and_b32 v245,v252,v245
mm v[124:127],a[172:175],a[124:127],v[124:127]
mm v[120:123],a[168:171],a[124:127],0
s_add_u32 s40,s40,15360
v_and_b32 v246,v252,v246
)" R"(mm v[120:123],a[136:139],a[92:95],v[120:123]
v_and_b32 v247,v252,v247
mm v[116:119],a[132:135],a[92:95],0
bl a[212:215],v248,s[20:23],s40 offen
mm v[116:119],a[164:167],a[124:127],v[116:119]
mm v[112:115],a[160:163],a[124:127],0
s_waitcnt vmcnt(20) lgkmcnt(0)
s_barrier
mm v[112:115],a[128:131],a[92:95],v[112:115]
mm v[128:131],a[64:67],a[144:147],0
s_and_b32 s63,s24,1
dt a[128:129],v192 offset:32768
s_add_u32 m0,s34,0
mm v[128:131],a[96:99],a[176:179],v[128:131]
s_lshl_b32 s62,s61,1
bl v208,s[16:19],s33 offen offset:0 lds
mm v[132:135],a[96:99],a[180:183],0
s_add_u32 s62,s62,s63
mm v[132:135],a[64:67],a[148:151],v[132:135]
dt a[130:131],v192 offset:33792
s_lshl_b32 s62,s62,6
mm v[136:139],a[64:67],a[152:155],0
v_and_b32 v223,3,v221
mm v[136:139],a[96:99],a[184:187],v[136:139]
v_lshlrev_b32 v223,4,v223
mm v[140:143],a[96:99],a[188:191],0
bl v209,s[16:19],s33 offen offset:1024 lds
v_lshl_add_u32 v223,v222,8,v223
mm v[140:143],a[64:67],a[156:159],v[140:143]
dt a[160:161],v192 offset:40960
v_lshrrev_b32 v222,2,v221
mm a[12:15],a[68:71],a[156:159],0
dr a[64:67],v196 offset:32768
v_lshl_add_u32 v251,v222,10,v223
mm a[12:15],a[100:103],a[188:191],a[12:15]
v_add_u32 v251,s62,v251
mm a[8:11],a[100:103],a[184:187],0
s_lshl_b32 s62,s63,10
dt a[162:163],v192 offset:41984
mm a[8:11],a[68:71],a[152:155],a[8:11]
s_add_u32 s62,s62,5120
dr a[96:99],v197 offset:32768
bl v210,s[16:19],s33 offen offset:2048 lds
mm a[4:7],a[68:71],a[148:151],0
v_add_u32 v252,s62,v253
mm a[4:7],a[100:103],a[180:183],a[4:7]
s_cmp_eq_u32 s61,2
s_cselect_b64 vcc,-1,0
dt a[132:133],v193 offset:32768
mm a[0:3],a[100:103],a[176:179],0
v_cndmask_b32 v249,v251,v252,vcc
mm a[0:3],a[68:71],a[144:147],a[0:3]
v_add_u32 v251,0x1000,v251
mm v[144:147],a[72:75],a[144:147],0
dr a[68:71],v196 offset:34816
bl v211,s[16:19],s33 offen offset:3072 lds
mm v[144:147],a[104:107],a[176:179],v[144:147]
dt a[134:135],v193 offset:33792
v_lshl_add_u32 v252,v222,7,v223
mm v[148:151],a[104:107],a[180:183],0
s_lshl_b32 s62,s63,6
s_add_u32 s62,s62,7168
mm v[148:151],a[72:75],a[148:151],v[148:151]
s_add_u32 m0,s34,4096
)" R"(dr a[100:103],v197 offset:34816
v_add_u32 v252,s62,v252
mm v[152:155],a[72:75],a[152:155],0
dt a[164:165],v193 offset:40960
mm v[152:155],a[104:107],a[184:187],v[152:155]
bl v212,s[16:19],s33 offen offset:0 lds
v_cndmask_b32 v250,v251,v252,vcc
mm v[156:159],a[104:107],a[188:191],0
s_cmp_eq_u32 s61,2
s_cselect_b32 s62,8,4
mm v[156:159],a[72:75],a[156:159],v[156:159]
v_mov_b32 v223,0x40000000
mm a[28:31],a[76:79],a[156:159],0
dr a[72:75],v196 offset:36864
v_cmp_gt_u32 vcc,s62,v221
mm a[28:31],a[108:111],a[188:191],a[28:31]
dt a[166:167],v193 offset:41984
v_cndmask_b32 v250,v223,v250,vcc
mm a[24:27],a[108:111],a[184:187],0
s_lshl_b32 s60,s29,2
bl v213,s[16:19],s33 offen offset:1024 lds
mm a[24:27],a[76:79],a[152:155],a[24:27]
s_add_u32 s60,s60,s24
dr a[104:107],v197 offset:36864
mm a[20:23],a[76:79],a[148:151],0
s_mul_i32 s61,s60,6554
dt a[136:137],v194 offset:32768
mm a[20:23],a[108:111],a[180:183],a[20:23]
s_lshr_b32 s61,s61,16
mm a[16:19],a[108:111],a[176:179],0
dt a[138:139],v194 offset:33792
s_mul_i32 s62,s61,10
mm a[16:19],a[76:79],a[144:147],a[16:19]
s_sub_u32 s62,s60,s62
bl v214,s[16:19],s33 offen offset:2048 lds
mm v[160:163],a[80:83],a[144:147],0
s_mul_i32 s43,s61,40960
dr a[76:79],v196 offset:38912
mm v[160:163],a[112:115],a[176:179],v[160:163]
s_lshl_b32 s63,s62,7
mm v[164:167],a[112:115],a[180:183],0
s_add_u32 s43,s43,s63
dt a[168:169],v194 offset:40960
mm v[164:167],a[80:83],a[148:151],v[164:167]
s_mov_b32 s44,s61
dr a[108:111],v197 offset:38912
mm v[168:171],a[80:83],a[152:155],0
s_lshl_b32 s49,s62,1
bl v215,s[16:19],s33 offen offset:3072 lds
s_add_u32 s33,s33,0x80000
mm v[168:171],a[112:115],a[184:187],v[168:171]
s_add_u32 s61,s29,144
dt a[170:171],v194 offset:41984
s_bitset0_b32 s33,22
mm v[172:175],a[112:115],a[188:191],0
s_and_b32 s61,s61,255
mm v[172:175],a[80:83],a[156:159],v[172:175]
s_lshl_b32 s60,s61,2
s_add_u32 m0,s35,65536
mm a[44:47],a[84:87],a[156:159],0
s_add_u32 s60,s60,s24
dr a[80:83],v196 offset:40960
mm a[44:47],a[116:119],a[188:191],a[44:47]
dt a[140:141],v195 offset:32768
)" R"(bl v200,s[12:15],s32 offen offset:0 lds
mm a[40:43],a[116:119],a[184:187],0
s_mul_i32 s62,s24,48
mm a[40:43],a[84:87],a[152:155],a[40:43]
s_add_u32 s62,s62,s61
dr a[112:115],v197 offset:40960
mm a[36:39],a[84:87],a[148:151],0
dt a[142:143],v195 offset:33792
s_add_u32 s62,s62,624
mm a[36:39],a[116:119],a[180:183],a[36:39]
s_cmp_ge_u32 s61,208
s_cselect_b32 s60,s62,s60
bl v201,s[12:15],s32 offen offset:1024 lds
mm a[32:35],a[116:119],a[176:179],0
s_add_u32 s60,s60,2048
mm a[32:35],a[84:87],a[144:147],a[32:35]
s_mul_i32 s61,s60,3277
mm v[176:179],a[88:91],a[144:147],0
s_lshr_b32 s61,s61,16
dr a[84:87],v196 offset:43008
mm v[176:179],a[120:123],a[176:179],v[176:179]
s_mul_i32 s62,s61,20
bl v202,s[12:15],s32 offen offset:2048 lds
dt a[172:173],v195 offset:40960
mm v[180:183],a[120:123],a[180:183],0
s_sub_u32 s62,s60,s62
mm v[180:183],a[88:91],a[148:151],v[180:183]
s_mov_b32 s46,s61
dr a[116:119],v197 offset:43008
mm v[184:187],a[88:91],a[152:155],0
s_mov_b32 s50,s62
dt a[174:175],v195 offset:41984
mm v[184:187],a[120:123],a[184:187],v[184:187]
s_lshl_b32 s62,s62,6
bl v203,s[12:15],s32 offen offset:3072 lds
mm v[188:191],a[120:123],a[188:191],0
s_lshl_b32 s45,s61,5
mm v[188:191],a[88:91],a[156:159],v[188:191]
s_mov_b32 s47,16
s_add_u32 m0,s35,69632
mm a[60:63],a[92:95],a[156:159],0
s_mov_b32 s60,4
dr a[88:91],v196 offset:45056
mm a[60:63],a[124:127],a[188:191],a[60:63]
s_mov_b32 s63,2
bl v204,s[12:15],s32 offen offset:0 lds
mm a[56:59],a[124:127],a[184:187],0
s_mul_i32 s101,s61,40
mm a[56:59],a[92:95],a[152:155],a[56:59]
s_sub_u32 s101,s101,5120
dr a[120:123],v197 offset:45056
mm a[52:55],a[92:95],a[148:151],0
s_cmp_ge_u32 s61,128
s_cselect_b32 s45,s101,s45
s_cselect_b32 s47,4,s47
s_cselect_b32 s60,10,s60
s_cselect_b32 s52,s70,s82
s_cselect_b32 s53,s71,s83
s_mov_b32 s51,0x500000
s_cselect_b32 s51,0xc8000,s51
mm a[52:55],a[124:127],a[180:183],a[52:55]
E0_0:
s_cbranch_execz E0_4
s_mul_i32 s101,s61,160
)" R"(bl v205,s[12:15],s32 offen offset:1024 lds
mm a[48:51],a[124:127],a[176:179],0
mm a[48:51],a[92:95],a[144:147],a[48:51]
s_waitcnt lgkmcnt(1)
mm v[0:3],a[128:131],a[64:67],v[0:3]
s_sub_u32 s101,s101,23032
dr a[92:95],v196 offset:47104
mm v[0:3],a[160:163],a[96:99],v[0:3]
bl v206,s[12:15],s32 offen offset:2048 lds
mm v[4:7],a[164:167],a[96:99],v[4:7]
s_cmp_ge_u32 s61,144
s_cselect_b32 s45,s101,s45
s_cselect_b32 s47,80,s47
s_cselect_b32 s63,40,s63
mm v[4:7],a[132:135],a[64:67],v[4:7]
s_mul_i32 s45,s45,1280
dr a[124:127],v197 offset:47104
mm v[8:11],a[136:139],a[64:67],v[8:11]
s_add_u32 s45,s45,s62
mm v[8:11],a[168:171],a[96:99],v[8:11]
bl v207,s[12:15],s32 offen offset:3072 lds
s_add_u32 s32,s32,128
mm v[12:15],a[172:175],a[96:99],v[12:15]
s_mul_i32 s47,s47,1280
dt a[144:145],v192 offset:49152
s_bitset0_b32 s32,10
mm v[12:15],a[140:143],a[64:67],v[12:15]
s_add_u32 m0,s35,0
mm v[28:31],a[140:143],a[68:71],v[28:31]
s_cmp_ge_u32 s61,148
s_cselect_b32 s45,0x80000000,s45
dt a[146:147],v192 offset:50176
mm v[28:31],a[172:175],a[100:103],v[28:31]
bl v200,s[12:15],s32 offen offset:0 lds
v_mul_u32_u24 v252,s60,v222
mm v[24:27],a[168:171],a[100:103],v[24:27]
mm v[24:27],a[136:139],a[68:71],v[24:27]
dt a[176:177],v192 offset:57344
v_bfe_u32 v223,v221,1,1
mm v[20:23],a[132:135],a[68:71],v[20:23]
v_mad_u32_u24 v252,s63,v223,v252
mm v[20:23],a[164:167],a[100:103],v[20:23]
bl v201,s[12:15],s32 offen offset:1024 lds
mm v[16:19],a[160:163],a[100:103],v[16:19]
dt a[178:179],v192 offset:58368
v_and_b32 v223,1,v221
mm v[16:19],a[128:131],a[68:71],v[16:19]
mm v[32:35],a[128:131],a[72:75],v[32:35]
v_add_u32 v252,v252,v223
mm v[32:35],a[160:163],a[104:107],v[32:35]
dt a[148:149],v193 offset:49152
bl v202,s[12:15],s32 offen offset:2048 lds
mm v[36:39],a[164:167],a[104:107],v[36:39]
v_and_b32 v223,48,v220
mm v[36:39],a[132:135],a[72:75],v[36:39]
dt a[150:151],v193 offset:50176
s_movk_i32 s60,1280
mm v[40:43],a[136:139],a[72:75],v[40:43]
v_mad_u32_u24 v252,s60,v252,v223
mm v[40:43],a[168:171],a[104:107],v[40:43]
bl v203,s[12:15],s32 offen offset:3072 lds
mm v[44:47],a[172:175],a[104:107],v[44:47]
dt a[180:181],v193 offset:57344
v_mad_u32_u24 v251,s60,v221,v223
)" R"(mm v[44:47],a[140:143],a[72:75],v[44:47]
s_add_u32 m0,s35,4096
mm v[60:63],a[140:143],a[76:79],v[60:63]
s_mov_b32 s20,s82
mm v[60:63],a[172:175],a[108:111],v[60:63]
s_and_b32 s21,s83,0xffff
dt a[182:183],v193 offset:58368
bl v204,s[12:15],s32 offen offset:0 lds
mm v[56:59],a[168:171],a[108:111],v[56:59]
mm v[56:59],a[136:139],a[76:79],v[56:59]
s_mov_b32 s22,0x500000
mm v[52:55],a[132:135],a[76:79],v[52:55]
s_mov_b32 s23,0x20000
dt a[152:153],v194 offset:49152
mm v[52:55],a[164:167],a[108:111],v[52:55]
bl v205,s[12:15],s32 offen offset:1024 lds
mm v[48:51],a[160:163],a[108:111],v[48:51]
s_mov_b32 s56,s52
mm v[48:51],a[128:131],a[76:79],v[48:51]
dt a[154:155],v194 offset:50176
mm v[64:67],a[128:131],a[80:83],v[64:67]
s_and_b32 s57,s53,0xffff
mm v[64:67],a[160:163],a[112:115],v[64:67]
s_mov_b32 s58,s51
bl v206,s[12:15],s32 offen offset:2048 lds
mm v[68:71],a[164:167],a[112:115],v[68:71]
dt a[184:185],v194 offset:57344
mm v[68:71],a[132:135],a[80:83],v[68:71]
s_waitcnt vmcnt(41)
v_lshl_or_b32 v224,v225,16,v224
mm v[72:75],a[136:139],a[80:83],v[72:75]
dt a[186:187],v194 offset:58368
v_or_b32 v240,v240,v224
mm v[72:75],a[168:171],a[112:115],v[72:75]
bl v207,s[12:15],s32 offen offset:3072 lds
s_add_u32 s32,s32,128
mm v[76:79],a[172:175],a[112:115],v[76:79]
s_waitcnt vmcnt(40)
v_lshl_or_b32 v226,v227,16,v226
s_bitset0_b32 s32,10
mm v[76:79],a[140:143],a[80:83],v[76:79]
dt a[156:157],v195 offset:49152
mm v[92:95],a[140:143],a[84:87],v[92:95]
v_or_b32 v241,v241,v226
mm v[92:95],a[172:175],a[116:119],v[92:95]
s_waitcnt vmcnt(38)
v_lshl_or_b32 v228,v229,16,v228
mm v[88:91],a[168:171],a[116:119],v[88:91]
dt a[158:159],v195 offset:50176
mm v[88:91],a[136:139],a[84:87],v[88:91]
v_or_b32 v242,v242,v228
mm v[84:87],a[132:135],a[84:87],v[84:87]
s_waitcnt vmcnt(36)
v_lshl_or_b32 v230,v231,16,v230
mm v[84:87],a[164:167],a[116:119],v[84:87]
dt a[188:189],v195 offset:57344
mm v[80:83],a[160:163],a[116:119],v[80:83]
v_or_b32 v243,v243,v230
mm v[80:83],a[128:131],a[84:87],v[80:83]
mm v[96:99],a[128:131],a[88:91],v[96:99]
s_waitcnt lgkmcnt(2)
dt a[190:191],v195 offset:58368
s_waitcnt vmcnt(34)
v_lshl_or_b32 v232,v233,16,v232
)" R"(mm v[96:99],a[160:163],a[120:123],v[96:99]
v_or_b32 v244,v244,v232
mm v[100:103],a[164:167],a[120:123],v[100:103]
mm v[100:103],a[132:135],a[88:91],v[100:103]
s_waitcnt vmcnt(32)
v_lshl_or_b32 v234,v235,16,v234
mm v[104:107],a[136:139],a[88:91],v[104:107]
v_or_b32 v245,v245,v234
mm v[104:107],a[168:171],a[120:123],v[104:107]
mm v[108:111],a[172:175],a[120:123],v[108:111]
s_waitcnt vmcnt(30)
v_lshl_or_b32 v236,v237,16,v236
mm v[108:111],a[140:143],a[88:91],v[108:111]
mm v[124:127],a[140:143],a[92:95],v[124:127]
v_or_b32 v246,v246,v236
mm v[124:127],a[172:175],a[124:127],v[124:127]
s_waitcnt vmcnt(28)
v_lshl_or_b32 v238,v239,16,v238
mm v[120:123],a[168:171],a[124:127],v[120:123]
mm v[120:123],a[136:139],a[92:95],v[120:123]
v_or_b32 v247,v247,v238
mm v[116:119],a[132:135],a[92:95],v[116:119]
mm v[116:119],a[164:167],a[124:127],v[116:119]
mm v[112:115],a[160:163],a[124:127],v[112:115]
s_waitcnt vmcnt(8) lgkmcnt(0)
s_barrier
mm v[112:115],a[128:131],a[92:95],v[112:115]
mm v[128:131],a[64:67],a[144:147],v[128:131]
dt a[128:129],v192 offset:0
s_add_u32 m0,s34,32768
mm v[128:131],a[96:99],a[176:179],v[128:131]
bl v208,s[16:19],s33 offen offset:0 lds
mm v[132:135],a[96:99],a[180:183],v[132:135]
mm v[132:135],a[64:67],a[148:151],v[132:135]
dt a[130:131],v192 offset:1024
mm v[136:139],a[64:67],a[152:155],v[136:139]
mm v[136:139],a[96:99],a[184:187],v[136:139]
mm v[140:143],a[96:99],a[188:191],v[140:143]
dt a[160:161],v192 offset:8192
bl v209,s[16:19],s33 offen offset:1024 lds
mm v[140:143],a[64:67],a[156:159],v[140:143]
mm a[12:15],a[68:71],a[156:159],a[12:15]
dr a[64:67],v198 offset:0
mm a[12:15],a[100:103],a[188:191],a[12:15]
mm a[8:11],a[100:103],a[184:187],a[8:11]
dt a[162:163],v192 offset:9216
mm a[8:11],a[68:71],a[152:155],a[8:11]
dr a[96:99],v199 offset:0
bl v210,s[16:19],s33 offen offset:2048 lds
mm a[4:7],a[68:71],a[148:151],a[4:7]
mm a[4:7],a[100:103],a[180:183],a[4:7]
dt a[132:133],v193 offset:0
mm a[0:3],a[100:103],a[176:179],a[0:3]
mm a[0:3],a[68:71],a[144:147],a[0:3]
mm v[144:147],a[72:75],a[144:147],v[144:147]
dr a[68:71],v198 offset:2048
bl v211,s[16:19],s33 offen offset:3072 lds
mm v[144:147],a[104:107],a[176:179],v[144:147]
dt a[134:135],v193 offset:1024
mm v[148:151],a[104:107],a[180:183],v[148:151]
mm v[148:151],a[72:75],a[148:151],v[148:151]
s_add_u32 m0,s34,36864
dr a[100:103],v199 offset:2048
mm v[152:155],a[72:75],a[152:155],v[152:155]
)" R"(dt a[164:165],v193 offset:8192
mm v[152:155],a[104:107],a[184:187],v[152:155]
bl v212,s[16:19],s33 offen offset:0 lds
mm v[156:159],a[104:107],a[188:191],v[156:159]
mm v[156:159],a[72:75],a[156:159],v[156:159]
mm a[28:31],a[76:79],a[156:159],a[28:31]
dr a[72:75],v198 offset:4096
mm v[224:227],a[200:203],v[240:243],0
dt a[166:167],v193 offset:9216
mm v[228:231],a[204:207],v[240:243],0
bl v213,s[16:19],s33 offen offset:1024 lds
mm a[28:31],a[108:111],a[188:191],a[28:31]
dr a[104:107],v199 offset:4096
mm a[24:27],a[108:111],a[184:187],a[24:27]
dt a[136:137],v194 offset:0
s_nop 0
v_mul_f32 v224,s42,v224
mm a[24:27],a[76:79],a[152:155],a[24:27]
v_mul_f32 v225,s42,v225
mm a[20:23],a[76:79],a[148:151],a[20:23]
dt a[138:139],v194 offset:1024
v_mul_f32 v226,s42,v226
mm a[20:23],a[108:111],a[180:183],a[20:23]
bl v214,s[16:19],s33 offen offset:2048 lds
v_mul_f32 v227,s42,v227
mm a[16:19],a[108:111],a[176:179],a[16:19]
v_mul_f32 v228,s42,v228
mm a[16:19],a[76:79],a[144:147],a[16:19]
mm v[160:163],a[80:83],a[144:147],v[160:163]
dr a[76:79],v198 offset:6144
v_mul_f32 v229,s42,v229
mm v[160:163],a[112:115],a[176:179],v[160:163]
dt a[168:169],v194 offset:8192
v_mul_f32 v230,s42,v230
mm v[164:167],a[112:115],a[180:183],v[164:167]
bl v215,s[16:19],s33 offen offset:3072 lds
s_add_u32 s33,s33,0x80000
mm v[164:167],a[80:83],a[148:151],v[164:167]
v_mul_f32 v231,s42,v231
s_bitset0_b32 s33,22
mm v[168:171],a[80:83],a[152:155],v[168:171]
dr a[108:111],v199 offset:6144
cv v224,v224,v225
mm v[168:171],a[112:115],a[184:187],v[168:171]
s_add_u32 m0,s35,32768
dt a[170:171],v194 offset:9216
cv v225,v226,v227
mm v[172:175],a[112:115],a[188:191],v[172:175]
cv v226,v228,v229
mm v[172:175],a[80:83],a[156:159],v[172:175]
dt a[140:141],v195 offset:0
bl v200,s[12:15],s32 offen offset:0 lds
mm a[44:47],a[84:87],a[156:159],a[44:47]
dr a[80:83],v198 offset:8192
cv v227,v230,v231
mm a[44:47],a[116:119],a[188:191],a[44:47]
bs v[224:227],v249,s[8:11],s41 offen sc1
mm a[40:43],a[116:119],a[184:187],a[40:43]
dt a[142:143],v195 offset:1024
mm v[232:235],a[200:203],v[244:247],0
dr a[112:115],v199 offset:8192
bl v201,s[12:15],s32 offen offset:1024 lds
mm v[236:239],a[204:207],v[244:247],0
mm a[40:43],a[84:87],a[152:155],a[40:43]
)" R"(mm a[36:39],a[84:87],a[148:151],a[36:39]
dt a[172:173],v195 offset:8192
s_nop 1
v_mul_f32 v232,s42,v232
mm a[36:39],a[116:119],a[180:183],a[36:39]
bl v202,s[12:15],s32 offen offset:2048 lds
v_mul_f32 v233,s42,v233
mm a[32:35],a[116:119],a[176:179],a[32:35]
v_mul_f32 v234,s42,v234
mm a[32:35],a[84:87],a[144:147],a[32:35]
dt a[174:175],v195 offset:9216
v_mul_f32 v235,s42,v235
mm v[176:179],a[88:91],a[144:147],v[176:179]
dr a[84:87],v198 offset:10240
v_mul_f32 v236,s42,v236
mm v[176:179],a[120:123],a[176:179],v[176:179]
bl v203,s[12:15],s32 offen offset:3072 lds
v_mul_f32 v237,s42,v237
mm v[180:183],a[120:123],a[180:183],v[180:183]
v_mul_f32 v238,s42,v238
mm v[180:183],a[88:91],a[148:151],v[180:183]
s_add_u32 m0,s35,36864
dr a[116:119],v199 offset:10240
mm v[184:187],a[88:91],a[152:155],v[184:187]
v_mul_f32 v239,s42,v239
mm v[184:187],a[120:123],a[184:187],v[184:187]
bl v204,s[12:15],s32 offen offset:0 lds
cv v232,v232,v233
mm v[188:191],a[120:123],a[188:191],v[188:191]
cv v233,v234,v235
mm v[188:191],a[88:91],a[156:159],v[188:191]
cv v234,v236,v237
mm a[60:63],a[92:95],a[156:159],a[60:63]
dr a[88:91],v198 offset:12288
cv v235,v238,v239
mm a[60:63],a[124:127],a[188:191],a[60:63]
bs v[232:235],v250,s[8:11],s41 offen sc1
bl v205,s[12:15],s32 offen offset:1024 lds
mm a[56:59],a[124:127],a[184:187],a[56:59]
s_add_u32 s41,s41,8192
mm a[56:59],a[92:95],a[152:155],a[56:59]
dr a[120:123],v199 offset:12288
mm a[52:55],a[92:95],a[148:151],a[52:55]
mm a[52:55],a[124:127],a[180:183],a[52:55]
bl v206,s[12:15],s32 offen offset:2048 lds
mm a[48:51],a[124:127],a[176:179],a[48:51]
mm a[48:51],a[92:95],a[144:147],a[48:51]
s_waitcnt lgkmcnt(1)
mm v[0:3],a[128:131],a[64:67],v[0:3]
dr a[92:95],v198 offset:14336
mm v[0:3],a[160:163],a[96:99],v[0:3]
bl v207,s[12:15],s32 offen offset:3072 lds
s_add_u32 s32,s32,128
mm v[4:7],a[164:167],a[96:99],v[4:7]
dr a[124:127],v199 offset:14336
s_bitset0_b32 s32,10
mm v[224:227],a[208:211],v[240:243],0
mm v[228:231],a[212:215],v[240:243],0
mm v[4:7],a[132:135],a[64:67],v[4:7]
s_bitcmp0_b32 s26,0
s_cbranch_scc0 CND1
bl a[216:219],v251,s[20:23],s43 offen offset:0
bl a[224:227],v251,s[20:23],s43 offen offset:64
bl a[232:235],v252,s[56:59],s45 offen
)" R"(s_add_u32 s43,s43,20480
s_add_u32 s45,s45,s47
bl a[220:223],v251,s[20:23],s43 offen offset:0
bl a[228:231],v251,s[20:23],s43 offen offset:64
bl a[236:239],v252,s[56:59],s45 offen
CND1:
mm v[8:11],a[136:139],a[64:67],v[8:11]
dt a[144:145],v192 offset:16384
v_mul_f32 v224,s42,v224
mm v[8:11],a[168:171],a[96:99],v[8:11]
v_mul_f32 v225,s42,v225
mm v[12:15],a[172:175],a[96:99],v[12:15]
dt a[146:147],v192 offset:17408
mm v[12:15],a[140:143],a[64:67],v[12:15]
v_mul_f32 v226,s42,v226
mm v[28:31],a[140:143],a[68:71],v[28:31]
mm v[28:31],a[172:175],a[100:103],v[28:31]
dt a[176:177],v192 offset:24576
v_mul_f32 v227,s42,v227
mm v[24:27],a[168:171],a[100:103],v[24:27]
v_mul_f32 v228,s42,v228
mm v[24:27],a[136:139],a[68:71],v[24:27]
mm v[20:23],a[132:135],a[68:71],v[20:23]
dt a[178:179],v192 offset:25600
v_mul_f32 v229,s42,v229
mm v[20:23],a[164:167],a[100:103],v[20:23]
mm v[16:19],a[160:163],a[100:103],v[16:19]
v_mul_f32 v230,s42,v230
mm v[16:19],a[128:131],a[68:71],v[16:19]
dt a[148:149],v193 offset:16384
v_mul_f32 v231,s42,v231
mm v[32:35],a[128:131],a[72:75],v[32:35]
mm v[32:35],a[160:163],a[104:107],v[32:35]
dt a[150:151],v193 offset:17408
cv v224,v224,v225
mm v[36:39],a[164:167],a[104:107],v[36:39]
mm v[36:39],a[132:135],a[72:75],v[36:39]
cv v225,v226,v227
mm v[40:43],a[136:139],a[72:75],v[40:43]
dt a[180:181],v193 offset:24576
cv v226,v228,v229
mm v[40:43],a[168:171],a[104:107],v[40:43]
mm v[44:47],a[172:175],a[104:107],v[44:47]
cv v227,v230,v231
mm v[44:47],a[140:143],a[72:75],v[44:47]
dt a[182:183],v193 offset:25600
mm v[60:63],a[140:143],a[76:79],v[60:63]
bs v[224:227],v249,s[8:11],s41 offen sc1
mm v[60:63],a[172:175],a[108:111],v[60:63]
mm v[56:59],a[168:171],a[108:111],v[56:59]
dt a[152:153],v194 offset:16384
mm v[232:235],a[208:211],v[244:247],0
mm v[236:239],a[212:215],v[244:247],0
mm v[56:59],a[136:139],a[76:79],v[56:59]
dt a[154:155],v194 offset:17408
mm v[52:55],a[132:135],a[76:79],v[52:55]
s_nop 3
v_mul_f32 v232,s42,v232
mm v[52:55],a[164:167],a[108:111],v[52:55]
v_mul_f32 v233,s42,v233
mm v[48:51],a[160:163],a[108:111],v[48:51]
dt a[184:185],v194 offset:24576
mm v[48:51],a[128:131],a[76:79],v[48:51]
v_mul_f32 v234,s42,v234
)" R"(mm v[64:67],a[128:131],a[80:83],v[64:67]
dt a[186:187],v194 offset:25600
mm v[64:67],a[160:163],a[112:115],v[64:67]
v_mul_f32 v235,s42,v235
mm v[68:71],a[164:167],a[112:115],v[68:71]
v_mul_f32 v236,s42,v236
mm v[68:71],a[132:135],a[80:83],v[68:71]
dt a[156:157],v195 offset:16384
mm v[72:75],a[136:139],a[80:83],v[72:75]
v_mul_f32 v237,s42,v237
mm v[72:75],a[168:171],a[112:115],v[72:75]
mm v[76:79],a[172:175],a[112:115],v[76:79]
dt a[158:159],v195 offset:17408
v_mul_f32 v238,s42,v238
mm v[76:79],a[140:143],a[80:83],v[76:79]
v_mul_f32 v239,s42,v239
mm v[92:95],a[140:143],a[84:87],v[92:95]
mm v[92:95],a[172:175],a[116:119],v[92:95]
dt a[188:189],v195 offset:24576
cv v232,v232,v233
mm v[88:91],a[168:171],a[116:119],v[88:91]
mm v[88:91],a[136:139],a[84:87],v[88:91]
cv v233,v234,v235
mm v[84:87],a[132:135],a[84:87],v[84:87]
dt a[190:191],v195 offset:25600
cv v234,v236,v237
mm v[84:87],a[164:167],a[116:119],v[84:87]
mm v[80:83],a[160:163],a[116:119],v[80:83]
cv v235,v238,v239
mm v[80:83],a[128:131],a[84:87],v[80:83]
mm v[96:99],a[128:131],a[88:91],v[96:99]
s_waitcnt lgkmcnt(1)
bs v[232:235],v250,s[8:11],s41 offen sc1
mm v[96:99],a[160:163],a[120:123],v[96:99]
mm v[100:103],a[164:167],a[120:123],v[100:103]
mm v[100:103],a[132:135],a[88:91],v[100:103]
mm v[104:107],a[136:139],a[88:91],v[104:107]
mm v[104:107],a[168:171],a[120:123],v[104:107]
mm v[108:111],a[172:175],a[120:123],v[108:111]
mm v[108:111],a[140:143],a[88:91],v[108:111]
mm v[124:127],a[140:143],a[92:95],v[124:127]
mm v[124:127],a[172:175],a[124:127],v[124:127]
mm v[120:123],a[168:171],a[124:127],v[120:123]
mm v[120:123],a[136:139],a[92:95],v[120:123]
mm v[116:119],a[132:135],a[92:95],v[116:119]
mm v[116:119],a[164:167],a[124:127],v[116:119]
mm v[112:115],a[160:163],a[124:127],v[112:115]
s_waitcnt vmcnt(12) lgkmcnt(0)
s_barrier
mm v[112:115],a[128:131],a[92:95],v[112:115]
mm v[128:131],a[64:67],a[144:147],v[128:131]
s_mul_i32 s60,s49,52
dt a[128:129],v192 offset:32768
s_add_u32 m0,s34,0
mm v[128:131],a[96:99],a[176:179],v[128:131]
s_lshr_b32 s60,s60,8
bl v208,s[16:19],s33 offen offset:0 lds
mm v[132:135],a[96:99],a[180:183],v[132:135]
mm v[132:135],a[64:67],a[148:151],v[132:135]
s_mul_i32 s61,s60,5
dt a[130:131],v192 offset:33792
mm v[136:139],a[64:67],a[152:155],v[136:139]
mm v[136:139],a[96:99],a[184:187],v[136:139]
s_sub_u32 s61,s49,s61
)" R"(mm v[140:143],a[96:99],a[188:191],v[140:143]
dt a[160:161],v192 offset:40960
bl v209,s[16:19],s33 offen offset:1024 lds
mm v[140:143],a[64:67],a[156:159],v[140:143]
s_lshl_b32 s62,s60,2
mm a[12:15],a[68:71],a[156:159],a[12:15]
s_sub_u32 s62,s44,s62
dr a[64:67],v196 offset:0
mm a[12:15],a[100:103],a[188:191],a[12:15]
mm a[8:11],a[100:103],a[184:187],a[8:11]
s_and_b32 s62,s62,15
dt a[162:163],v192 offset:41984
mm a[8:11],a[68:71],a[152:155],a[8:11]
dr a[96:99],v197 offset:0
bl v210,s[16:19],s33 offen offset:2048 lds
mm a[4:7],a[68:71],a[148:151],a[4:7]
s_lshr_b32 s63,s44,4
mm a[4:7],a[100:103],a[180:183],a[4:7]
dt a[132:133],v193 offset:32768
mm a[0:3],a[100:103],a[176:179],a[0:3]
s_lshl_b32 s63,s63,2
mm a[0:3],a[68:71],a[144:147],a[0:3]
s_add_u32 s60,s60,s63
mm v[144:147],a[72:75],a[144:147],v[144:147]
dr a[68:71],v196 offset:2048
bl v211,s[16:19],s33 offen offset:3072 lds
mm v[144:147],a[104:107],a[176:179],v[144:147]
s_mul_i32 s60,s60,80
dt a[134:135],v193 offset:33792
mm v[148:151],a[104:107],a[180:183],v[148:151]
mm v[148:151],a[72:75],a[148:151],v[148:151]
s_add_u32 m0,s34,4096
s_lshl_b32 s63,s61,4
dr a[100:103],v197 offset:2048
mm v[152:155],a[72:75],a[152:155],v[152:155]
dt a[164:165],v193 offset:40960
mm v[152:155],a[104:107],a[184:187],v[152:155]
s_add_u32 s63,s63,s62
bl v212,s[16:19],s33 offen offset:0 lds
mm v[156:159],a[104:107],a[188:191],v[156:159]
s_add_u32 s60,s60,s63
mm v[156:159],a[72:75],a[156:159],v[156:159]
mm a[28:31],a[76:79],a[156:159],a[28:31]
s_lshl_b32 s101,s60,1
dr a[72:75],v196 offset:4096
mm a[28:31],a[108:111],a[188:191],a[28:31]
dt a[166:167],v193 offset:41984
mm a[24:27],a[108:111],a[184:187],a[24:27]
s_add_u32 s49,s49,1
bl v213,s[16:19],s33 offen offset:1024 lds
mm a[24:27],a[76:79],a[152:155],a[24:27]
dr a[104:107],v197 offset:4096
mm a[20:23],a[76:79],a[148:151],a[20:23]
s_mul_i32 s60,s49,52
dt a[136:137],v194 offset:32768
mm a[20:23],a[108:111],a[180:183],a[20:23]
s_lshr_b32 s60,s60,8
mm a[16:19],a[108:111],a[176:179],a[16:19]
dt a[138:139],v194 offset:33792
mm a[16:19],a[76:79],a[144:147],a[16:19]
s_mul_i32 s61,s60,5
bl v214,s[16:19],s33 offen offset:2048 lds
mm v[160:163],a[80:83],a[144:147],v[160:163]
dr a[76:79],v196 offset:6144
)" R"(mm v[160:163],a[112:115],a[176:179],v[160:163]
s_sub_u32 s61,s49,s61
mm v[164:167],a[112:115],a[180:183],v[164:167]
dt a[168:169],v194 offset:40960
mm v[164:167],a[80:83],a[148:151],v[164:167]
s_lshl_b32 s62,s60,2
dr a[108:111],v197 offset:6144
mm v[168:171],a[80:83],a[152:155],v[168:171]
s_sub_u32 s62,s44,s62
bl v215,s[16:19],s33 offen offset:3072 lds
s_add_u32 s33,s33,0x80000
mm v[168:171],a[112:115],a[184:187],v[168:171]
dt a[170:171],v194 offset:41984
s_bitset0_b32 s33,22
mm v[172:175],a[112:115],a[188:191],v[172:175]
s_and_b32 s62,s62,15
mm v[172:175],a[80:83],a[156:159],v[172:175]
s_add_u32 m0,s35,65536
mm a[44:47],a[84:87],a[156:159],a[44:47]
s_lshr_b32 s63,s44,4
dr a[80:83],v196 offset:8192
mm a[44:47],a[116:119],a[188:191],a[44:47]
dt a[140:141],v195 offset:32768
bl v200,s[12:15],s32 offen offset:0 lds
mm a[40:43],a[116:119],a[184:187],a[40:43]
s_lshl_b32 s63,s63,2
mm a[40:43],a[84:87],a[152:155],a[40:43]
s_add_u32 s60,s60,s63
dr a[112:115],v197 offset:8192
mm a[36:39],a[84:87],a[148:151],a[36:39]
dt a[142:143],v195 offset:33792
mm a[36:39],a[116:119],a[180:183],a[36:39]
s_mul_i32 s60,s60,80
bl v201,s[12:15],s32 offen offset:1024 lds
mm a[32:35],a[116:119],a[176:179],a[32:35]
mm a[32:35],a[84:87],a[144:147],a[32:35]
s_lshl_b32 s63,s61,4
mm v[176:179],a[88:91],a[144:147],v[176:179]
dr a[84:87],v196 offset:10240
mm v[176:179],a[120:123],a[176:179],v[176:179]
s_add_u32 s63,s63,s62
bl v202,s[12:15],s32 offen offset:2048 lds
dt a[172:173],v195 offset:40960
mm v[180:183],a[120:123],a[180:183],v[180:183]
s_add_u32 s60,s60,s63
mm v[180:183],a[88:91],a[148:151],v[180:183]
dr a[116:119],v197 offset:10240
mm v[184:187],a[88:91],a[152:155],v[184:187]
s_lshl_b32 s49,s60,1
dt a[174:175],v195 offset:41984
mm v[184:187],a[120:123],a[184:187],v[184:187]
bl v203,s[12:15],s32 offen offset:3072 lds
mm v[188:191],a[120:123],a[188:191],v[188:191]
E0_1:
s_cbranch_execz E0_5
s_mov_b32 s44,s101
mm v[188:191],a[88:91],a[156:159],v[188:191]
s_add_u32 m0,s35,69632
mm a[60:63],a[92:95],a[156:159],a[60:63]
s_lshl_b32 s44,s44,10
dr a[88:91],v196 offset:12288
mm a[60:63],a[124:127],a[188:191],a[60:63]
s_add_u32 s44,s44,0x3000000
bl v204,s[12:15],s32 offen offset:0 lds
)" R"(mm a[56:59],a[124:127],a[184:187],a[56:59]
mm a[56:59],a[92:95],a[152:155],a[56:59]
s_lshl_b32 s49,s49,10
dr a[120:123],v197 offset:12288
mm a[52:55],a[92:95],a[148:151],a[52:55]
mm a[52:55],a[124:127],a[180:183],a[52:55]
s_add_u32 s49,s49,0x3000000
bl v205,s[12:15],s32 offen offset:1024 lds
mm a[48:51],a[124:127],a[176:179],a[48:51]
mm a[48:51],a[92:95],a[144:147],a[48:51]
s_waitcnt lgkmcnt(1)
mm v[0:3],a[128:131],a[64:67],v[0:3]
s_mul_i32 s60,s50,52
dr a[92:95],v196 offset:14336
mm v[0:3],a[160:163],a[96:99],v[0:3]
s_lshr_b32 s60,s60,8
bl v206,s[12:15],s32 offen offset:2048 lds
mm v[4:7],a[164:167],a[96:99],v[4:7]
dr a[124:127],v197 offset:14336
mm v[4:7],a[132:135],a[64:67],v[4:7]
s_mul_i32 s61,s60,5
mm v[8:11],a[136:139],a[64:67],v[8:11]
mm v[8:11],a[168:171],a[96:99],v[8:11]
s_sub_u32 s61,s50,s61
bl v207,s[12:15],s32 offen offset:3072 lds
s_add_u32 s32,s32,128
mm v[12:15],a[172:175],a[96:99],v[12:15]
dt a[144:145],v192 offset:49152
s_bitset0_b32 s32,10
mm v[12:15],a[140:143],a[64:67],v[12:15]
s_lshl_b32 s62,s60,2
mm v[28:31],a[140:143],a[68:71],v[28:31]
s_sub_u32 s62,s46,s62
dt a[146:147],v192 offset:50176
mm v[28:31],a[172:175],a[100:103],v[28:31]
s_bitcmp1_b32 s26,0
s_cbranch_scc0 CND2
bl a[216:219],v251,s[20:23],s43 offen offset:0
bl a[224:227],v251,s[20:23],s43 offen offset:64
bl a[232:235],v252,s[56:59],s45 offen
s_add_u32 s43,s43,20480
s_add_u32 s45,s45,s47
bl a[220:223],v251,s[20:23],s43 offen offset:0
bl a[228:231],v251,s[20:23],s43 offen offset:64
bl a[236:239],v252,s[56:59],s45 offen
CND2:
mm v[24:27],a[168:171],a[100:103],v[24:27]
s_and_b32 s62,s62,15
mm v[24:27],a[136:139],a[68:71],v[24:27]
dt a[176:177],v192 offset:57344
mm v[20:23],a[132:135],a[68:71],v[20:23]
s_lshr_b32 s63,s46,4
mm v[20:23],a[164:167],a[100:103],v[20:23]
mm v[16:19],a[160:163],a[100:103],v[16:19]
s_lshl_b32 s63,s63,2
dt a[178:179],v192 offset:58368
mm v[16:19],a[128:131],a[68:71],v[16:19]
mm v[32:35],a[128:131],a[72:75],v[32:35]
s_add_u32 s60,s60,s63
mm v[32:35],a[160:163],a[104:107],v[32:35]
s_mul_i32 s60,s60,80
dt a[148:149],v193 offset:49152
mm v[36:39],a[164:167],a[104:107],v[36:39]
mm v[36:39],a[132:135],a[72:75],v[36:39]
)" R"(dt a[150:151],v193 offset:50176
s_lshl_b32 s63,s61,4
mm v[40:43],a[136:139],a[72:75],v[40:43]
mm v[40:43],a[168:171],a[104:107],v[40:43]
s_add_u32 s63,s63,s62
mm v[44:47],a[172:175],a[104:107],v[44:47]
dt a[180:181],v193 offset:57344
mm v[44:47],a[140:143],a[72:75],v[44:47]
s_add_u32 s60,s60,s63
mm v[60:63],a[140:143],a[76:79],v[60:63]
mm v[60:63],a[172:175],a[108:111],v[60:63]
dt a[182:183],v193 offset:58368
s_lshl_b32 s101,s60,1
mm v[56:59],a[168:171],a[108:111],v[56:59]
s_lshl_b32 s60,s50,1
mm v[56:59],a[136:139],a[76:79],v[56:59]
mm v[52:55],a[132:135],a[76:79],v[52:55]
s_mul_i32 s62,s46,5
dt a[152:153],v194 offset:49152
mm v[52:55],a[164:167],a[108:111],v[52:55]
mm v[48:51],a[160:163],a[108:111],v[48:51]
s_add_u32 s62,s62,s60
mm v[48:51],a[128:131],a[76:79],v[48:51]
dt a[154:155],v194 offset:50176
mm v[64:67],a[128:131],a[80:83],v[64:67]
s_add_u32 s62,s62,4480
mm v[64:67],a[160:163],a[112:115],v[64:67]
mm v[68:71],a[164:167],a[112:115],v[68:71]
s_mul_i32 s60,s60,52
dt a[184:185],v194 offset:57344
mm v[68:71],a[132:135],a[80:83],v[68:71]
s_add_u32 s61,s60,52
mm v[72:75],a[136:139],a[80:83],v[72:75]
dt a[186:187],v194 offset:58368
mm v[72:75],a[168:171],a[112:115],v[72:75]
s_lshr_b32 s60,s60,8
mm v[76:79],a[172:175],a[112:115],v[76:79]
mm v[76:79],a[140:143],a[80:83],v[76:79]
dt a[156:157],v195 offset:49152
s_lshr_b32 s61,s61,8
mm v[92:95],a[140:143],a[84:87],v[92:95]
mm v[92:95],a[172:175],a[116:119],v[92:95]
s_mul_i32 s60,s60,95
mm v[88:91],a[168:171],a[116:119],v[88:91]
dt a[158:159],v195 offset:50176
mm v[88:91],a[136:139],a[84:87],v[88:91]
s_mul_i32 s61,s61,95
mm v[84:87],a[132:135],a[84:87],v[84:87]
s_add_u32 s60,s60,s62
mm v[84:87],a[164:167],a[116:119],v[84:87]
dt a[188:189],v195 offset:57344
mm v[80:83],a[160:163],a[116:119],v[80:83]
s_add_u32 s61,s61,s62
mm v[80:83],a[128:131],a[84:87],v[80:83]
mm v[96:99],a[128:131],a[88:91],v[96:99]
s_waitcnt lgkmcnt(2)
s_mov_b32 s63,s46
dt a[190:191],v195 offset:58368
mm v[96:99],a[160:163],a[120:123],v[96:99]
mm v[100:103],a[164:167],a[120:123],v[100:103]
s_cmp_ge_u32 s63,128
s_cselect_b32 s46,s60,s101
s_cselect_b32 s50,s61,s101
mm v[100:103],a[132:135],a[88:91],v[100:103]
)" R"(mm v[104:107],a[136:139],a[88:91],v[104:107]
s_lshl_b32 s46,s46,10
mm v[104:107],a[168:171],a[120:123],v[104:107]
s_add_u32 s46,s46,0x3000000
mm v[108:111],a[172:175],a[120:123],v[108:111]
mm v[108:111],a[140:143],a[88:91],v[108:111]
s_lshl_b32 s50,s50,10
mm v[124:127],a[140:143],a[92:95],v[124:127]
mm v[124:127],a[172:175],a[124:127],v[124:127]
s_add_u32 s50,s50,0x3000000
mm v[120:123],a[168:171],a[124:127],v[120:123]
mm v[120:123],a[136:139],a[92:95],v[120:123]
s_cmp_ge_u32 s63,148
s_cselect_b32 s46,0x80000000,s46
s_cselect_b32 s50,0x80000000,s50
mm v[116:119],a[132:135],a[92:95],v[116:119]
mm v[116:119],a[164:167],a[124:127],v[116:119]
mm v[112:115],a[160:163],a[124:127],v[112:115]
s_waitcnt vmcnt(8) lgkmcnt(0)
s_barrier
mm v[112:115],a[128:131],a[92:95],v[112:115]
mm v[128:131],a[64:67],a[144:147],v[128:131]
s_lshl_b32 s61,s25,3
dt a[128:129],v192 offset:0
s_add_u32 m0,s34,32768
mm v[128:131],a[96:99],a[176:179],v[128:131]
bl v208,s[16:19],s33 offen offset:0 lds
mm v[132:135],a[96:99],a[180:183],v[132:135]
s_add_u32 s61,s61,s26
mm v[132:135],a[64:67],a[148:151],v[132:135]
dt a[130:131],v192 offset:1024
mm v[136:139],a[64:67],a[152:155],v[136:139]
mm v[136:139],a[96:99],a[184:187],v[136:139]
s_lshl_b32 s62,s61,3
mm v[140:143],a[96:99],a[188:191],v[140:143]
dt a[160:161],v192 offset:8192
bl v209,s[16:19],s33 offen offset:1024 lds
mm v[140:143],a[64:67],a[156:159],v[140:143]
mm a[12:15],a[68:71],a[156:159],a[12:15]
s_add_u32 s62,s62,0x4002000
dr a[64:67],v196 offset:32768
mm a[12:15],a[100:103],a[188:191],a[12:15]
mm a[8:11],a[100:103],a[184:187],a[8:11]
dt a[162:163],v192 offset:9216
mm a[8:11],a[68:71],a[152:155],a[8:11]
dr a[96:99],v197 offset:32768
bl v210,s[16:19],s33 offen offset:2048 lds
mm a[4:7],a[68:71],a[148:151],a[4:7]
s_cmp_eq_u32 s24,0
s_cselect_b32 s62,s62,0x80000000
mm a[4:7],a[100:103],a[180:183],a[4:7]
dt a[132:133],v193 offset:0
mm a[0:3],a[100:103],a[176:179],a[0:3]
v_mov_b32 v220,s54
mm a[0:3],a[68:71],a[144:147],a[0:3]
mm v[144:147],a[72:75],a[144:147],v[144:147]
dr a[68:71],v196 offset:34816
bl v211,s[16:19],s33 offen offset:3072 lds
mm v[144:147],a[104:107],a[176:179],v[144:147]
dt a[134:135],v193 offset:1024
v_mov_b32 v221,s55
mm v[148:151],a[104:107],a[180:183],v[148:151]
mm v[148:151],a[72:75],a[148:151],v[148:151]
s_add_u32 m0,s34,36864
)" R"(dr a[100:103],v197 offset:34816
mm v[152:155],a[72:75],a[152:155],v[152:155]
dt a[164:165],v193 offset:8192
v_mov_b32 v222,0
mm v[152:155],a[104:107],a[184:187],v[152:155]
bl v212,s[16:19],s33 offen offset:0 lds
mm v[156:159],a[104:107],a[188:191],v[156:159]
mm v[156:159],a[72:75],a[156:159],v[156:159]
s_mov_b64 exec,1
buffer_store_dwordx2 v[220:221],v222,s[8:11],s62 offen sc1
s_mov_b64 exec,-1
mm a[28:31],a[76:79],a[156:159],a[28:31]
dr a[72:75],v196 offset:36864
mm a[28:31],a[108:111],a[188:191],a[28:31]
dt a[166:167],v193 offset:9216
mm a[24:27],a[108:111],a[184:187],a[24:27]
bl v213,s[16:19],s33 offen offset:1024 lds
v_mbcnt_lo_u32_b32 v251,-1,0
mm a[24:27],a[76:79],a[152:155],a[24:27]
dr a[104:107],v197 offset:36864
mm a[20:23],a[76:79],a[148:151],a[20:23]
dt a[136:137],v194 offset:0
mm a[20:23],a[108:111],a[180:183],a[20:23]
v_mbcnt_hi_u32_b32 v251,-1,v251
mm a[16:19],a[108:111],a[176:179],a[16:19]
dt a[138:139],v194 offset:1024
mm a[16:19],a[76:79],a[144:147],a[16:19]
bl v214,s[16:19],s33 offen offset:2048 lds
mm v[160:163],a[80:83],a[144:147],v[160:163]
s_cmp_eq_u32 s27,0
s_cselect_b32 s20,s64,s72
s_cselect_b32 s21,s65,s73
mm v[160:163],a[112:115],a[176:179],v[160:163]
dr a[76:79],v196 offset:38912
mm v[164:167],a[112:115],a[180:183],v[164:167]
s_and_b32 s21,s21,0xffff
dt a[168:169],v194 offset:8192
mm v[164:167],a[80:83],a[148:151],v[164:167]
dr a[108:111],v197 offset:38912
mm v[168:171],a[80:83],a[152:155],v[168:171]
bl v215,s[16:19],s33 offen offset:3072 lds
s_add_u32 s33,s33,0x80000
mm v[168:171],a[112:115],a[184:187],v[168:171]
s_mov_b32 s22,0x280000
dt a[170:171],v194 offset:9216
s_bitset0_b32 s33,22
mm v[172:175],a[112:115],a[188:191],v[172:175]
mm v[172:175],a[80:83],a[156:159],v[172:175]
s_add_u32 m0,s35,0
mm a[44:47],a[84:87],a[156:159],a[44:47]
dr a[80:83],v196 offset:40960
v_min_u32 v251,55,v251
mm a[44:47],a[116:119],a[188:191],a[44:47]
dt a[140:141],v195 offset:0
bl v200,s[12:15],s32 offen offset:0 lds
mm a[40:43],a[116:119],a[184:187],a[40:43]
mm a[40:43],a[84:87],a[152:155],a[40:43]
dr a[112:115],v197 offset:40960
v_lshlrev_b32 v252,7,v251
mm a[36:39],a[84:87],a[148:151],a[36:39]
dt a[142:143],v195 offset:1024
mm a[36:39],a[116:119],a[180:183],a[36:39]
bl v201,s[12:15],s32 offen offset:1024 lds
mm a[32:35],a[116:119],a[176:179],a[32:35]
)" R"(s_add_u32 s60,s29,128
mm a[32:35],a[84:87],a[144:147],a[32:35]
mm v[176:179],a[88:91],a[144:147],v[176:179]
dr a[84:87],v196 offset:43008
mm v[176:179],a[120:123],a[176:179],v[176:179]
s_and_b32 s60,s60,255
bl v202,s[12:15],s32 offen offset:2048 lds
dt a[172:173],v195 offset:8192
mm v[180:183],a[120:123],a[180:183],v[180:183]
mm v[180:183],a[88:91],a[148:151],v[180:183]
dr a[116:119],v197 offset:43008
mm v[184:187],a[88:91],a[152:155],v[184:187]
s_lshl_b32 s60,s60,1
dt a[174:175],v195 offset:9216
mm v[184:187],a[120:123],a[184:187],v[184:187]
bl v203,s[12:15],s32 offen offset:3072 lds
mm v[188:191],a[120:123],a[188:191],v[188:191]
mm v[188:191],a[88:91],a[156:159],v[188:191]
s_add_u32 m0,s35,4096
s_add_u32 s60,s60,s28
mm a[60:63],a[92:95],a[156:159],a[60:63]
dr a[88:91],v196 offset:45056
mm a[60:63],a[124:127],a[188:191],a[60:63]
bl v204,s[12:15],s32 offen offset:0 lds
mm a[56:59],a[124:127],a[184:187],a[56:59]
s_mul_i32 s48,s60,7168
mm a[56:59],a[92:95],a[152:155],a[56:59]
dr a[120:123],v197 offset:45056
mm a[52:55],a[92:95],a[148:151],a[52:55]
mm a[52:55],a[124:127],a[180:183],a[52:55]
bl v205,s[12:15],s32 offen offset:1024 lds
mm a[48:51],a[124:127],a[176:179],a[48:51]
mm a[48:51],a[92:95],a[144:147],a[48:51]
s_waitcnt lgkmcnt(1)
mm v[0:3],a[128:131],a[64:67],v[0:3]
dr a[92:95],v196 offset:47104
mm v[0:3],a[160:163],a[96:99],v[0:3]
bl v206,s[12:15],s32 offen offset:2048 lds
mm v[4:7],a[164:167],a[96:99],v[4:7]
s_waitcnt vmcnt(15)
dr a[124:127],v197 offset:47104
mm v[224:227],a[216:219],a[192:195],0
mm v[228:231],a[220:223],a[192:195],0
mm v[4:7],a[132:135],a[64:67],v[4:7]
bl v207,s[12:15],s32 offen offset:3072 lds
s_add_u32 s32,s32,128
mm v[8:11],a[136:139],a[64:67],v[8:11]
dt a[144:145],v192 offset:16384
s_bitset0_b32 s32,10
mm v[8:11],a[168:171],a[96:99],v[8:11]
cv v224,v224,v225
mm v[12:15],a[172:175],a[96:99],v[12:15]
dt a[146:147],v192 offset:17408
mm v[12:15],a[140:143],a[64:67],v[12:15]
s_bitcmp0_b32 s26,0
s_cbranch_scc0 CND3
buffer_load_dword v255,v252,s[20:23],s48 offen
CND3:
mm v[28:31],a[140:143],a[68:71],v[28:31]
cv v225,v226,v227
mm v[28:31],a[172:175],a[100:103],v[28:31]
dt a[176:177],v192 offset:24576
mm v[24:27],a[168:171],a[100:103],v[24:27]
cv v226,v228,v229
)" R"(mm v[24:27],a[136:139],a[68:71],v[24:27]
mm v[20:23],a[132:135],a[68:71],v[20:23]
dt a[178:179],v192 offset:25600
mm v[20:23],a[164:167],a[100:103],v[20:23]
cv v227,v230,v231
mm v[16:19],a[160:163],a[100:103],v[16:19]
mm v[16:19],a[128:131],a[68:71],v[16:19]
dt a[148:149],v193 offset:16384
mm v[32:35],a[128:131],a[72:75],v[32:35]
bs v[224:227],v253,s[8:11],s44 offen offset:0 sc1
mm v[32:35],a[160:163],a[104:107],v[32:35]
dt a[150:151],v193 offset:17408
mm v[36:39],a[164:167],a[104:107],v[36:39]
mm v[36:39],a[132:135],a[72:75],v[36:39]
mm v[40:43],a[136:139],a[72:75],v[40:43]
dt a[180:181],v193 offset:24576
mm v[232:235],a[216:219],a[196:199],0
mm v[236:239],a[220:223],a[196:199],0
mm v[40:43],a[168:171],a[104:107],v[40:43]
dt a[182:183],v193 offset:25600
mm v[44:47],a[172:175],a[104:107],v[44:47]
s_nop 3
cv v232,v232,v233
mm v[44:47],a[140:143],a[72:75],v[44:47]
mm v[60:63],a[140:143],a[76:79],v[60:63]
dt a[152:153],v194 offset:16384
mm v[60:63],a[172:175],a[108:111],v[60:63]
cv v233,v234,v235
mm v[56:59],a[168:171],a[108:111],v[56:59]
mm v[56:59],a[136:139],a[76:79],v[56:59]
dt a[154:155],v194 offset:17408
mm v[52:55],a[132:135],a[76:79],v[52:55]
cv v234,v236,v237
mm v[52:55],a[164:167],a[108:111],v[52:55]
mm v[48:51],a[160:163],a[108:111],v[48:51]
dt a[184:185],v194 offset:24576
mm v[48:51],a[128:131],a[76:79],v[48:51]
cv v235,v238,v239
mm v[64:67],a[128:131],a[80:83],v[64:67]
dt a[186:187],v194 offset:25600
mm v[64:67],a[160:163],a[112:115],v[64:67]
mm v[68:71],a[164:167],a[112:115],v[68:71]
bs v[232:235],v253,s[8:11],s44 offen offset:1024 sc1
mm v[68:71],a[132:135],a[80:83],v[68:71]
dt a[156:157],v195 offset:16384
mm v[72:75],a[136:139],a[80:83],v[72:75]
mm v[72:75],a[168:171],a[112:115],v[72:75]
mm v[76:79],a[172:175],a[112:115],v[76:79]
dt a[158:159],v195 offset:17408
mm v[224:227],a[224:227],a[192:195],0
mm v[228:231],a[228:231],a[192:195],0
mm v[76:79],a[140:143],a[80:83],v[76:79]
dt a[188:189],v195 offset:24576
mm v[92:95],a[140:143],a[84:87],v[92:95]
s_nop 3
cv v224,v224,v225
mm v[92:95],a[172:175],a[116:119],v[92:95]
mm v[88:91],a[168:171],a[116:119],v[88:91]
dt a[190:191],v195 offset:25600
mm v[88:91],a[136:139],a[84:87],v[88:91]
cv v225,v226,v227
mm v[84:87],a[132:135],a[84:87],v[84:87]
mm v[84:87],a[164:167],a[116:119],v[84:87]
mm v[80:83],a[160:163],a[116:119],v[80:83]
)" R"(cv v226,v228,v229
mm v[80:83],a[128:131],a[84:87],v[80:83]
mm v[96:99],a[128:131],a[88:91],v[96:99]
s_waitcnt lgkmcnt(1)
mm v[96:99],a[160:163],a[120:123],v[96:99]
cv v227,v230,v231
mm v[100:103],a[164:167],a[120:123],v[100:103]
mm v[100:103],a[132:135],a[88:91],v[100:103]
mm v[104:107],a[136:139],a[88:91],v[104:107]
bs v[224:227],v253,s[8:11],s49 offen offset:0 sc1
mm v[104:107],a[168:171],a[120:123],v[104:107]
mm v[108:111],a[172:175],a[120:123],v[108:111]
mm v[108:111],a[140:143],a[88:91],v[108:111]
mm v[124:127],a[140:143],a[92:95],v[124:127]
mm v[124:127],a[172:175],a[124:127],v[124:127]
mm v[120:123],a[168:171],a[124:127],v[120:123]
mm v[120:123],a[136:139],a[92:95],v[120:123]
mm v[116:119],a[132:135],a[92:95],v[116:119]
mm v[116:119],a[164:167],a[124:127],v[116:119]
mm v[112:115],a[160:163],a[124:127],v[112:115]
s_waitcnt vmcnt(11) lgkmcnt(0)
s_barrier
mm v[112:115],a[128:131],a[92:95],v[112:115]
mm v[128:131],a[64:67],a[144:147],v[128:131]
dt a[128:129],v192 offset:32768
s_add_u32 m0,s34,0
mm v[128:131],a[96:99],a[176:179],v[128:131]
bl v208,s[16:19],s33 offen offset:0 lds
mm v[132:135],a[96:99],a[180:183],v[132:135]
mm v[232:235],a[224:227],a[196:199],0
dt a[130:131],v192 offset:33792
mm v[236:239],a[228:231],a[196:199],0
mm v[132:135],a[64:67],a[148:151],v[132:135]
mm v[136:139],a[64:67],a[152:155],v[136:139]
bl v209,s[16:19],s33 offen offset:1024 lds
s_nop 2
cv v232,v232,v233
mm v[136:139],a[96:99],a[184:187],v[136:139]
dt a[160:161],v192 offset:40960
mm v[140:143],a[96:99],a[188:191],v[140:143]
mm v[140:143],a[64:67],a[156:159],v[140:143]
cv v233,v234,v235
mm a[12:15],a[68:71],a[156:159],a[12:15]
dr a[64:67],v198 offset:0
mm a[12:15],a[100:103],a[188:191],a[12:15]
bl v210,s[16:19],s33 offen offset:2048 lds
dt a[162:163],v192 offset:41984
mm a[8:11],a[100:103],a[184:187],a[8:11]
cv v234,v236,v237
mm a[8:11],a[68:71],a[152:155],a[8:11]
dr a[96:99],v199 offset:0
mm a[4:7],a[68:71],a[148:151],a[4:7]
dt a[132:133],v193 offset:32768
mm a[4:7],a[100:103],a[180:183],a[4:7]
cv v235,v238,v239
mm a[0:3],a[100:103],a[176:179],a[0:3]
bl v211,s[16:19],s33 offen offset:3072 lds
mm a[0:3],a[68:71],a[144:147],a[0:3]
dt a[134:135],v193 offset:33792
mm v[144:147],a[72:75],a[144:147],v[144:147]
bs v[232:235],v253,s[8:11],s49 offen offset:1024 sc1
dr a[68:71],v198 offset:2048
mm v[144:147],a[104:107],a[176:179],v[144:147]
s_add_u32 m0,s34,4096
)" R"(mm v[148:151],a[104:107],a[180:183],v[148:151]
dt a[164:165],v193 offset:40960
mm v[148:151],a[72:75],a[148:151],v[148:151]
dr a[100:103],v199 offset:2048
bl v212,s[16:19],s33 offen offset:0 lds
mm v[152:155],a[72:75],a[152:155],v[152:155]
mm v[224:227],a[232:235],a[192:195],0
mm v[228:231],a[236:239],a[192:195],0
dt a[166:167],v193 offset:41984
mm v[152:155],a[104:107],a[184:187],v[152:155]
mm v[156:159],a[104:107],a[188:191],v[156:159]
bl v213,s[16:19],s33 offen offset:1024 lds
s_nop 2
cv v224,v224,v225
mm v[156:159],a[72:75],a[156:159],v[156:159]
dt a[136:137],v194 offset:32768
mm a[28:31],a[76:79],a[156:159],a[28:31]
dr a[72:75],v198 offset:4096
mm a[28:31],a[108:111],a[188:191],a[28:31]
cv v225,v226,v227
mm a[24:27],a[108:111],a[184:187],a[24:27]
dt a[138:139],v194 offset:33792
mm a[24:27],a[76:79],a[152:155],a[24:27]
dr a[104:107],v199 offset:4096
bl v214,s[16:19],s33 offen offset:2048 lds
mm a[20:23],a[76:79],a[148:151],a[20:23]
cv v226,v228,v229
mm a[20:23],a[108:111],a[180:183],a[20:23]
mm a[16:19],a[108:111],a[176:179],a[16:19]
dt a[168:169],v194 offset:40960
mm a[16:19],a[76:79],a[144:147],a[16:19]
cv v227,v230,v231
mm v[160:163],a[80:83],a[144:147],v[160:163]
dr a[76:79],v198 offset:6144
bl v215,s[16:19],s33 offen offset:3072 lds
mm v[160:163],a[112:115],a[176:179],v[160:163]
s_add_u32 s33,s33,0x80000
dt a[170:171],v194 offset:41984
s_bitset0_b32 s33,22
mm v[164:167],a[112:115],a[180:183],v[164:167]
bs v[224:227],v253,s[8:11],s46 offen offset:0 sc1
mm v[164:167],a[80:83],a[148:151],v[164:167]
s_add_u32 m0,s35,32768
dr a[108:111],v199 offset:6144
mm v[168:171],a[80:83],a[152:155],v[168:171]
mm v[168:171],a[112:115],a[184:187],v[168:171]
dt a[140:141],v195 offset:32768
bl v200,s[12:15],s32 offen offset:0 lds
mm v[172:175],a[112:115],a[188:191],v[172:175]
mm v[232:235],a[232:235],a[196:199],0
mm v[236:239],a[236:239],a[196:199],0
dt a[142:143],v195 offset:33792
mm v[172:175],a[80:83],a[156:159],v[172:175]
bl v201,s[12:15],s32 offen offset:1024 lds
mm a[44:47],a[84:87],a[156:159],a[44:47]
dr a[80:83],v198 offset:8192
s_nop 1
cv v232,v232,v233
mm a[44:47],a[116:119],a[188:191],a[44:47]
mm a[40:43],a[116:119],a[184:187],a[40:43]
dt a[172:173],v195 offset:40960
mm a[40:43],a[84:87],a[152:155],a[40:43]
dr a[112:115],v199 offset:8192
bl v202,s[12:15],s32 offen offset:2048 lds
)" R"(mm a[36:39],a[84:87],a[148:151],a[36:39]
cv v233,v234,v235
mm a[36:39],a[116:119],a[180:183],a[36:39]
dt a[174:175],v195 offset:41984
mm a[32:35],a[116:119],a[176:179],a[32:35]
cv v234,v236,v237
mm a[32:35],a[84:87],a[144:147],a[32:35]
bl v203,s[12:15],s32 offen offset:3072 lds
mm v[176:179],a[88:91],a[144:147],v[176:179]
dr a[84:87],v198 offset:10240
mm v[176:179],a[120:123],a[176:179],v[176:179]
cv v235,v238,v239
s_add_u32 m0,s35,36864
mm v[180:183],a[120:123],a[180:183],v[180:183]
mm v[180:183],a[88:91],a[148:151],v[180:183]
dr a[116:119],v199 offset:10240
bl v204,s[12:15],s32 offen offset:0 lds
mm v[184:187],a[88:91],a[152:155],v[184:187]
bs v[232:235],v253,s[8:11],s50 offen offset:1024 sc1
mm v[184:187],a[120:123],a[184:187],v[184:187]
mm v[188:191],a[120:123],a[188:191],v[188:191]
mm v[188:191],a[88:91],a[156:159],v[188:191]
bl v205,s[12:15],s32 offen offset:1024 lds
mm a[60:63],a[92:95],a[156:159],a[60:63]
dr a[88:91],v198 offset:12288
mm a[60:63],a[124:127],a[188:191],a[60:63]
mm a[56:59],a[124:127],a[184:187],a[56:59]
mm a[56:59],a[92:95],a[152:155],a[56:59]
E0_2:
s_cbranch_execz E0_6
dr a[120:123],v199 offset:12288
bl v206,s[12:15],s32 offen offset:2048 lds
mm a[52:55],a[92:95],a[148:151],a[52:55]
mm a[52:55],a[124:127],a[180:183],a[52:55]
mm a[48:51],a[124:127],a[176:179],a[48:51]
mm a[48:51],a[92:95],a[144:147],a[48:51]
s_waitcnt lgkmcnt(1)
bl v207,s[12:15],s32 offen offset:3072 lds
s_add_u32 s32,s32,128
s_bitset0_b32 s32,10
mm v[0:3],a[128:131],a[64:67],v[0:3]
dr a[92:95],v198 offset:14336
mm v[0:3],a[160:163],a[96:99],v[0:3]
mm v[4:7],a[164:167],a[96:99],v[4:7]
dr a[124:127],v199 offset:14336
mm v[4:7],a[132:135],a[64:67],v[4:7]
s_bitcmp1_b32 s26,0
s_cbranch_scc0 CND4
buffer_load_dword v255,v252,s[20:23],s48 offen
CND4:
mm v[8:11],a[136:139],a[64:67],v[8:11]
mm v[8:11],a[168:171],a[96:99],v[8:11]
dt a[144:145],v192 offset:49152
mm v[12:15],a[172:175],a[96:99],v[12:15]
mm v[12:15],a[140:143],a[64:67],v[12:15]
mm v[28:31],a[140:143],a[68:71],v[28:31]
dt a[146:147],v192 offset:50176
mm v[28:31],a[172:175],a[100:103],v[28:31]
mm v[24:27],a[168:171],a[100:103],v[24:27]
mm v[24:27],a[136:139],a[68:71],v[24:27]
dt a[176:177],v192 offset:57344
mm v[20:23],a[132:135],a[68:71],v[20:23]
mm v[20:23],a[164:167],a[100:103],v[20:23]
mm v[16:19],a[160:163],a[100:103],v[16:19]
)" R"(dt a[178:179],v192 offset:58368
mm v[16:19],a[128:131],a[68:71],v[16:19]
mm v[32:35],a[128:131],a[72:75],v[32:35]
mm v[32:35],a[160:163],a[104:107],v[32:35]
dt a[148:149],v193 offset:49152
mm v[36:39],a[164:167],a[104:107],v[36:39]
mm v[36:39],a[132:135],a[72:75],v[36:39]
dt a[150:151],v193 offset:50176
mm v[40:43],a[136:139],a[72:75],v[40:43]
mm v[40:43],a[168:171],a[104:107],v[40:43]
mm v[44:47],a[172:175],a[104:107],v[44:47]
dt a[180:181],v193 offset:57344
mm v[44:47],a[140:143],a[72:75],v[44:47]
mm v[60:63],a[140:143],a[76:79],v[60:63]
mm v[60:63],a[172:175],a[108:111],v[60:63]
dt a[182:183],v193 offset:58368
mm v[56:59],a[168:171],a[108:111],v[56:59]
mm v[56:59],a[136:139],a[76:79],v[56:59]
mm v[52:55],a[132:135],a[76:79],v[52:55]
dt a[152:153],v194 offset:49152
mm v[52:55],a[164:167],a[108:111],v[52:55]
mm v[48:51],a[160:163],a[108:111],v[48:51]
s_mov_b32 s12,s64
s_and_b32 s13,s65,0xffff
mm v[48:51],a[128:131],a[76:79],v[48:51]
s_mov_b32 s14,0x280000
dt a[154:155],v194 offset:50176
mm v[64:67],a[128:131],a[80:83],v[64:67]
s_mov_b32 s15,0x20000
mm v[64:67],a[160:163],a[112:115],v[64:67]
v_mbcnt_lo_u32_b32 v220,-1,0
mm v[68:71],a[164:167],a[112:115],v[68:71]
dt a[184:185],v194 offset:57344
v_mbcnt_hi_u32_b32 v220,-1,v220
mm v[68:71],a[132:135],a[80:83],v[68:71]
v_lshrrev_b32 v221,3,v220
v_mul_u32_u24 v221,1280,v221
mm v[72:75],a[136:139],a[80:83],v[72:75]
dt a[186:187],v194 offset:58368
v_and_b32 v222,7,v220
mm v[72:75],a[168:171],a[112:115],v[72:75]
v_lshrrev_b32 v223,4,v220
mm v[76:79],a[172:175],a[112:115],v[76:79]
v_xor_b32 v222,v222,v223
mm v[76:79],a[140:143],a[80:83],v[76:79]
dt a[156:157],v195 offset:49152
v_lshl_add_u32 v200,v222,4,v221
mm v[92:95],a[140:143],a[84:87],v[92:95]
v_xor_b32 v221,64,v200
v_add_u32 v201,0x2400,v221
mm v[92:95],a[172:175],a[116:119],v[92:95]
v_add_u32 v202,0x4800,v200
mm v[88:91],a[168:171],a[116:119],v[88:91]
dt a[158:159],v195 offset:50176
v_add_u32 v203,0x6c00,v221
mm v[88:91],a[136:139],a[84:87],v[88:91]
v_add_u32 v204,0xa000,v200
mm v[84:87],a[132:135],a[84:87],v[84:87]
v_add_u32 v205,0xc400,v221
mm v[84:87],a[164:167],a[116:119],v[84:87]
dt a[188:189],v195 offset:57344
v_add_u32 v206,0xe800,v200
mm v[80:83],a[160:163],a[116:119],v[80:83]
v_add_u32 v207,0x10c00,v221
)" R"(s_lshr_b32 s32,s29,5
mm v[80:83],a[128:131],a[84:87],v[80:83]
s_and_b32 s62,s29,4
mm v[96:99],a[128:131],a[88:91],v[96:99]
s_waitcnt lgkmcnt(2)
s_lshl_b32 s62,s62,1
dt a[190:191],v195 offset:58368
mm v[96:99],a[160:163],a[120:123],v[96:99]
s_add_u32 s32,s32,s62
mm v[100:103],a[164:167],a[120:123],v[100:103]
s_bfe_u32 s61,s29,0x20003
s_and_b32 s62,s29,3
mm v[100:103],a[132:135],a[88:91],v[100:103]
s_lshl_b32 s62,s62,2
mm v[104:107],a[136:139],a[88:91],v[104:107]
s_add_u32 s61,s61,s62
mm v[104:107],a[168:171],a[120:123],v[104:107]
s_lshl_b32 s32,s32,2
mm v[108:111],a[172:175],a[120:123],v[108:111]
s_add_u32 s32,s32,s24
mm v[108:111],a[140:143],a[88:91],v[108:111]
s_mul_i32 s32,s32,40960
s_lshl_b32 s35,s24,12
mm v[124:127],a[140:143],a[92:95],v[124:127]
v_and_b32 v219,31,v220
mm v[124:127],a[172:175],a[124:127],v[124:127]
v_lshlrev_b32 v219,3,v219
mm v[120:123],a[168:171],a[124:127],v[120:123]
s_lshr_b32 s63,s61,1
mm v[120:123],a[136:139],a[92:95],v[120:123]
s_lshl_b32 s63,s63,8
mm v[116:119],a[132:135],a[92:95],v[116:119]
s_add_u32 s63,s63,0x4002000
mm v[116:119],a[164:167],a[124:127],v[116:119]
mm v[112:115],a[160:163],a[124:127],v[112:115]
s_waitcnt vmcnt(10) lgkmcnt(0)
s_barrier
mm v[112:115],a[128:131],a[92:95],v[112:115]
mm v[128:131],a[64:67],a[144:147],v[128:131]
dt a[128:129],v192 offset:0
s_add_u32 m0,s34,32768
mm v[128:131],a[96:99],a[176:179],v[128:131]
bl v208,s[16:19],s33 offen offset:0 lds
mm v[132:135],a[96:99],a[180:183],v[132:135]
mm v[132:135],a[64:67],a[148:151],v[132:135]
dt a[130:131],v192 offset:1024
mm v[136:139],a[64:67],a[152:155],v[136:139]
mm v[136:139],a[96:99],a[184:187],v[136:139]
mm v[140:143],a[96:99],a[188:191],v[140:143]
dt a[160:161],v192 offset:8192
bl v209,s[16:19],s33 offen offset:1024 lds
mm v[140:143],a[64:67],a[156:159],v[140:143]
mm a[12:15],a[68:71],a[156:159],a[12:15]
dr a[64:67],v196 offset:0
mm a[12:15],a[100:103],a[188:191],a[12:15]
mm a[8:11],a[100:103],a[184:187],a[8:11]
dt a[162:163],v192 offset:9216
mm a[8:11],a[68:71],a[152:155],a[8:11]
dr a[96:99],v197 offset:0
bl v210,s[16:19],s33 offen offset:2048 lds
mm a[4:7],a[68:71],a[148:151],a[4:7]
mm a[4:7],a[100:103],a[180:183],a[4:7]
dt a[132:133],v193 offset:0
mm a[0:3],a[100:103],a[176:179],a[0:3]
)" R"(mm a[0:3],a[68:71],a[144:147],a[0:3]
mm v[144:147],a[72:75],a[144:147],v[144:147]
dr a[68:71],v196 offset:2048
bl v211,s[16:19],s33 offen offset:3072 lds
mm v[144:147],a[104:107],a[176:179],v[144:147]
dt a[134:135],v193 offset:1024
mm v[148:151],a[104:107],a[180:183],v[148:151]
mm v[148:151],a[72:75],a[148:151],v[148:151]
s_add_u32 m0,s34,36864
dr a[100:103],v197 offset:2048
mm v[152:155],a[72:75],a[152:155],v[152:155]
dt a[164:165],v193 offset:8192
mm v[152:155],a[104:107],a[184:187],v[152:155]
bl v212,s[16:19],s33 offen offset:0 lds
mm v[156:159],a[104:107],a[188:191],v[156:159]
mm v[156:159],a[72:75],a[156:159],v[156:159]
mm a[28:31],a[76:79],a[156:159],a[28:31]
dr a[72:75],v196 offset:4096
mm a[28:31],a[108:111],a[188:191],a[28:31]
dt a[166:167],v193 offset:9216
mm a[24:27],a[108:111],a[184:187],a[24:27]
bl v213,s[16:19],s33 offen offset:1024 lds
mm a[24:27],a[76:79],a[152:155],a[24:27]
dr a[104:107],v197 offset:4096
mm a[20:23],a[76:79],a[148:151],a[20:23]
dt a[136:137],v194 offset:0
mm a[20:23],a[108:111],a[180:183],a[20:23]
mm a[16:19],a[108:111],a[176:179],a[16:19]
dt a[138:139],v194 offset:1024
mm a[16:19],a[76:79],a[144:147],a[16:19]
bl v214,s[16:19],s33 offen offset:2048 lds
mm v[160:163],a[80:83],a[144:147],v[160:163]
dr a[76:79],v196 offset:6144
mm v[160:163],a[112:115],a[176:179],v[160:163]
mm v[164:167],a[112:115],a[180:183],v[164:167]
dt a[168:169],v194 offset:8192
mm v[164:167],a[80:83],a[148:151],v[164:167]
dr a[108:111],v197 offset:6144
mm v[168:171],a[80:83],a[152:155],v[168:171]
bl v215,s[16:19],s33 offen offset:3072 lds
s_add_u32 s33,s33,0x80000
mm v[168:171],a[112:115],a[184:187],v[168:171]
s_bitset0_b32 s33,22
s_mov_b32 s20,s76
s_and_b32 s21,s77,0xffff
mm v[172:175],a[112:115],a[188:191],v[172:175]
s_mov_b32 s22,0x500000
v_lshrrev_b32 v251,3,v251
v_and_b32 v252,0x380,v252
mm v[172:175],a[80:83],a[156:159],v[172:175]
v_lshl_add_u32 v252,v251,13,v252
s_add_u32 s60,s29,128
mm a[44:47],a[84:87],a[156:159],a[44:47]
s_and_b32 s60,s60,255
s_lshr_b32 s60,s60,3
s_lshl_b32 s60,s60,2
mm a[44:47],a[116:119],a[188:191],a[44:47]
s_add_u32 s60,s60,s24
s_mul_i32 s60,s60,57344
s_and_b32 s48,s25,28
mm a[40:43],a[116:119],a[184:187],a[40:43]
s_lshl_b32 s48,s48,8
dt a[170:171],v194 offset:9216
s_add_u32 s48,s48,s60
)" R"(mm a[40:43],a[84:87],a[152:155],a[40:43]
s_add_u32 m0,s35,131072
dr a[80:83],v196 offset:8192
s_add_u32 s60,s32,256
mm a[36:39],a[84:87],a[148:151],a[36:39]
dt a[140:141],v195 offset:0
bl v200,s[12:15],s60 offen offset:0 lds
mm a[36:39],a[116:119],a[180:183],a[36:39]
bl v201,s[12:15],s60 offen offset:1024 lds
dr a[112:115],v197 offset:8192
mm a[32:35],a[116:119],a[176:179],a[32:35]
dt a[142:143],v195 offset:1024
mm a[32:35],a[84:87],a[144:147],a[32:35]
mm v[176:179],a[88:91],a[144:147],v[176:179]
dr a[84:87],v196 offset:10240
mm v[176:179],a[120:123],a[176:179],v[176:179]
bl v202,s[12:15],s60 offen offset:2048 lds
dt a[172:173],v195 offset:8192
mm v[180:183],a[120:123],a[180:183],v[180:183]
mm v[180:183],a[88:91],a[148:151],v[180:183]
dr a[116:119],v197 offset:10240
mm v[184:187],a[88:91],a[152:155],v[184:187]
dt a[174:175],v195 offset:9216
mm v[184:187],a[120:123],a[184:187],v[184:187]
bl v203,s[12:15],s60 offen offset:3072 lds
mm v[188:191],a[120:123],a[188:191],v[188:191]
s_add_u32 m0,s35,147456
mm v[188:191],a[88:91],a[156:159],v[188:191]
s_add_u32 s60,s32,384
mm a[60:63],a[92:95],a[156:159],a[60:63]
dr a[88:91],v196 offset:12288
mm a[60:63],a[124:127],a[188:191],a[60:63]
bl v200,s[12:15],s60 offen offset:0 lds
mm a[56:59],a[124:127],a[184:187],a[56:59]
mm a[56:59],a[92:95],a[152:155],a[56:59]
dr a[120:123],v197 offset:12288
mm a[52:55],a[92:95],a[148:151],a[52:55]
mm a[52:55],a[124:127],a[180:183],a[52:55]
bl v201,s[12:15],s60 offen offset:1024 lds
mm a[48:51],a[124:127],a[176:179],a[48:51]
mm a[48:51],a[92:95],a[144:147],a[48:51]
s_waitcnt lgkmcnt(1)
mm v[0:3],a[128:131],a[64:67],v[0:3]
dr a[92:95],v196 offset:14336
mm v[0:3],a[160:163],a[96:99],v[0:3]
bl v202,s[12:15],s60 offen offset:2048 lds
mm v[4:7],a[164:167],a[96:99],v[4:7]
dr a[124:127],v197 offset:14336
mm v[4:7],a[132:135],a[64:67],v[4:7]
mm v[8:11],a[136:139],a[64:67],v[8:11]
mm v[8:11],a[168:171],a[96:99],v[8:11]
dt a[144:145],v192 offset:16384
bl v203,s[12:15],s60 offen offset:3072 lds
mm v[12:15],a[172:175],a[96:99],v[12:15]
mm v[12:15],a[140:143],a[64:67],v[12:15]
mm v[28:31],a[140:143],a[68:71],v[28:31]
dt a[146:147],v192 offset:17408
mm v[28:31],a[172:175],a[100:103],v[28:31]
buffer_load_dwordx2 v[248:249],v219,s[8:11],s63 offen sc1
mm v[24:27],a[168:171],a[100:103],v[24:27]
mm v[24:27],a[136:139],a[68:71],v[24:27]
dt a[176:177],v192 offset:24576
mm v[20:23],a[132:135],a[68:71],v[20:23]
mm v[20:23],a[164:167],a[100:103],v[20:23]
)" R"(s_bitcmp0_b32 s26,0
s_cbranch_scc0 CND5
buffer_load_dword v255,v252,s[20:23],s48 offen
CND5:
mm v[16:19],a[160:163],a[100:103],v[16:19]
dt a[178:179],v192 offset:25600
s_getpc_b64 s[60:61]
CT1:
mm v[16:19],a[128:131],a[68:71],v[16:19]
s_lshr_b32 s62,s29,3
s_lshl_b32 s62,s62,2
mm v[32:35],a[128:131],a[72:75],v[32:35]
s_add_u32 s62,s62,s24
mm v[32:35],a[160:163],a[104:107],v[32:35]
s_lshl_b32 s62,s62,7
dt a[148:149],v193 offset:16384
mm v[36:39],a[164:167],a[104:107],v[36:39]
s_add_u32 s62,s62,A1START-CT1
mm v[36:39],a[132:135],a[72:75],v[36:39]
dt a[150:151],v193 offset:17408
v_mbcnt_lo_u32_b32 v232,-1,0
mm v[40:43],a[136:139],a[72:75],v[40:43]
v_mbcnt_hi_u32_b32 v232,-1,v232
mm v[40:43],a[168:171],a[104:107],v[40:43]
v_min_u32 v232,1,v232
mm v[44:47],a[172:175],a[104:107],v[44:47]
dt a[180:181],v193 offset:24576
v_lshlrev_b32 v232,14,v232
mm v[44:47],a[140:143],a[72:75],v[44:47]
v_add_u32 v232,s62,v232
mm v[60:63],a[140:143],a[76:79],v[60:63]
v_min_u32 v232,THEEND-CT1-4,v232
mm v[60:63],a[172:175],a[108:111],v[60:63]
dt a[182:183],v193 offset:25600
global_load_dword v255,v232,s[60:61]
mm v[56:59],a[168:171],a[108:111],v[56:59]
v_mbcnt_lo_u32_b32 v232,-1,0
mm v[56:59],a[136:139],a[76:79],v[56:59]
v_mbcnt_hi_u32_b32 v232,-1,v232
mm v[52:55],a[132:135],a[76:79],v[52:55]
dt a[152:153],v194 offset:16384
v_lshlrev_b32 v226,4,v232
mm v[52:55],a[164:167],a[108:111],v[52:55]
v_add_u32 v227,0x1000,v226
mm v[48:51],a[160:163],a[108:111],v[48:51]
v_add_u32 v224,0x8000,v226
mm v[48:51],a[128:131],a[76:79],v[48:51]
dt a[154:155],v194 offset:17408
v_add_u32 v225,0x18000,v226
mm v[64:67],a[128:131],a[80:83],v[64:67]
s_lshr_b32 s61,s29,5
mm v[64:67],a[160:163],a[112:115],v[64:67]
s_and_b32 s62,s29,4
mm v[68:71],a[164:167],a[112:115],v[68:71]
s_lshl_b32 s62,s62,1
dt a[184:185],v194 offset:24576
mm v[68:71],a[132:135],a[80:83],v[68:71]
s_add_u32 s61,s61,s62
mm v[72:75],a[136:139],a[80:83],v[72:75]
dt a[186:187],v194 offset:25600
s_bfe_u32 s33,s29,0x20003
mm v[72:75],a[168:171],a[112:115],v[72:75]
s_and_b32 s62,s29,3
s_lshl_b32 s62,s62,2
)" R"(mm v[76:79],a[172:175],a[112:115],v[76:79]
s_add_u32 s33,s33,s62
mm v[76:79],a[140:143],a[80:83],v[76:79]
s_mul_i32 s33,s33,20
dt a[156:157],v195 offset:16384
mm v[92:95],a[140:143],a[84:87],v[92:95]
s_add_u32 s33,s33,s24
mm v[92:95],a[172:175],a[116:119],v[92:95]
s_lshl_b32 s33,s33,13
mm v[88:91],a[168:171],a[116:119],v[88:91]
s_add_u32 s33,s33,0x4200000
dt a[158:159],v195 offset:17408
mm v[88:91],a[136:139],a[84:87],v[88:91]
s_lshl_b32 s34,s24,13
mm v[84:87],a[132:135],a[84:87],v[84:87]
v_and_b32 v233,15,v232
mm v[84:87],a[164:167],a[116:119],v[84:87]
dt a[188:189],v195 offset:24576
v_cmp_eq_u32 vcc,8,v233
mm v[80:83],a[160:163],a[116:119],v[80:83]
s_nop 0
v_cndmask_b32 v228,0,1.0,vcc
mm v[80:83],a[128:131],a[84:87],v[80:83]
v_cndmask_b32 v229,0,1.0,vcc
mm v[96:99],a[128:131],a[88:91],v[96:99]
s_waitcnt lgkmcnt(2)
dt a[190:191],v195 offset:25600
v_cndmask_b32 v230,0,1.0,vcc
mm v[96:99],a[160:163],a[120:123],v[96:99]
v_cndmask_b32 v231,0,1.0,vcc
mm v[100:103],a[164:167],a[120:123],v[100:103]
v_lshrrev_b32 v234,1,v233
mm v[100:103],a[132:135],a[88:91],v[100:103]
v_lshrrev_b32 v235,4,v232
mm v[104:107],a[136:139],a[88:91],v[104:107]
v_xor_b32 v234,v234,v235
mm v[104:107],a[168:171],a[120:123],v[104:107]
v_lshlrev_b32 v233,7,v233
mm v[108:111],a[172:175],a[120:123],v[108:111]
v_lshl_add_u32 v233,v234,4,v233
mm v[108:111],a[140:143],a[88:91],v[108:111]
v_add_u32 v220,s35,v233
mm v[124:127],a[140:143],a[92:95],v[124:127]
v_xor_b32 v221,64,v220
mm v[124:127],a[172:175],a[124:127],v[124:127]
v_add_u32 v222,0x20000,v220
mm v[120:123],a[168:171],a[124:127],v[120:123]
v_add_u32 v223,0x20000,v221
mm v[120:123],a[136:139],a[92:95],v[120:123]
mm v[116:119],a[132:135],a[92:95],v[116:119]
mm v[116:119],a[164:167],a[124:127],v[116:119]
mm v[112:115],a[160:163],a[124:127],v[112:115]
s_waitcnt vmcnt(10) lgkmcnt(0)
s_barrier
mm v[112:115],a[128:131],a[92:95],v[112:115]
mm v[128:131],a[64:67],a[144:147],v[128:131]
s_waitcnt vmcnt(1)
v_cmp_ne_u64 vcc,s[54:55],v[248:249]
s_cbranch_vccnz FW_S
FW_D:
mm v[128:131],a[96:99],a[176:179],v[128:131]
s_add_u32 s62,s33,0
dt a[128:129],v192 offset:32768
mm v[132:135],a[96:99],a[180:183],v[132:135]
)" R"(s_add_u32 m0,s34,65536
mm v[132:135],a[64:67],a[148:151],v[132:135]
dt a[130:131],v192 offset:33792
mm v[136:139],a[64:67],a[152:155],v[136:139]
bl v226,s[8:11],s62 offen offset:0 sc1 lds
mm v[136:139],a[96:99],a[184:187],v[136:139]
mm v[140:143],a[96:99],a[188:191],v[140:143]
dt a[160:161],v192 offset:40960
mm v[140:143],a[64:67],a[156:159],v[140:143]
mm a[12:15],a[68:71],a[156:159],a[12:15]
dr a[64:67],v196 offset:32768
bl v226,s[8:11],s62 offen offset:1024 sc1 lds
mm a[12:15],a[100:103],a[188:191],a[12:15]
mm a[8:11],a[100:103],a[184:187],a[8:11]
dt a[162:163],v192 offset:41984
mm a[8:11],a[68:71],a[152:155],a[8:11]
dr a[96:99],v197 offset:32768
mm a[4:7],a[68:71],a[148:151],a[4:7]
bl v226,s[8:11],s62 offen offset:2048 sc1 lds
mm a[4:7],a[100:103],a[180:183],a[4:7]
dt a[132:133],v193 offset:32768
mm a[0:3],a[100:103],a[176:179],a[0:3]
mm a[0:3],a[68:71],a[144:147],a[0:3]
mm v[144:147],a[72:75],a[144:147],v[144:147]
dr a[68:71],v196 offset:34816
bl v226,s[8:11],s62 offen offset:3072 sc1 lds
mm v[144:147],a[104:107],a[176:179],v[144:147]
dt a[134:135],v193 offset:33792
mm v[148:151],a[104:107],a[180:183],v[148:151]
s_add_u32 m0,s34,69632
mm v[148:151],a[72:75],a[148:151],v[148:151]
dr a[100:103],v197 offset:34816
mm v[152:155],a[72:75],a[152:155],v[152:155]
dt a[164:165],v193 offset:40960
bl v227,s[8:11],s62 offen offset:0 sc1 lds
mm v[152:155],a[104:107],a[184:187],v[152:155]
mm v[156:159],a[104:107],a[188:191],v[156:159]
mm v[156:159],a[72:75],a[156:159],v[156:159]
mm a[28:31],a[76:79],a[156:159],a[28:31]
dr a[72:75],v196 offset:36864
bl v227,s[8:11],s62 offen offset:1024 sc1 lds
mm a[28:31],a[108:111],a[188:191],a[28:31]
dt a[166:167],v193 offset:41984
mm a[24:27],a[108:111],a[184:187],a[24:27]
mm a[24:27],a[76:79],a[152:155],a[24:27]
dr a[104:107],v197 offset:36864
mm a[20:23],a[76:79],a[148:151],a[20:23]
bl v227,s[8:11],s62 offen offset:2048 sc1 lds
dt a[136:137],v194 offset:32768
mm a[20:23],a[108:111],a[180:183],a[20:23]
mm a[16:19],a[108:111],a[176:179],a[16:19]
dt a[138:139],v194 offset:33792
mm a[16:19],a[76:79],a[144:147],a[16:19]
mm v[160:163],a[80:83],a[144:147],v[160:163]
dr a[76:79],v196 offset:38912
bl v227,s[8:11],s62 offen offset:3072 sc1 lds
mm v[160:163],a[112:115],a[176:179],v[160:163]
s_add_u32 m0,s35,0
mm v[164:167],a[112:115],a[180:183],v[164:167]
s_add_u32 s60,s32,0
dt a[168:169],v194 offset:40960
mm v[164:167],a[80:83],a[148:151],v[164:167]
dr a[108:111],v197 offset:38912
mm v[168:171],a[80:83],a[152:155],v[168:171]
)" R"(bl v200,s[12:15],s60 offen offset:0 lds
mm v[168:171],a[112:115],a[184:187],v[168:171]
dt a[170:171],v194 offset:41984
mm v[172:175],a[112:115],a[188:191],v[172:175]
mm v[172:175],a[80:83],a[156:159],v[172:175]
mm a[44:47],a[84:87],a[156:159],a[44:47]
dr a[80:83],v196 offset:40960
bl v201,s[12:15],s60 offen offset:1024 lds
mm a[44:47],a[116:119],a[188:191],a[44:47]
dt a[140:141],v195 offset:32768
mm a[40:43],a[116:119],a[184:187],a[40:43]
mm a[40:43],a[84:87],a[152:155],a[40:43]
dr a[112:115],v197 offset:40960
mm a[36:39],a[84:87],a[148:151],a[36:39]
dt a[142:143],v195 offset:33792
bl v202,s[12:15],s60 offen offset:2048 lds
mm a[36:39],a[116:119],a[180:183],a[36:39]
mm a[32:35],a[116:119],a[176:179],a[32:35]
mm a[32:35],a[84:87],a[144:147],a[32:35]
mm v[176:179],a[88:91],a[144:147],v[176:179]
dr a[84:87],v196 offset:43008
bl v203,s[12:15],s60 offen offset:3072 lds
mm v[176:179],a[120:123],a[176:179],v[176:179]
s_add_u32 m0,s35,16384
dt a[172:173],v195 offset:40960
mm v[180:183],a[120:123],a[180:183],v[180:183]
s_add_u32 s60,s32,128
mm v[180:183],a[88:91],a[148:151],v[180:183]
dr a[116:119],v197 offset:43008
mm v[184:187],a[88:91],a[152:155],v[184:187]
bl v200,s[12:15],s60 offen offset:0 lds
dt a[174:175],v195 offset:41984
mm v[184:187],a[120:123],a[184:187],v[184:187]
mm v[188:191],a[120:123],a[188:191],v[188:191]
mm v[188:191],a[88:91],a[156:159],v[188:191]
mm a[60:63],a[92:95],a[156:159],a[60:63]
dr a[88:91],v196 offset:45056
bl v201,s[12:15],s60 offen offset:1024 lds
mm a[60:63],a[124:127],a[188:191],a[60:63]
mm a[56:59],a[124:127],a[184:187],a[56:59]
mm a[56:59],a[92:95],a[152:155],a[56:59]
dr a[120:123],v197 offset:45056
mm a[52:55],a[92:95],a[148:151],a[52:55]
bl v202,s[12:15],s60 offen offset:2048 lds
mm a[52:55],a[124:127],a[180:183],a[52:55]
mm a[48:51],a[124:127],a[176:179],a[48:51]
mm a[48:51],a[92:95],a[144:147],a[48:51]
s_waitcnt lgkmcnt(1)
mm v[0:3],a[128:131],a[64:67],v[0:3]
bl v203,s[12:15],s60 offen offset:3072 lds
s_lshl_b32 s62,s29,3
mm v[0:3],a[160:163],a[96:99],v[0:3]
s_add_u32 s62,s62,0x4004000
s_cmp_eq_u32 s24,0
s_cselect_b32 s62,s62,0x80000000
mm v[4:7],a[164:167],a[96:99],v[4:7]
dr a[92:95],v196 offset:47104
v_mov_b32 v236,s54
mm v[4:7],a[132:135],a[64:67],v[4:7]
dr a[124:127],v197 offset:47104
v_mov_b32 v237,s55
mm v[8:11],a[136:139],a[64:67],v[8:11]
dt a[144:145],v192 offset:49152
v_mov_b32 v238,0
)" R"(mm v[8:11],a[168:171],a[96:99],v[8:11]
s_mov_b64 exec,1
buffer_store_dwordx2 v[236:237],v238,s[8:11],s62 offen sc1
s_mov_b64 exec,-1
dt a[146:147],v192 offset:50176
mm v[12:15],a[172:175],a[96:99],v[12:15]
dt a[176:177],v192 offset:57344
mm v[12:15],a[140:143],a[64:67],v[12:15]
dt a[178:179],v192 offset:58368
mm v[28:31],a[140:143],a[68:71],v[28:31]
s_bitcmp1_b32 s26,0
s_cbranch_scc0 CND6
buffer_load_dword v255,v252,s[20:23],s48 offen
CND6:
mm v[28:31],a[172:175],a[100:103],v[28:31]
E0_3:
s_cbranch_execz E0_7
dt a[148:149],v193 offset:49152
mm v[24:27],a[168:171],a[100:103],v[24:27]
dt a[150:151],v193 offset:50176
mm v[24:27],a[136:139],a[68:71],v[24:27]
dt a[180:181],v193 offset:57344
mm v[20:23],a[132:135],a[68:71],v[20:23]
dt a[182:183],v193 offset:58368
mm v[20:23],a[164:167],a[100:103],v[20:23]
dt a[152:153],v194 offset:49152
mm v[16:19],a[160:163],a[100:103],v[16:19]
mm v[16:19],a[128:131],a[68:71],v[16:19]
dt a[154:155],v194 offset:50176
mm v[32:35],a[128:131],a[72:75],v[32:35]
dt a[184:185],v194 offset:57344
mm v[32:35],a[160:163],a[104:107],v[32:35]
mm v[36:39],a[164:167],a[104:107],v[36:39]
dt a[186:187],v194 offset:58368
mm v[36:39],a[132:135],a[72:75],v[36:39]
dt a[156:157],v195 offset:49152
mm v[40:43],a[136:139],a[72:75],v[40:43]
dt a[158:159],v195 offset:50176
mm v[40:43],a[168:171],a[104:107],v[40:43]
mm v[44:47],a[172:175],a[104:107],v[44:47]
dt a[188:189],v195 offset:57344
mm v[44:47],a[140:143],a[72:75],v[44:47]
dt a[190:191],v195 offset:58368
mm v[60:63],a[140:143],a[76:79],v[60:63]
mm v[60:63],a[172:175],a[108:111],v[60:63]
mm v[56:59],a[168:171],a[108:111],v[56:59]
mm v[56:59],a[136:139],a[76:79],v[56:59]
mm v[52:55],a[132:135],a[76:79],v[52:55]
mm v[52:55],a[164:167],a[108:111],v[52:55]
mm v[48:51],a[160:163],a[108:111],v[48:51]
mm v[48:51],a[128:131],a[76:79],v[48:51]
s_waitcnt lgkmcnt(0)
s_barrier
mm v[64:67],a[128:131],a[80:83],v[64:67]
s_add_u32 s62,s33,32768
s_add_u32 m0,s34,98304
mm v[64:67],a[160:163],a[112:115],v[64:67]
bl v226,s[8:11],s62 offen offset:0 sc1 lds
mm v[68:71],a[164:167],a[112:115],v[68:71]
mm v[68:71],a[132:135],a[80:83],v[68:71]
mm v[72:75],a[136:139],a[80:83],v[72:75]
mm v[72:75],a[168:171],a[112:115],v[72:75]
bl v226,s[8:11],s62 offen offset:1024 sc1 lds
mm v[76:79],a[172:175],a[112:115],v[76:79]
)" R"(cv v0,v0,v1
mm v[76:79],a[140:143],a[80:83],v[76:79]
cv v1,v2,v3
mm v[92:95],a[140:143],a[84:87],v[92:95]
cv v2,v4,v5
mm v[92:95],a[172:175],a[116:119],v[92:95]
bl v226,s[8:11],s62 offen offset:2048 sc1 lds
cv v3,v6,v7
mm v[88:91],a[168:171],a[116:119],v[88:91]
cv v8,v8,v9
mm v[88:91],a[136:139],a[84:87],v[88:91]
cv v9,v10,v11
mm v[84:87],a[132:135],a[84:87],v[84:87]
cv v10,v12,v13
mm v[84:87],a[164:167],a[116:119],v[84:87]
bl v226,s[8:11],s62 offen offset:3072 sc1 lds
cv v11,v14,v15
mm v[80:83],a[160:163],a[116:119],v[80:83]
cv v16,v16,v17
mm v[80:83],a[128:131],a[84:87],v[80:83]
s_add_u32 m0,s34,102400
cv v17,v18,v19
mm v[96:99],a[128:131],a[88:91],v[96:99]
cv v18,v20,v21
mm v[96:99],a[160:163],a[120:123],v[96:99]
bl v227,s[8:11],s62 offen offset:0 sc1 lds
cv v19,v22,v23
mm v[100:103],a[164:167],a[120:123],v[100:103]
cv v24,v24,v25
mm v[100:103],a[132:135],a[88:91],v[100:103]
cv v25,v26,v27
mm v[104:107],a[136:139],a[88:91],v[104:107]
cv v26,v28,v29
mm v[104:107],a[168:171],a[120:123],v[104:107]
bl v227,s[8:11],s62 offen offset:1024 sc1 lds
cv v27,v30,v31
mm v[108:111],a[172:175],a[120:123],v[108:111]
cv v32,v32,v33
mm v[108:111],a[140:143],a[88:91],v[108:111]
cv v33,v34,v35
mm v[124:127],a[140:143],a[92:95],v[124:127]
cv v34,v36,v37
mm v[124:127],a[172:175],a[124:127],v[124:127]
bl v227,s[8:11],s62 offen offset:2048 sc1 lds
cv v35,v38,v39
mm v[120:123],a[168:171],a[124:127],v[120:123]
cv v40,v40,v41
mm v[120:123],a[136:139],a[92:95],v[120:123]
cv v41,v42,v43
mm v[116:119],a[132:135],a[92:95],v[116:119]
cv v42,v44,v45
mm v[116:119],a[164:167],a[124:127],v[116:119]
bl v227,s[8:11],s62 offen offset:3072 sc1 lds
cv v43,v46,v47
mm v[112:115],a[160:163],a[124:127],v[112:115]
cv v48,v48,v49
s_add_u32 s62,s33,65536
mm v[112:115],a[128:131],a[92:95],v[112:115]
s_add_u32 m0,s34,32768
cv v49,v50,v51
mm v[128:131],a[64:67],a[144:147],v[128:131]
cv v50,v52,v53
mm v[128:131],a[96:99],a[176:179],v[128:131]
bl v226,s[8:11],s62 offen offset:0 sc1 lds
)" R"(cv v51,v54,v55
mm v[132:135],a[96:99],a[180:183],v[132:135]
cv v56,v56,v57
mm v[132:135],a[64:67],a[148:151],v[132:135]
cv v57,v58,v59
mm v[136:139],a[64:67],a[152:155],v[136:139]
cv v58,v60,v61
mm v[136:139],a[96:99],a[184:187],v[136:139]
bl v226,s[8:11],s62 offen offset:1024 sc1 lds
cv v59,v62,v63
mm v[140:143],a[96:99],a[188:191],v[140:143]
cv v64,v64,v65
mm v[140:143],a[64:67],a[156:159],v[140:143]
cv v65,v66,v67
mm a[12:15],a[68:71],a[156:159],a[12:15]
cv v66,v68,v69
mm a[12:15],a[100:103],a[188:191],a[12:15]
bl v226,s[8:11],s62 offen offset:2048 sc1 lds
cv v67,v70,v71
mm a[8:11],a[100:103],a[184:187],a[8:11]
cv v72,v72,v73
mm a[8:11],a[68:71],a[152:155],a[8:11]
cv v73,v74,v75
mm a[4:7],a[68:71],a[148:151],a[4:7]
cv v74,v76,v77
mm a[4:7],a[100:103],a[180:183],a[4:7]
bl v226,s[8:11],s62 offen offset:3072 sc1 lds
cv v75,v78,v79
mm a[0:3],a[100:103],a[176:179],a[0:3]
cv v80,v80,v81
mm a[0:3],a[68:71],a[144:147],a[0:3]
cv v81,v82,v83
s_add_u32 m0,s34,36864
mm v[144:147],a[72:75],a[144:147],v[144:147]
cv v82,v84,v85
mm v[144:147],a[104:107],a[176:179],v[144:147]
bl v227,s[8:11],s62 offen offset:0 sc1 lds
cv v83,v86,v87
mm v[148:151],a[104:107],a[180:183],v[148:151]
cv v88,v88,v89
mm v[148:151],a[72:75],a[148:151],v[148:151]
cv v89,v90,v91
mm v[152:155],a[72:75],a[152:155],v[152:155]
cv v90,v92,v93
mm v[152:155],a[104:107],a[184:187],v[152:155]
bl v227,s[8:11],s62 offen offset:1024 sc1 lds
cv v91,v94,v95
mm v[156:159],a[104:107],a[188:191],v[156:159]
cv v96,v96,v97
mm v[156:159],a[72:75],a[156:159],v[156:159]
cv v97,v98,v99
mm a[28:31],a[76:79],a[156:159],a[28:31]
cv v98,v100,v101
mm a[28:31],a[108:111],a[188:191],a[28:31]
bl v227,s[8:11],s62 offen offset:2048 sc1 lds
cv v99,v102,v103
mm a[24:27],a[108:111],a[184:187],a[24:27]
cv v104,v104,v105
mm a[24:27],a[76:79],a[152:155],a[24:27]
cv v105,v106,v107
mm a[20:23],a[76:79],a[148:151],a[20:23]
cv v106,v108,v109
mm a[20:23],a[108:111],a[180:183],a[20:23]
bl v227,s[8:11],s62 offen offset:3072 sc1 lds
)" R"(cv v107,v110,v111
mm a[16:19],a[108:111],a[176:179],a[16:19]
bs v[0:3],v216,s[8:11],s36 offen offset:0 sc1
cv v112,v112,v113
mm a[16:19],a[76:79],a[144:147],a[16:19]
cv v113,v114,v115
mm v[160:163],a[80:83],a[144:147],v[160:163]
cv v114,v116,v117
mm v[160:163],a[112:115],a[176:179],v[160:163]
cv v115,v118,v119
mm v[164:167],a[112:115],a[180:183],v[164:167]
cv v120,v120,v121
mm v[164:167],a[80:83],a[148:151],v[164:167]
bs v[8:11],v216,s[8:11],s36 offen offset:1024 sc1
cv v121,v122,v123
mm v[168:171],a[80:83],a[152:155],v[168:171]
cv v122,v124,v125
mm v[168:171],a[112:115],a[184:187],v[168:171]
cv v123,v126,v127
mm v[172:175],a[112:115],a[188:191],v[172:175]
mm v[172:175],a[80:83],a[156:159],v[172:175]
mm a[44:47],a[84:87],a[156:159],a[44:47]
mm a[44:47],a[116:119],a[188:191],a[44:47]
bs v[16:19],v217,s[8:11],s36 offen offset:0 sc1
mm a[40:43],a[116:119],a[184:187],a[40:43]
mm a[40:43],a[84:87],a[152:155],a[40:43]
mm a[36:39],a[84:87],a[148:151],a[36:39]
mm a[36:39],a[116:119],a[180:183],a[36:39]
mm a[32:35],a[116:119],a[176:179],a[32:35]
bs v[24:27],v217,s[8:11],s36 offen offset:1024 sc1
mm a[32:35],a[84:87],a[144:147],a[32:35]
mm v[176:179],a[88:91],a[144:147],v[176:179]
s_waitcnt vmcnt(29)
s_barrier
mm v[176:179],a[120:123],a[176:179],v[176:179]
s_waitcnt vmcnt(25)
dr a[192:195],v220 offset:0
mm v[180:183],a[120:123],a[180:183],v[180:183]
dr a[196:199],v220 offset:2048
mm v[180:183],a[88:91],a[148:151],v[180:183]
dr a[200:203],v224 offset:32768
mm v[184:187],a[88:91],a[152:155],v[184:187]
bs v[32:35],v216,s[8:11],s37 offen offset:0 sc1
mm v[184:187],a[120:123],a[184:187],v[184:187]
dr a[204:207],v224 offset:33792
mm v[188:191],a[120:123],a[188:191],v[188:191]
dr a[208:211],v224 offset:34816
mm v[188:191],a[88:91],a[156:159],v[188:191]
dr a[212:215],v224 offset:35840
mm a[60:63],a[92:95],a[156:159],a[60:63]
dr a[216:219],v224 offset:36864
mm a[60:63],a[124:127],a[188:191],a[60:63]
bs v[40:43],v216,s[8:11],s37 offen offset:1024 sc1
mm a[56:59],a[124:127],a[184:187],a[56:59]
dr a[220:223],v224 offset:37888
mm a[56:59],a[92:95],a[152:155],a[56:59]
dr a[224:227],v224 offset:38912
mm a[52:55],a[92:95],a[148:151],a[52:55]
dr a[228:231],v224 offset:39936
mm a[52:55],a[124:127],a[180:183],a[52:55]
mm a[48:51],a[124:127],a[176:179],a[48:51]
mm a[48:51],a[92:95],a[144:147],a[48:51]
A1START:
dr a[88:91],v224 offset:45056
)" R"(dr a[92:95],v224 offset:46080
dr a[96:99],v224 offset:47104
s_waitcnt lgkmcnt(6)
mm v[4:7],a[200:203],a[192:195],0
s_add_u32 m0,s35,0
dr a[100:103],v224 offset:48128
s_add_u32 s60,s32,512
mm v[68:71],a[200:203],a[196:199],0
s_add_u32 s62,s33,98304
dr a[64:67],v221 offset:0
ard v236,a0
mm v[76:79],a[204:207],a[196:199],0
dr a[68:71],v221 offset:2048
ard v237,a1
mm v[12:15],a[204:207],a[192:195],0
bs v[48:51],v217,s[8:11],s37 offen offset:0 sc1
ard v238,a2
mm v[20:23],a[208:211],a[192:195],0
ard v239,a3
mm v[84:87],a[208:211],a[196:199],0
cv v128,v128,v129
mm v[92:95],a[212:215],a[196:199],0
mm v[28:31],a[212:215],a[192:195],0
cv v129,v130,v131
mm v[36:39],a[216:219],a[192:195],0
s_waitcnt lgkmcnt(1)
bs v[56:59],v217,s[8:11],s37 offen offset:1024 sc1
dr a[200:203],v224 offset:49152
cv v130,v236,v237
mm v[36:39],a[88:91],a[64:67],v[36:39]
dr a[72:75],v224 offset:40960
mm v[100:103],a[216:219],a[196:199],0
s_waitcnt lgkmcnt(2)
cv v131,v238,v239
mm v[100:103],a[88:91],a[68:71],v[100:103]
dr a[204:207],v224 offset:50176
ard v236,a4
mm v[108:111],a[196:199],a[220:223],0
dr a[208:211],v224 offset:51200
ard v237,a5
mm v[108:111],a[68:71],a[92:95],v[108:111]
mm v[44:47],a[192:195],a[220:223],0
dr a[212:215],v224 offset:52224
ard v238,a6
mm v[44:47],a[64:67],a[92:95],v[44:47]
s_waitcnt vmcnt(25)
dr a[104:107],v220 offset:16384
ard v239,a7
mm v[52:55],a[192:195],a[224:227],0
dr a[76:79],v224 offset:41984
mm v[52:55],a[64:67],a[96:99],v[52:55]
dr a[108:111],v220 offset:18432
cv v132,v132,v133
mm v[116:119],a[196:199],a[224:227],0
bl v200,s[12:15],s60 offen offset:0 lds
mm v[116:119],a[68:71],a[96:99],v[116:119]
cv v133,v134,v135
mm v[124:127],a[196:199],a[228:231],v[228:231]
dr a[80:83],v224 offset:43008
cv v134,v236,v237
mm v[124:127],a[68:71],a[100:103],v[124:127]
dr a[84:87],v224 offset:44032
cv v135,v238,v239
mm v[60:63],a[192:195],a[228:231],v[228:231]
)" R"(bl v201,s[12:15],s60 offen offset:1024 lds
mm v[60:63],a[64:67],a[100:103],v[60:63]
s_waitcnt lgkmcnt(3)
dr a[88:91],v224 offset:61440
ard v236,a8
mm v[4:7],a[72:75],a[64:67],v[4:7]
dr a[216:219],v224 offset:53248
ard v237,a9
mm v[4:7],a[200:203],a[104:107],v[4:7]
s_waitcnt lgkmcnt(4)
ard v238,a10
mm v[68:71],a[200:203],a[108:111],v[68:71]
dr a[92:95],v224 offset:62464
mm v[68:71],a[72:75],a[68:71],v[68:71]
dr a[96:99],v224 offset:63488
ard v239,a11
mm v[76:79],a[76:79],a[68:71],v[76:79]
bl v202,s[12:15],s60 offen offset:2048 lds
mm v[76:79],a[204:207],a[108:111],v[76:79]
dr a[100:103],v224 offset:64512
cv v136,v136,v137
mm v[12:15],a[204:207],a[104:107],v[12:15]
dr a[72:75],v224 offset:57344
cv v137,v138,v139
mm v[12:15],a[76:79],a[64:67],v[12:15]
s_waitcnt lgkmcnt(4)
dr a[112:115],v221 offset:16384
cv v138,v236,v237
mm v[20:23],a[80:83],a[64:67],v[20:23]
dr a[220:223],v224 offset:54272
mm v[20:23],a[208:211],a[104:107],v[20:23]
bl v203,s[12:15],s60 offen offset:3072 lds
mm v[84:87],a[208:211],a[108:111],v[84:87]
s_add_u32 m0,s35,16384
dr a[116:119],v221 offset:18432
s_add_u32 s60,s32,640
mm v[84:87],a[80:83],a[68:71],v[84:87]
cv v139,v238,v239
ard v236,a12
mm v[92:95],a[84:87],a[68:71],v[92:95]
dr a[76:79],v224 offset:58368
ard v237,a13
mm v[92:95],a[212:215],a[108:111],v[92:95]
dr a[224:227],v224 offset:55296
ard v238,a14
mm v[28:31],a[212:215],a[104:107],v[28:31]
dr a[80:83],v224 offset:59392
ard v239,a15
mm v[28:31],a[84:87],a[64:67],v[28:31]
dr a[228:231],v224 offset:56320
mm v[36:39],a[216:219],a[104:107],v[36:39]
s_waitcnt lgkmcnt(5)
dr a[84:87],v224 offset:60416
cv v140,v140,v141
mm v[36:39],a[88:91],a[112:115],v[36:39]
s_waitcnt lgkmcnt(5)
cv v141,v142,v143
mm v[100:103],a[88:91],a[116:119],v[100:103]
mm v[100:103],a[216:219],a[108:111],v[100:103]
cv v142,v236,v237
mm v[108:111],a[108:111],a[220:223],v[108:111]
cv v143,v238,v239
mm v[108:111],a[116:119],a[92:95],v[108:111]
mm v[44:47],a[112:115],a[92:95],v[44:47]
)" R"(s_waitcnt vmcnt(20) lgkmcnt(0)
s_barrier
dr a[200:203],v225 offset:0
dr a[204:207],v225 offset:1024
mm v[44:47],a[104:107],a[220:223],v[44:47]
dr a[208:211],v225 offset:2048
aw a0,v96
mm v[52:55],a[104:107],a[224:227],v[52:55]
dr a[212:215],v225 offset:3072
aw a1,v97
mm v[52:55],a[112:115],a[96:99],v[52:55]
dr a[192:195],v222 offset:0
aw a2,v98
mm v[116:119],a[116:119],a[96:99],v[116:119]
dr a[196:199],v222 offset:2048
aw a3,v99
mm v[116:119],a[108:111],a[224:227],v[116:119]
bl v200,s[12:15],s60 offen offset:0 lds
mm v[124:127],a[108:111],a[228:231],v[124:127]
aw a4,v104
mm v[124:127],a[116:119],a[100:103],v[124:127]
aw a5,v105
mm v[60:63],a[112:115],a[100:103],v[60:63]
aw a6,v106
mm v[60:63],a[104:107],a[228:231],v[60:63]
bl v201,s[12:15],s60 offen offset:1024 lds
mm v[4:7],a[72:75],a[112:115],v[4:7]
s_waitcnt lgkmcnt(1)
dr a[88:91],v225 offset:12288
aw a7,v107
mm v[4:7],a[200:203],a[192:195],v[4:7]
s_waitcnt lgkmcnt(1)
dr a[216:219],v225 offset:4096
aw a8,v112
mm v[68:71],a[200:203],a[196:199],v[68:71]
aw a9,v113
mm v[68:71],a[72:75],a[116:119],v[68:71]
dr a[92:95],v225 offset:13312
mm v[76:79],a[76:79],a[116:119],v[76:79]
bl v202,s[12:15],s60 offen offset:2048 lds
mm v[76:79],a[204:207],a[196:199],v[76:79]
dr a[96:99],v225 offset:14336
aw a10,v114
mm v[12:15],a[204:207],a[192:195],v[12:15]
dr a[100:103],v225 offset:15360
aw a11,v115
mm v[12:15],a[76:79],a[112:115],v[12:15]
dr a[64:67],v223 offset:0
aw a12,v120
mm v[20:23],a[80:83],a[112:115],v[20:23]
dr a[220:223],v225 offset:5120
aw a13,v121
mm v[20:23],a[208:211],a[192:195],v[20:23]
bl v203,s[12:15],s60 offen offset:3072 lds
mm v[84:87],a[208:211],a[196:199],v[84:87]
s_add_u32 m0,s34,65536
dr a[68:71],v223 offset:2048
s_add_u32 s60,s32,768
mm v[84:87],a[80:83],a[116:119],v[84:87]
aw a14,v122
aw a15,v123
mm v[92:95],a[84:87],a[116:119],v[92:95]
dr a[224:227],v225 offset:6144
mm v[92:95],a[212:215],a[196:199],v[92:95]
)" R"(bl v226,s[8:11],s62 offen offset:0 sc1 lds
mm v[28:31],a[212:215],a[192:195],v[28:31]
dr a[228:231],v225 offset:7168
ard v236,a16
mm v[28:31],a[84:87],a[112:115],v[28:31]
s_waitcnt lgkmcnt(3)
dr a[200:203],v225 offset:16384
ard v237,a17
mm v[36:39],a[216:219],a[192:195],v[36:39]
ard v238,a18
mm v[36:39],a[88:91],a[64:67],v[36:39]
s_waitcnt lgkmcnt(3)
bl v226,s[8:11],s62 offen offset:1024 sc1 lds
mm v[100:103],a[88:91],a[68:71],v[100:103]
dr a[72:75],v225 offset:8192
ard v239,a19
mm v[100:103],a[216:219],a[196:199],v[100:103]
dr a[204:207],v225 offset:17408
cv v144,v144,v145
mm v[108:111],a[196:199],a[220:223],v[108:111]
dr a[208:211],v225 offset:18432
cv v145,v146,v147
mm v[108:111],a[68:71],a[92:95],v[108:111]
dr a[212:215],v225 offset:19456
cv v146,v236,v237
mm v[44:47],a[64:67],a[92:95],v[44:47]
dr a[104:107],v222 offset:16384
mm v[44:47],a[192:195],a[220:223],v[44:47]
s_waitcnt lgkmcnt(5)
bl v226,s[8:11],s62 offen offset:2048 sc1 lds
mm v[52:55],a[192:195],a[224:227],v[52:55]
dr a[76:79],v225 offset:9216
cv v147,v238,v239
mm v[52:55],a[64:67],a[96:99],v[52:55]
dr a[108:111],v222 offset:18432
ard v236,a20
mm v[116:119],a[68:71],a[96:99],v[116:119]
ard v237,a21
mm v[116:119],a[196:199],a[224:227],v[116:119]
bl v226,s[8:11],s62 offen offset:3072 sc1 lds
mm v[124:127],a[196:199],a[228:231],v[124:127]
s_add_u32 m0,s34,69632
dr a[80:83],v225 offset:10240
ard v238,a22
mm v[124:127],a[68:71],a[100:103],v[124:127]
ard v239,a23
mm v[60:63],a[64:67],a[100:103],v[60:63]
dr a[84:87],v225 offset:11264
cv v148,v148,v149
mm v[60:63],a[192:195],a[228:231],v[60:63]
s_waitcnt lgkmcnt(3)
bl v227,s[8:11],s62 offen offset:0 sc1 lds
mm v[4:7],a[72:75],a[64:67],v[4:7]
dr a[88:91],v225 offset:28672
cv v149,v150,v151
mm v[4:7],a[200:203],a[104:107],v[4:7]
s_waitcnt lgkmcnt(3)
dr a[216:219],v225 offset:20480
cv v150,v236,v237
mm v[68:71],a[200:203],a[108:111],v[68:71]
cv v151,v238,v239
mm v[68:71],a[72:75],a[68:71],v[68:71]
dr a[92:95],v225 offset:29696
mm v[76:79],a[76:79],a[68:71],v[76:79]
)" R"(bl v227,s[8:11],s62 offen offset:1024 sc1 lds
mm v[76:79],a[204:207],a[108:111],v[76:79]
dr a[96:99],v225 offset:30720
ard v236,a24
mm v[12:15],a[204:207],a[104:107],v[12:15]
dr a[100:103],v225 offset:31744
ard v237,a25
mm v[12:15],a[76:79],a[64:67],v[12:15]
s_waitcnt lgkmcnt(4)
dr a[72:75],v225 offset:24576
ard v238,a26
mm v[20:23],a[80:83],a[64:67],v[20:23]
dr a[112:115],v223 offset:16384
ard v239,a27
mm v[20:23],a[208:211],a[104:107],v[20:23]
dr a[220:223],v225 offset:21504
cv v152,v152,v153
bl v227,s[8:11],s62 offen offset:2048 sc1 lds
mm v[84:87],a[208:211],a[108:111],v[84:87]
dr a[116:119],v223 offset:18432
mm v[84:87],a[80:83],a[68:71],v[84:87]
cv v153,v154,v155
mm v[92:95],a[84:87],a[68:71],v[92:95]
s_mov_b32 s16,s76
dr a[76:79],v225 offset:25600
s_and_b32 s17,s77,0xffff
mm v[92:95],a[212:215],a[108:111],v[92:95]
s_mov_b32 s18,0x500000
bl v227,s[8:11],s62 offen offset:3072 sc1 lds
mm v[28:31],a[212:215],a[104:107],v[28:31]
E0_4:
s_cbranch_execz E0_R
s_add_u32 m0,s35,131072
dr a[224:227],v225 offset:22528
s_add_u32 s62,s33,131072
mm v[28:31],a[84:87],a[64:67],v[28:31]
s_waitcnt lgkmcnt(4)
dr a[80:83],v225 offset:26624
s_mov_b32 s19,0x20000
mm v[36:39],a[216:219],a[104:107],v[36:39]
s_lshl_b32 s51,s24,12
dr a[228:231],v225 offset:23552
s_lshl_b32 s49,s24,17
mm v[36:39],a[88:91],a[112:115],v[36:39]
s_waitcnt lgkmcnt(4)
dr a[84:87],v225 offset:27648
s_lshl_b32 s52,s25,8
mm v[100:103],a[88:91],a[116:119],v[100:103]
s_add_u32 s49,s49,s52
bl v200,s[12:15],s60 offen offset:0 lds
mm v[100:103],a[216:219],a[108:111],v[100:103]
cv v154,v236,v237
cv v155,v238,v239
mm v[108:111],a[108:111],a[220:223],v[108:111]
ard v236,a28
ard v237,a29
bl v201,s[12:15],s60 offen offset:1024 lds
mm v[108:111],a[116:119],a[92:95],v[108:111]
ard v238,a30
ard v239,a31
mm v[44:47],a[112:115],a[92:95],v[44:47]
s_waitcnt vmcnt(26) lgkmcnt(0)
s_barrier
dr a[200:203],v224 offset:0
)" R"(dr a[204:207],v224 offset:1024
mm v[44:47],a[104:107],a[220:223],v[44:47]
dr a[208:211],v224 offset:2048
cv v156,v156,v157
mm v[52:55],a[104:107],a[224:227],v[52:55]
dr a[212:215],v224 offset:3072
cv v157,v158,v159
mm v[52:55],a[112:115],a[96:99],v[52:55]
s_waitcnt vmcnt(14)
dr a[192:195],v220 offset:0
cv v158,v236,v237
mm v[116:119],a[116:119],a[96:99],v[116:119]
dr a[196:199],v220 offset:2048
cv v159,v238,v239
mm v[116:119],a[108:111],a[224:227],v[116:119]
bl v202,s[12:15],s60 offen offset:2048 lds
mm v[124:127],a[108:111],a[228:231],v[124:127]
aw a16,v64
aw a17,v65
mm v[124:127],a[116:119],a[100:103],v[124:127]
aw a18,v66
mm v[60:63],a[112:115],a[100:103],v[60:63]
mm v[60:63],a[104:107],a[228:231],v[60:63]
bl v203,s[12:15],s60 offen offset:3072 lds
mm v[4:7],a[72:75],a[112:115],v[4:7]
s_waitcnt lgkmcnt(1)
dr a[88:91],v224 offset:12288
s_add_u32 m0,s35,147456
mm v[4:7],a[200:203],a[192:195],v[4:7]
s_waitcnt lgkmcnt(1)
dr a[216:219],v224 offset:4096
s_add_u32 s60,s32,896
mm v[68:71],a[200:203],a[196:199],v[68:71]
aw a19,v67
aw a20,v72
mm v[68:71],a[72:75],a[116:119],v[68:71]
bl v200,s[12:15],s60 offen offset:0 lds
mm v[76:79],a[76:79],a[116:119],v[76:79]
dr a[92:95],v224 offset:13312
aw a21,v73
mm v[76:79],a[204:207],a[196:199],v[76:79]
dr a[96:99],v224 offset:14336
aw a22,v74
mm v[12:15],a[204:207],a[192:195],v[12:15]
dr a[100:103],v224 offset:15360
aw a23,v75
mm v[12:15],a[76:79],a[112:115],v[12:15]
dr a[64:67],v221 offset:0
aw a24,v80
mm v[20:23],a[80:83],a[112:115],v[20:23]
bl v201,s[12:15],s60 offen offset:1024 lds
mm v[20:23],a[208:211],a[192:195],v[20:23]
dr a[220:223],v224 offset:5120
aw a25,v81
mm v[84:87],a[208:211],a[196:199],v[84:87]
dr a[68:71],v221 offset:2048
aw a26,v82
mm v[84:87],a[80:83],a[116:119],v[84:87]
aw a27,v83
mm v[92:95],a[84:87],a[116:119],v[92:95]
bl v202,s[12:15],s60 offen offset:2048 lds
mm v[92:95],a[212:215],a[196:199],v[92:95]
dr a[224:227],v224 offset:6144
aw a28,v88
)" R"(mm v[28:31],a[212:215],a[192:195],v[28:31]
dr a[228:231],v224 offset:7168
aw a29,v89
mm v[28:31],a[84:87],a[112:115],v[28:31]
s_waitcnt lgkmcnt(4)
aw a30,v90
mm v[36:39],a[216:219],a[192:195],v[36:39]
bl v203,s[12:15],s60 offen offset:3072 lds
mm v[36:39],a[88:91],a[64:67],v[36:39]
s_waitcnt lgkmcnt(2)
dr a[200:203],v224 offset:16384
s_add_u32 m0,s34,98304
mm v[100:103],a[88:91],a[68:71],v[100:103]
s_add_u32 s60,s32,1024
dr a[72:75],v224 offset:8192
aw a31,v91
mm v[100:103],a[216:219],a[196:199],v[100:103]
dr a[204:207],v224 offset:17408
ard v236,a32
mm v[108:111],a[196:199],a[220:223],v[108:111]
dr a[208:211],v224 offset:18432
ard v237,a33
mm v[108:111],a[68:71],a[92:95],v[108:111]
dr a[212:215],v224 offset:19456
ard v238,a34
mm v[44:47],a[64:67],a[92:95],v[44:47]
s_waitcnt vmcnt(16)
dr a[104:107],v220 offset:16384
ard v239,a35
bl v226,s[8:11],s62 offen offset:0 sc1 lds
mm v[44:47],a[192:195],a[220:223],v[44:47]
s_waitcnt lgkmcnt(6)
cv v160,v160,v161
mm v[52:55],a[192:195],a[224:227],v[52:55]
dr a[76:79],v224 offset:9216
mm v[52:55],a[64:67],a[96:99],v[52:55]
dr a[108:111],v220 offset:18432
cv v161,v162,v163
mm v[116:119],a[68:71],a[96:99],v[116:119]
bl v226,s[8:11],s62 offen offset:1024 sc1 lds
mm v[116:119],a[196:199],a[224:227],v[116:119]
cv v162,v236,v237
mm v[124:127],a[196:199],a[228:231],v[124:127]
cv v163,v238,v239
mm v[124:127],a[68:71],a[100:103],v[124:127]
dr a[80:83],v224 offset:10240
ard v236,a36
mm v[60:63],a[64:67],a[100:103],v[60:63]
bl v226,s[8:11],s62 offen offset:2048 sc1 lds
mm v[60:63],a[192:195],a[228:231],v[60:63]
s_waitcnt lgkmcnt(2)
dr a[84:87],v224 offset:11264
ard v237,a37
mm v[4:7],a[72:75],a[64:67],v[4:7]
dr a[88:91],v224 offset:28672
ard v238,a38
mm v[4:7],a[200:203],a[104:107],v[4:7]
s_waitcnt lgkmcnt(3)
ard v239,a39
mm v[68:71],a[200:203],a[108:111],v[68:71]
dr a[216:219],v224 offset:20480
mm v[68:71],a[72:75],a[68:71],v[68:71]
bl v226,s[8:11],s62 offen offset:3072 sc1 lds
mm v[76:79],a[76:79],a[68:71],v[76:79]
)" R"(s_add_u32 m0,s34,102400
dr a[92:95],v224 offset:29696
cv v164,v164,v165
mm v[76:79],a[204:207],a[108:111],v[76:79]
dr a[96:99],v224 offset:30720
cv v165,v166,v167
mm v[12:15],a[204:207],a[104:107],v[12:15]
dr a[100:103],v224 offset:31744
cv v166,v236,v237
mm v[12:15],a[76:79],a[64:67],v[12:15]
s_waitcnt lgkmcnt(4)
dr a[72:75],v224 offset:24576
cv v167,v238,v239
mm v[20:23],a[80:83],a[64:67],v[20:23]
dr a[112:115],v221 offset:16384
bl v227,s[8:11],s62 offen offset:0 sc1 lds
mm v[20:23],a[208:211],a[104:107],v[20:23]
dr a[220:223],v224 offset:21504
ard v236,a40
mm v[84:87],a[208:211],a[108:111],v[84:87]
dr a[116:119],v221 offset:18432
ard v237,a41
mm v[84:87],a[80:83],a[68:71],v[84:87]
ard v238,a42
mm v[92:95],a[84:87],a[68:71],v[92:95]
bl v227,s[8:11],s62 offen offset:1024 sc1 lds
mm v[92:95],a[212:215],a[108:111],v[92:95]
dr a[76:79],v224 offset:25600
ard v239,a43
mm v[28:31],a[212:215],a[104:107],v[28:31]
dr a[224:227],v224 offset:22528
cv v168,v168,v169
mm v[28:31],a[84:87],a[64:67],v[28:31]
s_waitcnt lgkmcnt(4)
dr a[80:83],v224 offset:26624
mm v[36:39],a[216:219],a[104:107],v[36:39]
dr a[228:231],v224 offset:23552
cv v169,v170,v171
mm v[36:39],a[88:91],a[112:115],v[36:39]
s_waitcnt lgkmcnt(4)
dr a[84:87],v224 offset:27648
cv v170,v236,v237
bl v227,s[8:11],s62 offen offset:2048 sc1 lds
mm v[100:103],a[88:91],a[116:119],v[100:103]
mm v[100:103],a[216:219],a[108:111],v[100:103]
cv v171,v238,v239
mm v[108:111],a[108:111],a[220:223],v[108:111]
ard v236,a44
bl v227,s[8:11],s62 offen offset:3072 sc1 lds
mm v[108:111],a[116:119],a[92:95],v[108:111]
s_add_u32 m0,s35,0
ard v237,a45
mm v[44:47],a[112:115],a[92:95],v[44:47]
s_waitcnt vmcnt(16) lgkmcnt(0)
s_barrier
dr a[200:203],v224 offset:32768
dr a[204:207],v224 offset:33792
mm v[44:47],a[104:107],a[220:223],v[44:47]
dr a[208:211],v224 offset:34816
ard v238,a46
mm v[52:55],a[104:107],a[224:227],v[52:55]
dr a[212:215],v224 offset:35840
ard v239,a47
mm v[52:55],a[112:115],a[96:99],v[52:55]
)" R"(s_waitcnt vmcnt(12)
dr a[192:195],v222 offset:0
mm v[116:119],a[116:119],a[96:99],v[116:119]
dr a[196:199],v222 offset:2048
cv v172,v172,v173
bl v200,s[12:15],s60 offen offset:0 lds
mm v[116:119],a[108:111],a[224:227],v[116:119]
cv v173,v174,v175
mm v[124:127],a[108:111],a[228:231],v[124:127]
mm v[124:127],a[116:119],a[100:103],v[124:127]
cv v174,v236,v237
mm v[60:63],a[112:115],a[100:103],v[60:63]
bl v201,s[12:15],s60 offen offset:1024 lds
mm v[60:63],a[104:107],a[228:231],v[60:63]
dr a[88:91],v224 offset:45056
cv v175,v238,v239
mm v[4:7],a[72:75],a[112:115],v[4:7]
s_waitcnt lgkmcnt(2)
dr a[216:219],v224 offset:36864
ard v236,a48
mm v[4:7],a[200:203],a[192:195],v[4:7]
s_waitcnt lgkmcnt(2)
ard v237,a49
mm v[68:71],a[200:203],a[196:199],v[68:71]
bl v202,s[12:15],s60 offen offset:2048 lds
mm v[68:71],a[72:75],a[116:119],v[68:71]
dr a[92:95],v224 offset:46080
ard v238,a50
mm v[76:79],a[76:79],a[116:119],v[76:79]
dr a[96:99],v224 offset:47104
ard v239,a51
mm v[76:79],a[204:207],a[196:199],v[76:79]
cv v176,v176,v177
mm v[12:15],a[204:207],a[192:195],v[12:15]
dr a[100:103],v224 offset:48128
mm v[12:15],a[76:79],a[112:115],v[12:15]
bl v203,s[12:15],s60 offen offset:3072 lds
mm v[20:23],a[80:83],a[112:115],v[20:23]
s_add_u32 m0,s35,16384
dr a[64:67],v223 offset:0
s_add_u32 s60,s32,1152
mm v[20:23],a[208:211],a[192:195],v[20:23]
dr a[220:223],v224 offset:37888
cv v177,v178,v179
mm v[84:87],a[208:211],a[196:199],v[84:87]
dr a[68:71],v223 offset:2048
cv v178,v236,v237
mm v[84:87],a[80:83],a[116:119],v[84:87]
bl v200,s[12:15],s60 offen offset:0 lds
mm v[92:95],a[84:87],a[116:119],v[92:95]
dr a[224:227],v224 offset:38912
cv v179,v238,v239
mm v[92:95],a[212:215],a[196:199],v[92:95]
ard v236,a52
ard v237,a53
mm v[28:31],a[212:215],a[192:195],v[28:31]
dr a[228:231],v224 offset:39936
ard v238,a54
mm v[28:31],a[84:87],a[112:115],v[28:31]
s_waitcnt lgkmcnt(4)
bl v201,s[12:15],s60 offen offset:1024 lds
mm v[36:39],a[216:219],a[192:195],v[36:39]
dr a[200:203],v224 offset:49152
ard v239,a55
)" R"(mm v[36:39],a[88:91],a[64:67],v[36:39]
s_waitcnt lgkmcnt(3)
dr a[72:75],v224 offset:40960
cv v180,v180,v181
mm v[100:103],a[88:91],a[68:71],v[100:103]
cv v181,v182,v183
mm v[100:103],a[216:219],a[196:199],v[100:103]
dr a[204:207],v224 offset:50176
mm v[108:111],a[196:199],a[220:223],v[108:111]
bl v202,s[12:15],s60 offen offset:2048 lds
mm v[108:111],a[68:71],a[92:95],v[108:111]
dr a[208:211],v224 offset:51200
cv v182,v236,v237
mm v[44:47],a[64:67],a[92:95],v[44:47]
dr a[212:215],v224 offset:52224
cv v183,v238,v239
mm v[44:47],a[192:195],a[220:223],v[44:47]
s_waitcnt lgkmcnt(4)
s_waitcnt vmcnt(15)
dr a[104:107],v222 offset:16384
mm v[52:55],a[192:195],a[224:227],v[52:55]
s_mul_i32 s48,s24,-40960
dr a[76:79],v224 offset:41984
s_add_u32 s48,s48,0x1e000
mm v[52:55],a[64:67],a[96:99],v[52:55]
s_mul_i32 s52,s26,-327680
bl v203,s[12:15],s60 offen offset:3072 lds
mm v[116:119],a[68:71],a[96:99],v[116:119]
s_add_u32 m0,s51,32768
dr a[108:111],v222 offset:18432
s_mov_b32 s12,s72
mm v[116:119],a[196:199],a[224:227],v[116:119]
s_and_b32 s13,s73,0xffff
s_mov_b32 s14,0x280000
s_mov_b32 s15,0x20000
mm v[124:127],a[196:199],a[228:231],v[124:127]
s_add_u32 s52,s52,0x24e000
dr a[80:83],v224 offset:43008
v_add_u32 v202,s48,v202
mm v[124:127],a[68:71],a[100:103],v[124:127]
s_lshl_b32 s50,s24,13
bl v208,s[16:19],s49 offen offset:0 lds
mm v[60:63],a[64:67],a[100:103],v[60:63]
dr a[84:87],v224 offset:44032
v_add_u32 v203,s48,v203
mm v[60:63],a[192:195],a[228:231],v[60:63]
s_waitcnt lgkmcnt(3)
dr a[88:91],v224 offset:61440
v_add_u32 v206,s48,v206
mm v[4:7],a[72:75],a[64:67],v[4:7]
v_add_u32 v207,s48,v207
v_add_u32 v204,s52,v204
mm v[4:7],a[200:203],a[104:107],v[4:7]
s_waitcnt lgkmcnt(3)
dr a[216:219],v224 offset:53248
v_add_u32 v205,s52,v205
mm v[68:71],a[200:203],a[108:111],v[68:71]
s_mul_i32 s48,s26,163840
dr a[92:95],v224 offset:62464
v_add_u32 v206,s52,v206
mm v[68:71],a[72:75],a[68:71],v[68:71]
bl v209,s[16:19],s49 offen offset:1024 lds
mm v[76:79],a[76:79],a[68:71],v[76:79]
dr a[96:99],v224 offset:63488
)" R"(v_add_u32 v207,s52,v207
mm v[76:79],a[204:207],a[108:111],v[76:79]
s_mul_i32 s52,s24,20480
dr a[100:103],v224 offset:64512
s_add_u32 s48,s48,s52
mm v[12:15],a[204:207],a[104:107],v[12:15]
dr a[72:75],v224 offset:57344
ard v236,a56
mm v[12:15],a[76:79],a[64:67],v[12:15]
s_waitcnt lgkmcnt(5)
dr a[112:115],v223 offset:16384
ard v237,a57
mm v[20:23],a[80:83],a[64:67],v[20:23]
dr a[220:223],v224 offset:54272
v_mbcnt_lo_u32_b32 v240,-1,0
mm v[20:23],a[208:211],a[104:107],v[20:23]
dr a[116:119],v223 offset:18432
v_mbcnt_hi_u32_b32 v240,-1,v240
mm v[84:87],a[208:211],a[108:111],v[84:87]
bl v210,s[16:19],s49 offen offset:2048 lds
mm v[84:87],a[80:83],a[68:71],v[84:87]
v_lshrrev_b32 v243,4,v240
v_bfe_u32 v242,v240,2,2
mm v[92:95],a[84:87],a[68:71],v[92:95]
dr a[76:79],v224 offset:58368
v_lshl_add_u32 v241,v243,3,v242
mm v[92:95],a[212:215],a[108:111],v[92:95]
dr a[224:227],v224 offset:55296
v_lshlrev_b32 v241,8,v241
mm v[28:31],a[212:215],a[104:107],v[28:31]
dr a[80:83],v224 offset:59392
v_and_b32 v243,1,v243
mm v[28:31],a[84:87],a[64:67],v[28:31]
s_waitcnt lgkmcnt(4)
dr a[228:231],v224 offset:56320
v_lshl_add_u32 v242,v243,2,v242
mm v[36:39],a[216:219],a[104:107],v[36:39]
bl v211,s[16:19],s49 offen offset:3072 lds
mm v[36:39],a[88:91],a[112:115],v[36:39]
s_waitcnt lgkmcnt(4)
dr a[84:87],v224 offset:60416
s_add_u32 m0,s50,65536
mm v[100:103],a[88:91],a[116:119],v[100:103]
s_add_u32 s49,s49,0x80000
v_lshl_add_u32 v241,v242,5,v241
v_and_b32 v242,3,v240
mm v[100:103],a[216:219],a[108:111],v[100:103]
v_lshl_add_u32 v241,v242,3,v241
v_add_u32 v192,0x4000,v241
mm v[108:111],a[108:111],a[220:223],v[108:111]
v_xor_b32 v193,32,v192
v_xor_b32 v194,64,v192
mm v[108:111],a[116:119],a[92:95],v[108:111]
v_xor_b32 v195,96,v192
v_xor_b32 v196,128,v192
mm v[44:47],a[112:115],a[92:95],v[44:47]
s_waitcnt vmcnt(12) lgkmcnt(0)
s_barrier
dr a[200:203],v225 offset:0
dr a[204:207],v225 offset:1024
mm v[44:47],a[104:107],a[220:223],v[44:47]
dr a[208:211],v225 offset:2048
v_xor_b32 v197,160,v192
mm v[52:55],a[104:107],a[224:227],v[52:55]
)" R"(s_mul_i32 s62,s24,5120
dr a[212:215],v225 offset:3072
v_xor_b32 v198,192,v192
mm v[52:55],a[112:115],a[96:99],v[52:55]
s_lshr_b32 s61,s29,5
s_waitcnt vmcnt(8)
dr a[120:123],v220 offset:0
bl v200,s[12:15],s48 offen offset:0 lds
mm v[116:119],a[116:119],a[96:99],v[116:119]
s_and_b32 s63,s29,4
dr a[124:127],v220 offset:2048
v_xor_b32 v199,224,v192
mm v[116:119],a[108:111],a[224:227],v[116:119]
s_lshl_b32 s63,s63,1
v_and_b32 v241,15,v240
v_lshrrev_b32 v242,1,v241
mm v[124:127],a[108:111],a[228:231],v[124:127]
s_add_u32 s61,s61,s63
v_lshrrev_b32 v243,4,v240
v_xor_b32 v242,v242,v243
mm v[124:127],a[116:119],a[100:103],v[124:127]
s_and_b32 s63,s29,3
bl v201,s[12:15],s48 offen offset:1024 lds
mm v[60:63],a[112:115],a[100:103],v[60:63]
s_lshl_b32 s63,s63,2
v_lshlrev_b32 v241,7,v241
v_lshl_add_u32 v241,v242,4,v241
mm v[60:63],a[104:107],a[228:231],v[60:63]
dr a[88:91],v225 offset:12288
v_add_u32 v241,s50,v241
mm v[4:7],a[72:75],a[112:115],v[4:7]
s_waitcnt lgkmcnt(2)
dr a[216:219],v225 offset:4096
v_add_u32 v212,0x10000,v241
mm v[4:7],a[200:203],a[120:123],v[4:7]
s_waitcnt lgkmcnt(2)
v_xor_b32 v213,64,v212
v_add_u32 v214,0x20000,v241
mm v[68:71],a[200:203],a[124:127],v[68:71]
dr a[92:95],v225 offset:13312
v_xor_b32 v215,64,v214
mm v[68:71],a[72:75],a[116:119],v[68:71]
bl v202,s[12:15],s48 offen offset:2048 lds
mm v[76:79],a[76:79],a[116:119],v[76:79]
dr a[96:99],v225 offset:14336
ard v238,a58
mm v[76:79],a[204:207],a[124:127],v[76:79]
dr a[100:103],v225 offset:15360
ard v239,a59
mm v[12:15],a[204:207],a[120:123],v[12:15]
cv v184,v184,v185
cv v185,v186,v187
mm v[12:15],a[76:79],a[112:115],v[12:15]
dr a[128:131],v221 offset:0
cv v186,v236,v237
mm v[20:23],a[80:83],a[112:115],v[20:23]
dr a[220:223],v225 offset:5120
cv v187,v238,v239
mm v[20:23],a[208:211],a[120:123],v[20:23]
dr a[132:135],v221 offset:2048
ard v236,a60
mm v[84:87],a[208:211],a[124:127],v[84:87]
bl v203,s[12:15],s48 offen offset:3072 lds
mm v[84:87],a[80:83],a[116:119],v[84:87]
)" R"(s_add_u32 m0,s50,69632
dr a[224:227],v225 offset:6144
ard v237,a61
mm v[92:95],a[84:87],a[116:119],v[92:95]
ard v238,a62
ard v239,a63
mm v[92:95],a[212:215],a[124:127],v[92:95]
dr a[228:231],v225 offset:7168
cv v188,v188,v189
mm v[28:31],a[212:215],a[120:123],v[28:31]
dr a[72:75],v225 offset:8192
cv v189,v190,v191
mm v[28:31],a[84:87],a[112:115],v[28:31]
s_waitcnt lgkmcnt(4)
bl v204,s[12:15],s48 offen offset:0 lds
mm v[36:39],a[216:219],a[120:123],v[36:39]
dr a[200:203],v225 offset:16384
cv v190,v236,v237
mm v[36:39],a[88:91],a[128:131],v[36:39]
s_waitcnt lgkmcnt(4)
dr a[204:207],v225 offset:17408
cv v191,v238,v239
mm v[100:103],a[88:91],a[132:135],v[100:103]
v_mbcnt_lo_u32_b32 v232,-1,0
v_mbcnt_hi_u32_b32 v232,-1,v232
mm v[100:103],a[216:219],a[124:127],v[100:103]
dr a[76:79],v225 offset:9216
v_and_b32 v233,15,v232
mm v[108:111],a[124:127],a[220:223],v[108:111]
dr a[208:211],v225 offset:18432
v_mul_u32_u24 v233,160,v233
mm v[108:111],a[132:135],a[92:95],v[108:111]
bl v205,s[12:15],s48 offen offset:1024 lds
mm v[44:47],a[128:131],a[92:95],v[44:47]
dr a[212:215],v225 offset:19456
v_bfe_u32 v234,v232,4,1
mm v[44:47],a[120:123],a[220:223],v[44:47]
s_waitcnt lgkmcnt(4)
dr a[80:83],v225 offset:10240
v_mul_u32_u24 v234,80,v234
mm v[52:55],a[120:123],a[224:227],v[52:55]
v_add3_u32 v236,v233,v234,s62
v_bfi_b32 v233,v232,0,32
mm v[52:55],a[128:131],a[96:99],v[52:55]
s_mul_i32 s62,s24,3072
s_waitcnt vmcnt(10)
dr a[192:195],v220 offset:16384
mm v[116:119],a[132:135],a[96:99],v[116:119]
s_add_u32 s62,s62,20480
dr a[196:199],v220 offset:18432
v_lshl_or_b32 v236,v233,15,v236
mm v[116:119],a[124:127],a[224:227],v[116:119]
bl v206,s[12:15],s48 offen offset:2048 lds
mm v[124:127],a[124:127],a[228:231],v[124:127]
dr a[216:219],v225 offset:20480
v_add_u32 v237,s62,v226
mm v[124:127],a[132:135],a[100:103],v[124:127]
s_mul_i32 s62,s27,80
dr a[84:87],v225 offset:11264
v_lshrrev_b32 v233,2,v232
mm v[60:63],a[128:131],a[100:103],v[60:63]
v_mul_u32_u24 v233,160,v233
v_and_b32 v234,3,v232
mm v[60:63],a[120:123],a[228:231],v[60:63]
)" R"(dr a[220:223],v225 offset:21504
v_lshlrev_b32 v234,3,v234
mm v[4:7],a[72:75],a[128:131],v[4:7]
dr a[224:227],v225 offset:22528
v_add3_u32 v238,v233,v234,s62
mm v[68:71],a[72:75],a[132:135],v[68:71]
s_waitcnt lgkmcnt(4)
bl v207,s[12:15],s48 offen offset:3072 lds
mm v[76:79],a[76:79],a[132:135],v[76:79]
E0_5:
s_cbranch_execz E0_R
s_cmp_eq_u32 s28,0
s_cselect_b32 s62,s27,2
dr a[228:231],v225 offset:23552
mm v[12:15],a[76:79],a[128:131],v[12:15]
s_lshl_b32 s62,s62,10
dr a[72:75],v225 offset:24576
s_add_u32 s62,s62,20480
mm v[20:23],a[80:83],a[128:131],v[20:23]
s_add_u32 s48,s48,128
v_add_u32 v239,s62,v226
s_bfe_u32 s62,s29,0x20003
mm v[84:87],a[80:83],a[132:135],v[84:87]
s_waitcnt lgkmcnt(4)
bs v[128:131],v218,s[8:11],s36 offen offset:0 sc1
dr a[76:79],v225 offset:25600
s_add_u32 s62,s62,s63
s_mul_i32 s62,s62,12
s_mul_i32 s63,s24,3
s_add_u32 s62,s62,s63
s_lshl_b32 s62,s62,4
mm v[92:95],a[84:87],a[132:135],v[92:95]
s_add_u32 s62,s62,s61
dr a[80:83],v225 offset:26624
s_lshl_b32 s62,s62,10
mm v[28:31],a[84:87],a[128:131],v[28:31]
s_add_u32 s70,s62,0x3c00000
mm v[4:7],a[200:203],a[192:195],v[4:7]
dr a[84:87],v225 offset:27648
mm v[68:71],a[200:203],a[196:199],v[68:71]
dr a[64:67],v221 offset:16384
mm v[76:79],a[204:207],a[196:199],v[76:79]
bs v[132:135],v218,s[8:11],s36 offen offset:1024 sc1
mm v[12:15],a[204:207],a[192:195],v[12:15]
dr a[68:71],v221 offset:18432
mm v[20:23],a[208:211],a[192:195],v[20:23]
dr a[88:91],v225 offset:28672
mm v[84:87],a[208:211],a[196:199],v[84:87]
bs v[136:139],v218,s[8:11],s36 offen offset:2048 sc1
mm v[92:95],a[212:215],a[196:199],v[92:95]
dr a[92:95],v225 offset:29696
mm v[28:31],a[212:215],a[192:195],v[28:31]
dr a[96:99],v225 offset:30720
mm v[36:39],a[216:219],a[192:195],v[36:39]
dr a[100:103],v225 offset:31744
mm v[100:103],a[216:219],a[196:199],v[100:103]
s_waitcnt lgkmcnt(5)
bs v[140:143],v218,s[8:11],s36 offen offset:3072 sc1
mm v[4:7],a[72:75],a[64:67],v[4:7]
mm v[12:15],a[76:79],a[64:67],v[12:15]
mm v[20:23],a[80:83],a[64:67],v[20:23]
mm v[28:31],a[84:87],a[64:67],v[28:31]
s_waitcnt lgkmcnt(2)
mm v[36:39],a[88:91],a[64:67],v[36:39]
)" R"(s_waitcnt vmcnt(12) lgkmcnt(0)
s_barrier
s_add_u32 m0,s51,49152
mm v[68:71],a[72:75],a[68:71],v[68:71]
bl v208,s[16:19],s49 offen offset:0 lds
mm v[76:79],a[76:79],a[68:71],v[76:79]
v_min3_f32 v242,v4,v5,v6
v_max3_f32 v240,v4,v5,v6
mm v[84:87],a[80:83],a[68:71],v[84:87]
v_min3_f32 v242,v242,v7,v12
v_max3_f32 v240,v240,v7,v12
mm v[92:95],a[84:87],a[68:71],v[92:95]
v_min3_f32 v242,v242,v13,v14
v_max3_f32 v240,v240,v13,v14
mm v[100:103],a[88:91],a[68:71],v[100:103]
bl v209,s[16:19],s49 offen offset:1024 lds
v_min3_f32 v242,v242,v15,v20
v_max3_f32 v240,v240,v15,v20
v_min3_f32 v242,v242,v21,v22
v_max3_f32 v240,v240,v21,v22
v_min3_f32 v242,v242,v23,v28
v_max3_f32 v240,v240,v23,v28
v_min3_f32 v242,v242,v29,v30
v_max3_f32 v240,v240,v29,v30
mm v[108:111],a[68:71],a[92:95],v[108:111]
bl v210,s[16:19],s49 offen offset:2048 lds
v_min3_f32 v242,v242,v31,v36
v_max3_f32 v240,v240,v31,v36
v_min3_f32 v242,v242,v37,v38
v_max3_f32 v240,v240,v37,v38
v_min_f32 v242,v242,v39
v_max_f32 v240,v240,v39
v_pk_add_f32 v[0:1],v[242:243],v[4:5] op_sel_hi:[0,1] neg_lo:[0,1] neg_hi:[0,1]
v_pk_add_f32 v[4:5],v[4:5],v[240:241] op_sel_hi:[1,0] neg_lo:[0,1] neg_hi:[0,1]
v_pk_add_f32 v[2:3],v[242:243],v[6:7] op_sel_hi:[0,1] neg_lo:[0,1] neg_hi:[0,1]
v_pk_add_f32 v[6:7],v[6:7],v[240:241] op_sel_hi:[1,0] neg_lo:[0,1] neg_hi:[0,1]
v_pk_add_f32 v[8:9],v[242:243],v[12:13] op_sel_hi:[0,1] neg_lo:[0,1] neg_hi:[0,1]
v_pk_add_f32 v[12:13],v[12:13],v[240:241] op_sel_hi:[1,0] neg_lo:[0,1] neg_hi:[0,1]
v_pk_add_f32 v[10:11],v[242:243],v[14:15] op_sel_hi:[0,1] neg_lo:[0,1] neg_hi:[0,1]
v_pk_add_f32 v[14:15],v[14:15],v[240:241] op_sel_hi:[1,0] neg_lo:[0,1] neg_hi:[0,1]
v_pk_add_f32 v[16:17],v[242:243],v[20:21] op_sel_hi:[0,1] neg_lo:[0,1] neg_hi:[0,1]
v_pk_add_f32 v[20:21],v[20:21],v[240:241] op_sel_hi:[1,0] neg_lo:[0,1] neg_hi:[0,1]
v_pk_add_f32 v[18:19],v[242:243],v[22:23] op_sel_hi:[0,1] neg_lo:[0,1] neg_hi:[0,1]
v_pk_add_f32 v[22:23],v[22:23],v[240:241] op_sel_hi:[1,0] neg_lo:[0,1] neg_hi:[0,1]
v_pk_add_f32 v[24:25],v[242:243],v[28:29] op_sel_hi:[0,1] neg_lo:[0,1] neg_hi:[0,1]
v_pk_add_f32 v[28:29],v[28:29],v[240:241] op_sel_hi:[1,0] neg_lo:[0,1] neg_hi:[0,1]
v_pk_add_f32 v[26:27],v[242:243],v[30:31] op_sel_hi:[0,1] neg_lo:[0,1] neg_hi:[0,1]
v_pk_add_f32 v[30:31],v[30:31],v[240:241] op_sel_hi:[1,0] neg_lo:[0,1] neg_hi:[0,1]
v_pk_add_f32 v[32:33],v[242:243],v[36:37] op_sel_hi:[0,1] neg_lo:[0,1] neg_hi:[0,1]
v_pk_add_f32 v[36:37],v[36:37],v[240:241] op_sel_hi:[1,0] neg_lo:[0,1] neg_hi:[0,1]
v_pk_add_f32 v[34:35],v[242:243],v[38:39] op_sel_hi:[0,1] neg_lo:[0,1] neg_hi:[0,1]
v_pk_add_f32 v[38:39],v[38:39],v[240:241] op_sel_hi:[1,0] neg_lo:[0,1] neg_hi:[0,1]
mm v[44:47],a[64:67],a[92:95],v[44:47]
bl v211,s[16:19],s49 offen offset:3072 lds
ex2 v4,v4
ex2 v0,v0
ex2 v5,v5
ex2 v1,v1
ex2 v6,v6
ex2 v2,v2
s_add_u32 s49,s49,0x80000
s_add_u32 m0,s50,131072
mm v[52:55],a[64:67],a[96:99],v[52:55]
bl v200,s[12:15],s48 offen offset:0 lds
)" R"(ex2 v7,v7
ex2 v3,v3
ex2 v12,v12
ex2 v8,v8
ex2 v13,v13
ex2 v9,v9
mm v[116:119],a[68:71],a[96:99],v[116:119]
bl v201,s[12:15],s48 offen offset:1024 lds
ex2 v14,v14
ex2 v10,v10
ex2 v15,v15
ex2 v11,v11
ex2 v20,v20
ex2 v16,v16
mm v[124:127],a[68:71],a[100:103],v[124:127]
bl v202,s[12:15],s48 offen offset:2048 lds
ex2 v21,v21
ex2 v17,v17
ex2 v22,v22
ex2 v18,v18
ex2 v23,v23
ex2 v19,v19
mm v[60:63],a[64:67],a[100:103],v[60:63]
bl v203,s[12:15],s48 offen offset:3072 lds
ex2 v28,v28
ex2 v24,v24
ex2 v29,v29
ex2 v25,v25
ex2 v30,v30
ex2 v26,v26
s_add_u32 m0,s50,135168
mm v[108:111],a[196:199],a[220:223],v[108:111]
bl v204,s[12:15],s48 offen offset:0 lds
ex2 v31,v31
ex2 v27,v27
ex2 v36,v36
ex2 v32,v32
ex2 v37,v37
ex2 v33,v33
mm v[44:47],a[192:195],a[220:223],v[44:47]
bl v205,s[12:15],s48 offen offset:1024 lds
ex2 v38,v38
ex2 v34,v34
ex2 v39,v39
ex2 v35,v35
v_pk_add_f32 v[244:245],v[4:5],v[6:7]
v_pk_add_f32 v[246:247],v[0:1],v[2:3]
v_pk_add_f32 v[244:245],v[244:245],v[12:13]
v_pk_add_f32 v[246:247],v[246:247],v[8:9]
v_pk_add_f32 v[244:245],v[244:245],v[14:15]
v_pk_add_f32 v[246:247],v[246:247],v[10:11]
v_pk_add_f32 v[244:245],v[244:245],v[20:21]
v_pk_add_f32 v[246:247],v[246:247],v[16:17]
v_pk_add_f32 v[244:245],v[244:245],v[22:23]
v_pk_add_f32 v[246:247],v[246:247],v[18:19]
v_pk_add_f32 v[244:245],v[244:245],v[28:29]
v_pk_add_f32 v[246:247],v[246:247],v[24:25]
v_pk_add_f32 v[244:245],v[244:245],v[30:31]
v_pk_add_f32 v[246:247],v[246:247],v[26:27]
v_pk_add_f32 v[244:245],v[244:245],v[36:37]
v_pk_add_f32 v[246:247],v[246:247],v[32:33]
v_pk_add_f32 v[244:245],v[244:245],v[38:39]
v_pk_add_f32 v[246:247],v[246:247],v[34:35]
mm v[52:55],a[192:195],a[224:227],v[52:55]
)" R"(bl v206,s[12:15],s48 offen offset:2048 lds
v_add_f32 v244,v244,v245
v_add_f32 v246,v246,v247
v_rcp_f32 v244,v244
v_rcp_f32 v246,v246
v_pk_mul_f32 v[4:5],v[4:5],v[244:245] op_sel_hi:[1,0]
v_pk_mul_f32 v[6:7],v[6:7],v[244:245] op_sel_hi:[1,0]
v_pk_mul_f32 v[12:13],v[12:13],v[244:245] op_sel_hi:[1,0]
v_pk_mul_f32 v[14:15],v[14:15],v[244:245] op_sel_hi:[1,0]
v_pk_mul_f32 v[20:21],v[20:21],v[244:245] op_sel_hi:[1,0]
v_pk_mul_f32 v[22:23],v[22:23],v[244:245] op_sel_hi:[1,0]
v_pk_mul_f32 v[28:29],v[28:29],v[244:245] op_sel_hi:[1,0]
v_pk_mul_f32 v[30:31],v[30:31],v[244:245] op_sel_hi:[1,0]
v_pk_mul_f32 v[36:37],v[36:37],v[244:245] op_sel_hi:[1,0]
v_pk_mul_f32 v[38:39],v[38:39],v[244:245] op_sel_hi:[1,0]
v_pk_mul_f32 v[0:1],v[0:1],v[246:247] op_sel_hi:[1,0]
v_pk_mul_f32 v[2:3],v[2:3],v[246:247] op_sel_hi:[1,0]
v_pk_mul_f32 v[8:9],v[8:9],v[246:247] op_sel_hi:[1,0]
v_pk_mul_f32 v[10:11],v[10:11],v[246:247] op_sel_hi:[1,0]
v_pk_mul_f32 v[16:17],v[16:17],v[246:247] op_sel_hi:[1,0]
v_pk_mul_f32 v[18:19],v[18:19],v[246:247] op_sel_hi:[1,0]
v_pk_mul_f32 v[24:25],v[24:25],v[246:247] op_sel_hi:[1,0]
v_pk_mul_f32 v[26:27],v[26:27],v[246:247] op_sel_hi:[1,0]
v_pk_mul_f32 v[32:33],v[32:33],v[246:247] op_sel_hi:[1,0]
v_pk_mul_f32 v[34:35],v[34:35],v[246:247] op_sel_hi:[1,0]
mm v[116:119],a[196:199],a[224:227],v[116:119]
bl v207,s[12:15],s48 offen offset:3072 lds
s_waitcnt vmcnt(16)
dt a[96:97],v192 offset:16384
dt a[98:99],v192 offset:17408
dt a[100:101],v193 offset:16384
dt a[102:103],v193 offset:17408
dt a[104:105],v194 offset:16384
dt a[106:107],v194 offset:17408
dt a[108:109],v195 offset:16384
dt a[110:111],v195 offset:17408
dt a[112:113],v196 offset:16384
dt a[114:115],v196 offset:17408
dt a[116:117],v197 offset:16384
dt a[118:119],v197 offset:17408
dt a[120:121],v198 offset:16384
dt a[122:123],v198 offset:17408
dt a[124:125],v199 offset:16384
dt a[126:127],v199 offset:17408
dr a[64:67],v212 offset:0
dr a[72:75],v212 offset:2048
dr a[80:83],v212 offset:4096
dr a[88:91],v212 offset:6144
dt a[148:149],v192 offset:24576
dt a[150:151],v192 offset:25600
dt a[152:153],v193 offset:24576
dt a[154:155],v193 offset:25600
dt a[156:157],v194 offset:24576
dt a[158:159],v194 offset:25600
dt a[160:161],v195 offset:24576
dt a[162:163],v195 offset:25600
dt a[164:165],v196 offset:24576
dt a[166:167],v196 offset:25600
dt a[168:169],v197 offset:24576
dt a[170:171],v197 offset:25600
dt a[172:173],v198 offset:24576
dt a[174:175],v198 offset:25600
dt a[176:177],v199 offset:24576
dt a[178:179],v199 offset:25600
)" R"(dr a[68:71],v213 offset:0
dr a[76:79],v213 offset:2048
dr a[84:87],v213 offset:4096
dr a[92:95],v213 offset:6144
mm v[124:127],a[196:199],a[228:231],v[124:127]
cv v128,v4,v5
cv v129,v6,v7
mm v[60:63],a[192:195],a[228:231],v[60:63]
cv v130,v12,v13
cv v131,v14,v15
cv v132,v20,v21
cv v133,v22,v23
cv v134,v28,v29
cv v135,v30,v31
cv v136,v36,v37
cv v137,v38,v39
cv v138,v0,v1
cv v139,v2,v3
cv v140,v8,v9
cv v141,v10,v11
cv v142,v16,v17
cv v143,v18,v19
cv v228,v24,v25
cv v229,v26,v27
cv v230,v32,v33
cv v231,v34,v35
s_add_u32 s48,s48,128
s_waitcnt lgkmcnt(15)
mm v[0:3],a[96:99],a[64:67],0
s_waitcnt lgkmcnt(3)
ds_write_b128 v236,v[128:131] offset:0
cv v111,v110,v111
cv v110,v108,v109
s_add_u32 m0,s50,98304
mm v[0:3],a[148:151],a[68:71],v[0:3]
bl v200,s[12:15],s48 offen offset:0 lds
cv v108,v44,v45
cv v109,v46,v47
mm v[4:7],a[152:155],a[68:71],0
bs v[144:147],v218,s[8:11],s37 offen offset:0 sc1
cv v119,v118,v119
cv v118,v116,v117
cv v116,v52,v53
cv v117,v54,v55
cv v127,v126,v127
cv v126,v124,v125
mm v[4:7],a[100:103],a[64:67],v[4:7]
ds_write_b128 v236,v[132:135] offset:16
mm v[8:11],a[104:107],a[64:67],0
bl v201,s[12:15],s48 offen offset:1024 lds
cv v124,v60,v61
cv v125,v62,v63
v_min3_f32 v242,v68,v69,v70
v_max3_f32 v240,v68,v69,v70
mm v[8:11],a[156:159],a[68:71],v[8:11]
bs v[148:151],v218,s[8:11],s37 offen offset:1024 sc1
v_min3_f32 v242,v242,v71,v76
v_max3_f32 v240,v240,v71,v76
v_min3_f32 v242,v242,v77,v78
v_max3_f32 v240,v240,v77,v78
v_min3_f32 v242,v242,v79,v84
v_max3_f32 v240,v240,v79,v84
mm v[12:15],a[160:163],a[68:71],0
ds_write_b128 v236,v[136:139] offset:32
)" R"(mm v[12:15],a[108:111],a[64:67],v[12:15]
bl v202,s[12:15],s48 offen offset:2048 lds
v_min3_f32 v242,v242,v85,v86
v_max3_f32 v240,v240,v85,v86
v_min3_f32 v242,v242,v87,v92
v_max3_f32 v240,v240,v87,v92
mm v[16:19],a[112:115],a[64:67],0
bs v[152:155],v218,s[8:11],s37 offen offset:2048 sc1
v_min3_f32 v242,v242,v93,v94
v_max3_f32 v240,v240,v93,v94
v_min3_f32 v242,v242,v95,v100
v_max3_f32 v240,v240,v95,v100
v_min3_f32 v242,v242,v101,v102
v_max3_f32 v240,v240,v101,v102
v_min_f32 v242,v242,v103
v_max_f32 v240,v240,v103
v_pk_add_f32 v[64:65],v[242:243],v[68:69] op_sel_hi:[0,1] neg_lo:[0,1] neg_hi:[0,1]
v_pk_add_f32 v[68:69],v[68:69],v[240:241] op_sel_hi:[1,0] neg_lo:[0,1] neg_hi:[0,1]
v_pk_add_f32 v[66:67],v[242:243],v[70:71] op_sel_hi:[0,1] neg_lo:[0,1] neg_hi:[0,1]
v_pk_add_f32 v[70:71],v[70:71],v[240:241] op_sel_hi:[1,0] neg_lo:[0,1] neg_hi:[0,1]
v_pk_add_f32 v[72:73],v[242:243],v[76:77] op_sel_hi:[0,1] neg_lo:[0,1] neg_hi:[0,1]
v_pk_add_f32 v[76:77],v[76:77],v[240:241] op_sel_hi:[1,0] neg_lo:[0,1] neg_hi:[0,1]
v_pk_add_f32 v[74:75],v[242:243],v[78:79] op_sel_hi:[0,1] neg_lo:[0,1] neg_hi:[0,1]
v_pk_add_f32 v[78:79],v[78:79],v[240:241] op_sel_hi:[1,0] neg_lo:[0,1] neg_hi:[0,1]
v_pk_add_f32 v[80:81],v[242:243],v[84:85] op_sel_hi:[0,1] neg_lo:[0,1] neg_hi:[0,1]
v_pk_add_f32 v[84:85],v[84:85],v[240:241] op_sel_hi:[1,0] neg_lo:[0,1] neg_hi:[0,1]
v_pk_add_f32 v[82:83],v[242:243],v[86:87] op_sel_hi:[0,1] neg_lo:[0,1] neg_hi:[0,1]
v_pk_add_f32 v[86:87],v[86:87],v[240:241] op_sel_hi:[1,0] neg_lo:[0,1] neg_hi:[0,1]
v_pk_add_f32 v[88:89],v[242:243],v[92:93] op_sel_hi:[0,1] neg_lo:[0,1] neg_hi:[0,1]
v_pk_add_f32 v[92:93],v[92:93],v[240:241] op_sel_hi:[1,0] neg_lo:[0,1] neg_hi:[0,1]
v_pk_add_f32 v[90:91],v[242:243],v[94:95] op_sel_hi:[0,1] neg_lo:[0,1] neg_hi:[0,1]
v_pk_add_f32 v[94:95],v[94:95],v[240:241] op_sel_hi:[1,0] neg_lo:[0,1] neg_hi:[0,1]
v_pk_add_f32 v[96:97],v[242:243],v[100:101] op_sel_hi:[0,1] neg_lo:[0,1] neg_hi:[0,1]
v_pk_add_f32 v[100:101],v[100:101],v[240:241] op_sel_hi:[1,0] neg_lo:[0,1] neg_hi:[0,1]
v_pk_add_f32 v[98:99],v[242:243],v[102:103] op_sel_hi:[0,1] neg_lo:[0,1] neg_hi:[0,1]
v_pk_add_f32 v[102:103],v[102:103],v[240:241] op_sel_hi:[1,0] neg_lo:[0,1] neg_hi:[0,1]
mm v[16:19],a[164:167],a[68:71],v[16:19]
ds_write_b128 v236,v[140:143] offset:48
mm v[20:23],a[168:171],a[68:71],0
bl v203,s[12:15],s48 offen offset:3072 lds
ex2 v68,v68
ex2 v64,v64
ex2 v69,v69
ex2 v65,v65
mm v[20:23],a[116:119],a[64:67],v[20:23]
bs v[156:159],v218,s[8:11],s37 offen offset:3072 sc1
ex2 v70,v70
ex2 v66,v66
ex2 v71,v71
ex2 v67,v67
mm v[24:27],a[120:123],a[64:67],0
ds_write_b128 v236,v[228:231] offset:64
s_add_u32 m0,s50,102400
mm v[24:27],a[172:175],a[68:71],v[24:27]
bl v204,s[12:15],s48 offen offset:0 lds
ex2 v76,v76
ex2 v72,v72
ex2 v77,v77
ex2 v73,v73
mm v[28:31],a[176:179],a[68:71],0
ex2 v78,v78
mm v[28:31],a[124:127],a[64:67],v[28:31]
ds_write_b128 v237,v[108:111] offset:0
mm v[60:63],a[124:127],a[72:75],0
)" R"(s_waitcnt lgkmcnt(2)
bl v205,s[12:15],s48 offen offset:1024 lds
ex2 v74,v74
ex2 v79,v79
ex2 v75,v75
ex2 v84,v84
ex2 v80,v80
mm v[60:63],a[176:179],a[76:79],v[60:63]
ds_write_b128 v237,v[116:119] offset:1024
mm v[56:59],a[172:175],a[76:79],0
ex2 v85,v85
ex2 v81,v81
ex2 v86,v86
mm v[56:59],a[120:123],a[72:75],v[56:59]
bl v206,s[12:15],s48 offen offset:2048 lds
ex2 v82,v82
ex2 v87,v87
ex2 v83,v83
ex2 v92,v92
mm v[52:55],a[116:119],a[72:75],0
ds_write_b128 v237,v[124:127] offset:2048
ex2 v88,v88
ex2 v93,v93
ex2 v89,v89
ex2 v94,v94
ex2 v90,v90
mm v[52:55],a[168:171],a[76:79],v[52:55]
bl v207,s[12:15],s48 offen offset:3072 lds
ex2 v95,v95
ex2 v91,v91
ex2 v100,v100
ex2 v96,v96
ex2 v101,v101
ex2 v97,v97
ex2 v102,v102
ex2 v98,v98
ex2 v103,v103
ex2 v99,v99
v_pk_add_f32 v[244:245],v[68:69],v[70:71]
v_pk_add_f32 v[246:247],v[64:65],v[66:67]
v_pk_add_f32 v[244:245],v[244:245],v[76:77]
v_pk_add_f32 v[246:247],v[246:247],v[72:73]
v_pk_add_f32 v[244:245],v[244:245],v[78:79]
v_pk_add_f32 v[246:247],v[246:247],v[74:75]
v_pk_add_f32 v[244:245],v[244:245],v[84:85]
v_pk_add_f32 v[246:247],v[246:247],v[80:81]
v_pk_add_f32 v[244:245],v[244:245],v[86:87]
v_pk_add_f32 v[246:247],v[246:247],v[82:83]
v_pk_add_f32 v[244:245],v[244:245],v[92:93]
v_pk_add_f32 v[246:247],v[246:247],v[88:89]
v_pk_add_f32 v[244:245],v[244:245],v[94:95]
v_pk_add_f32 v[246:247],v[246:247],v[90:91]
v_pk_add_f32 v[244:245],v[244:245],v[100:101]
v_pk_add_f32 v[246:247],v[246:247],v[96:97]
v_pk_add_f32 v[244:245],v[244:245],v[102:103]
v_pk_add_f32 v[246:247],v[246:247],v[98:99]
mm v[48:51],a[164:167],a[76:79],0
v_add_f32 v244,v244,v245
v_add_f32 v246,v246,v247
v_rcp_f32 v244,v244
v_rcp_f32 v246,v246
v_pk_mul_f32 v[68:69],v[68:69],v[244:245] op_sel_hi:[1,0]
v_pk_mul_f32 v[70:71],v[70:71],v[244:245] op_sel_hi:[1,0]
v_pk_mul_f32 v[76:77],v[76:77],v[244:245] op_sel_hi:[1,0]
)" R"(v_pk_mul_f32 v[78:79],v[78:79],v[244:245] op_sel_hi:[1,0]
v_pk_mul_f32 v[84:85],v[84:85],v[244:245] op_sel_hi:[1,0]
v_pk_mul_f32 v[86:87],v[86:87],v[244:245] op_sel_hi:[1,0]
v_pk_mul_f32 v[92:93],v[92:93],v[244:245] op_sel_hi:[1,0]
v_pk_mul_f32 v[94:95],v[94:95],v[244:245] op_sel_hi:[1,0]
v_pk_mul_f32 v[100:101],v[100:101],v[244:245] op_sel_hi:[1,0]
v_pk_mul_f32 v[102:103],v[102:103],v[244:245] op_sel_hi:[1,0]
v_pk_mul_f32 v[64:65],v[64:65],v[246:247] op_sel_hi:[1,0]
v_pk_mul_f32 v[66:67],v[66:67],v[246:247] op_sel_hi:[1,0]
v_pk_mul_f32 v[72:73],v[72:73],v[246:247] op_sel_hi:[1,0]
v_pk_mul_f32 v[74:75],v[74:75],v[246:247] op_sel_hi:[1,0]
v_pk_mul_f32 v[80:81],v[80:81],v[246:247] op_sel_hi:[1,0]
v_pk_mul_f32 v[82:83],v[82:83],v[246:247] op_sel_hi:[1,0]
v_pk_mul_f32 v[88:89],v[88:89],v[246:247] op_sel_hi:[1,0]
v_pk_mul_f32 v[90:91],v[90:91],v[246:247] op_sel_hi:[1,0]
v_pk_mul_f32 v[96:97],v[96:97],v[246:247] op_sel_hi:[1,0]
v_pk_mul_f32 v[98:99],v[98:99],v[246:247] op_sel_hi:[1,0]
mm v[48:51],a[112:115],a[72:75],v[48:51]
cv v144,v68,v69
cv v145,v70,v71
mm v[44:47],a[108:111],a[72:75],0
cv v146,v76,v77
cv v147,v78,v79
mm v[44:47],a[160:163],a[76:79],v[44:47]
ds_write_b128 v236,v[144:147] offset:2560
mm v[40:43],a[156:159],a[76:79],0
cv v148,v84,v85
cv v149,v86,v87
cv v150,v92,v93
cv v151,v94,v95
mm v[40:43],a[104:107],a[72:75],v[40:43]
ds_write_b128 v236,v[148:151] offset:2576
mm v[36:39],a[100:103],a[72:75],0
cv v152,v100,v101
cv v153,v102,v103
cv v154,v64,v65
cv v155,v66,v67
mm v[36:39],a[152:155],a[76:79],v[36:39]
ds_write_b128 v236,v[152:155] offset:2592
cv v156,v72,v73
cv v157,v74,v75
cv v158,v80,v81
cv v159,v82,v83
mm v[32:35],a[148:151],a[76:79],0
ds_write_b128 v236,v[156:159] offset:2608
cv v220,v88,v89
cv v221,v90,v91
cv v222,v96,v97
cv v223,v98,v99
mm v[32:35],a[96:99],a[72:75],v[32:35]
ds_write_b128 v236,v[220:223] offset:2624
s_add_u32 s48,s48,128
mm v[64:67],a[96:99],a[80:83],0
s_add_u32 m0,s51,32768
aw a252,v220
aw a232,v228
mm v[64:67],a[148:151],a[84:87],v[64:67]
s_mov_b32 s20,s84
aw a233,v229
v_mbcnt_lo_u32_b32 v220,-1,0
mm v[68:71],a[152:155],a[84:87],0
s_and_b32 s21,s85,0xffff
v_mbcnt_hi_u32_b32 v220,-1,v220
aw a254,v222
)" R"(mm v[68:71],a[100:103],a[80:83],v[68:71]
s_lshl_b32 s71,s24,9
s_add_u32 s71,s71,0x4004000
v_mbcnt_lo_u32_b32 v222,-1,0
mm v[72:75],a[104:107],a[80:83],0
s_mov_b32 s22,0x500000
v_mbcnt_hi_u32_b32 v222,-1,v222
v_mov_b32 v254,0x40000000
s_waitcnt vmcnt(12) lgkmcnt(0)
s_barrier
dr a[44:47],v239 offset:0
dt a[180:181],v238 offset:0
dt a[182:183],v238 offset:2560
dt a[184:185],v238 offset:32
dt a[186:187],v238 offset:2592
dt a[188:189],v238 offset:64
dt a[190:191],v238 offset:2624
dr a[48:51],v239 offset:3072
dt a[192:193],v238 offset:5120
dt a[194:195],v238 offset:7680
dt a[196:197],v238 offset:5152
dt a[198:199],v238 offset:7712
dt a[200:201],v238 offset:5184
dt a[202:203],v238 offset:7744
dr a[52:55],v239 offset:6144
mm v[72:75],a[156:159],a[84:87],v[72:75]
s_mov_b32 s23,0x20000
dt a[204:205],v238 offset:10240
v_mov_b32 v252,s54
mm v[76:79],a[160:163],a[84:87],0
s_mov_b32 s52,0x3e0293ee
dt a[206:207],v238 offset:12800
v_mov_b32 v253,s55
mm v[76:79],a[108:111],a[80:83],v[76:79]
s_mov_b32 s53,0x3e0293ee
bl v208,s[16:19],s49 offen offset:0 lds
dt a[208:209],v238 offset:10272
mm v[108:111],a[108:111],a[88:91],0
dt a[210:211],v238 offset:12832
aw a216,v128
mm v[108:111],a[160:163],a[92:95],v[108:111]
dt a[212:213],v238 offset:10304
aw a217,v129
mm v[104:107],a[156:159],a[92:95],0
dt a[214:215],v238 offset:12864
aw a238,v146
mm v[104:107],a[104:107],a[88:91],v[104:107]
dr a[56:59],v239 offset:9216
aw a218,v130
mm v[100:103],a[100:103],a[88:91],0
bl v209,s[16:19],s49 offen offset:1024 lds
dt a[32:33],v238 offset:15360
mm v[100:103],a[152:155],a[92:95],v[100:103]
dt a[34:35],v238 offset:17920
aw a236,v144
mm v[96:99],a[148:151],a[92:95],0
dt a[36:37],v238 offset:15392
aw a219,v131
mm v[96:99],a[96:99],a[88:91],v[96:99]
dt a[38:39],v238 offset:17952
aw a237,v145
mm v[80:83],a[112:115],a[80:83],0
dt a[40:41],v238 offset:15424
aw a220,v132
)" R"(mm v[80:83],a[164:167],a[84:87],v[80:83]
bl v210,s[16:19],s49 offen offset:2048 lds
dt a[42:43],v238 offset:17984
mm v[84:87],a[168:171],a[84:87],0
E0_6:
s_cbranch_execz E0_R
dt a[108:109],v195 offset:32768
aw a221,v133
mm v[84:87],a[116:119],a[80:83],v[84:87]
dt a[110:111],v195 offset:33792
aw a222,v134
mm v[88:91],a[120:123],a[80:83],0
dt a[160:161],v195 offset:40960
aw a223,v135
mm v[88:91],a[172:175],a[84:87],v[88:91]
dt a[162:163],v195 offset:41984
aw a224,v136
mm v[92:95],a[176:179],a[84:87],0
bl v211,s[16:19],s49 offen offset:3072 lds
dt a[104:105],v194 offset:32768
mm v[92:95],a[124:127],a[80:83],v[92:95]
s_add_u32 m0,s50,65536
dt a[106:107],v194 offset:33792
s_add_u32 s49,s49,0x80000
mm v[124:127],a[124:127],a[88:91],0
dt a[156:157],v194 offset:40960
aw a225,v137
mm v[124:127],a[176:179],a[92:95],v[124:127]
dt a[158:159],v194 offset:41984
aw a226,v138
mm v[120:123],a[172:175],a[92:95],0
dr a[64:67],v214 offset:0
aw a227,v139
mm v[120:123],a[120:123],a[88:91],v[120:123]
bl v200,s[12:15],s48 offen offset:0 lds
dr a[68:71],v215 offset:0
mm v[116:119],a[116:119],a[88:91],0
dt a[100:101],v193 offset:32768
aw a228,v140
mm v[116:119],a[168:171],a[92:95],v[116:119]
dt a[102:103],v193 offset:33792
aw a229,v141
mm v[112:115],a[164:167],a[92:95],0
dt a[152:153],v193 offset:40960
aw a230,v142
mm v[112:115],a[112:115],a[88:91],v[112:115]
s_waitcnt lgkmcnt(6)
dt a[154:155],v193 offset:41984
aw a231,v143
mm v[240:243],a[180:183],a[44:47],0
bl v201,s[12:15],s48 offen offset:1024 lds
dr a[72:75],v214 offset:2048
mm v[244:247],a[184:187],a[44:47],0
dr a[76:79],v215 offset:2048
aw a239,v147
mm v[248:251],a[188:191],a[44:47],0
dt a[96:97],v192 offset:32768
aw a240,v148
mm v[240:243],a[192:195],a[48:51],v[240:243]
dt a[98:99],v192 offset:33792
aw a241,v149
mm v[244:247],a[196:199],a[48:51],v[244:247]
dt a[148:149],v192 offset:40960
aw a242,v150
)" R"(mm v[248:251],a[200:203],a[48:51],v[248:251]
bl v202,s[12:15],s48 offen offset:2048 lds
dt a[150:151],v192 offset:41984
mm v[240:243],a[204:207],a[52:55],v[240:243]
aw a243,v151
aw a244,v152
mm v[244:247],a[208:211],a[52:55],v[244:247]
aw a245,v153
aw a246,v154
mm v[248:251],a[212:215],a[52:55],v[248:251]
aw a247,v155
aw a248,v156
mm v[240:243],a[32:35],a[56:59],v[240:243]
bl v203,s[12:15],s48 offen offset:3072 lds
mm v[244:247],a[36:39],a[56:59],v[244:247]
s_add_u32 m0,s50,69632
aw a249,v157
aw a250,v158
mm v[248:251],a[40:43],a[56:59],v[248:251]
s_waitcnt lgkmcnt(1)
aw a251,v159
aw a253,v221
mm v[0:3],a[96:99],a[64:67],v[0:3]
s_waitcnt lgkmcnt(0)
aw a255,v223
aw a234,v230
mm v[0:3],a[148:151],a[68:71],v[0:3]
dr a[80:83],v214 offset:4096
aw a235,v231
mm v[4:7],a[152:155],a[68:71],v[4:7]
dr a[84:87],v215 offset:4096
mm v[4:7],a[100:103],a[64:67],v[4:7]
dr a[88:91],v214 offset:6144
mm v[8:11],a[104:107],a[64:67],v[8:11]
bs v[240:243],v226,s[8:11],s70 offen sc1
dr a[92:95],v215 offset:6144
s_add_u32 s70,s70,16384
mm v[8:11],a[156:159],a[68:71],v[8:11]
dt a[112:113],v196 offset:32768
mm v[12:15],a[160:163],a[68:71],v[12:15]
dt a[114:115],v196 offset:33792
mm v[12:15],a[108:111],a[64:67],v[12:15]
dt a[164:165],v196 offset:40960
mm v[44:47],a[108:111],a[72:75],v[44:47]
dt a[166:167],v196 offset:41984
mm v[44:47],a[160:163],a[76:79],v[44:47]
dt a[116:117],v197 offset:32768
mm v[40:43],a[156:159],a[76:79],v[40:43]
bs v[244:247],v226,s[8:11],s70 offen sc1
dt a[118:119],v197 offset:33792
s_add_u32 s70,s70,16384
mm v[40:43],a[104:107],a[72:75],v[40:43]
dt a[168:169],v197 offset:40960
mm v[36:39],a[100:103],a[72:75],v[36:39]
dt a[170:171],v197 offset:41984
mm v[36:39],a[152:155],a[76:79],v[36:39]
dt a[120:121],v198 offset:32768
mm v[32:35],a[148:151],a[76:79],v[32:35]
dt a[122:123],v198 offset:33792
mm v[32:35],a[96:99],a[72:75],v[32:35]
s_waitcnt lgkmcnt(6)
dt a[172:173],v198 offset:40960
mm v[64:67],a[96:99],a[80:83],v[64:67]
bs v[248:251],v226,s[8:11],s70 offen sc1
)" R"(dt a[174:175],v198 offset:41984
v_lshlrev_b32 v226,3,v220
v_and_b32 v227,504,v226
mm v[64:67],a[148:151],a[84:87],v[64:67]
dt a[124:125],v199 offset:32768
mm v[68:71],a[152:155],a[84:87],v[68:71]
dt a[126:127],v199 offset:33792
mm v[68:71],a[100:103],a[80:83],v[68:71]
dt a[176:177],v199 offset:40960
mm v[72:75],a[104:107],a[80:83],v[72:75]
bl v204,s[12:15],s48 offen offset:0 lds
mm v[72:75],a[156:159],a[84:87],v[72:75]
dt a[178:179],v199 offset:41984
mm v[76:79],a[160:163],a[84:87],v[76:79]
mm v[76:79],a[108:111],a[80:83],v[76:79]
mm v[108:111],a[108:111],a[88:91],v[108:111]
bl v205,s[12:15],s48 offen offset:1024 lds
mm v[108:111],a[160:163],a[92:95],v[108:111]
mm v[104:107],a[156:159],a[92:95],v[104:107]
s_waitcnt vmcnt(9) lgkmcnt(0)
s_barrier
mm v[104:107],a[104:107],a[88:91],v[104:107]
mm v[100:103],a[100:103],a[88:91],v[100:103]
bl v206,s[12:15],s48 offen offset:2048 lds
mm v[100:103],a[152:155],a[92:95],v[100:103]
mm v[96:99],a[148:151],a[92:95],v[96:99]
mm v[96:99],a[96:99],a[88:91],v[96:99]
mm v[16:19],a[112:115],a[64:67],v[16:19]
bl v207,s[12:15],s48 offen offset:3072 lds
mm v[16:19],a[164:167],a[68:71],v[16:19]
s_add_u32 m0,s51,16384
s_add_u32 s48,s48,128
mm v[20:23],a[168:171],a[68:71],v[20:23]
dt a[108:109],v195 offset:16384
mm v[20:23],a[116:119],a[64:67],v[20:23]
dt a[110:111],v195 offset:17408
mm v[24:27],a[120:123],a[64:67],v[24:27]
bl v208,s[16:19],s49 offen offset:0 lds
mm v[24:27],a[172:175],a[68:71],v[24:27]
dt a[160:161],v195 offset:24576
mm v[28:31],a[176:179],a[68:71],v[28:31]
dt a[162:163],v195 offset:25600
mm v[28:31],a[124:127],a[64:67],v[28:31]
dt a[104:105],v194 offset:16384
mm v[60:63],a[124:127],a[72:75],v[60:63]
bl v209,s[16:19],s49 offen offset:1024 lds
mm v[60:63],a[176:179],a[76:79],v[60:63]
dt a[106:107],v194 offset:17408
mm v[56:59],a[172:175],a[76:79],v[56:59]
dt a[156:157],v194 offset:24576
mm v[56:59],a[120:123],a[72:75],v[56:59]
dt a[158:159],v194 offset:25600
mm v[52:55],a[116:119],a[72:75],v[52:55]
bl v210,s[16:19],s49 offen offset:2048 lds
mm v[52:55],a[168:171],a[76:79],v[52:55]
dr a[64:67],v212 offset:32768
mm v[48:51],a[164:167],a[76:79],v[48:51]
dr a[68:71],v213 offset:32768
mm v[48:51],a[112:115],a[72:75],v[48:51]
dt a[100:101],v193 offset:16384
mm v[80:83],a[112:115],a[80:83],v[80:83]
dt a[102:103],v193 offset:17408
mm v[80:83],a[164:167],a[84:87],v[80:83]
dt a[152:153],v193 offset:24576
)" R"(mm v[84:87],a[168:171],a[84:87],v[84:87]
buffer_load_dwordx2 v[228:229],v226,s[8:11],s71 offen sc1
mm v[84:87],a[116:119],a[80:83],v[84:87]
dt a[154:155],v193 offset:25600
bl v211,s[16:19],s49 offen offset:3072 lds
mm v[88:91],a[120:123],a[80:83],v[88:91]
s_add_u32 m0,s50,131072
dr a[72:75],v212 offset:34816
s_add_u32 s49,s49,0x80000
mm v[88:91],a[172:175],a[84:87],v[88:91]
dr a[76:79],v213 offset:34816
mm v[92:95],a[176:179],a[84:87],v[92:95]
dt a[96:97],v192 offset:16384
mm v[92:95],a[124:127],a[80:83],v[92:95]
dt a[98:99],v192 offset:17408
bl v200,s[12:15],s48 offen offset:0 lds
mm v[124:127],a[124:127],a[88:91],v[124:127]
s_lshr_b32 s62,s29,5
dt a[148:149],v192 offset:24576
s_and_b32 s63,s29,4
mm v[124:127],a[176:179],a[92:95],v[124:127]
s_lshl_b32 s63,s63,1
dt a[150:151],v192 offset:25600
s_add_u32 s62,s62,s63
mm v[120:123],a[172:175],a[92:95],v[120:123]
s_bfe_u32 s61,s29,0x20003
s_and_b32 s63,s29,3
s_lshl_b32 s63,s63,2
mm v[120:123],a[120:123],a[88:91],v[120:123]
s_add_u32 s61,s61,s63
bl v201,s[12:15],s48 offen offset:1024 lds
mm v[116:119],a[116:119],a[88:91],v[116:119]
s_lshl_b32 s61,s61,4
dr a[80:83],v212 offset:36864
s_add_u32 s61,s61,s62
mm v[116:119],a[168:171],a[92:95],v[116:119]
s_lshl_b32 s61,s61,3
dr a[84:87],v213 offset:36864
s_add_u32 s61,s61,0x4006000
mm v[112:115],a[164:167],a[92:95],v[112:115]
s_cmp_eq_u32 s24,0
s_cselect_b32 s61,s61,0x80000000
s_lshr_b32 s62,s36,15
mm v[112:115],a[112:115],a[88:91],v[112:115]
s_waitcnt lgkmcnt(3)
bl v202,s[12:15],s48 offen offset:2048 lds
mm v[0:3],a[96:99],a[64:67],v[0:3]
s_waitcnt lgkmcnt(2)
dr a[88:91],v212 offset:38912
s_lshl_b32 s62,s62,2
mm v[0:3],a[148:151],a[68:71],v[0:3]
s_add_u32 s62,s62,s24
dr a[92:95],v213 offset:38912
s_lshl_b32 s62,s62,3
mm v[4:7],a[152:155],a[68:71],v[4:7]
s_add_u32 s62,s62,0x4018000
aw a196,v160
aw a197,v161
mm v[4:7],a[100:103],a[64:67],v[4:7]
bl v203,s[12:15],s48 offen offset:3072 lds
mm v[8:11],a[104:107],a[64:67],v[8:11]
s_add_u32 m0,s50,135168
dt a[112:113],v196 offset:16384
aw a198,v162
)" R"(mm v[8:11],a[156:159],a[68:71],v[8:11]
dt a[114:115],v196 offset:17408
aw a199,v163
mm v[12:15],a[160:163],a[68:71],v[12:15]
dt a[164:165],v196 offset:24576
aw a200,v164
mm v[12:15],a[108:111],a[64:67],v[12:15]
dt a[166:167],v196 offset:25600
aw a201,v165
mm v[44:47],a[108:111],a[72:75],v[44:47]
bl v204,s[12:15],s48 offen offset:0 lds
mm v[44:47],a[160:163],a[76:79],v[44:47]
dt a[116:117],v197 offset:16384
aw a202,v166
mm v[40:43],a[156:159],a[76:79],v[40:43]
dt a[118:119],v197 offset:17408
aw a203,v167
mm v[40:43],a[104:107],a[72:75],v[40:43]
dt a[168:169],v197 offset:24576
aw a204,v168
mm v[36:39],a[100:103],a[72:75],v[36:39]
dt a[170:171],v197 offset:25600
aw a205,v169
mm v[36:39],a[152:155],a[76:79],v[36:39]
dt a[120:121],v198 offset:16384
aw a206,v170
mm v[32:35],a[148:151],a[76:79],v[32:35]
bl v205,s[12:15],s48 offen offset:1024 lds
mm v[32:35],a[96:99],a[72:75],v[32:35]
s_waitcnt lgkmcnt(5)
dt a[122:123],v198 offset:17408
aw a207,v171
mm v[64:67],a[96:99],a[80:83],v[64:67]
dt a[172:173],v198 offset:24576
aw a208,v172
mm v[64:67],a[148:151],a[84:87],v[64:67]
dt a[174:175],v198 offset:25600
aw a209,v173
mm v[68:71],a[152:155],a[84:87],v[68:71]
dt a[124:125],v199 offset:16384
aw a210,v174
mm v[68:71],a[100:103],a[80:83],v[68:71]
dt a[126:127],v199 offset:17408
aw a211,v175
bl v206,s[12:15],s48 offen offset:2048 lds
mm v[72:75],a[104:107],a[80:83],v[72:75]
dt a[176:177],v199 offset:24576
aw a180,v176
mm v[72:75],a[156:159],a[84:87],v[72:75]
dt a[178:179],v199 offset:25600
aw a181,v177
mm v[76:79],a[160:163],a[84:87],v[76:79]
aw a182,v178
aw a183,v179
mm v[76:79],a[108:111],a[80:83],v[76:79]
bl v207,s[12:15],s48 offen offset:3072 lds
mm v[108:111],a[108:111],a[88:91],v[108:111]
s_add_u32 m0,s51,49152
s_add_u32 s48,s48,128
aw a184,v180
mm v[108:111],a[160:163],a[92:95],v[108:111]
aw a185,v181
aw a186,v182
mm v[104:107],a[156:159],a[92:95],v[104:107]
)" R"(s_waitcnt vmcnt(8) lgkmcnt(0)
s_barrier
mm v[104:107],a[104:107],a[88:91],v[104:107]
bl v208,s[16:19],s49 offen offset:0 lds
mm v[100:103],a[100:103],a[88:91],v[100:103]
dt a[108:109],v195 offset:0
aw a187,v183
mm v[100:103],a[152:155],a[92:95],v[100:103]
dt a[110:111],v195 offset:1024
aw a188,v184
mm v[96:99],a[148:151],a[92:95],v[96:99]
dt a[160:161],v195 offset:8192
aw a189,v185
mm v[96:99],a[96:99],a[88:91],v[96:99]
dt a[162:163],v195 offset:9216
aw a190,v186
mm v[16:19],a[112:115],a[64:67],v[16:19]
bl v209,s[16:19],s49 offen offset:1024 lds
mm v[16:19],a[164:167],a[68:71],v[16:19]
dt a[104:105],v194 offset:0
aw a191,v187
mm v[20:23],a[168:171],a[68:71],v[20:23]
dt a[106:107],v194 offset:1024
aw a192,v188
mm v[20:23],a[116:119],a[64:67],v[20:23]
dt a[156:157],v194 offset:8192
aw a193,v189
mm v[24:27],a[120:123],a[64:67],v[24:27]
dt a[158:159],v194 offset:9216
aw a194,v190
mm v[24:27],a[172:175],a[68:71],v[24:27]
bl v210,s[16:19],s49 offen offset:2048 lds
mm v[28:31],a[176:179],a[68:71],v[28:31]
aw a195,v191
mm v[28:31],a[124:127],a[64:67],v[28:31]
dr a[64:67],v212 offset:0
mm v[60:63],a[124:127],a[72:75],v[60:63]
dr a[68:71],v213 offset:0
mm v[60:63],a[176:179],a[76:79],v[60:63]
mm v[56:59],a[172:175],a[76:79],v[56:59]
bl v211,s[16:19],s49 offen offset:3072 lds
mm v[56:59],a[120:123],a[72:75],v[56:59]
s_add_u32 m0,s50,98304
dt a[100:101],v193 offset:0
s_add_u32 s49,s49,0x80000
mm v[52:55],a[116:119],a[72:75],v[52:55]
dt a[102:103],v193 offset:1024
mm v[52:55],a[168:171],a[76:79],v[52:55]
dt a[152:153],v193 offset:8192
mm v[48:51],a[164:167],a[76:79],v[48:51]
dt a[154:155],v193 offset:9216
mm v[48:51],a[112:115],a[72:75],v[48:51]
bl v200,s[12:15],s48 offen offset:0 lds
mm v[80:83],a[112:115],a[80:83],v[80:83]
dr a[72:75],v212 offset:2048
mm v[80:83],a[164:167],a[84:87],v[80:83]
dr a[76:79],v213 offset:2048
mm v[84:87],a[168:171],a[84:87],v[84:87]
mm v[84:87],a[116:119],a[80:83],v[84:87]
dt a[96:97],v192 offset:0
mm v[88:91],a[120:123],a[80:83],v[88:91]
bl v201,s[12:15],s48 offen offset:1024 lds
mm v[88:91],a[172:175],a[84:87],v[88:91]
dt a[98:99],v192 offset:1024
)" R"(mm v[92:95],a[176:179],a[84:87],v[92:95]
dt a[148:149],v192 offset:8192
mm v[92:95],a[124:127],a[80:83],v[92:95]
dt a[150:151],v192 offset:9216
mm v[124:127],a[124:127],a[88:91],v[124:127]
dr a[80:83],v212 offset:4096
mm v[124:127],a[176:179],a[92:95],v[124:127]
bl v202,s[12:15],s48 offen offset:2048 lds
mm v[120:123],a[172:175],a[92:95],v[120:123]
dr a[84:87],v213 offset:4096
mm v[120:123],a[120:123],a[88:91],v[120:123]
mm v[116:119],a[116:119],a[88:91],v[116:119]
mm v[116:119],a[168:171],a[92:95],v[116:119]
mm v[112:115],a[164:167],a[92:95],v[112:115]
bl v203,s[12:15],s48 offen offset:3072 lds
mm v[112:115],a[112:115],a[88:91],v[112:115]
s_waitcnt lgkmcnt(1)
dr a[88:91],v212 offset:6144
s_add_u32 m0,s50,102400
mm v[0:3],a[96:99],a[64:67],v[0:3]
dr a[92:95],v213 offset:6144
mm v[0:3],a[148:151],a[68:71],v[0:3]
mm v[4:7],a[152:155],a[68:71],v[4:7]
dt a[112:113],v196 offset:0
mm v[4:7],a[100:103],a[64:67],v[4:7]
bl v204,s[12:15],s48 offen offset:0 lds
mm v[8:11],a[104:107],a[64:67],v[8:11]
dt a[114:115],v196 offset:1024
mm v[8:11],a[156:159],a[68:71],v[8:11]
dt a[164:165],v196 offset:8192
mm v[12:15],a[160:163],a[68:71],v[12:15]
dt a[166:167],v196 offset:9216
mm v[12:15],a[108:111],a[64:67],v[12:15]
dt a[116:117],v197 offset:0
mm v[44:47],a[108:111],a[72:75],v[44:47]
bl v205,s[12:15],s48 offen offset:1024 lds
mm v[44:47],a[160:163],a[76:79],v[44:47]
dt a[118:119],v197 offset:1024
mm v[40:43],a[156:159],a[76:79],v[40:43]
dt a[168:169],v197 offset:8192
mm v[40:43],a[104:107],a[72:75],v[40:43]
dt a[170:171],v197 offset:9216
v_cmp_eq_u32 vcc,0,v222
s_nop 1
v_cndmask_b32 v254,v254,v222,vcc
mm v[36:39],a[100:103],a[72:75],v[36:39]
v_cmp_ne_u64 vcc,s[54:55],v[228:229]
s_cbranch_vccnz FWO_S
FWO_D:
mm v[36:39],a[152:155],a[76:79],v[36:39]
dt a[120:121],v198 offset:0
mm v[32:35],a[148:151],a[76:79],v[32:35]
dt a[122:123],v198 offset:1024
mm v[32:35],a[96:99],a[72:75],v[32:35]
bl v206,s[12:15],s48 offen offset:2048 lds
mm v[64:67],a[96:99],a[80:83],v[64:67]
s_waitcnt lgkmcnt(4)
dt a[172:173],v198 offset:8192
mm v[64:67],a[148:151],a[84:87],v[64:67]
dt a[174:175],v198 offset:9216
mm v[68:71],a[152:155],a[84:87],v[68:71]
dt a[124:125],v199 offset:0
mm v[68:71],a[100:103],a[80:83],v[68:71]
dt a[126:127],v199 offset:1024
)" R"(mm v[72:75],a[104:107],a[80:83],v[72:75]
dt a[176:177],v199 offset:8192
bl v207,s[12:15],s48 offen offset:3072 lds
mm v[72:75],a[156:159],a[84:87],v[72:75]
s_add_u32 m0,s51,32768
dt a[178:179],v199 offset:9216
s_add_u32 s48,s48,128
mm v[76:79],a[160:163],a[84:87],v[76:79]
mm v[76:79],a[108:111],a[80:83],v[76:79]
mm v[108:111],a[108:111],a[88:91],v[108:111]
bl v208,s[16:19],s49 offen offset:0 lds
mm v[108:111],a[160:163],a[92:95],v[108:111]
mm v[104:107],a[156:159],a[92:95],v[104:107]
s_waitcnt vmcnt(9) lgkmcnt(0)
s_barrier
mm v[104:107],a[104:107],a[88:91],v[104:107]
dt a[108:109],v195 offset:32768
mm v[100:103],a[100:103],a[88:91],v[100:103]
buffer_store_dwordx2 v[252:253],v254,s[8:11],s61 offen sc1
dt a[110:111],v195 offset:33792
s_or_b32 s61,s29,s24
mm v[100:103],a[152:155],a[92:95],v[100:103]
s_cmp_eq_u32 s61,0
s_mov_b32 s61,0x4000000
s_cselect_b32 s61,s61,0x80000000
mm v[96:99],a[148:151],a[92:95],v[96:99]
bl v209,s[16:19],s49 offen offset:1024 lds
mm v[96:99],a[96:99],a[88:91],v[96:99]
dt a[160:161],v195 offset:40960
mm v[16:19],a[112:115],a[64:67],v[16:19]
dt a[162:163],v195 offset:41984
mm v[16:19],a[164:167],a[68:71],v[16:19]
dt a[104:105],v194 offset:32768
mm v[20:23],a[168:171],a[68:71],v[20:23]
buffer_store_dwordx2 v[252:253],v254,s[8:11],s62 offen sc1
s_lshr_b32 s62,s37,15
s_lshl_b32 s62,s62,2
s_add_u32 s62,s62,s24
mm v[20:23],a[116:119],a[64:67],v[20:23]
s_lshl_b32 s62,s62,3
bl v210,s[16:19],s49 offen offset:2048 lds
mm v[24:27],a[120:123],a[64:67],v[24:27]
s_add_u32 s62,s62,0x4018000
dt a[106:107],v194 offset:33792
mm v[24:27],a[172:175],a[68:71],v[24:27]
dt a[156:157],v194 offset:40960
mm v[28:31],a[176:179],a[68:71],v[28:31]
dt a[158:159],v194 offset:41984
mm v[28:31],a[124:127],a[64:67],v[28:31]
buffer_store_dwordx2 v[252:253],v254,s[8:11],s62 offen sc1
dr a[64:67],v214 offset:0
mm v[60:63],a[124:127],a[72:75],v[60:63]
bl v211,s[16:19],s49 offen offset:3072 lds
mm v[60:63],a[176:179],a[76:79],v[60:63]
s_add_u32 m0,s50,65536
dr a[68:71],v215 offset:0
s_add_u32 s49,s49,0x80000
mm v[56:59],a[172:175],a[76:79],v[56:59]
dt a[100:101],v193 offset:32768
mm v[56:59],a[120:123],a[72:75],v[56:59]
dt a[102:103],v193 offset:33792
mm v[52:55],a[116:119],a[72:75],v[52:55]
dt a[152:153],v193 offset:40960
mm v[52:55],a[168:171],a[76:79],v[52:55]
)" R"(buffer_store_dwordx2 v[252:253],v254,s[8:11],s61 offen sc1
dt a[154:155],v193 offset:41984
mm v[48:51],a[164:167],a[76:79],v[48:51]
bl v200,s[12:15],s48 offen offset:0 lds
mm v[48:51],a[112:115],a[72:75],v[48:51]
dr a[72:75],v214 offset:2048
mm v[80:83],a[112:115],a[80:83],v[80:83]
dr a[76:79],v215 offset:2048
mm v[80:83],a[164:167],a[84:87],v[80:83]
mm v[84:87],a[168:171],a[84:87],v[84:87]
bl v201,s[12:15],s48 offen offset:1024 lds
mm v[84:87],a[116:119],a[80:83],v[84:87]
dt a[96:97],v192 offset:32768
mm v[88:91],a[120:123],a[80:83],v[88:91]
dt a[98:99],v192 offset:33792
mm v[88:91],a[172:175],a[84:87],v[88:91]
dt a[148:149],v192 offset:40960
mm v[92:95],a[176:179],a[84:87],v[92:95]
bl v202,s[12:15],s48 offen offset:2048 lds
mm v[92:95],a[124:127],a[80:83],v[92:95]
dt a[150:151],v192 offset:41984
mm v[124:127],a[124:127],a[88:91],v[124:127]
dr a[80:83],v214 offset:4096
mm v[124:127],a[176:179],a[92:95],v[124:127]
dr a[84:87],v215 offset:4096
mm v[120:123],a[172:175],a[92:95],v[120:123]
bl v203,s[12:15],s48 offen offset:3072 lds
mm v[120:123],a[120:123],a[88:91],v[120:123]
E0_7:
s_cbranch_execz E0_R
s_add_u32 m0,s50,69632
mm v[116:119],a[116:119],a[88:91],v[116:119]
mm v[116:119],a[168:171],a[92:95],v[116:119]
mm v[112:115],a[164:167],a[92:95],v[112:115]
mm v[112:115],a[112:115],a[88:91],v[112:115]
s_waitcnt lgkmcnt(1)
bl v204,s[12:15],s48 offen offset:0 lds
mm v[0:3],a[96:99],a[64:67],v[0:3]
dr a[88:91],v214 offset:6144
v_mbcnt_lo_u32_b32 v128,-1,0
mm v[0:3],a[148:151],a[68:71],v[0:3]
dr a[92:95],v215 offset:6144
v_mbcnt_hi_u32_b32 v128,-1,v128
mm v[4:7],a[152:155],a[68:71],v[4:7]
s_lshr_b32 s64,s29,5
mm v[4:7],a[100:103],a[64:67],v[4:7]
s_and_b32 s66,s29,4
dt a[112:113],v196 offset:32768
mm v[8:11],a[104:107],a[64:67],v[8:11]
s_lshl_b32 s66,s66,1
bl v205,s[12:15],s48 offen offset:1024 lds
mm v[8:11],a[156:159],a[68:71],v[8:11]
s_add_u32 s64,s64,s66
dt a[114:115],v196 offset:33792
mm v[12:15],a[160:163],a[68:71],v[12:15]
s_bfe_u32 s65,s29,0x20003
dt a[164:165],v196 offset:40960
mm v[12:15],a[108:111],a[64:67],v[12:15]
s_and_b32 s66,s29,3
dt a[166:167],v196 offset:41984
mm v[44:47],a[108:111],a[72:75],v[44:47]
s_lshl_b32 s66,s66,2
dt a[116:117],v197 offset:32768
mm v[44:47],a[160:163],a[76:79],v[44:47]
)" R"(s_add_u32 s65,s65,s66
dt a[118:119],v197 offset:33792
mm v[40:43],a[156:159],a[76:79],v[40:43]
bl v206,s[12:15],s48 offen offset:2048 lds
mm v[40:43],a[104:107],a[72:75],v[40:43]
dt a[168:169],v197 offset:40960
v_and_b32 v129,15,v128
mm v[36:39],a[100:103],a[72:75],v[36:39]
dt a[170:171],v197 offset:41984
v_lshlrev_b32 v146,3,v129
mm v[36:39],a[152:155],a[76:79],v[36:39]
dt a[120:121],v198 offset:32768
v_lshrrev_b32 v128,4,v128
mm v[32:35],a[148:151],a[76:79],v[32:35]
dt a[122:123],v198 offset:33792
v_lshlrev_b32 v130,12,v128
mm v[32:35],a[96:99],a[72:75],v[32:35]
s_mul_i32 s68,s64,86
dt a[172:173],v198 offset:40960
v_lshl_add_u32 v144,v129,4,v130
mm v[64:67],a[96:99],a[80:83],v[64:67]
s_waitcnt lgkmcnt(5)
bl v207,s[12:15],s48 offen offset:3072 lds
mm v[64:67],a[148:151],a[84:87],v[64:67]
s_add_u32 m0,s51,16384
dt a[174:175],v198 offset:41984
s_lshr_b32 s68,s68,8
mm v[68:71],a[152:155],a[84:87],v[68:71]
s_mul_i32 s67,s68,3
dt a[124:125],v199 offset:32768
s_sub_u32 s67,s64,s67
mm v[68:71],a[100:103],a[80:83],v[68:71]
s_lshr_b32 s69,s68,1
dt a[126:127],v199 offset:33792
s_add_u32 s48,s48,128
mm v[72:75],a[104:107],a[80:83],v[72:75]
s_and_b32 s68,s68,1
dt a[176:177],v199 offset:40960
mm v[72:75],a[156:159],a[84:87],v[72:75]
s_lshl_b32 s66,s69,2
bl v208,s[16:19],s49 offen offset:0 lds
dt a[178:179],v199 offset:41984
mm v[76:79],a[160:163],a[84:87],v[76:79]
s_sub_u32 s66,16,s66
mm v[76:79],a[108:111],a[80:83],v[76:79]
s_mul_i32 s66,s66,s68
mm v[108:111],a[108:111],a[88:91],v[108:111]
v_add_u32 v130,s66,v129
mm v[108:111],a[160:163],a[92:95],v[108:111]
bl v209,s[16:19],s49 offen offset:1024 lds
mm v[104:107],a[156:159],a[92:95],v[104:107]
s_waitcnt vmcnt(11) lgkmcnt(0)
s_barrier
mm v[104:107],a[104:107],a[88:91],v[104:107]
dt a[108:109],v195 offset:16384
v_cmp_le_u32 vcc,10,v130
mm v[100:103],a[100:103],a[88:91],v[100:103]
s_lshl_b32 s66,s68,4
dt a[110:111],v195 offset:17408
v_cndmask_b32 v131,0,1,vcc
mm v[100:103],a[152:155],a[92:95],v[100:103]
bl v210,s[16:19],s49 offen offset:2048 lds
mm v[96:99],a[148:151],a[92:95],v[96:99]
dt a[160:161],v195 offset:24576
)" R"(v_mad_i32_i24 v145,v131,-10,v130
mm v[96:99],a[96:99],a[88:91],v[96:99]
dt a[162:163],v195 offset:25600
v_subrev_u32 v130,s66,v130
mm v[16:19],a[112:115],a[64:67],v[16:19]
s_mul_i32 s66,s68,24
dt a[104:105],v194 offset:16384
s_xor_b32 s66,s66,16
mm v[16:19],a[164:167],a[68:71],v[16:19]
dt a[106:107],v194 offset:17408
v_cmp_eq_u32 vcc,s66,v129
mm v[20:23],a[168:171],a[68:71],v[20:23]
bl v211,s[16:19],s49 offen offset:3072 lds
mm v[20:23],a[116:119],a[64:67],v[20:23]
s_add_u32 m0,s50,131072
dt a[156:157],v194 offset:24576
v_cndmask_b32 v145,v145,10,vcc
mm v[24:27],a[120:123],a[64:67],v[24:27]
s_add_u32 s49,s49,0x80000
dt a[158:159],v194 offset:25600
v_cndmask_b32 v131,v131,v128,vcc
mm v[24:27],a[172:175],a[68:71],v[24:27]
s_getpc_b64 s[60:61]
CT2:
v_cndmask_b32 v130,v130,0,vcc
v_and_b32 v129,3,v145
mm v[28:31],a[176:179],a[68:71],v[28:31]
s_lshr_b32 s62,s29,3
v_lshl_add_u32 v129,v131,2,v129
v_lshrrev_b32 v145,2,v145
mm v[28:31],a[124:127],a[64:67],v[28:31]
s_lshl_b32 s62,s62,2
bl v200,s[12:15],s48 offen offset:0 lds
mm v[60:63],a[124:127],a[72:75],v[60:63]
s_add_u32 s62,s62,s24
dr a[64:67],v212 offset:32768
v_mul_u32_u24 v145,1280,v145
mm v[60:63],a[176:179],a[76:79],v[60:63]
s_mul_i32 s66,s68,12
dr a[68:71],v213 offset:32768
v_lshl_add_u32 v145,v129,4,v145
mm v[56:59],a[172:175],a[76:79],v[56:59]
s_sub_u32 s66,16,s66
s_lshl_b32 s62,s62,7
s_add_u32 s62,s62,ATT-CT2
mm v[56:59],a[120:123],a[72:75],v[56:59]
dt a[100:101],v193 offset:16384
v_mov_b32 v129,0x40000000
mm v[52:55],a[116:119],a[72:75],v[52:55]
bl v201,s[12:15],s48 offen offset:1024 lds
mm v[52:55],a[168:171],a[76:79],v[52:55]
dt a[102:103],v193 offset:17408
v_cmp_gt_u32 vcc,s66,v130
mm v[48:51],a[164:167],a[76:79],v[48:51]
dt a[152:153],v193 offset:24576
v_cndmask_b32 v145,v129,v145,vcc
mm v[48:51],a[112:115],a[72:75],v[48:51]
dt a[154:155],v193 offset:25600
v_cmp_gt_u32 vcc,2,v128
mm v[80:83],a[112:115],a[80:83],v[80:83]
s_lshl_b32 s67,s67,2
dr a[72:75],v212 offset:34816
v_cndmask_b32 v145,v129,v145,vcc
mm v[80:83],a[164:167],a[84:87],v[80:83]
)" R"(s_add_u32 s67,s67,s24
bl v202,s[12:15],s48 offen offset:2048 lds
mm v[84:87],a[168:171],a[84:87],v[84:87]
s_lshl_b32 s69,s69,7
dr a[76:79],v213 offset:34816
v_mbcnt_lo_u32_b32 v222,-1,0
mm v[84:87],a[116:119],a[80:83],v[84:87]
s_lshr_b32 s66,s67,1
dt a[96:97],v192 offset:16384
v_mbcnt_hi_u32_b32 v222,-1,v222
mm v[88:91],a[120:123],a[80:83],v[88:91]
s_lshl_b32 s66,s66,8
dt a[98:99],v192 offset:17408
v_min_u32 v222,3,v222
mm v[88:91],a[172:175],a[84:87],v[88:91]
s_add_u32 s69,s69,s66
dt a[148:149],v192 offset:24576
v_lshlrev_b32 v222,14,v222
mm v[92:95],a[176:179],a[84:87],v[92:95]
s_lshl_b32 s66,s28,3
bl v203,s[12:15],s48 offen offset:3072 lds
mm v[92:95],a[124:127],a[80:83],v[92:95]
s_add_u32 m0,s50,135168
dt a[150:151],v192 offset:25600
s_add_u32 s69,s69,s66
mm v[124:127],a[124:127],a[88:91],v[124:127]
s_lshl_b32 s66,s65,12
dr a[80:83],v212 offset:36864
v_add_u32 v222,s62,v222
mm v[124:127],a[176:179],a[92:95],v[124:127]
s_add_u32 s69,s69,s66
dr a[84:87],v213 offset:36864
v_min_u32 v222,THEEND-CT2-4,v222
mm v[120:123],a[172:175],a[92:95],v[120:123]
s_add_u32 s69,s69,0x3f00000
mm v[120:123],a[120:123],a[88:91],v[120:123]
s_cmp_lt_u32 s67,10
s_cselect_b32 s67,s69,0x80000000
mm v[116:119],a[116:119],a[88:91],v[116:119]
s_mul_i32 s68,s65,12
bl v204,s[12:15],s48 offen offset:0 lds
mm v[116:119],a[168:171],a[92:95],v[116:119]
s_add_u32 s68,s68,s64
mm v[112:115],a[164:167],a[92:95],v[112:115]
s_lshl_b32 s68,s68,14
mm v[112:115],a[112:115],a[88:91],v[112:115]
s_waitcnt lgkmcnt(1)
dr a[88:91],v212 offset:38912
s_lshl_b32 s66,s24,8
mm v[0:3],a[96:99],a[64:67],v[0:3]
s_add_u32 s68,s68,s66
bl v205,s[12:15],s48 offen offset:1024 lds
mm v[0:3],a[148:151],a[68:71],v[0:3]
s_add_u32 s68,s68,0x3c00000
dr a[92:95],v213 offset:38912
v_mbcnt_lo_u32_b32 v250,-1,0
mm v[4:7],a[152:155],a[68:71],v[4:7]
s_cmp_lt_u32 s64,12
s_cselect_b32 s67,s67,0x80000000
s_cselect_b32 s68,s68,0x80000000
mm v[4:7],a[100:103],a[64:67],v[4:7]
s_lshl_b32 s69,s65,7
dt a[112:113],v196 offset:16384
v_mbcnt_hi_u32_b32 v250,-1,v250
)" R"(mm v[8:11],a[104:107],a[64:67],v[8:11]
s_add_u32 s69,s69,0x4006000
dt a[114:115],v196 offset:17408
v_lshlrev_b32 v232,4,v250
mm v[8:11],a[156:159],a[68:71],v[8:11]
bl v206,s[12:15],s48 offen offset:2048 lds
mm v[12:15],a[160:163],a[68:71],v[12:15]
dt a[164:165],v196 offset:24576
v_add_u32 v233,0x1000,v232
mm v[12:15],a[108:111],a[64:67],v[12:15]
dt a[166:167],v196 offset:25600
v_add_u32 v234,0x10000,v232
mm v[44:47],a[108:111],a[72:75],v[44:47]
dt a[116:117],v197 offset:16384
v_add_u32 v235,0x20000,v232
mm v[44:47],a[160:163],a[76:79],v[44:47]
dt a[118:119],v197 offset:17408
v_mov_b32 v236,0xff7fffff
mm v[40:43],a[156:159],a[76:79],v[40:43]
dt a[168:169],v197 offset:24576
v_and_b32 v251,15,v250
mm v[40:43],a[104:107],a[72:75],v[40:43]
bl v207,s[12:15],s48 offen offset:3072 lds
mm v[36:39],a[100:103],a[72:75],v[36:39]
s_add_u32 m0,s51,49152
dt a[170:171],v197 offset:25600
v_lshrrev_b32 v240,4,v250
mm v[36:39],a[152:155],a[76:79],v[36:39]
s_add_u32 s48,s48,128
dt a[120:121],v198 offset:16384
v_lshlrev_b32 v240,2,v240
mm v[32:35],a[148:151],a[76:79],v[32:35]
s_lshl_b32 s71,s25,10
dt a[122:123],v198 offset:17408
s_add_u32 s71,s71,0x4018000
mm v[32:35],a[96:99],a[72:75],v[32:35]
dt a[172:173],v198 offset:24576
v_sub_u32 v251,v251,v240
mm v[64:67],a[96:99],a[80:83],v[64:67]
s_waitcnt lgkmcnt(5)
dt a[174:175],v198 offset:25600
s_lshl_b32 s40,s24,4
mm v[64:67],a[148:151],a[84:87],v[64:67]
bl v208,s[16:19],s49 offen offset:0 lds
mm v[68:71],a[152:155],a[84:87],v[68:71]
dt a[124:125],v199 offset:16384
v_add_u32 v237,s40,v251
mm v[68:71],a[100:103],a[80:83],v[68:71]
s_sub_u32 s40,48,s40
dt a[126:127],v199 offset:17408
mm v[72:75],a[104:107],a[80:83],v[72:75]
dt a[176:177],v199 offset:24576
mm v[72:75],a[156:159],a[84:87],v[72:75]
dt a[178:179],v199 offset:25600
v_add_u32 v238,s40,v251
mm v[76:79],a[160:163],a[84:87],v[76:79]
bl v209,s[16:19],s49 offen offset:1024 lds
mm v[76:79],a[108:111],a[80:83],v[76:79]
v_mov_b32 v251,0x3f803f80
mm v[108:111],a[108:111],a[88:91],v[108:111]
aw a144,v251
mm v[108:111],a[160:163],a[92:95],v[108:111]
aw a145,v251
mm v[104:107],a[156:159],a[92:95],v[104:107]
)" R"(bl v210,s[16:19],s49 offen offset:2048 lds
s_waitcnt vmcnt(11) lgkmcnt(0)
s_barrier
mm v[104:107],a[104:107],a[88:91],v[104:107]
dt a[108:109],v195 offset:0
aw a146,v251
mm v[100:103],a[100:103],a[88:91],v[100:103]
dt a[110:111],v195 offset:1024
aw a147,v251
mm v[100:103],a[152:155],a[92:95],v[100:103]
dt a[160:161],v195 offset:8192
mm v[96:99],a[148:151],a[92:95],v[96:99]
bl v211,s[16:19],s49 offen offset:3072 lds
mm v[96:99],a[96:99],a[88:91],v[96:99]
s_add_u32 m0,s50,98304
dt a[162:163],v195 offset:9216
v_bfe_i32 v246,v250,0,1
mm v[16:19],a[112:115],a[64:67],v[16:19]
s_add_u32 s49,s49,0x80000
dt a[104:105],v194 offset:0
v_and_b32 v246,0xaaaaaaaa,v246
mm v[16:19],a[164:167],a[68:71],v[16:19]
dt a[106:107],v194 offset:1024
v_or_b32 v246,0x44444444,v246
mm v[20:23],a[168:171],a[68:71],v[20:23]
s_lshl_b32 s32,s25,20
dt a[156:157],v194 offset:8192
mm v[20:23],a[116:119],a[64:67],v[20:23]
bl v200,s[12:15],s48 offen offset:0 lds
mm v[24:27],a[120:123],a[64:67],v[24:27]
s_lshl_b32 s40,s27,14
dt a[158:159],v194 offset:9216
mm v[24:27],a[172:175],a[68:71],v[24:27]
s_add_u32 s32,s32,s40
mm v[28:31],a[176:179],a[68:71],v[28:31]
mm v[28:31],a[124:127],a[64:67],v[28:31]
s_lshl_b32 s40,s28,12
dr a[64:67],v212 offset:0
mm v[60:63],a[124:127],a[72:75],v[60:63]
s_add_u32 s32,s32,s40
dr a[68:71],v213 offset:0
mm v[60:63],a[176:179],a[76:79],v[60:63]
mm v[56:59],a[172:175],a[76:79],v[56:59]
s_mov_b32 s31,s32
buffer_load_dwordx2 v[128:129],v146,s[8:11],s69 offen sc1
mm v[56:59],a[120:123],a[72:75],v[56:59]
s_mov_b32 s30,s32
bl v201,s[12:15],s48 offen offset:1024 lds
mm v[52:55],a[116:119],a[72:75],v[52:55]
dt a[100:101],v193 offset:0
mm v[52:55],a[168:171],a[76:79],v[52:55]
s_lshl_b32 s33,s24,12
dt a[102:103],v193 offset:1024
mm v[48:51],a[164:167],a[76:79],v[48:51]
s_lshl_b32 s34,s24,11
dt a[152:153],v193 offset:8192
mm v[48:51],a[112:115],a[72:75],v[48:51]
bl v202,s[12:15],s48 offen offset:2048 lds
mm v[80:83],a[112:115],a[80:83],v[80:83]
s_lshr_b32 s56,s25,2
dt a[154:155],v193 offset:9216
mm v[80:83],a[164:167],a[84:87],v[80:83]
s_and_b32 s57,s25,3
dr a[72:75],v212 offset:2048
)" R"(mm v[84:87],a[168:171],a[84:87],v[84:87]
dr a[76:79],v213 offset:2048
mm v[84:87],a[116:119],a[80:83],v[84:87]
s_mul_i32 s35,s25,163840
bl v203,s[12:15],s48 offen offset:3072 lds
mm v[88:91],a[120:123],a[80:83],v[88:91]
s_add_u32 m0,s50,102400
dt a[96:97],v192 offset:0
s_add_u32 s35,s35,s34
mm v[88:91],a[172:175],a[84:87],v[88:91]
dt a[98:99],v192 offset:1024
mm v[92:95],a[176:179],a[84:87],v[92:95]
s_add_u32 s35,s35,0x3000000
dt a[148:149],v192 offset:8192
mm v[92:95],a[124:127],a[80:83],v[92:95]
s_lshl_b32 s58,s27,11
dt a[150:151],v192 offset:9216
mm v[124:127],a[124:127],a[88:91],v[124:127]
bl v204,s[12:15],s48 offen offset:0 lds
mm v[124:127],a[176:179],a[92:95],v[124:127]
s_lshl_b32 s59,s27,12
dr a[80:83],v212 offset:4096
mm v[120:123],a[172:175],a[92:95],v[120:123]
s_add_u32 s59,s59,0x2000
dr a[84:87],v213 offset:4096
mm v[120:123],a[120:123],a[88:91],v[120:123]
mm v[116:119],a[116:119],a[88:91],v[116:119]
bl v205,s[12:15],s48 offen offset:1024 lds
mm v[116:119],a[168:171],a[92:95],v[116:119]
mm v[112:115],a[164:167],a[92:95],v[112:115]
mm v[112:115],a[112:115],a[88:91],v[112:115]
s_waitcnt lgkmcnt(2)
dr a[88:91],v212 offset:6144
mm v[0:3],a[96:99],a[64:67],v[0:3]
global_load_dword v255,v222,s[60:61]
mm v[0:3],a[148:151],a[68:71],v[0:3]
bl v206,s[12:15],s48 offen offset:2048 lds
mm v[4:7],a[152:155],a[68:71],v[4:7]
dr a[92:95],v213 offset:6144
mm v[4:7],a[100:103],a[64:67],v[4:7]
dt a[112:113],v196 offset:0
mm v[8:11],a[104:107],a[64:67],v[8:11]
dt a[114:115],v196 offset:1024
mm v[8:11],a[156:159],a[68:71],v[8:11]
dt a[164:165],v196 offset:8192
mm v[12:15],a[160:163],a[68:71],v[12:15]
bl v207,s[12:15],s48 offen offset:3072 lds
mm v[12:15],a[108:111],a[64:67],v[12:15]
s_add_u32 m0,s51,32768
dt a[166:167],v196 offset:9216
s_add_u32 s48,s48,128
mm v[44:47],a[108:111],a[72:75],v[44:47]
dt a[116:117],v197 offset:0
mm v[44:47],a[160:163],a[76:79],v[44:47]
dt a[118:119],v197 offset:1024
mm v[40:43],a[156:159],a[76:79],v[40:43]
dt a[168:169],v197 offset:8192
mm v[40:43],a[104:107],a[72:75],v[40:43]
dt a[170:171],v197 offset:9216
mm v[36:39],a[100:103],a[72:75],v[36:39]
bl v208,s[16:19],s49 offen offset:0 lds
mm v[36:39],a[152:155],a[76:79],v[36:39]
dt a[120:121],v198 offset:0
mm v[32:35],a[148:151],a[76:79],v[32:35]
)" R"(dt a[122:123],v198 offset:1024
mm v[32:35],a[96:99],a[72:75],v[32:35]
s_waitcnt lgkmcnt(5)
dt a[172:173],v198 offset:8192
mm v[64:67],a[96:99],a[80:83],v[64:67]
dt a[174:175],v198 offset:9216
mm v[64:67],a[148:151],a[84:87],v[64:67]
dt a[124:125],v199 offset:0
mm v[68:71],a[152:155],a[84:87],v[68:71]
bl v209,s[16:19],s49 offen offset:1024 lds
mm v[68:71],a[100:103],a[80:83],v[68:71]
dt a[126:127],v199 offset:1024
mm v[72:75],a[104:107],a[80:83],v[72:75]
s_add_u32 s40,s31,32768
dt a[176:177],v199 offset:8192
mm v[72:75],a[156:159],a[84:87],v[72:75]
dt a[178:179],v199 offset:9216
mm v[76:79],a[160:163],a[84:87],v[76:79]
bl v210,s[16:19],s49 offen offset:2048 lds
mm v[76:79],a[108:111],a[80:83],v[76:79]
mm v[108:111],a[108:111],a[88:91],v[108:111]
mm v[108:111],a[160:163],a[92:95],v[108:111]
mm v[104:107],a[156:159],a[92:95],v[104:107]
s_waitcnt vmcnt(13) lgkmcnt(0)
s_barrier
mm v[104:107],a[104:107],a[88:91],v[104:107]
bl v211,s[16:19],s49 offen offset:3072 lds
mm v[100:103],a[100:103],a[88:91],v[100:103]
s_add_u32 m0,s50,65536
dt a[108:109],v195 offset:32768
s_add_u32 s49,s49,0x80000
mm v[100:103],a[152:155],a[92:95],v[100:103]
dt a[110:111],v195 offset:33792
mm v[96:99],a[148:151],a[92:95],v[96:99]
dt a[160:161],v195 offset:40960
mm v[96:99],a[96:99],a[88:91],v[96:99]
bl v200,s[12:15],s48 offen offset:0 lds
mm v[16:19],a[112:115],a[64:67],v[16:19]
s_waitcnt vmcnt(13)
v_cmp_ne_u64 vcc,s[54:55],v[128:129]
s_cbranch_vccnz RED_S
RED_D:
mm v[16:19],a[164:167],a[68:71],v[16:19]
dt a[162:163],v195 offset:41984
mm v[20:23],a[168:171],a[68:71],v[20:23]
dt a[104:105],v194 offset:32768
mm v[20:23],a[116:119],a[64:67],v[20:23]
bl v[128:131],v144,s[8:11],s68 offen offset:0 sc1
mm v[24:27],a[120:123],a[64:67],v[24:27]
dt a[106:107],v194 offset:33792
mm v[24:27],a[172:175],a[68:71],v[24:27]
dt a[156:157],v194 offset:40960
mm v[28:31],a[176:179],a[68:71],v[28:31]
dt a[158:159],v194 offset:41984
mm v[28:31],a[124:127],a[64:67],v[28:31]
bl v201,s[12:15],s48 offen offset:1024 lds
mm v[60:63],a[124:127],a[72:75],v[60:63]
dr a[64:67],v214 offset:0
mm v[60:63],a[176:179],a[76:79],v[60:63]
dr a[68:71],v215 offset:0
mm v[56:59],a[172:175],a[76:79],v[56:59]
mm v[56:59],a[120:123],a[72:75],v[56:59]
dt a[100:101],v193 offset:32768
mm v[52:55],a[116:119],a[72:75],v[52:55]
)" R"(bl v[132:135],v144,s[8:11],s68 offen offset:1024 sc1
mm v[52:55],a[168:171],a[76:79],v[52:55]
dt a[102:103],v193 offset:33792
mm v[48:51],a[164:167],a[76:79],v[48:51]
dt a[152:153],v193 offset:40960
mm v[48:51],a[112:115],a[72:75],v[48:51]
dt a[154:155],v193 offset:41984
mm v[80:83],a[112:115],a[80:83],v[80:83]
s_lshr_b32 s62,s29,5
buffer_load_dwordx2 v[228:229],v227,s[8:11],s71 offen sc1
mm v[80:83],a[164:167],a[84:87],v[80:83]
s_and_b32 s63,s29,4
bl v202,s[12:15],s48 offen offset:2048 lds
mm v[84:87],a[168:171],a[84:87],v[84:87]
s_lshl_b32 s63,s63,1
dr a[72:75],v214 offset:2048
s_add_u32 s62,s62,s63
mm v[84:87],a[116:119],a[80:83],v[84:87]
s_bfe_u32 s61,s29,0x20003
dr a[76:79],v215 offset:2048
s_and_b32 s63,s29,3
mm v[88:91],a[120:123],a[80:83],v[88:91]
s_lshl_b32 s63,s63,2
s_add_u32 s61,s61,s63
s_lshl_b32 s61,s61,4
mm v[88:91],a[172:175],a[84:87],v[88:91]
s_add_u32 s61,s61,s62
dt a[96:97],v192 offset:32768
s_lshl_b32 s61,s61,3
mm v[92:95],a[176:179],a[84:87],v[92:95]
s_add_u32 s61,s61,0x4008000
dt a[98:99],v192 offset:33792
mm v[92:95],a[124:127],a[80:83],v[92:95]
s_cmp_eq_u32 s24,0
s_cselect_b32 s74,s61,0x80000000
dt a[148:149],v192 offset:40960
bl v[136:139],v144,s[8:11],s68 offen offset:2048 sc1
mm v[124:127],a[124:127],a[88:91],v[124:127]
dt a[150:151],v192 offset:41984
mm v[124:127],a[176:179],a[92:95],v[124:127]
dr a[80:83],v214 offset:4096
mm v[120:123],a[172:175],a[92:95],v[120:123]
dr a[84:87],v215 offset:4096
mm v[120:123],a[120:123],a[88:91],v[120:123]
bl v203,s[12:15],s48 offen offset:3072 lds
mm v[116:119],a[116:119],a[88:91],v[116:119]
s_add_u32 m0,s50,69632
mm v[116:119],a[168:171],a[92:95],v[116:119]
mm v[112:115],a[164:167],a[92:95],v[112:115]
mm v[112:115],a[112:115],a[88:91],v[112:115]
s_waitcnt lgkmcnt(2)
bl v[140:143],v144,s[8:11],s68 offen offset:3072 sc1
mm v[0:3],a[96:99],a[64:67],v[0:3]
dr a[88:91],v214 offset:6144
mm v[0:3],a[148:151],a[68:71],v[0:3]
dr a[92:95],v215 offset:6144
mm v[4:7],a[152:155],a[68:71],v[4:7]
mm v[4:7],a[100:103],a[64:67],v[4:7]
bl v204,s[12:15],s48 offen offset:0 lds
mm v[8:11],a[104:107],a[64:67],v[8:11]
dt a[112:113],v196 offset:32768
mm v[8:11],a[156:159],a[68:71],v[8:11]
dt a[114:115],v196 offset:33792
mm v[12:15],a[160:163],a[68:71],v[12:15]
)" R"(dt a[164:165],v196 offset:40960
mm v[12:15],a[108:111],a[64:67],v[12:15]
bl v205,s[12:15],s48 offen offset:1024 lds
mm v[44:47],a[108:111],a[72:75],v[44:47]
dt a[166:167],v196 offset:41984
mm v[44:47],a[160:163],a[76:79],v[44:47]
dt a[116:117],v197 offset:32768
mm v[40:43],a[156:159],a[76:79],v[40:43]
dt a[118:119],v197 offset:33792
mm v[40:43],a[104:107],a[72:75],v[40:43]
dt a[168:169],v197 offset:40960
mm v[36:39],a[100:103],a[72:75],v[36:39]
bl v206,s[12:15],s48 offen offset:2048 lds
mm v[36:39],a[152:155],a[76:79],v[36:39]
dt a[170:171],v197 offset:41984
mm v[32:35],a[148:151],a[76:79],v[32:35]
dt a[120:121],v198 offset:32768
mm v[32:35],a[96:99],a[72:75],v[32:35]
s_waitcnt lgkmcnt(5)
dt a[122:123],v198 offset:33792
mm v[64:67],a[96:99],a[80:83],v[64:67]
dt a[172:173],v198 offset:40960
bl v207,s[12:15],s48 offen offset:3072 lds
mm v[64:67],a[148:151],a[84:87],v[64:67]
EA_9:
s_cbranch_execz EA_R
s_add_u32 m0,s51,16384
dt a[174:175],v198 offset:41984
s_add_u32 s48,s48,128
mm v[68:71],a[152:155],a[84:87],v[68:71]
dt a[124:125],v199 offset:32768
mm v[68:71],a[100:103],a[80:83],v[68:71]
dt a[126:127],v199 offset:33792
mm v[72:75],a[104:107],a[80:83],v[72:75]
dt a[176:177],v199 offset:40960
bl v208,s[16:19],s49 offen offset:0 lds
mm v[72:75],a[156:159],a[84:87],v[72:75]
dt a[178:179],v199 offset:41984
mm v[76:79],a[160:163],a[84:87],v[76:79]
mm v[76:79],a[108:111],a[80:83],v[76:79]
mm v[108:111],a[108:111],a[88:91],v[108:111]
bl v209,s[16:19],s49 offen offset:1024 lds
mm v[108:111],a[160:163],a[92:95],v[108:111]
s_waitcnt vmcnt(10)
v_cmp_ne_u64 vcc,s[54:55],v[228:229]
s_cbranch_vccnz FKV_S
FKV_D:
mm v[104:107],a[156:159],a[92:95],v[104:107]
s_waitcnt lgkmcnt(0)
s_barrier
mm v[104:107],a[104:107],a[88:91],v[104:107]
dt a[108:109],v195 offset:16384
v_add_f32 v128,v128,v132
mm v[100:103],a[100:103],a[88:91],v[100:103]
bl v210,s[16:19],s49 offen offset:2048 lds
mm v[100:103],a[152:155],a[92:95],v[100:103]
dt a[110:111],v195 offset:17408
v_add_f32 v129,v129,v133
mm v[96:99],a[148:151],a[92:95],v[96:99]
dt a[160:161],v195 offset:24576
v_add_f32 v130,v130,v134
mm v[96:99],a[96:99],a[88:91],v[96:99]
dt a[162:163],v195 offset:25600
v_add_f32 v131,v131,v135
)" R"(mm v[16:19],a[112:115],a[64:67],v[16:19]
s_mov_b32 s12,0xfff0ffff
bl v211,s[16:19],s49 offen offset:3072 lds
mm v[16:19],a[164:167],a[68:71],v[16:19]
s_add_u32 m0,s33,0
dt a[104:105],v194 offset:16384
s_add_u32 s49,s49,0x80000
mm v[20:23],a[168:171],a[68:71],v[20:23]
s_mov_b32 s13,0xf000ff00
dt a[106:107],v194 offset:17408
s_mov_b32 s14,0xffe0fffe
mm v[20:23],a[116:119],a[64:67],v[20:23]
s_mov_b32 s15,0xe000fe00
dt a[156:157],v194 offset:24576
s_mov_b32 s16,0xffc0fffc
mm v[24:27],a[120:123],a[64:67],v[24:27]
s_mov_b32 s17,0xc000fc00
bl v232,s[8:11],s31 offen offset:0 sc1 lds
mm v[24:27],a[172:175],a[68:71],v[24:27]
s_mov_b32 s18,0xff80fff8
dt a[158:159],v194 offset:25600
s_mov_b32 s19,0x8000f800
mm v[28:31],a[176:179],a[68:71],v[28:31]
s_mov_b32 s36,0x34127801
s_mov_b32 s37,0x01781234
s_lshl_b32 s48,s24,4
mm v[28:31],a[124:127],a[64:67],v[28:31]
s_lshr_b64 s[36:37],s[36:37],s48
dr a[64:67],v212 offset:32768
mm v[60:63],a[124:127],a[72:75],v[60:63]
bl v232,s[8:11],s31 offen offset:1024 sc1 lds
mm v[60:63],a[176:179],a[76:79],v[60:63]
dr a[68:71],v213 offset:32768
mm v[56:59],a[172:175],a[76:79],v[56:59]
dt a[100:101],v193 offset:16384
mm v[56:59],a[120:123],a[72:75],v[56:59]
dt a[102:103],v193 offset:17408
mm v[52:55],a[116:119],a[72:75],v[52:55]
dt a[152:153],v193 offset:24576
s_waitcnt vmcnt(10)
mm v[52:55],a[168:171],a[76:79],v[52:55]
bl v232,s[8:11],s31 offen offset:2048 sc1 lds
mm v[48:51],a[164:167],a[76:79],v[48:51]
dt a[154:155],v193 offset:25600
v_add_f32 v136,v136,v140
mm v[48:51],a[112:115],a[72:75],v[48:51]
dr a[72:75],v212 offset:34816
v_add_f32 v137,v137,v141
mm v[80:83],a[112:115],a[80:83],v[80:83]
dr a[76:79],v213 offset:34816
v_add_f32 v138,v138,v142
mm v[80:83],a[164:167],a[84:87],v[80:83]
v_add_f32 v139,v139,v143
v_add_f32 v128,v128,v136
mm v[84:87],a[168:171],a[84:87],v[84:87]
bl v232,s[8:11],s31 offen offset:3072 sc1 lds
mm v[84:87],a[116:119],a[80:83],v[84:87]
s_add_u32 m0,s33,131072
dt a[96:97],v192 offset:16384
v_add_f32 v129,v129,v137
mm v[88:91],a[120:123],a[80:83],v[88:91]
dt a[98:99],v192 offset:17408
v_add_f32 v130,v130,v138
mm v[88:91],a[172:175],a[84:87],v[88:91]
)" R"(dt a[148:149],v192 offset:24576
v_add_f32 v131,v131,v139
mm v[92:95],a[176:179],a[84:87],v[92:95]
dt a[150:151],v192 offset:25600
v_mov_b32 v132,v128
mm v[92:95],a[124:127],a[80:83],v[92:95]
bl v232,s[8:11],s40 offen offset:0 sc1 lds
mm v[124:127],a[124:127],a[88:91],v[124:127]
dr a[80:83],v212 offset:36864
v_mov_b32 v133,v129
mm v[124:127],a[176:179],a[92:95],v[124:127]
dr a[84:87],v213 offset:36864
v_mov_b32 v134,v130
mm v[120:123],a[172:175],a[92:95],v[120:123]
v_permlane32_swap_b32 v128,v132
mm v[120:123],a[120:123],a[88:91],v[120:123]
v_permlane32_swap_b32 v129,v133
mm v[116:119],a[116:119],a[88:91],v[116:119]
bl v232,s[8:11],s40 offen offset:1024 sc1 lds
mm v[116:119],a[168:171],a[92:95],v[116:119]
v_permlane32_swap_b32 v130,v134
mm v[112:115],a[164:167],a[92:95],v[112:115]
v_mov_b32 v135,v131
v_add_f32 v128,v128,v132
mm v[112:115],a[112:115],a[88:91],v[112:115]
s_waitcnt lgkmcnt(1)
dr a[88:91],v212 offset:38912
v_add_f32 v129,v129,v133
mm v[0:3],a[96:99],a[64:67],v[0:3]
dr a[92:95],v213 offset:38912
v_add_f32 v130,v130,v134
mm v[0:3],a[148:151],a[68:71],v[0:3]
bl v232,s[8:11],s40 offen offset:2048 sc1 lds
mm v[4:7],a[152:155],a[68:71],v[4:7]
dt a[112:113],v196 offset:16384
v_mov_b32 v132,v128
mm v[4:7],a[100:103],a[64:67],v[4:7]
dt a[114:115],v196 offset:17408
v_mov_b32 v133,v129
mm v[8:11],a[104:107],a[64:67],v[8:11]
dt a[164:165],v196 offset:24576
v_mov_b32 v134,v130
mm v[8:11],a[156:159],a[68:71],v[8:11]
dt a[166:167],v196 offset:25600
mm v[12:15],a[160:163],a[68:71],v[12:15]
s_getpc_b64 s[0:1]
EB_G:
bl v232,s[8:11],s40 offen offset:3072 sc1 lds
mm v[12:15],a[108:111],a[64:67],v[12:15]
s_add_u32 s40,s31,8192
dt a[116:117],v197 offset:16384
s_add_u32 m0,s33,147456
mm v[44:47],a[108:111],a[72:75],v[44:47]
s_add_u32 s0,s0,s2
s_addc_u32 s1,s1,0
dt a[118:119],v197 offset:17408
mm v[44:47],a[160:163],a[76:79],v[44:47]
dt a[168:169],v197 offset:24576
mm v[40:43],a[156:159],a[76:79],v[40:43]
dt a[170:171],v197 offset:25600
mm v[40:43],a[104:107],a[72:75],v[40:43]
bl v232,s[8:11],s40 offen offset:0 sc1 lds
mm v[36:39],a[100:103],a[72:75],v[36:39]
dt a[120:121],v198 offset:16384
)" R"(mm v[36:39],a[152:155],a[76:79],v[36:39]
dt a[122:123],v198 offset:17408
mm v[32:35],a[148:151],a[76:79],v[32:35]
dt a[172:173],v198 offset:24576
mm v[32:35],a[96:99],a[72:75],v[32:35]
dt a[174:175],v198 offset:25600
mm v[64:67],a[96:99],a[80:83],v[64:67]
s_waitcnt lgkmcnt(5)
dt a[124:125],v199 offset:16384
mm v[64:67],a[148:151],a[84:87],v[64:67]
bl v232,s[8:11],s40 offen offset:1024 sc1 lds
mm v[68:71],a[152:155],a[84:87],v[68:71]
dt a[126:127],v199 offset:17408
mm v[68:71],a[100:103],a[80:83],v[68:71]
dt a[176:177],v199 offset:24576
mm v[72:75],a[104:107],a[80:83],v[72:75]
v_permlane32_swap_b32 v131,v135
mm v[72:75],a[156:159],a[84:87],v[72:75]
dt a[178:179],v199 offset:25600
v_add_f32 v131,v131,v135
v_mov_b32 v135,v131
v_permlane16_swap_b32 v128,v132
v_permlane16_swap_b32 v129,v133
v_permlane16_swap_b32 v130,v134
v_permlane16_swap_b32 v131,v135
mm v[76:79],a[160:163],a[84:87],v[76:79]
v_add_f32 v128,v128,v132
v_add_f32 v129,v129,v133
v_add_f32 v130,v130,v134
v_add_f32 v131,v131,v135
cv v128,v128,v129
cv v129,v130,v131
buffer_store_dwordx2 v[128:129],v145,s[8:11],s67 offen sc1
mm v[76:79],a[108:111],a[80:83],v[76:79]
bl v232,s[8:11],s40 offen offset:2048 sc1 lds
mm v[108:111],a[108:111],a[88:91],v[108:111]
mm v[108:111],a[160:163],a[92:95],v[108:111]
s_mov_b64 exec,0
s_setpc_b64 s[0:1]
EB_R:
s_mov_b64 exec,-1
mm v[104:107],a[156:159],a[92:95],v[104:107]
s_waitcnt vmcnt(12) lgkmcnt(0)
s_barrier
mm v[104:107],a[104:107],a[88:91],v[104:107]
bl v232,s[8:11],s40 offen offset:3072 sc1 lds
mm v[100:103],a[100:103],a[88:91],v[100:103]
s_add_u32 s40,s30,40960
dt a[108:109],v195 offset:0
s_add_u32 m0,s33,49152
mm v[100:103],a[152:155],a[92:95],v[100:103]
dt a[110:111],v195 offset:1024
mm v[96:99],a[148:151],a[92:95],v[96:99]
dt a[160:161],v195 offset:8192
mm v[96:99],a[96:99],a[88:91],v[96:99]
bl v232,s[8:11],s40 offen offset:0 sc1 lds
mm v[16:19],a[112:115],a[64:67],v[16:19]
dt a[162:163],v195 offset:9216
mm v[16:19],a[164:167],a[68:71],v[16:19]
dt a[104:105],v194 offset:0
mm v[20:23],a[168:171],a[68:71],v[20:23]
dt a[106:107],v194 offset:1024
mm v[20:23],a[116:119],a[64:67],v[20:23]
dt a[156:157],v194 offset:8192
)" R"(mm v[24:27],a[120:123],a[64:67],v[24:27]
bl v232,s[8:11],s40 offen offset:1024 sc1 lds
mm v[24:27],a[172:175],a[68:71],v[24:27]
dt a[158:159],v194 offset:9216
mm v[28:31],a[176:179],a[68:71],v[28:31]
mm v[28:31],a[124:127],a[64:67],v[28:31]
dr a[64:67],v212 offset:0
mm v[60:63],a[124:127],a[72:75],v[60:63]
dr a[68:71],v213 offset:0
mm v[60:63],a[176:179],a[76:79],v[60:63]
bl v232,s[8:11],s40 offen offset:2048 sc1 lds
mm v[56:59],a[172:175],a[76:79],v[56:59]
dt a[100:101],v193 offset:0
mm v[56:59],a[120:123],a[72:75],v[56:59]
dt a[102:103],v193 offset:1024
mm v[52:55],a[116:119],a[72:75],v[52:55]
dt a[152:153],v193 offset:8192
mm v[52:55],a[168:171],a[76:79],v[52:55]
dt a[154:155],v193 offset:9216
mm v[48:51],a[164:167],a[76:79],v[48:51]
bl v232,s[8:11],s40 offen offset:3072 sc1 lds
mm v[48:51],a[112:115],a[72:75],v[48:51]
dr a[72:75],v212 offset:2048
mm v[80:83],a[112:115],a[80:83],v[80:83]
dr a[76:79],v213 offset:2048
mm v[80:83],a[164:167],a[84:87],v[80:83]
dt a[96:97],v192 offset:0
mm v[84:87],a[168:171],a[84:87],v[84:87]
dt a[98:99],v192 offset:1024
mm v[84:87],a[116:119],a[80:83],v[84:87]
dt a[148:149],v192 offset:8192
mm v[88:91],a[120:123],a[80:83],v[88:91]
dt a[150:151],v192 offset:9216
mm v[88:91],a[172:175],a[84:87],v[88:91]
mm v[92:95],a[176:179],a[84:87],v[92:95]
mm v[92:95],a[124:127],a[80:83],v[92:95]
dr a[80:83],v212 offset:4096
dr a[84:87],v213 offset:4096
mm v[124:127],a[124:127],a[88:91],v[124:127]
mm v[124:127],a[176:179],a[92:95],v[124:127]
mm v[120:123],a[172:175],a[92:95],v[120:123]
mm v[120:123],a[120:123],a[88:91],v[120:123]
mm v[116:119],a[116:119],a[88:91],v[116:119]
mm v[116:119],a[168:171],a[92:95],v[116:119]
mm v[112:115],a[164:167],a[92:95],v[112:115]
mm v[112:115],a[112:115],a[88:91],v[112:115]
s_waitcnt lgkmcnt(0)
dt a[112:113],v196 offset:0
dt a[114:115],v196 offset:1024
dt a[164:165],v196 offset:8192
dt a[166:167],v196 offset:9216
dt a[116:117],v197 offset:0
dt a[118:119],v197 offset:1024
dt a[168:169],v197 offset:8192
dt a[170:171],v197 offset:9216
dt a[120:121],v198 offset:0
mm v[0:3],a[96:99],a[64:67],v[0:3]
dt a[122:123],v198 offset:1024
mm v[0:3],a[148:151],a[68:71],v[0:3]
dt a[172:173],v198 offset:8192
mm v[4:7],a[152:155],a[68:71],v[4:7]
dt a[174:175],v198 offset:9216
mm v[4:7],a[100:103],a[64:67],v[4:7]
dt a[124:125],v199 offset:0
)" R"(mm v[8:11],a[104:107],a[64:67],v[8:11]
dt a[126:127],v199 offset:1024
mm v[8:11],a[156:159],a[68:71],v[8:11]
dt a[176:177],v199 offset:8192
mm v[12:15],a[160:163],a[68:71],v[12:15]
dt a[178:179],v199 offset:9216
mm v[12:15],a[108:111],a[64:67],v[12:15]
s_waitcnt lgkmcnt(6)
mm v[16:19],a[112:115],a[64:67],v[16:19]
mm v[16:19],a[164:167],a[68:71],v[16:19]
mm v[20:23],a[168:171],a[68:71],v[20:23]
mm v[20:23],a[116:119],a[64:67],v[20:23]
mm v[24:27],a[120:123],a[64:67],v[24:27]
s_waitcnt lgkmcnt(1)
mm v[24:27],a[172:175],a[68:71],v[24:27]
s_waitcnt lgkmcnt(0)
mm v[28:31],a[176:179],a[68:71],v[28:31]
mm v[28:31],a[124:127],a[64:67],v[28:31]
dr a[88:91],v212 offset:6144
v_mul_f32 v0,s52,v0
mm v[60:63],a[124:127],a[72:75],v[60:63]
dr a[92:95],v213 offset:6144
v_mul_f32 v1,s52,v1
mm v[60:63],a[176:179],a[76:79],v[60:63]
v_mul_f32 v2,s52,v2
v_mul_f32 v3,s52,v3
mm v[56:59],a[172:175],a[76:79],v[56:59]
v_mul_f32 v4,s52,v4
v_mul_f32 v5,s52,v5
mm v[56:59],a[120:123],a[72:75],v[56:59]
v_mul_f32 v6,s52,v6
v_mul_f32 v7,s52,v7
mm v[52:55],a[116:119],a[72:75],v[52:55]
cv v0,v0,v1
cv v1,v2,v3
mm v[52:55],a[168:171],a[76:79],v[52:55]
cv v2,v4,v5
cv v3,v6,v7
mm v[48:51],a[164:167],a[76:79],v[48:51]
v_mul_f32 v8,s52,v8
v_mul_f32 v9,s52,v9
mm v[48:51],a[112:115],a[72:75],v[48:51]
v_mul_f32 v10,s52,v10
v_mul_f32 v11,s52,v11
mm v[44:47],a[108:111],a[72:75],v[44:47]
v_mul_f32 v12,s52,v12
v_mul_f32 v13,s52,v13
mm v[44:47],a[160:163],a[76:79],v[44:47]
v_mul_f32 v14,s52,v14
v_mul_f32 v15,s52,v15
mm v[40:43],a[156:159],a[76:79],v[40:43]
cv v8,v8,v9
cv v9,v10,v11
mm v[40:43],a[104:107],a[72:75],v[40:43]
cv v10,v12,v13
cv v11,v14,v15
mm v[36:39],a[100:103],a[72:75],v[36:39]
v_mul_f32 v16,s52,v16
v_mul_f32 v17,s52,v17
mm v[36:39],a[152:155],a[76:79],v[36:39]
v_mul_f32 v18,s52,v18
v_mul_f32 v19,s52,v19
mm v[32:35],a[148:151],a[76:79],v[32:35]
v_mul_f32 v20,s52,v20
)" R"(v_mul_f32 v21,s52,v21
mm v[32:35],a[96:99],a[72:75],v[32:35]
v_mul_f32 v22,s52,v22
v_mul_f32 v23,s52,v23
mm v[64:67],a[96:99],a[80:83],v[64:67]
cv v16,v16,v17
cv v17,v18,v19
mm v[64:67],a[148:151],a[84:87],v[64:67]
cv v18,v20,v21
cv v19,v22,v23
mm v[68:71],a[152:155],a[84:87],v[68:71]
v_mul_f32 v24,s52,v24
v_mul_f32 v25,s52,v25
mm v[68:71],a[100:103],a[80:83],v[68:71]
v_mul_f32 v26,s52,v26
v_mul_f32 v27,s52,v27
mm v[72:75],a[104:107],a[80:83],v[72:75]
v_mul_f32 v28,s52,v28
v_mul_f32 v29,s52,v29
mm v[72:75],a[156:159],a[84:87],v[72:75]
v_mul_f32 v30,s52,v30
v_mul_f32 v31,s52,v31
mm v[76:79],a[160:163],a[84:87],v[76:79]
cv v24,v24,v25
cv v25,v26,v27
mm v[76:79],a[108:111],a[80:83],v[76:79]
cv v26,v28,v29
cv v27,v30,v31
mm v[80:83],a[112:115],a[80:83],v[80:83]
s_waitcnt vmcnt(6) lgkmcnt(0)
s_barrier
mm v[80:83],a[164:167],a[84:87],v[80:83]
dr a[64:67],v232 offset:0
v_mul_f32 v32,s52,v32
mm v[84:87],a[168:171],a[84:87],v[84:87]
dr a[68:71],v232 offset:1024
v_mul_f32 v33,s52,v33
mm v[84:87],a[116:119],a[80:83],v[84:87]
v_mul_f32 v34,s52,v34
v_mul_f32 v35,s52,v35
mm v[88:91],a[120:123],a[80:83],v[88:91]
dr a[72:75],v232 offset:2048
v_mul_f32 v36,s52,v36
mm v[88:91],a[172:175],a[84:87],v[88:91]
dr a[76:79],v232 offset:3072
v_mul_f32 v37,s52,v37
mm v[92:95],a[176:179],a[84:87],v[92:95]
v_mul_f32 v38,s52,v38
v_mul_f32 v39,s52,v39
mm v[92:95],a[124:127],a[80:83],v[92:95]
dr a[80:83],v232 offset:4096
cv v32,v32,v33
mm v[124:127],a[124:127],a[88:91],v[124:127]
dr a[84:87],v232 offset:5120
cv v33,v34,v35
mm v[124:127],a[176:179],a[92:95],v[124:127]
cv v34,v36,v37
cv v35,v38,v39
mm v[120:123],a[172:175],a[92:95],v[120:123]
v_mul_f32 v40,s52,v40
v_mul_f32 v41,s52,v41
mm v[120:123],a[120:123],a[88:91],v[120:123]
v_mul_f32 v42,s52,v42
v_mul_f32 v43,s52,v43
)" R"(mm v[116:119],a[116:119],a[88:91],v[116:119]
v_mul_f32 v44,s52,v44
v_mul_f32 v45,s52,v45
mm v[116:119],a[168:171],a[92:95],v[116:119]
v_mul_f32 v46,s52,v46
v_mul_f32 v47,s52,v47
mm v[112:115],a[164:167],a[92:95],v[112:115]
cv v40,v40,v41
cv v41,v42,v43
mm v[112:115],a[112:115],a[88:91],v[112:115]
cv v42,v44,v45
cv v43,v46,v47
mm v[108:111],a[108:111],a[88:91],v[108:111]
v_mul_f32 v48,s52,v48
v_mul_f32 v49,s52,v49
mm v[108:111],a[160:163],a[92:95],v[108:111]
v_mul_f32 v50,s52,v50
v_mul_f32 v51,s52,v51
mm v[104:107],a[156:159],a[92:95],v[104:107]
v_mul_f32 v52,s52,v52
v_mul_f32 v53,s52,v53
mm v[104:107],a[104:107],a[88:91],v[104:107]
v_mul_f32 v54,s52,v54
v_mul_f32 v55,s52,v55
mm v[100:103],a[100:103],a[88:91],v[100:103]
cv v48,v48,v49
cv v49,v50,v51
mm v[100:103],a[152:155],a[92:95],v[100:103]
cv v50,v52,v53
cv v51,v54,v55
mm v[96:99],a[148:151],a[92:95],v[96:99]
v_mul_f32 v56,s52,v56
v_mul_f32 v57,s52,v57
mm v[96:99],a[96:99],a[88:91],v[96:99]
dr a[88:91],v232 offset:6144
v_mul_f32 v58,s52,v58
dr a[92:95],v232 offset:7168
dr a[96:99],v232 offset:8192
dr a[100:103],v232 offset:9216
dr a[104:107],v232 offset:10240
dr a[108:111],v232 offset:11264
dr a[112:115],v232 offset:12288
dr a[116:119],v232 offset:13312
dr a[120:123],v232 offset:14336
dr a[124:127],v232 offset:15360
v_mul_f32 v59,s52,v59
v_mul_f32 v60,s52,v60
v_mul_f32 v61,s52,v61
v_mul_f32 v62,s52,v62
v_mul_f32 v63,s52,v63
cv v56,v56,v57
cv v57,v58,v59
cv v58,v60,v61
cv v59,v62,v63
v_pk_mul_f32 v[64:65],v[64:65],s[52:53]
v_pk_mul_f32 v[66:67],v[66:67],s[52:53]
v_pk_mul_f32 v[68:69],v[68:69],s[52:53]
v_pk_mul_f32 v[70:71],v[70:71],s[52:53]
cv v64,v64,v65
cv v65,v66,v67
cv v66,v68,v69
cv v67,v70,v71
v_pk_mul_f32 v[72:73],v[72:73],s[52:53]
v_pk_mul_f32 v[74:75],v[74:75],s[52:53]
)" R"(v_pk_mul_f32 v[76:77],v[76:77],s[52:53]
v_pk_mul_f32 v[78:79],v[78:79],s[52:53]
cv v72,v72,v73
cv v73,v74,v75
cv v74,v76,v77
cv v75,v78,v79
v_pk_mul_f32 v[80:81],v[80:81],s[52:53]
v_pk_mul_f32 v[82:83],v[82:83],s[52:53]
v_pk_mul_f32 v[84:85],v[84:85],s[52:53]
v_pk_mul_f32 v[86:87],v[86:87],s[52:53]
cv v80,v80,v81
cv v81,v82,v83
cv v82,v84,v85
cv v83,v86,v87
v_pk_mul_f32 v[88:89],v[88:89],s[52:53]
v_pk_mul_f32 v[90:91],v[90:91],s[52:53]
v_pk_mul_f32 v[92:93],v[92:93],s[52:53]
v_pk_mul_f32 v[94:95],v[94:95],s[52:53]
cv v88,v88,v89
cv v89,v90,v91
cv v90,v92,v93
cv v91,v94,v95
v_pk_mul_f32 v[96:97],v[96:97],s[52:53]
v_pk_mul_f32 v[98:99],v[98:99],s[52:53]
v_pk_mul_f32 v[100:101],v[100:101],s[52:53]
v_pk_mul_f32 v[102:103],v[102:103],s[52:53]
cv v96,v96,v97
cv v97,v98,v99
cv v98,v100,v101
cv v99,v102,v103
v_pk_mul_f32 v[104:105],v[104:105],s[52:53]
v_pk_mul_f32 v[106:107],v[106:107],s[52:53]
v_pk_mul_f32 v[108:109],v[108:109],s[52:53]
v_pk_mul_f32 v[110:111],v[110:111],s[52:53]
cv v104,v104,v105
cv v105,v106,v107
cv v106,v108,v109
cv v107,v110,v111
v_pk_mul_f32 v[112:113],v[112:113],s[52:53]
v_pk_mul_f32 v[114:115],v[114:115],s[52:53]
v_pk_mul_f32 v[116:117],v[116:117],s[52:53]
v_pk_mul_f32 v[118:119],v[118:119],s[52:53]
cv v112,v112,v113
cv v113,v114,v115
cv v114,v116,v117
cv v115,v118,v119
v_pk_mul_f32 v[120:121],v[120:121],s[52:53]
v_pk_mul_f32 v[122:123],v[122:123],s[52:53]
v_pk_mul_f32 v[124:125],v[124:125],s[52:53]
v_pk_mul_f32 v[126:127],v[126:127],s[52:53]
cv v120,v120,v121
cv v121,v122,v123
cv v122,v124,v125
cv v123,v126,v127
ATT:
s_cmp_eq_u32 s26,0
s_cbranch_scc1 SB
B0:
s_waitcnt lgkmcnt(10)
mm v[128:131],a[64:67],v[0:3],0
s_add_u32 s31,s31,65536
buffer_store_dwordx2 v[252:253],v254,s[8:11],s74 offen sc1
s_add_u32 m0,s33,0
mm v[144:147],a[64:67],v[32:35],0
)" R"(mm v[144:147],a[68:71],v[40:43],v[144:147]
mm v[128:131],a[68:71],v[8:11],v[128:131]
mm v[128:131],a[72:75],v[16:19],v[128:131]
mm v[144:147],a[72:75],v[48:51],v[144:147]
mm v[144:147],a[76:79],v[56:59],v[144:147]
s_waitcnt lgkmcnt(0)
s_barrier
mm v[128:131],a[76:79],v[24:27],v[128:131]
bl v232,s[8:11],s31 offen offset:0 sc1 lds
mm v[132:135],a[80:83],v[0:3],0
mm v[148:151],a[80:83],v[32:35],0
mm v[148:151],a[84:87],v[40:43],v[148:151]
mm v[132:135],a[84:87],v[8:11],v[132:135]
bl v232,s[8:11],s31 offen offset:1024 sc1 lds
mm v[132:135],a[88:91],v[16:19],v[132:135]
mm v[148:151],a[88:91],v[48:51],v[148:151]
mm v[148:151],a[92:95],v[56:59],v[148:151]
mm v[132:135],a[92:95],v[24:27],v[132:135]
bl v232,s[8:11],s31 offen offset:2048 sc1 lds
mm v[168:171],a[64:67],v[64:67],0
v_max3_f32 v224,v128,v129,v130
v_max3_f32 v225,v144,v145,v146
mm v[184:187],a[64:67],v[96:99],0
dr a[64:67],v235 offset:0
mm v[184:187],a[68:71],v[104:107],v[184:187]
v_max3_f32 v225,v225,v147,v148
v_max3_f32 v224,v224,v131,v132
mm v[168:171],a[68:71],v[72:75],v[168:171]
bl v232,s[8:11],s31 offen offset:3072 sc1 lds
mm v[168:171],a[72:75],v[80:83],v[168:171]
dr a[68:71],v235 offset:1024
v_max3_f32 v224,v224,v133,v134
mm v[184:187],a[72:75],v[112:115],v[184:187]
EA_10:
s_cbranch_execz EA_R
dr a[72:75],v235 offset:2048
v_max_f32 v224,v224,v135
mm v[184:187],a[76:79],v[120:123],v[184:187]
v_mov_b32 v226,v224
v_max3_f32 v225,v225,v149,v150
mm v[168:171],a[76:79],v[88:91],v[168:171]
v_permlane32_swap_b32 v224,v226
mm v[172:175],a[80:83],v[64:67],0
dr a[76:79],v235 offset:3072
v_max_f32 v224,v224,v226
mm v[188:191],a[80:83],v[96:99],0
dr a[80:83],v235 offset:4096
v_mov_b32 v226,v224
mm v[188:191],a[84:87],v[104:107],v[188:191]
v_max_f32 v225,v225,v151
v_mov_b32 v227,v225
mm v[172:175],a[84:87],v[72:75],v[172:175]
v_permlane16_swap_b32 v224,v226
mm v[172:175],a[88:91],v[80:83],v[172:175]
dr a[84:87],v235 offset:5120
v_max_f32 v224,v224,v226
mm v[188:191],a[88:91],v[112:115],v[188:191]
dr a[88:91],v235 offset:6144
v_sub_f32 v192,0xc2400000,v224
mm v[188:191],a[92:95],v[120:123],v[188:191]
v_sub_f32 v193,0xc2400000,v224
v_sub_f32 v194,0xc2400000,v224
v_sub_f32 v195,0xc2400000,v224
mm v[172:175],a[92:95],v[88:91],v[172:175]
)" R"(v_permlane32_swap_b32 v225,v227
mm v[136:139],a[96:99],v[0:3],v[192:195]
s_bfe_i64 s[48:49],s[36:37],0x10000
dr a[92:95],v235 offset:7168
v_max_f32 v225,v225,v227
mm v[136:139],a[100:103],v[8:11],v[136:139]
s_bfe_i64 s[50:51],s[36:37],0x10004
v_mov_b32 v227,v225
s_add_u32 s41,s38,s58
s_nop 0
v_permlane16_swap_b32 v225,v227
mm v[136:139],a[104:107],v[16:19],v[136:139]
s_and_b64 s[52:53],s[12:13],s[48:49]
v_max_f32 v225,v225,v227
v_sub_f32 v196,0xc2400000,v225
v_sub_f32 v197,0xc2400000,v225
v_sub_f32 v198,0xc2400000,v225
v_sub_f32 v199,0xc2400000,v225
mm v[136:139],a[108:111],v[24:27],v[136:139]
s_or_b64 s[52:53],s[52:53],s[50:51]
s_add_u32 m0,s34,16384
v_max3_f32 v228,v168,v169,v170
mm v[152:155],a[108:111],v[56:59],v[196:199]
s_add_u32 s40,s31,32768
v_max3_f32 v228,v228,v171,v172
v_max3_f32 v229,v184,v185,v186
mm v[152:155],a[104:107],v[48:51],v[152:155]
ex2 v136,v136
mm v[152:155],a[100:103],v[40:43],v[152:155]
s_and_b64 s[86:87],s[14:15],s[48:49]
ex2 v137,v137
mm v[152:155],a[96:99],v[32:35],v[152:155]
s_or_b64 s[86:87],s[86:87],s[50:51]
dr a[96:99],v235 offset:8192
v_cndmask_b32_e64 v208,v236,v192,s[52:53]
mm v[156:159],a[112:115],v[32:35],v[196:199]
dr a[100:103],v235 offset:9216
v_cndmask_b32_e64 v209,v236,v192,s[86:87]
mm v[156:159],a[116:119],v[40:43],v[156:159]
ex2 v138,v138
mm v[156:159],a[120:123],v[48:51],v[156:159]
s_and_b64 s[52:53],s[16:17],s[48:49]
dr a[104:107],v235 offset:10240
v_max3_f32 v229,v229,v187,v188
mm v[156:159],a[124:127],v[56:59],v[156:159]
s_or_b64 s[52:53],s[52:53],s[50:51]
dr a[108:111],v235 offset:11264
v_max3_f32 v229,v229,v189,v190
mm v[140:143],a[124:127],v[24:27],v[192:195]
ex2 v152,v152
mm v[140:143],a[120:123],v[16:19],v[140:143]
s_and_b64 s[86:87],s[18:19],s[48:49]
ex2 v153,v153
mm v[140:143],a[116:119],v[8:11],v[140:143]
ex2 v154,v154
mm v[140:143],a[112:115],v[0:3],v[140:143]
s_waitcnt lgkmcnt(2)
dr a[112:115],v235 offset:12288
v_cndmask_b32_e64 v210,v236,v192,s[52:53]
mm v[160:163],a[64:67],v[0:3],v[192:195]
s_or_b64 s[86:87],s[86:87],s[50:51]
dr a[116:119],v235 offset:13312
v_cndmask_b32_e64 v211,v236,v192,s[86:87]
mm v[160:163],a[68:71],v[8:11],v[160:163]
)" R"(s_bfe_i64 s[48:49],s[36:37],0x10001
ex2 v155,v155
mm v[160:163],a[72:75],v[16:19],v[160:163]
s_bfe_i64 s[50:51],s[36:37],0x10005
bs a[16:19],v232,s[8:11],s41 offen offset:0 sc1
dr a[120:123],v235 offset:14336
s_add_u32 s41,s35,0
v_max_f32 v229,v229,v191
v_max3_f32 v228,v228,v173,v174
v_max_f32 v228,v228,v175
v_add_f32 v144,v196,v144
mm v[160:163],a[76:79],v[24:27],v[160:163]
dr a[124:127],v235 offset:15360
v_add_f32 v145,v196,v145
mm v[176:179],a[76:79],v[56:59],v[196:199]
s_and_b64 s[52:53],s[12:13],s[48:49]
ex2 v144,v144
mm v[176:179],a[72:75],v[48:51],v[176:179]
s_or_b64 s[52:53],s[52:53],s[50:51]
ex2 v145,v145
mm v[176:179],a[68:71],v[40:43],v[176:179]
ex2 v156,v156
mm v[176:179],a[64:67],v[32:35],v[176:179]
s_and_b64 s[86:87],s[14:15],s[48:49]
ex2 v157,v157
mm v[180:183],a[80:83],v[32:35],v[196:199]
bl v232,s[8:11],s41 offen offset:0 sc1 lds
mm v[180:183],a[84:87],v[40:43],v[180:183]
s_add_u32 m0,s33,131072
ex2 v158,v158
mm v[180:183],a[88:91],v[48:51],v[180:183]
s_or_b64 s[86:87],s[86:87],s[50:51]
ex2 v159,v159
mm v[180:183],a[92:95],v[56:59],v[180:183]
s_add_u32 s41,s38,s58
ex2 v139,v139
mm v[164:167],a[92:95],v[24:27],v[192:195]
s_waitcnt vmcnt(11) lgkmcnt(0)
s_barrier
mm v[164:167],a[88:91],v[16:19],v[164:167]
bl v232,s[8:11],s40 offen offset:0 sc1 lds
mm v[164:167],a[84:87],v[8:11],v[164:167]
dr a[64:67],v235 offset:16384
v_cndmask_b32_e64 v212,v236,v192,s[52:53]
mm v[164:167],a[80:83],v[0:3],v[164:167]
s_and_b64 s[52:53],s[16:17],s[48:49]
dr a[68:71],v235 offset:24576
v_cndmask_b32_e64 v213,v236,v192,s[86:87]
mm v[168:171],a[96:99],v[0:3],v[192:195]
s_or_b64 s[52:53],s[52:53],s[50:51]
ex2 v140,v140
mm v[168:171],a[100:103],v[8:11],v[168:171]
s_and_b64 s[86:87],s[18:19],s[48:49]
bl v232,s[8:11],s40 offen offset:1024 sc1 lds
mm v[168:171],a[104:107],v[16:19],v[168:171]
s_or_b64 s[86:87],s[86:87],s[50:51]
dr a[72:75],v235 offset:17408
v_cndmask_b32_e64 v214,v236,v192,s[52:53]
mm v[168:171],a[108:111],v[24:27],v[168:171]
s_bfe_i64 s[48:49],s[36:37],0x10002
dr a[76:79],v235 offset:25600
v_cndmask_b32_e64 v215,v236,v192,s[86:87]
mm v[184:187],a[108:111],v[56:59],v[196:199]
s_bfe_i64 s[50:51],s[36:37],0x10006
)" R"(ex2 v141,v141
mm v[184:187],a[104:107],v[48:51],v[184:187]
s_and_b64 s[52:53],s[12:13],s[48:49]
bl v232,s[8:11],s40 offen offset:2048 sc1 lds
mm v[184:187],a[100:103],v[40:43],v[184:187]
s_or_b64 s[52:53],s[52:53],s[50:51]
dr a[80:83],v235 offset:18432
v_add_f32 v146,v196,v146
mm v[184:187],a[96:99],v[32:35],v[184:187]
ex2 v146,v146
mm v[188:191],a[112:115],v[32:35],v[196:199]
ex2 v142,v142
mm v[188:191],a[116:119],v[40:43],v[188:191]
s_and_b64 s[86:87],s[14:15],s[48:49]
bl v232,s[8:11],s40 offen offset:3072 sc1 lds
mm v[188:191],a[120:123],v[48:51],v[188:191]
s_add_u32 m0,s34,16384
ex2 v143,v143
mm v[188:191],a[124:127],v[56:59],v[188:191]
s_or_b64 s[86:87],s[86:87],s[50:51]
dr a[84:87],v235 offset:26624
v_add_f32 v147,v196,v147
mm v[172:175],a[124:127],v[24:27],v[192:195]
s_add_u32 s40,s31,8192
dr a[88:91],v235 offset:19456
v_add_f32 v148,v196,v148
mm v[172:175],a[120:123],v[16:19],v[172:175]
v_add_f32 v149,v196,v149
v_add_f32 v150,v196,v150
v_add_f32 v151,v196,v151
ex2 v150,v150
ex2 v151,v151
ex2 v148,v148
ex2 v149,v149
ex2 v147,v147
mm v[172:175],a[116:119],v[8:11],v[172:175]
dr a[92:95],v235 offset:27648
cv v151,v150,v151
cv v150,v148,v149
cv v148,v144,v145
cv v149,v146,v147
mm v[172:175],a[112:115],v[0:3],v[172:175]
s_waitcnt lgkmcnt(3)
dr a[96:99],v235 offset:20480
cv v152,v152,v153
bs a[20:23],v232,s[8:11],s41 offen offset:1024 sc1
cv v153,v154,v155
cv v154,v156,v157
cv v155,v158,v159
v_add_f32 v128,v192,v128
v_add_f32 v129,v192,v129
v_add_f32 v130,v192,v130
mm v[68:71],a[64:67],v[148:151],0
s_add_u32 s41,s35,0
v_add_f32 v131,v192,v131
v_add_f32 v132,v192,v132
v_add_f32 v133,v192,v133
v_add_f32 v134,v192,v134
v_add_f32 v135,v192,v135
ex2 v134,v134
ex2 v135,v135
ex2 v132,v132
ex2 v133,v133
ex2 v128,v128
)" R"(ex2 v129,v129
ex2 v130,v130
ex2 v131,v131
cv v136,v136,v137
cv v137,v138,v139
cv v138,v140,v141
cv v139,v142,v143
mm v[68:71],a[68:71],v[152:155],v[68:71]
dr a[100:103],v235 offset:28672
cv v135,v134,v135
cv v134,v132,v133
cv v132,v128,v129
cv v133,v130,v131
mm v[4:7],a[68:71],v[136:139],0
dr a[104:107],v235 offset:21504
v_cndmask_b32_e64 v216,v236,v192,s[52:53]
mm v[4:7],a[64:67],v[132:135],v[4:7]
s_and_b64 s[52:53],s[16:17],s[48:49]
bl v232,s[8:11],s41 offen offset:1024 sc1 lds
mm v[12:15],a[72:75],v[132:135],0
s_or_b64 s[52:53],s[52:53],s[50:51]
dr a[108:111],v235 offset:29696
v_cndmask_b32_e64 v217,v236,v192,s[86:87]
mm v[12:15],a[76:79],v[136:139],v[12:15]
s_and_b64 s[86:87],s[18:19],s[48:49]
dr a[112:115],v235 offset:22528
v_cndmask_b32_e64 v218,v236,v192,s[52:53]
mm v[76:79],a[76:79],v[152:155],0
s_or_b64 s[86:87],s[86:87],s[50:51]
dr a[116:119],v235 offset:30720
v_cndmask_b32_e64 v219,v236,v192,s[86:87]
mm v[76:79],a[72:75],v[148:151],v[76:79]
s_bfe_i64 s[48:49],s[36:37],0x10003
dr a[120:123],v235 offset:23552
aw a128,0
mm v[84:87],a[80:83],v[148:151],0
s_waitcnt lgkmcnt(5)
dr a[124:127],v235 offset:31744
aw a129,0
mm v[84:87],a[84:87],v[152:155],v[84:87]
s_bfe_i64 s[50:51],s[36:37],0x10007
ex2 v182,v182
mm v[20:23],a[84:87],v[136:139],0
s_and_b64 s[52:53],s[12:13],s[48:49]
ex2 v183,v183
mm v[20:23],a[80:83],v[132:135],v[20:23]
s_add_u32 m0,s33,147456
ex2 v180,v180
mm v[28:31],a[88:91],v[132:135],0
s_or_b64 s[52:53],s[52:53],s[50:51]
ex2 v181,v181
mm v[28:31],a[92:95],v[136:139],v[28:31]
s_and_b64 s[86:87],s[14:15],s[48:49]
aw a132,0
aw a133,0
mm v[92:95],a[92:95],v[152:155],0
s_or_b64 s[86:87],s[86:87],s[50:51]
ex2 v176,v176
s_waitcnt vmcnt(8) lgkmcnt(0)
s_barrier
mm v[92:95],a[88:91],v[148:151],v[92:95]
bl v232,s[8:11],s40 offen offset:0 sc1 lds
v_smfmac_f32_16x16x64_bf16 a[128:131],a[144:147],v[132:139],v246
dr a[64:67],v232 offset:0
)" R"(v_cndmask_b32_e64 v220,v236,v192,s[52:53]
v_smfmac_f32_16x16x64_bf16 a[132:135],a[144:147],v[148:155],v246
s_and_b64 s[52:53],s[16:17],s[48:49]
dr a[68:71],v232 offset:1024
v_cndmask_b32_e64 v221,v236,v192,s[86:87]
mm v[100:103],a[96:99],v[148:151],0
s_or_b64 s[52:53],s[52:53],s[50:51]
ex2 v177,v177
mm v[100:103],a[100:103],v[152:155],v[100:103]
s_and_b64 s[86:87],s[18:19],s[48:49]
bl v232,s[8:11],s40 offen offset:1024 sc1 lds
mm v[36:39],a[100:103],v[136:139],0
s_or_b64 s[86:87],s[86:87],s[50:51]
dr a[72:75],v232 offset:2048
v_cndmask_b32_e64 v222,v236,v192,s[52:53]
mm v[36:39],a[96:99],v[132:135],v[36:39]
dr a[76:79],v232 offset:3072
v_cndmask_b32_e64 v223,v236,v192,s[86:87]
mm v[44:47],a[104:107],v[132:135],0
ex2 v178,v178
mm v[44:47],a[108:111],v[136:139],v[44:47]
bl v232,s[8:11],s40 offen offset:2048 sc1 lds
mm v[108:111],a[108:111],v[152:155],0
dr a[80:83],v232 offset:4096
v_mov_b32 v230,v228
mm v[108:111],a[104:107],v[148:151],v[108:111]
dr a[84:87],v232 offset:5120
v_mov_b32 v231,v229
mm v[116:119],a[112:115],v[148:151],0
v_permlane32_swap_b32 v228,v230
mm v[116:119],a[116:119],v[152:155],v[116:119]
s_mov_b32 s30,s31
dr a[88:91],v232 offset:6144
v_max_f32 v228,v228,v230
mm v[52:55],a[116:119],v[136:139],0
s_add_u32 s31,s31,65536
dr a[92:95],v232 offset:7168
v_mov_b32 v230,v228
mm v[52:55],a[112:115],v[132:135],v[52:55]
bl v232,s[8:11],s40 offen offset:3072 sc1 lds
mm v[60:63],a[120:123],v[132:135],0
dr a[96:99],v232 offset:8192
cv v183,v182,v183
mm v[60:63],a[124:127],v[136:139],v[60:63]
v_permlane32_swap_b32 v229,v231
dr a[100:103],v232 offset:9216
mm v[124:127],a[124:127],v[152:155],0
dr a[104:107],v232 offset:10240
v_max_f32 v229,v229,v231
v_mov_b32 v231,v229
v_permlane16_swap_b32 v228,v230
s_nop 0
v_permlane16_swap_b32 v229,v231
ex2 v179,v179
ex2 v166,v166
ex2 v167,v167
ex2 v164,v164
ex2 v165,v165
ex2 v160,v160
ex2 v161,v161
ex2 v162,v162
ex2 v163,v163
mm v[124:127],a[120:123],v[148:151],v[124:127]
dr a[108:111],v232 offset:11264
)" R"(v_max_f32 v228,v228,v230
v_max_f32 v229,v229,v231
v_sub_f32 v200,0xc2400000,v228
v_sub_f32 v201,0xc2400000,v228
v_sub_f32 v202,0xc2400000,v228
v_sub_f32 v203,0xc2400000,v228
v_sub_f32 v204,0xc2400000,v229
v_sub_f32 v205,0xc2400000,v229
v_sub_f32 v206,0xc2400000,v229
v_sub_f32 v207,0xc2400000,v229
cv v182,v180,v181
cv v180,v176,v177
cv v181,v178,v179
cv v167,v166,v167
cv v166,v164,v165
cv v164,v160,v161
cv v165,v162,v163
s_cmp_eq_u32 s26,1
s_cbranch_scc1 D
B1:
s_waitcnt lgkmcnt(6)
mm v[128:131],a[64:67],v[0:3],v[192:195]
s_add_u32 s41,s38,s58
dr a[112:115],v232 offset:12288
s_add_u32 m0,s34,24576
mm v[128:131],a[68:71],v[8:11],v[128:131]
s_add_u32 s40,s30,40960
dr a[116:119],v232 offset:13312
mm v[128:131],a[72:75],v[16:19],v[128:131]
bs a[24:27],v233,s[8:11],s41 offen offset:0 sc1
s_add_u32 s41,s35,8192
ex2 v184,v184
ex2 v185,v185
mm v[128:131],a[76:79],v[24:27],v[128:131]
dr a[120:123],v232 offset:14336
cv v184,v184,v185
mm v[144:147],a[76:79],v[56:59],v[196:199]
dr a[124:127],v232 offset:15360
mm v[144:147],a[72:75],v[48:51],v[144:147]
ex2 v186,v186
mm v[144:147],a[68:71],v[40:43],v[144:147]
ex2 v187,v187
mm v[144:147],a[64:67],v[32:35],v[144:147]
ex2 v188,v188
mm v[148:151],a[80:83],v[32:35],v[196:199]
bl v232,s[8:11],s41 offen offset:0 sc1 lds
mm v[148:151],a[84:87],v[40:43],v[148:151]
s_waitcnt lgkmcnt(2)
ex2 v189,v189
mm v[148:151],a[88:91],v[48:51],v[148:151]
s_add_u32 m0,s33,0
ex2 v190,v190
mm v[148:151],a[92:95],v[56:59],v[148:151]
s_add_u32 s41,s35,8192
ex2 v191,v191
mm v[132:135],a[92:95],v[24:27],v[192:195]
s_waitcnt vmcnt(10) lgkmcnt(0)
s_barrier
mm v[132:135],a[88:91],v[16:19],v[132:135]
bl v232,s[8:11],s31 offen offset:0 sc1 lds
mm v[132:135],a[84:87],v[8:11],v[132:135]
dr a[64:67],v232 offset:49152
cv v185,v186,v187
mm v[132:135],a[80:83],v[0:3],v[132:135]
)" R"(dr a[68:71],v232 offset:57344
cv v186,v188,v189
mm v[136:139],a[96:99],v[0:3],v[192:195]
ex2 v168,v168
mm v[136:139],a[100:103],v[8:11],v[136:139]
bl v232,s[8:11],s31 offen offset:1024 sc1 lds
mm v[136:139],a[104:107],v[16:19],v[136:139]
dr a[72:75],v232 offset:50176
cv v187,v190,v191
mm v[136:139],a[108:111],v[24:27],v[136:139]
dr a[76:79],v232 offset:58368
mm v[152:155],a[108:111],v[56:59],v[196:199]
ex2 v169,v169
mm v[152:155],a[104:107],v[48:51],v[152:155]
bl v232,s[8:11],s31 offen offset:2048 sc1 lds
mm v[152:155],a[100:103],v[40:43],v[152:155]
dr a[80:83],v232 offset:51200
cv v168,v168,v169
mm v[152:155],a[96:99],v[32:35],v[152:155]
ex2 v170,v170
mm v[156:159],a[112:115],v[32:35],v[196:199]
ex2 v171,v171
mm v[156:159],a[116:119],v[40:43],v[156:159]
bl v232,s[8:11],s31 offen offset:3072 sc1 lds
mm v[156:159],a[120:123],v[48:51],v[156:159]
s_add_u32 m0,s34,24576
ex2 v172,v172
mm v[156:159],a[124:127],v[56:59],v[156:159]
ex2 v173,v173
mm v[140:143],a[124:127],v[24:27],v[192:195]
ex2 v174,v174
mm v[140:143],a[120:123],v[16:19],v[140:143]
ex2 v175,v175
mm v[140:143],a[116:119],v[8:11],v[140:143]
dr a[84:87],v232 offset:59392
cv v169,v170,v171
mm v[140:143],a[112:115],v[0:3],v[140:143]
s_waitcnt lgkmcnt(1)
dr a[88:91],v232 offset:52224
cv v170,v172,v173
mm v[68:71],a[64:67],v[180:183],v[68:71]
dr a[92:95],v232 offset:60416
cv v171,v174,v175
mm v[68:71],a[68:71],v[184:187],v[68:71]
dr a[96:99],v232 offset:53248
mm v[4:7],a[68:71],v[168:171],v[4:7]
dr a[100:103],v232 offset:61440
mm v[4:7],a[64:67],v[164:167],v[4:7]
dr a[104:107],v232 offset:54272
mm v[12:15],a[72:75],v[164:167],v[12:15]
bl v232,s[8:11],s41 offen offset:1024 sc1 lds
mm v[12:15],a[76:79],v[168:171],v[12:15]
s_add_u32 m0,s33,49152
dr a[108:111],v232 offset:62464
s_add_u32 s41,s38,s58
mm v[76:79],a[76:79],v[184:187],v[76:79]
dr a[112:115],v232 offset:55296
mm v[76:79],a[72:75],v[180:183],v[76:79]
dr a[116:119],v232 offset:63488
mm v[84:87],a[80:83],v[180:183],v[84:87]
s_waitcnt lgkmcnt(5)
dr a[120:123],v232 offset:56320
mm v[84:87],a[84:87],v[184:187],v[84:87]
dr a[124:127],v232 offset:64512
)" R"(mm v[20:23],a[84:87],v[168:171],v[20:23]
ex2 v150,v150
mm v[20:23],a[80:83],v[164:167],v[20:23]
ex2 v151,v151
mm v[28:31],a[88:91],v[164:167],v[28:31]
ex2 v148,v148
mm v[28:31],a[92:95],v[168:171],v[28:31]
ex2 v149,v149
mm v[92:95],a[92:95],v[184:187],v[92:95]
s_waitcnt vmcnt(11) lgkmcnt(0)
s_barrier
mm v[92:95],a[88:91],v[180:183],v[92:95]
bl v232,s[8:11],s40 offen offset:0 sc1 lds
v_smfmac_f32_16x16x64_bf16 a[128:131],a[144:147],v[164:171],v246
dr a[64:67],v235 offset:0
cv v151,v150,v151
v_smfmac_f32_16x16x64_bf16 a[132:135],a[144:147],v[180:187],v246
dr a[68:71],v235 offset:1024
cv v150,v148,v149
mm v[100:103],a[96:99],v[180:183],v[100:103]
ex2 v144,v144
mm v[100:103],a[100:103],v[184:187],v[100:103]
bl v232,s[8:11],s40 offen offset:1024 sc1 lds
mm v[36:39],a[100:103],v[168:171],v[36:39]
dr a[72:75],v235 offset:2048
mm v[36:39],a[96:99],v[164:167],v[36:39]
dr a[76:79],v235 offset:3072
mm v[44:47],a[104:107],v[164:167],v[44:47]
ex2 v145,v145
mm v[44:47],a[108:111],v[168:171],v[44:47]
bl v232,s[8:11],s40 offen offset:2048 sc1 lds
mm v[108:111],a[108:111],v[184:187],v[108:111]
dr a[80:83],v235 offset:4096
cv v148,v144,v145
mm v[108:111],a[104:107],v[180:183],v[108:111]
dr a[84:87],v235 offset:5120
mm v[116:119],a[112:115],v[180:183],v[116:119]
ex2 v146,v146
mm v[116:119],a[116:119],v[184:187],v[116:119]
bl v232,s[8:11],s40 offen offset:3072 sc1 lds
mm v[52:55],a[116:119],v[168:171],v[52:55]
s_add_u32 m0,s34,32768
dr a[88:91],v235 offset:6144
s_add_u32 s40,s31,32768
mm v[52:55],a[112:115],v[164:167],v[52:55]
dr a[92:95],v235 offset:7168
mm v[60:63],a[120:123],v[164:167],v[60:63]
ex2 v147,v147
mm v[60:63],a[124:127],v[168:171],v[60:63]
bs a[28:31],v233,s[8:11],s41 offen offset:1024 sc1
dr a[96:99],v235 offset:8192
s_add_u32 s41,s35,16384
cv v149,v146,v147
ex2 v152,v152
mm v[124:127],a[124:127],v[184:187],v[124:127]
dr a[100:103],v235 offset:9216
mm v[124:127],a[120:123],v[180:183],v[124:127]
s_waitcnt lgkmcnt(4)
ex2 v153,v153
mm v[160:163],a[64:67],v[0:3],v[192:195]
dr a[104:107],v235 offset:10240
cv v152,v152,v153
mm v[160:163],a[68:71],v[8:11],v[160:163]
dr a[108:111],v235 offset:11264
)" R"(mm v[160:163],a[72:75],v[16:19],v[160:163]
bl v232,s[8:11],s41 offen offset:0 sc1 lds
mm v[160:163],a[76:79],v[24:27],v[160:163]
s_add_u32 m0,s33,131072
dr a[112:115],v235 offset:12288
s_add_u32 s41,s35,16384
mm v[176:179],a[76:79],v[56:59],v[196:199]
dr a[116:119],v235 offset:13312
mm v[176:179],a[72:75],v[48:51],v[176:179]
dr a[120:123],v235 offset:14336
mm v[176:179],a[68:71],v[40:43],v[176:179]
dr a[124:127],v235 offset:15360
mm v[176:179],a[64:67],v[32:35],v[176:179]
ex2 v154,v154
mm v[180:183],a[80:83],v[32:35],v[196:199]
ex2 v155,v155
mm v[180:183],a[84:87],v[40:43],v[180:183]
s_waitcnt lgkmcnt(4)
ex2 v156,v156
mm v[180:183],a[88:91],v[48:51],v[180:183]
ex2 v157,v157
mm v[180:183],a[92:95],v[56:59],v[180:183]
ex2 v158,v158
mm v[164:167],a[92:95],v[24:27],v[192:195]
s_waitcnt vmcnt(13) lgkmcnt(0)
s_barrier
mm v[164:167],a[88:91],v[16:19],v[164:167]
bl v232,s[8:11],s40 offen offset:0 sc1 lds
mm v[164:167],a[84:87],v[8:11],v[164:167]
dr a[64:67],v235 offset:16384
cv v153,v154,v155
mm v[164:167],a[80:83],v[0:3],v[164:167]
dr a[68:71],v235 offset:24576
cv v154,v156,v157
mm v[168:171],a[96:99],v[0:3],v[192:195]
ex2 v159,v159
mm v[168:171],a[100:103],v[8:11],v[168:171]
bl v232,s[8:11],s40 offen offset:1024 sc1 lds
mm v[168:171],a[104:107],v[16:19],v[168:171]
dr a[72:75],v235 offset:17408
cv v155,v158,v159
mm v[168:171],a[108:111],v[24:27],v[168:171]
dr a[76:79],v235 offset:25600
mm v[184:187],a[108:111],v[56:59],v[196:199]
ex2 v136,v136
mm v[184:187],a[104:107],v[48:51],v[184:187]
bl v232,s[8:11],s40 offen offset:2048 sc1 lds
mm v[184:187],a[100:103],v[40:43],v[184:187]
dr a[80:83],v235 offset:18432
mm v[184:187],a[96:99],v[32:35],v[184:187]
ex2 v137,v137
mm v[188:191],a[112:115],v[32:35],v[196:199]
ex2 v138,v138
mm v[188:191],a[116:119],v[40:43],v[188:191]
bl v232,s[8:11],s40 offen offset:3072 sc1 lds
mm v[188:191],a[120:123],v[48:51],v[188:191]
s_add_u32 m0,s34,32768
ex2 v139,v139
mm v[188:191],a[124:127],v[56:59],v[188:191]
s_add_u32 s40,s31,8192
ex2 v140,v140
mm v[172:175],a[124:127],v[24:27],v[192:195]
ex2 v141,v141
mm v[172:175],a[120:123],v[16:19],v[172:175]
)" R"(ex2 v142,v142
mm v[172:175],a[116:119],v[8:11],v[172:175]
ex2 v143,v143
mm v[172:175],a[112:115],v[0:3],v[172:175]
EA_11:
s_cbranch_execz EA_R
s_waitcnt lgkmcnt(0)
dr a[84:87],v235 offset:26624
cv v136,v136,v137
bl v232,s[8:11],s41 offen offset:1024 sc1 lds
mm v[68:71],a[64:67],v[148:151],v[68:71]
s_add_u32 m0,s33,147456
dr a[88:91],v235 offset:19456
cv v137,v138,v139
ex2 v134,v134
ex2 v135,v135
ex2 v132,v132
ex2 v133,v133
ex2 v128,v128
ex2 v129,v129
ex2 v130,v130
ex2 v131,v131
cv v138,v140,v141
cv v139,v142,v143
mm v[68:71],a[68:71],v[152:155],v[68:71]
dr a[92:95],v235 offset:27648
cv v135,v134,v135
cv v134,v132,v133
cv v132,v128,v129
cv v133,v130,v131
mm v[4:7],a[68:71],v[136:139],v[4:7]
dr a[96:99],v235 offset:20480
mm v[4:7],a[64:67],v[132:135],v[4:7]
dr a[100:103],v235 offset:28672
mm v[12:15],a[72:75],v[132:135],v[12:15]
dr a[104:107],v235 offset:21504
mm v[12:15],a[76:79],v[136:139],v[12:15]
dr a[108:111],v235 offset:29696
mm v[76:79],a[76:79],v[152:155],v[76:79]
dr a[112:115],v235 offset:22528
mm v[76:79],a[72:75],v[148:151],v[76:79]
dr a[116:119],v235 offset:30720
mm v[84:87],a[80:83],v[148:151],v[84:87]
s_waitcnt lgkmcnt(6)
dr a[120:123],v235 offset:23552
mm v[84:87],a[84:87],v[152:155],v[84:87]
dr a[124:127],v235 offset:31744
mm v[20:23],a[84:87],v[136:139],v[20:23]
ex2 v182,v182
mm v[20:23],a[80:83],v[132:135],v[20:23]
ex2 v183,v183
mm v[28:31],a[88:91],v[132:135],v[28:31]
ex2 v180,v180
mm v[28:31],a[92:95],v[136:139],v[28:31]
ex2 v181,v181
mm v[92:95],a[92:95],v[152:155],v[92:95]
s_waitcnt vmcnt(12) lgkmcnt(0)
s_barrier
mm v[92:95],a[88:91],v[148:151],v[92:95]
bl v232,s[8:11],s40 offen offset:0 sc1 lds
v_smfmac_f32_16x16x64_bf16 a[128:131],a[144:147],v[132:139],v246
dr a[64:67],v232 offset:0
cv v183,v182,v183
v_smfmac_f32_16x16x64_bf16 a[132:135],a[144:147],v[148:155],v246
)" R"(dr a[68:71],v232 offset:1024
cv v182,v180,v181
mm v[100:103],a[96:99],v[148:151],v[100:103]
ex2 v176,v176
mm v[100:103],a[100:103],v[152:155],v[100:103]
bl v232,s[8:11],s40 offen offset:1024 sc1 lds
mm v[36:39],a[100:103],v[136:139],v[36:39]
dr a[72:75],v232 offset:2048
mm v[36:39],a[96:99],v[132:135],v[36:39]
dr a[76:79],v232 offset:3072
mm v[44:47],a[104:107],v[132:135],v[44:47]
ex2 v177,v177
mm v[44:47],a[108:111],v[136:139],v[44:47]
bl v232,s[8:11],s40 offen offset:2048 sc1 lds
mm v[108:111],a[108:111],v[152:155],v[108:111]
dr a[80:83],v232 offset:4096
cv v180,v176,v177
mm v[108:111],a[104:107],v[148:151],v[108:111]
dr a[84:87],v232 offset:5120
mm v[116:119],a[112:115],v[148:151],v[116:119]
ex2 v178,v178
mm v[116:119],a[116:119],v[152:155],v[116:119]
s_mov_b32 s30,s31
dr a[88:91],v232 offset:6144
s_add_u32 s31,s31,65536
mm v[52:55],a[116:119],v[136:139],v[52:55]
dr a[92:95],v232 offset:7168
mm v[52:55],a[112:115],v[132:135],v[52:55]
bl v232,s[8:11],s40 offen offset:3072 sc1 lds
mm v[60:63],a[120:123],v[132:135],v[60:63]
dr a[96:99],v232 offset:8192
mm v[60:63],a[124:127],v[136:139],v[60:63]
dr a[100:103],v232 offset:9216
mm v[124:127],a[124:127],v[152:155],v[124:127]
ex2 v179,v179
dr a[104:107],v232 offset:10240
ex2 v166,v166
ex2 v167,v167
ex2 v164,v164
ex2 v165,v165
ex2 v160,v160
ex2 v161,v161
ex2 v162,v162
ex2 v163,v163
mm v[124:127],a[120:123],v[148:151],v[124:127]
dr a[108:111],v232 offset:11264
cv v181,v178,v179
cv v167,v166,v167
cv v166,v164,v165
cv v164,v160,v161
cv v165,v162,v163
s_cmp_eq_u32 s26,2
s_cbranch_scc1 D
B2:
s_waitcnt lgkmcnt(6)
mm v[128:131],a[64:67],v[0:3],v[192:195]
s_add_u32 s41,s38,s59
dr a[112:115],v232 offset:12288
s_add_u32 m0,s34,40960
mm v[128:131],a[68:71],v[8:11],v[128:131]
s_add_u32 s40,s30,40960
dr a[116:119],v232 offset:13312
v_bfe_u32 v250,v232,7,1
mm v[128:131],a[72:75],v[16:19],v[128:131]
)" R"(s_lshr_b32 s61,s29,5
bs a[196:199],v232,s[8:11],s41 offen offset:0 sc1
s_add_u32 s41,s35,24576
ex2 v184,v184
ex2 v185,v185
v_lshrrev_b32 v251,8,v232
mm v[128:131],a[76:79],v[24:27],v[128:131]
s_and_b32 s63,s29,4
dr a[120:123],v232 offset:14336
cv v184,v184,v185
mm v[144:147],a[76:79],v[56:59],v[196:199]
s_lshl_b32 s63,s63,1
dr a[124:127],v232 offset:15360
s_add_u32 s61,s61,s63
mm v[144:147],a[72:75],v[48:51],v[144:147]
s_bfe_u32 s62,s29,0x20003
ex2 v186,v186
mm v[144:147],a[68:71],v[40:43],v[144:147]
s_and_b32 s63,s29,3
ex2 v187,v187
mm v[144:147],a[64:67],v[32:35],v[144:147]
s_lshl_b32 s63,s63,2
ex2 v188,v188
mm v[148:151],a[80:83],v[32:35],v[196:199]
s_add_u32 s62,s62,s63
bl v232,s[8:11],s41 offen offset:0 sc1 lds
mm v[148:151],a[84:87],v[40:43],v[148:151]
s_waitcnt lgkmcnt(2)
ex2 v189,v189
mm v[148:151],a[88:91],v[48:51],v[148:151]
s_add_u32 m0,s33,0
ex2 v190,v190
mm v[148:151],a[92:95],v[56:59],v[148:151]
s_add_u32 s41,s35,24576
ex2 v191,v191
mm v[132:135],a[92:95],v[24:27],v[192:195]
s_lshl_b32 s85,s61,2
ex2 v168,v168
s_waitcnt vmcnt(10) lgkmcnt(0)
s_barrier
mm v[132:135],a[88:91],v[16:19],v[132:135]
s_add_u32 s85,s85,s24
bl v232,s[8:11],s31 offen offset:0 sc1 lds
mm v[132:135],a[84:87],v[8:11],v[132:135]
s_lshl_b32 s85,s85,4
dr a[64:67],v232 offset:49152
cv v185,v186,v187
mm v[132:135],a[80:83],v[0:3],v[132:135]
s_add_u32 s85,s85,s62
dr a[68:71],v232 offset:57344
cv v186,v188,v189
mm v[136:139],a[96:99],v[0:3],v[192:195]
s_lshl_b32 s85,s85,3
ex2 v169,v169
mm v[136:139],a[100:103],v[8:11],v[136:139]
s_add_u32 s85,s85,0x400a000
bl v232,s[8:11],s31 offen offset:1024 sc1 lds
mm v[136:139],a[104:107],v[16:19],v[136:139]
s_lshl_b32 s61,s61,1
dr a[72:75],v232 offset:50176
cv v187,v190,v191
mm v[136:139],a[108:111],v[24:27],v[136:139]
s_add_u32 s61,s61,s27
dr a[76:79],v232 offset:58368
)" R"(cv v168,v168,v169
mm v[152:155],a[108:111],v[56:59],v[196:199]
s_mul_i32 s61,s61,20
ex2 v170,v170
mm v[152:155],a[104:107],v[48:51],v[152:155]
s_add_u32 s82,s61,s62
bl v232,s[8:11],s31 offen offset:2048 sc1 lds
mm v[152:155],a[100:103],v[40:43],v[152:155]
s_lshr_b32 s63,s62,2
dr a[80:83],v232 offset:51200
s_add_u32 s83,s61,s63
mm v[152:155],a[96:99],v[32:35],v[152:155]
s_add_u32 s83,s83,16
ex2 v171,v171
mm v[156:159],a[112:115],v[32:35],v[196:199]
s_lshl_b32 s61,s28,11
ex2 v172,v172
mm v[156:159],a[116:119],v[40:43],v[156:159]
s_add_u32 s61,s61,0x3800000
bl v232,s[8:11],s31 offen offset:3072 sc1 lds
mm v[156:159],a[120:123],v[48:51],v[156:159]
s_add_u32 m0,s34,40960
ex2 v173,v173
mm v[156:159],a[124:127],v[56:59],v[156:159]
s_lshl_b32 s82,s82,12
ex2 v174,v174
mm v[140:143],a[124:127],v[24:27],v[192:195]
s_add_u32 s82,s82,s61
ex2 v175,v175
mm v[140:143],a[120:123],v[16:19],v[140:143]
s_lshl_b32 s83,s83,12
dr a[84:87],v232 offset:59392
cv v169,v170,v171
mm v[140:143],a[116:119],v[8:11],v[140:143]
s_add_u32 s83,s83,s61
dr a[88:91],v232 offset:52224
cv v170,v172,v173
mm v[140:143],a[112:115],v[0:3],v[140:143]
s_waitcnt lgkmcnt(2)
cv v171,v174,v175
s_and_b32 s63,s62,3
mm v[68:71],a[64:67],v[180:183],v[68:71]
s_lshl_b32 s63,s63,8
dr a[92:95],v232 offset:60416
s_add_u32 s83,s83,s63
mm v[68:71],a[68:71],v[184:187],v[68:71]
s_lshl_b32 s81,s62,12
dr a[96:99],v232 offset:53248
s_add_u32 s81,s81,0x3f00000
mm v[4:7],a[68:71],v[168:171],v[4:7]
s_lshl_b32 s84,s62,7
dr a[100:103],v232 offset:61440
s_add_u32 s84,s84,0x4008000
mm v[4:7],a[64:67],v[164:167],v[4:7]
dr a[104:107],v232 offset:54272
v_lshrrev_b32 v249,2,v232
mm v[12:15],a[72:75],v[164:167],v[12:15]
bl v232,s[8:11],s41 offen offset:1024 sc1 lds
mm v[12:15],a[76:79],v[168:171],v[12:15]
s_add_u32 m0,s33,49152
dr a[108:111],v232 offset:62464
s_add_u32 s41,s38,s59
mm v[76:79],a[76:79],v[184:187],v[76:79]
dr a[112:115],v232 offset:55296
)" R"(mm v[76:79],a[72:75],v[180:183],v[76:79]
dr a[116:119],v232 offset:63488
mm v[84:87],a[80:83],v[180:183],v[84:87]
s_waitcnt lgkmcnt(5)
dr a[120:123],v232 offset:56320
mm v[84:87],a[84:87],v[184:187],v[84:87]
dr a[124:127],v232 offset:64512
mm v[20:23],a[84:87],v[168:171],v[20:23]
ex2 v150,v150
mm v[20:23],a[80:83],v[164:167],v[20:23]
ex2 v151,v151
mm v[28:31],a[88:91],v[164:167],v[28:31]
ex2 v148,v148
mm v[28:31],a[92:95],v[168:171],v[28:31]
ex2 v149,v149
mm v[92:95],a[92:95],v[184:187],v[92:95]
s_waitcnt vmcnt(11) lgkmcnt(0)
s_barrier
mm v[92:95],a[88:91],v[180:183],v[92:95]
bl v232,s[8:11],s40 offen offset:0 sc1 lds
v_smfmac_f32_16x16x64_bf16 a[128:131],a[144:147],v[164:171],v246
dr a[64:67],v235 offset:0
cv v151,v150,v151
v_smfmac_f32_16x16x64_bf16 a[132:135],a[144:147],v[180:187],v246
dr a[68:71],v235 offset:1024
cv v150,v148,v149
mm v[100:103],a[96:99],v[180:183],v[100:103]
ex2 v144,v144
mm v[100:103],a[100:103],v[184:187],v[100:103]
bl v232,s[8:11],s40 offen offset:1024 sc1 lds
mm v[36:39],a[100:103],v[168:171],v[36:39]
dr a[72:75],v235 offset:2048
mm v[36:39],a[96:99],v[164:167],v[36:39]
dr a[76:79],v235 offset:3072
mm v[44:47],a[104:107],v[164:167],v[44:47]
ex2 v145,v145
mm v[44:47],a[108:111],v[168:171],v[44:47]
bl v232,s[8:11],s40 offen offset:2048 sc1 lds
mm v[108:111],a[108:111],v[184:187],v[108:111]
dr a[80:83],v235 offset:4096
cv v148,v144,v145
mm v[108:111],a[104:107],v[180:183],v[108:111]
dr a[84:87],v235 offset:5120
mm v[116:119],a[112:115],v[180:183],v[116:119]
ex2 v146,v146
mm v[116:119],a[116:119],v[184:187],v[116:119]
bl v232,s[8:11],s40 offen offset:3072 sc1 lds
mm v[52:55],a[116:119],v[168:171],v[52:55]
s_add_u32 m0,s34,65536
dr a[88:91],v235 offset:6144
s_add_u32 s40,s31,32768
mm v[52:55],a[112:115],v[164:167],v[52:55]
dr a[92:95],v235 offset:7168
mm v[60:63],a[120:123],v[164:167],v[60:63]
ex2 v147,v147
mm v[60:63],a[124:127],v[168:171],v[60:63]
bs a[200:203],v232,s[8:11],s41 offen offset:1024 sc1
dr a[96:99],v235 offset:8192
s_add_u32 s41,s35,32768
cv v149,v146,v147
ex2 v152,v152
mm v[124:127],a[124:127],v[184:187],v[124:127]
dr a[100:103],v235 offset:9216
mm v[124:127],a[120:123],v[180:183],v[124:127]
)" R"(s_waitcnt lgkmcnt(4)
ex2 v153,v153
mm v[160:163],a[64:67],v[0:3],v[192:195]
dr a[104:107],v235 offset:10240
cv v152,v152,v153
mm v[160:163],a[68:71],v[8:11],v[160:163]
dr a[108:111],v235 offset:11264
mm v[160:163],a[72:75],v[16:19],v[160:163]
bl v232,s[8:11],s41 offen offset:0 sc1 lds
mm v[160:163],a[76:79],v[24:27],v[160:163]
s_add_u32 m0,s33,131072
dr a[112:115],v235 offset:12288
s_add_u32 s41,s35,32768
mm v[176:179],a[76:79],v[56:59],v[196:199]
dr a[116:119],v235 offset:13312
mm v[176:179],a[72:75],v[48:51],v[176:179]
dr a[120:123],v235 offset:14336
mm v[176:179],a[68:71],v[40:43],v[176:179]
dr a[124:127],v235 offset:15360
mm v[176:179],a[64:67],v[32:35],v[176:179]
ex2 v154,v154
mm v[180:183],a[80:83],v[32:35],v[196:199]
ex2 v155,v155
mm v[180:183],a[84:87],v[40:43],v[180:183]
s_waitcnt lgkmcnt(4)
ex2 v156,v156
mm v[180:183],a[88:91],v[48:51],v[180:183]
ex2 v157,v157
mm v[180:183],a[92:95],v[56:59],v[180:183]
ex2 v158,v158
mm v[164:167],a[92:95],v[24:27],v[192:195]
s_waitcnt vmcnt(13) lgkmcnt(0)
s_barrier
mm v[164:167],a[88:91],v[16:19],v[164:167]
bl v232,s[8:11],s40 offen offset:0 sc1 lds
mm v[164:167],a[84:87],v[8:11],v[164:167]
dr a[64:67],v235 offset:16384
cv v153,v154,v155
mm v[164:167],a[80:83],v[0:3],v[164:167]
dr a[68:71],v235 offset:24576
cv v154,v156,v157
mm v[168:171],a[96:99],v[0:3],v[192:195]
ex2 v159,v159
mm v[168:171],a[100:103],v[8:11],v[168:171]
bl v232,s[8:11],s40 offen offset:1024 sc1 lds
mm v[168:171],a[104:107],v[16:19],v[168:171]
dr a[72:75],v235 offset:17408
cv v155,v158,v159
mm v[168:171],a[108:111],v[24:27],v[168:171]
dr a[76:79],v235 offset:25600
mm v[184:187],a[108:111],v[56:59],v[196:199]
ex2 v136,v136
mm v[184:187],a[104:107],v[48:51],v[184:187]
bl v232,s[8:11],s40 offen offset:2048 sc1 lds
mm v[184:187],a[100:103],v[40:43],v[184:187]
dr a[80:83],v235 offset:18432
mm v[184:187],a[96:99],v[32:35],v[184:187]
ex2 v137,v137
mm v[188:191],a[112:115],v[32:35],v[196:199]
ex2 v138,v138
mm v[188:191],a[116:119],v[40:43],v[188:191]
bl v232,s[8:11],s40 offen offset:3072 sc1 lds
mm v[188:191],a[120:123],v[48:51],v[188:191]
s_add_u32 m0,s34,65536
)" R"(ex2 v139,v139
mm v[188:191],a[124:127],v[56:59],v[188:191]
s_add_u32 s40,s31,8192
ex2 v140,v140
mm v[172:175],a[124:127],v[24:27],v[192:195]
ex2 v141,v141
mm v[172:175],a[120:123],v[16:19],v[172:175]
ex2 v142,v142
mm v[172:175],a[116:119],v[8:11],v[172:175]
ex2 v143,v143
mm v[172:175],a[112:115],v[0:3],v[172:175]
s_waitcnt lgkmcnt(0)
dr a[84:87],v235 offset:26624
cv v136,v136,v137
bl v232,s[8:11],s41 offen offset:1024 sc1 lds
mm v[68:71],a[64:67],v[148:151],v[68:71]
s_add_u32 m0,s33,147456
dr a[88:91],v235 offset:19456
cv v137,v138,v139
ex2 v134,v134
ex2 v135,v135
ex2 v132,v132
ex2 v133,v133
ex2 v128,v128
ex2 v129,v129
ex2 v130,v130
ex2 v131,v131
cv v138,v140,v141
cv v139,v142,v143
mm v[68:71],a[68:71],v[152:155],v[68:71]
dr a[92:95],v235 offset:27648
cv v135,v134,v135
cv v134,v132,v133
cv v132,v128,v129
cv v133,v130,v131
mm v[4:7],a[68:71],v[136:139],v[4:7]
dr a[96:99],v235 offset:20480
mm v[4:7],a[64:67],v[132:135],v[4:7]
dr a[100:103],v235 offset:28672
mm v[12:15],a[72:75],v[132:135],v[12:15]
dr a[104:107],v235 offset:21504
mm v[12:15],a[76:79],v[136:139],v[12:15]
dr a[108:111],v235 offset:29696
mm v[76:79],a[76:79],v[152:155],v[76:79]
dr a[112:115],v235 offset:22528
mm v[76:79],a[72:75],v[148:151],v[76:79]
dr a[116:119],v235 offset:30720
mm v[84:87],a[80:83],v[148:151],v[84:87]
s_waitcnt lgkmcnt(6)
dr a[120:123],v235 offset:23552
mm v[84:87],a[84:87],v[152:155],v[84:87]
dr a[124:127],v235 offset:31744
mm v[20:23],a[84:87],v[136:139],v[20:23]
v_cmp_eq_u32 vcc,v250,v251
v_and_b32 v250,0xf0,v232
v_mov_b32 v251,0x1000
v_cndmask_b32 v239,v251,v250,vcc
mm v[20:23],a[80:83],v[132:135],v[20:23]
ex2 v182,v182
mm v[28:31],a[88:91],v[132:135],v[28:31]
ex2 v183,v183
mm v[28:31],a[92:95],v[136:139],v[28:31]
ex2 v180,v180
mm v[92:95],a[92:95],v[152:155],v[92:95]
)" R"(s_waitcnt vmcnt(12) lgkmcnt(0)
s_barrier
mm v[92:95],a[88:91],v[148:151],v[92:95]
bl v232,s[8:11],s40 offen offset:0 sc1 lds
v_smfmac_f32_16x16x64_bf16 a[128:131],a[144:147],v[132:139],v246
dr a[64:67],v232 offset:0
v_add_u32 v239,0x1e000,v239
v_smfmac_f32_16x16x64_bf16 a[132:135],a[144:147],v[148:155],v246
dr a[68:71],v232 offset:1024
v_xor_b32 v250,v232,v249
mm v[100:103],a[96:99],v[148:151],v[100:103]
ex2 v181,v181
mm v[100:103],a[100:103],v[152:155],v[100:103]
bl v232,s[8:11],s40 offen offset:1024 sc1 lds
mm v[36:39],a[100:103],v[136:139],v[36:39]
dr a[72:75],v232 offset:2048
v_and_b32 v250,0xc0,v250
mm v[36:39],a[96:99],v[132:135],v[36:39]
dr a[76:79],v232 offset:3072
v_lshl_add_u32 v250,v250,2,v250
mm v[44:47],a[104:107],v[132:135],v[44:47]
ex2 v176,v176
mm v[44:47],a[108:111],v[136:139],v[44:47]
bl v232,s[8:11],s40 offen offset:2048 sc1 lds
mm v[108:111],a[108:111],v[152:155],v[108:111]
dr a[80:83],v232 offset:4096
v_xor_b32 v243,v250,v232
mm v[108:111],a[104:107],v[148:151],v[108:111]
dr a[84:87],v232 offset:5120
cv v183,v182,v183
mm v[116:119],a[112:115],v[148:151],v[116:119]
ex2 v177,v177
mm v[116:119],a[116:119],v[152:155],v[116:119]
s_mov_b32 s30,s31
dr a[88:91],v232 offset:6144
cv v182,v180,v181
mm v[52:55],a[116:119],v[136:139],v[52:55]
s_add_u32 s31,s31,65536
dr a[92:95],v232 offset:7168
cv v180,v176,v177
mm v[52:55],a[112:115],v[132:135],v[52:55]
bl v232,s[8:11],s40 offen offset:3072 sc1 lds
mm v[60:63],a[120:123],v[132:135],v[60:63]
dr a[96:99],v232 offset:8192
mm v[60:63],a[124:127],v[136:139],v[60:63]
dr a[100:103],v232 offset:9216
mm v[124:127],a[124:127],v[152:155],v[124:127]
ex2 v178,v178
dr a[104:107],v232 offset:10240
ex2 v179,v179
ex2 v166,v166
ex2 v167,v167
ex2 v164,v164
ex2 v165,v165
ex2 v160,v160
ex2 v161,v161
ex2 v162,v162
ex2 v163,v163
mm v[124:127],a[120:123],v[148:151],v[124:127]
dr a[108:111],v232 offset:11264
cv v181,v178,v179
cv v167,v166,v167
cv v166,v164,v165
cv v164,v160,v161
)" R"(cv v165,v162,v163
s_cmp_eq_u32 s26,3
s_cbranch_scc1 D
B3:
s_waitcnt lgkmcnt(6)
mm v[128:131],a[64:67],v[0:3],v[192:195]
s_add_u32 s41,s38,s59
dr a[112:115],v232 offset:12288
s_add_u32 m0,s34,73728
mm v[128:131],a[68:71],v[8:11],v[128:131]
s_add_u32 s40,s30,40960
dr a[116:119],v232 offset:13312
mm v[128:131],a[72:75],v[16:19],v[128:131]
bs a[204:207],v232,s[8:11],s41 offen offset:2048 sc1
s_add_u32 s41,s35,40960
ex2 v184,v184
ex2 v185,v185
mm v[128:131],a[76:79],v[24:27],v[128:131]
dr a[120:123],v232 offset:14336
cv v184,v184,v185
mm v[144:147],a[76:79],v[56:59],v[196:199]
dr a[124:127],v232 offset:15360
mm v[144:147],a[72:75],v[48:51],v[144:147]
ex2 v186,v186
mm v[144:147],a[68:71],v[40:43],v[144:147]
ex2 v187,v187
mm v[144:147],a[64:67],v[32:35],v[144:147]
ex2 v188,v188
mm v[148:151],a[80:83],v[32:35],v[196:199]
bl v232,s[8:11],s41 offen offset:0 sc1 lds
mm v[148:151],a[84:87],v[40:43],v[148:151]
s_waitcnt lgkmcnt(2)
ex2 v189,v189
mm v[148:151],a[88:91],v[48:51],v[148:151]
s_add_u32 m0,s33,0
ex2 v190,v190
mm v[148:151],a[92:95],v[56:59],v[148:151]
s_add_u32 s41,s38,s59
ex2 v191,v191
mm v[132:135],a[92:95],v[24:27],v[192:195]
s_waitcnt vmcnt(10) lgkmcnt(0)
s_barrier
mm v[132:135],a[88:91],v[16:19],v[132:135]
bl v232,s[8:11],s31 offen offset:0 sc1 lds
mm v[132:135],a[84:87],v[8:11],v[132:135]
dr a[64:67],v232 offset:49152
cv v185,v186,v187
mm v[132:135],a[80:83],v[0:3],v[132:135]
dr a[68:71],v232 offset:57344
cv v186,v188,v189
mm v[136:139],a[96:99],v[0:3],v[192:195]
ex2 v168,v168
mm v[136:139],a[100:103],v[8:11],v[136:139]
bl v232,s[8:11],s31 offen offset:1024 sc1 lds
mm v[136:139],a[104:107],v[16:19],v[136:139]
dr a[72:75],v232 offset:50176
cv v187,v190,v191
mm v[136:139],a[108:111],v[24:27],v[136:139]
dr a[76:79],v232 offset:58368
mm v[152:155],a[108:111],v[56:59],v[196:199]
ex2 v169,v169
mm v[152:155],a[104:107],v[48:51],v[152:155]
bl v232,s[8:11],s31 offen offset:2048 sc1 lds
mm v[152:155],a[100:103],v[40:43],v[152:155]
)" R"(dr a[80:83],v232 offset:51200
cv v168,v168,v169
mm v[152:155],a[96:99],v[32:35],v[152:155]
ex2 v170,v170
mm v[156:159],a[112:115],v[32:35],v[196:199]
ex2 v171,v171
mm v[156:159],a[116:119],v[40:43],v[156:159]
bl v232,s[8:11],s31 offen offset:3072 sc1 lds
mm v[156:159],a[120:123],v[48:51],v[156:159]
s_add_u32 m0,s33,49152
ex2 v172,v172
mm v[156:159],a[124:127],v[56:59],v[156:159]
ex2 v173,v173
mm v[140:143],a[124:127],v[24:27],v[192:195]
ex2 v174,v174
mm v[140:143],a[120:123],v[16:19],v[140:143]
ex2 v175,v175
mm v[140:143],a[116:119],v[8:11],v[140:143]
dr a[84:87],v232 offset:59392
cv v169,v170,v171
mm v[140:143],a[112:115],v[0:3],v[140:143]
s_waitcnt lgkmcnt(1)
dr a[88:91],v232 offset:52224
cv v170,v172,v173
mm v[68:71],a[64:67],v[180:183],v[68:71]
dr a[92:95],v232 offset:60416
cv v171,v174,v175
mm v[68:71],a[68:71],v[184:187],v[68:71]
dr a[96:99],v232 offset:53248
mm v[4:7],a[68:71],v[168:171],v[4:7]
dr a[100:103],v232 offset:61440
mm v[4:7],a[64:67],v[164:167],v[4:7]
EA_12:
s_cbranch_execz EA_R
dr a[104:107],v232 offset:54272
mm v[12:15],a[72:75],v[164:167],v[12:15]
dr a[108:111],v232 offset:62464
mm v[12:15],a[76:79],v[168:171],v[12:15]
bs a[208:211],v232,s[8:11],s41 offen offset:3072 sc1
dr a[112:115],v232 offset:55296
s_add_u32 s41,s35,40960
ex2 v150,v150
ex2 v151,v151
mm v[76:79],a[76:79],v[184:187],v[76:79]
dr a[116:119],v232 offset:63488
cv v151,v150,v151
mm v[76:79],a[72:75],v[180:183],v[76:79]
dr a[120:123],v232 offset:56320
mm v[84:87],a[80:83],v[180:183],v[84:87]
s_waitcnt lgkmcnt(6)
dr a[124:127],v232 offset:64512
mm v[84:87],a[84:87],v[184:187],v[84:87]
ex2 v148,v148
mm v[20:23],a[84:87],v[168:171],v[20:23]
ex2 v149,v149
mm v[20:23],a[80:83],v[164:167],v[20:23]
ex2 v144,v144
mm v[28:31],a[88:91],v[164:167],v[28:31]
ex2 v145,v145
mm v[28:31],a[92:95],v[168:171],v[28:31]
ex2 v146,v146
mm v[92:95],a[92:95],v[184:187],v[92:95]
s_waitcnt vmcnt(11) lgkmcnt(0)
s_barrier
)" R"(mm v[92:95],a[88:91],v[180:183],v[92:95]
bl v232,s[8:11],s40 offen offset:0 sc1 lds
v_smfmac_f32_16x16x64_bf16 a[128:131],a[144:147],v[164:171],v246
dr a[64:67],v235 offset:0
cv v150,v148,v149
v_smfmac_f32_16x16x64_bf16 a[132:135],a[144:147],v[180:187],v246
dr a[68:71],v235 offset:1024
cv v148,v144,v145
mm v[100:103],a[96:99],v[180:183],v[100:103]
ex2 v147,v147
mm v[100:103],a[100:103],v[184:187],v[100:103]
bl v232,s[8:11],s40 offen offset:1024 sc1 lds
mm v[36:39],a[100:103],v[168:171],v[36:39]
dr a[72:75],v235 offset:2048
cv v149,v146,v147
mm v[36:39],a[96:99],v[164:167],v[36:39]
dr a[76:79],v235 offset:3072
mm v[44:47],a[104:107],v[164:167],v[44:47]
ex2 v152,v152
mm v[44:47],a[108:111],v[168:171],v[44:47]
bl v232,s[8:11],s40 offen offset:2048 sc1 lds
mm v[108:111],a[108:111],v[184:187],v[108:111]
dr a[80:83],v235 offset:4096
mm v[108:111],a[104:107],v[180:183],v[108:111]
dr a[84:87],v235 offset:5120
mm v[116:119],a[112:115],v[180:183],v[116:119]
ex2 v153,v153
mm v[116:119],a[116:119],v[184:187],v[116:119]
bl v232,s[8:11],s40 offen offset:3072 sc1 lds
mm v[52:55],a[116:119],v[168:171],v[52:55]
s_add_u32 m0,s34,73728
dr a[88:91],v235 offset:6144
s_add_u32 s40,s31,32768
mm v[52:55],a[112:115],v[164:167],v[52:55]
dr a[92:95],v235 offset:7168
cv v152,v152,v153
mm v[60:63],a[120:123],v[164:167],v[60:63]
ex2 v154,v154
mm v[60:63],a[124:127],v[168:171],v[60:63]
bl v232,s[8:11],s41 offen offset:1024 sc1 lds
mm v[124:127],a[124:127],v[184:187],v[124:127]
s_add_u32 s41,s39,s58
dr a[96:99],v235 offset:8192
s_add_u32 m0,s33,131072
mm v[124:127],a[120:123],v[180:183],v[124:127]
s_waitcnt lgkmcnt(3)
dr a[100:103],v235 offset:9216
mm v[160:163],a[64:67],v[0:3],v[192:195]
ex2 v155,v155
mm v[160:163],a[68:71],v[8:11],v[160:163]
dr a[104:107],v235 offset:10240
cv v153,v154,v155
mm v[160:163],a[72:75],v[16:19],v[160:163]
dr a[108:111],v235 offset:11264
mm v[160:163],a[76:79],v[24:27],v[160:163]
dr a[112:115],v235 offset:12288
mm v[176:179],a[76:79],v[56:59],v[196:199]
bs a[0:3],v232,s[8:11],s41 offen offset:0 sc1
dr a[116:119],v235 offset:13312
ex2 v156,v156
ex2 v157,v157
mm v[176:179],a[72:75],v[48:51],v[176:179]
dr a[120:123],v235 offset:14336
cv v154,v156,v157
)" R"(mm v[176:179],a[68:71],v[40:43],v[176:179]
dr a[124:127],v235 offset:15360
mm v[176:179],a[64:67],v[32:35],v[176:179]
ex2 v158,v158
mm v[180:183],a[80:83],v[32:35],v[196:199]
ex2 v159,v159
mm v[180:183],a[84:87],v[40:43],v[180:183]
s_waitcnt lgkmcnt(4)
ex2 v136,v136
mm v[180:183],a[88:91],v[48:51],v[180:183]
ex2 v137,v137
mm v[180:183],a[92:95],v[56:59],v[180:183]
ex2 v138,v138
mm v[164:167],a[92:95],v[24:27],v[192:195]
s_waitcnt vmcnt(13) lgkmcnt(0)
s_barrier
mm v[164:167],a[88:91],v[16:19],v[164:167]
bl v232,s[8:11],s40 offen offset:0 sc1 lds
mm v[164:167],a[84:87],v[8:11],v[164:167]
dr a[64:67],v235 offset:16384
cv v155,v158,v159
mm v[164:167],a[80:83],v[0:3],v[164:167]
dr a[68:71],v235 offset:24576
cv v136,v136,v137
mm v[168:171],a[96:99],v[0:3],v[192:195]
ex2 v139,v139
mm v[168:171],a[100:103],v[8:11],v[168:171]
bl v232,s[8:11],s40 offen offset:1024 sc1 lds
mm v[168:171],a[104:107],v[16:19],v[168:171]
dr a[72:75],v235 offset:17408
cv v137,v138,v139
mm v[168:171],a[108:111],v[24:27],v[168:171]
dr a[76:79],v235 offset:25600
mm v[184:187],a[108:111],v[56:59],v[196:199]
ex2 v140,v140
mm v[184:187],a[104:107],v[48:51],v[184:187]
bl v232,s[8:11],s40 offen offset:2048 sc1 lds
mm v[184:187],a[100:103],v[40:43],v[184:187]
dr a[80:83],v235 offset:18432
mm v[184:187],a[96:99],v[32:35],v[184:187]
ex2 v141,v141
mm v[188:191],a[112:115],v[32:35],v[196:199]
ex2 v142,v142
mm v[188:191],a[116:119],v[40:43],v[188:191]
bl v232,s[8:11],s40 offen offset:3072 sc1 lds
mm v[188:191],a[120:123],v[48:51],v[188:191]
s_add_u32 s40,s31,8192
ex2 v143,v143
mm v[188:191],a[124:127],v[56:59],v[188:191]
s_add_u32 m0,s33,147456
dr a[84:87],v235 offset:26624
cv v138,v140,v141
mm v[172:175],a[124:127],v[24:27],v[192:195]
ex2 v134,v134
mm v[172:175],a[120:123],v[16:19],v[172:175]
ex2 v135,v135
mm v[172:175],a[116:119],v[8:11],v[172:175]
dr a[88:91],v235 offset:19456
cv v139,v142,v143
mm v[172:175],a[112:115],v[0:3],v[172:175]
s_waitcnt lgkmcnt(2)
ex2 v132,v132
mm v[68:71],a[64:67],v[148:151],v[68:71]
s_lshr_b32 s46,s38,15
)" R"(ex2 v133,v133
ex2 v128,v128
ex2 v129,v129
ex2 v130,v130
ex2 v131,v131
mm v[68:71],a[68:71],v[152:155],v[68:71]
s_lshl_b32 s46,s46,2
dr a[92:95],v235 offset:27648
cv v135,v134,v135
cv v134,v132,v133
cv v132,v128,v129
cv v133,v130,v131
mm v[4:7],a[68:71],v[136:139],v[4:7]
s_add_u32 s46,s46,s24
dr a[96:99],v235 offset:20480
s_lshl_b32 s46,s46,3
mm v[4:7],a[64:67],v[132:135],v[4:7]
s_add_u32 s46,s46,0x4018000
dr a[100:103],v235 offset:28672
mm v[12:15],a[72:75],v[132:135],v[12:15]
dr a[104:107],v235 offset:21504
mm v[12:15],a[76:79],v[136:139],v[12:15]
dr a[108:111],v235 offset:29696
mm v[76:79],a[76:79],v[152:155],v[76:79]
dr a[112:115],v235 offset:22528
mm v[76:79],a[72:75],v[148:151],v[76:79]
dr a[116:119],v235 offset:30720
mm v[84:87],a[80:83],v[148:151],v[84:87]
s_waitcnt lgkmcnt(6)
dr a[120:123],v235 offset:23552
mm v[84:87],a[84:87],v[152:155],v[84:87]
dr a[124:127],v235 offset:31744
mm v[20:23],a[84:87],v[136:139],v[20:23]
ex2 v182,v182
mm v[20:23],a[80:83],v[132:135],v[20:23]
ex2 v183,v183
mm v[28:31],a[88:91],v[132:135],v[28:31]
ex2 v180,v180
mm v[28:31],a[92:95],v[136:139],v[28:31]
ex2 v181,v181
mm v[92:95],a[92:95],v[152:155],v[92:95]
s_waitcnt vmcnt(11) lgkmcnt(0)
s_barrier
mm v[92:95],a[88:91],v[148:151],v[92:95]
bl v232,s[8:11],s40 offen offset:0 sc1 lds
v_smfmac_f32_16x16x64_bf16 a[128:131],a[144:147],v[132:139],v246
dr a[64:67],v232 offset:0
cv v183,v182,v183
v_smfmac_f32_16x16x64_bf16 a[132:135],a[144:147],v[148:155],v246
dr a[68:71],v232 offset:1024
cv v182,v180,v181
mm v[100:103],a[96:99],v[148:151],v[100:103]
ex2 v176,v176
mm v[100:103],a[100:103],v[152:155],v[100:103]
bl v232,s[8:11],s40 offen offset:1024 sc1 lds
mm v[36:39],a[100:103],v[136:139],v[36:39]
dr a[72:75],v232 offset:2048
mm v[36:39],a[96:99],v[132:135],v[36:39]
dr a[76:79],v232 offset:3072
mm v[44:47],a[104:107],v[132:135],v[44:47]
ex2 v177,v177
mm v[44:47],a[108:111],v[136:139],v[44:47]
bl v232,s[8:11],s40 offen offset:2048 sc1 lds
mm v[108:111],a[108:111],v[152:155],v[108:111]
)" R"(dr a[80:83],v232 offset:4096
cv v180,v176,v177
mm v[108:111],a[104:107],v[148:151],v[108:111]
dr a[84:87],v232 offset:5120
mm v[116:119],a[112:115],v[148:151],v[116:119]
ex2 v178,v178
mm v[116:119],a[116:119],v[152:155],v[116:119]
s_mov_b32 s30,s31
dr a[88:91],v232 offset:6144
s_add_u32 s31,s31,65536
mm v[52:55],a[116:119],v[136:139],v[52:55]
dr a[92:95],v232 offset:7168
mm v[52:55],a[112:115],v[132:135],v[52:55]
bl v232,s[8:11],s40 offen offset:3072 sc1 lds
mm v[60:63],a[120:123],v[132:135],v[60:63]
dr a[96:99],v232 offset:8192
mm v[60:63],a[124:127],v[136:139],v[60:63]
dr a[100:103],v232 offset:9216
mm v[124:127],a[124:127],v[152:155],v[124:127]
ex2 v179,v179
dr a[104:107],v232 offset:10240
ex2 v166,v166
ex2 v167,v167
ex2 v164,v164
ex2 v165,v165
ex2 v160,v160
ex2 v161,v161
ex2 v162,v162
ex2 v163,v163
mm v[124:127],a[120:123],v[148:151],v[124:127]
dr a[108:111],v232 offset:11264
cv v181,v178,v179
s_waitcnt vmcnt(14)
buffer_store_dwordx2 v[252:253],v254,s[8:11],s46 offen sc1
cv v167,v166,v167
cv v166,v164,v165
cv v164,v160,v161
cv v165,v162,v163
s_cmp_eq_u32 s26,4
s_cbranch_scc1 D
B4:
s_waitcnt lgkmcnt(6)
mm v[128:131],a[64:67],v[0:3],v[192:195]
s_mov_b32 s46,s84
dr a[112:115],v232 offset:12288
v_lshrrev_b32 v242,1,v232
mm v[128:131],a[68:71],v[8:11],v[128:131]
s_add_u32 s41,s39,s58
dr a[116:119],v232 offset:13312
v_and_b32 v242,120,v242
mm v[128:131],a[72:75],v[16:19],v[128:131]
s_add_u32 m0,s34,81920
buffer_load_dwordx2 v[240:241],v242,s[8:11],s46 offen sc1
mm v[128:131],a[76:79],v[24:27],v[128:131]
s_add_u32 s40,s30,40960
bs a[4:7],v232,s[8:11],s41 offen offset:1024 sc1
dr a[120:123],v232 offset:14336
s_add_u32 s41,s35,49152
ex2 v184,v184
ex2 v185,v185
mm v[144:147],a[76:79],v[56:59],v[196:199]
dr a[124:127],v232 offset:15360
cv v184,v184,v185
mm v[144:147],a[72:75],v[48:51],v[144:147]
)" R"(ex2 v186,v186
mm v[144:147],a[68:71],v[40:43],v[144:147]
ex2 v187,v187
mm v[144:147],a[64:67],v[32:35],v[144:147]
ex2 v188,v188
mm v[148:151],a[80:83],v[32:35],v[196:199]
bl v232,s[8:11],s41 offen offset:0 sc1 lds
mm v[148:151],a[84:87],v[40:43],v[148:151]
s_waitcnt lgkmcnt(2)
ex2 v189,v189
mm v[148:151],a[88:91],v[48:51],v[148:151]
s_add_u32 m0,s33,0
ex2 v190,v190
mm v[148:151],a[92:95],v[56:59],v[148:151]
s_add_u32 s41,s39,s58
ex2 v191,v191
mm v[132:135],a[92:95],v[24:27],v[192:195]
s_waitcnt vmcnt(11) lgkmcnt(0)
s_barrier
mm v[132:135],a[88:91],v[16:19],v[132:135]
bl v232,s[8:11],s31 offen offset:0 sc1 lds
mm v[132:135],a[84:87],v[8:11],v[132:135]
dr a[64:67],v232 offset:49152
cv v185,v186,v187
mm v[132:135],a[80:83],v[0:3],v[132:135]
dr a[68:71],v232 offset:57344
cv v186,v188,v189
mm v[136:139],a[96:99],v[0:3],v[192:195]
ex2 v168,v168
mm v[136:139],a[100:103],v[8:11],v[136:139]
bl v232,s[8:11],s31 offen offset:1024 sc1 lds
mm v[136:139],a[104:107],v[16:19],v[136:139]
dr a[72:75],v232 offset:50176
cv v187,v190,v191
mm v[136:139],a[108:111],v[24:27],v[136:139]
dr a[76:79],v232 offset:58368
mm v[152:155],a[108:111],v[56:59],v[196:199]
ex2 v169,v169
mm v[152:155],a[104:107],v[48:51],v[152:155]
bl v232,s[8:11],s31 offen offset:2048 sc1 lds
mm v[152:155],a[100:103],v[40:43],v[152:155]
dr a[80:83],v232 offset:51200
cv v168,v168,v169
mm v[152:155],a[96:99],v[32:35],v[152:155]
ex2 v170,v170
mm v[156:159],a[112:115],v[32:35],v[196:199]
ex2 v171,v171
mm v[156:159],a[116:119],v[40:43],v[156:159]
bl v232,s[8:11],s31 offen offset:3072 sc1 lds
mm v[156:159],a[120:123],v[48:51],v[156:159]
s_add_u32 m0,s33,49152
ex2 v172,v172
mm v[156:159],a[124:127],v[56:59],v[156:159]
ex2 v173,v173
mm v[140:143],a[124:127],v[24:27],v[192:195]
ex2 v174,v174
mm v[140:143],a[120:123],v[16:19],v[140:143]
ex2 v175,v175
mm v[140:143],a[116:119],v[8:11],v[140:143]
dr a[84:87],v232 offset:59392
cv v169,v170,v171
mm v[140:143],a[112:115],v[0:3],v[140:143]
s_waitcnt lgkmcnt(1)
dr a[88:91],v232 offset:52224
)" R"(cv v170,v172,v173
mm v[68:71],a[64:67],v[180:183],v[68:71]
dr a[92:95],v232 offset:60416
cv v171,v174,v175
mm v[68:71],a[68:71],v[184:187],v[68:71]
dr a[96:99],v232 offset:53248
mm v[4:7],a[68:71],v[168:171],v[4:7]
dr a[100:103],v232 offset:61440
mm v[4:7],a[64:67],v[164:167],v[4:7]
dr a[104:107],v232 offset:54272
mm v[12:15],a[72:75],v[164:167],v[12:15]
dr a[108:111],v232 offset:62464
mm v[12:15],a[76:79],v[168:171],v[12:15]
dr a[112:115],v232 offset:55296
mm v[76:79],a[76:79],v[184:187],v[76:79]
dr a[116:119],v232 offset:63488
mm v[76:79],a[72:75],v[180:183],v[76:79]
dr a[120:123],v232 offset:56320
mm v[84:87],a[80:83],v[180:183],v[84:87]
s_waitcnt lgkmcnt(6)
dr a[124:127],v232 offset:64512
mm v[84:87],a[84:87],v[184:187],v[84:87]
ex2 v150,v150
mm v[20:23],a[84:87],v[168:171],v[20:23]
ex2 v151,v151
mm v[20:23],a[80:83],v[164:167],v[20:23]
ex2 v148,v148
mm v[28:31],a[88:91],v[164:167],v[28:31]
ex2 v149,v149
mm v[28:31],a[92:95],v[168:171],v[28:31]
ex2 v144,v144
mm v[92:95],a[92:95],v[184:187],v[92:95]
s_waitcnt vmcnt(11) lgkmcnt(0)
s_barrier
mm v[92:95],a[88:91],v[180:183],v[92:95]
bl v232,s[8:11],s40 offen offset:0 sc1 lds
v_smfmac_f32_16x16x64_bf16 a[128:131],a[144:147],v[164:171],v246
dr a[64:67],v235 offset:0
cv v151,v150,v151
v_smfmac_f32_16x16x64_bf16 a[132:135],a[144:147],v[180:187],v246
dr a[68:71],v235 offset:1024
cv v150,v148,v149
mm v[100:103],a[96:99],v[180:183],v[100:103]
ex2 v145,v145
mm v[100:103],a[100:103],v[184:187],v[100:103]
bl v232,s[8:11],s40 offen offset:1024 sc1 lds
mm v[36:39],a[100:103],v[168:171],v[36:39]
dr a[72:75],v235 offset:2048
cv v148,v144,v145
mm v[36:39],a[96:99],v[164:167],v[36:39]
dr a[76:79],v235 offset:3072
mm v[44:47],a[104:107],v[164:167],v[44:47]
ex2 v146,v146
mm v[44:47],a[108:111],v[168:171],v[44:47]
bl v232,s[8:11],s40 offen offset:2048 sc1 lds
mm v[108:111],a[108:111],v[184:187],v[108:111]
dr a[80:83],v235 offset:4096
mm v[108:111],a[104:107],v[180:183],v[108:111]
dr a[84:87],v235 offset:5120
mm v[116:119],a[112:115],v[180:183],v[116:119]
ex2 v147,v147
mm v[116:119],a[116:119],v[184:187],v[116:119]
bl v232,s[8:11],s40 offen offset:3072 sc1 lds
mm v[52:55],a[116:119],v[168:171],v[52:55]
)" R"(s_add_u32 m0,s34,81920
dr a[88:91],v235 offset:6144
s_add_u32 s40,s31,32768
mm v[52:55],a[112:115],v[164:167],v[52:55]
dr a[92:95],v235 offset:7168
cv v149,v146,v147
mm v[60:63],a[120:123],v[164:167],v[60:63]
ex2 v152,v152
mm v[60:63],a[124:127],v[168:171],v[60:63]
bs a[8:11],v233,s[8:11],s41 offen offset:0 sc1
dr a[96:99],v235 offset:8192
s_add_u32 s41,s35,49152
ex2 v153,v153
ex2 v154,v154
mm v[124:127],a[124:127],v[184:187],v[124:127]
dr a[100:103],v235 offset:9216
cv v152,v152,v153
mm v[124:127],a[120:123],v[180:183],v[124:127]
s_waitcnt lgkmcnt(4)
ex2 v155,v155
mm v[160:163],a[64:67],v[0:3],v[192:195]
dr a[104:107],v235 offset:10240
cv v153,v154,v155
mm v[160:163],a[68:71],v[8:11],v[160:163]
s_waitcnt vmcnt(11)
v_cmp_ne_u64 vcc,s[54:55],v[240:241]
s_cbranch_vccnz FA20_S
FA20_D:
mm v[160:163],a[72:75],v[16:19],v[160:163]
dr a[108:111],v235 offset:11264
mm v[160:163],a[76:79],v[24:27],v[160:163]
bl v232,s[8:11],s41 offen offset:1024 sc1 lds
mm v[176:179],a[76:79],v[56:59],v[196:199]
s_lshl_b32 s41,s24,10
dr a[112:115],v235 offset:12288
s_add_u32 m0,s41,122880
mm v[176:179],a[72:75],v[48:51],v[176:179]
s_add_u32 s41,s41,s81
dr a[116:119],v235 offset:13312
mm v[176:179],a[68:71],v[40:43],v[176:179]
dr a[120:123],v235 offset:14336
mm v[176:179],a[64:67],v[32:35],v[176:179]
dr a[124:127],v235 offset:15360
bl v232,s[8:11],s41 offen sc1 lds
mm v[180:183],a[80:83],v[32:35],v[196:199]
s_lshl_b32 s41,s24,10
ex2 v156,v156
mm v[180:183],a[84:87],v[40:43],v[180:183]
s_waitcnt lgkmcnt(4)
s_add_u32 m0,s41,126976
s_mov_b32 s41,0x80000000
mm v[180:183],a[88:91],v[48:51],v[180:183]
ex2 v157,v157
mm v[180:183],a[92:95],v[56:59],v[180:183]
bl v232,s[8:11],s41 offen sc1 lds
mm v[164:167],a[92:95],v[24:27],v[192:195]
s_add_u32 m0,s33,131072
ex2 v158,v158
s_waitcnt lgkmcnt(0)
s_barrier
mm v[164:167],a[88:91],v[16:19],v[164:167]
dr a[64:67],v235 offset:16384
cv v154,v156,v157
mm v[164:167],a[84:87],v[8:11],v[164:167]
)" R"(dr a[68:71],v235 offset:24576
mm v[164:167],a[80:83],v[0:3],v[164:167]
bl v232,s[8:11],s40 offen offset:0 sc1 lds
mm v[168:171],a[96:99],v[0:3],v[192:195]
dr a[72:75],v235 offset:17408
mm v[168:171],a[100:103],v[8:11],v[168:171]
dr a[76:79],v235 offset:25600
mm v[168:171],a[104:107],v[16:19],v[168:171]
ex2 v159,v159
mm v[168:171],a[108:111],v[24:27],v[168:171]
bl v232,s[8:11],s40 offen offset:1024 sc1 lds
mm v[184:187],a[108:111],v[56:59],v[196:199]
dr a[80:83],v235 offset:18432
cv v155,v158,v159
mm v[184:187],a[104:107],v[48:51],v[184:187]
ex2 v136,v136
mm v[184:187],a[100:103],v[40:43],v[184:187]
ex2 v137,v137
mm v[184:187],a[96:99],v[32:35],v[184:187]
bl v232,s[8:11],s40 offen offset:2048 sc1 lds
mm v[188:191],a[112:115],v[32:35],v[196:199]
ex2 v138,v138
mm v[188:191],a[116:119],v[40:43],v[188:191]
ex2 v139,v139
mm v[188:191],a[120:123],v[48:51],v[188:191]
ex2 v140,v140
mm v[188:191],a[124:127],v[56:59],v[188:191]
bl v232,s[8:11],s40 offen offset:3072 sc1 lds
mm v[172:175],a[124:127],v[24:27],v[192:195]
s_add_u32 s40,s31,8192
ex2 v141,v141
mm v[172:175],a[120:123],v[16:19],v[172:175]
s_add_u32 m0,s33,147456
ex2 v142,v142
mm v[172:175],a[116:119],v[8:11],v[172:175]
ex2 v143,v143
mm v[172:175],a[112:115],v[0:3],v[172:175]
s_waitcnt lgkmcnt(0)
dr a[84:87],v235 offset:26624
cv v136,v136,v137
mm v[68:71],a[64:67],v[148:151],v[68:71]
dr a[88:91],v235 offset:19456
cv v137,v138,v139
ex2 v134,v134
ex2 v135,v135
ex2 v132,v132
ex2 v133,v133
ex2 v128,v128
ex2 v129,v129
ex2 v130,v130
ex2 v131,v131
cv v138,v140,v141
cv v139,v142,v143
mm v[68:71],a[68:71],v[152:155],v[68:71]
dr a[92:95],v235 offset:27648
cv v135,v134,v135
cv v134,v132,v133
cv v132,v128,v129
cv v133,v130,v131
mm v[4:7],a[68:71],v[136:139],v[4:7]
dr a[96:99],v235 offset:20480
mm v[4:7],a[64:67],v[132:135],v[4:7]
dr a[100:103],v235 offset:28672
mm v[12:15],a[72:75],v[132:135],v[12:15]
)" R"(dr a[104:107],v235 offset:21504
mm v[12:15],a[76:79],v[136:139],v[12:15]
dr a[108:111],v235 offset:29696
mm v[76:79],a[76:79],v[152:155],v[76:79]
dr a[112:115],v235 offset:22528
mm v[76:79],a[72:75],v[148:151],v[76:79]
dr a[116:119],v235 offset:30720
mm v[84:87],a[80:83],v[148:151],v[84:87]
s_waitcnt lgkmcnt(6)
dr a[120:123],v235 offset:23552
mm v[84:87],a[84:87],v[152:155],v[84:87]
dr a[124:127],v235 offset:31744
mm v[20:23],a[84:87],v[136:139],v[20:23]
ex2 v182,v182
mm v[20:23],a[80:83],v[132:135],v[20:23]
ex2 v183,v183
mm v[28:31],a[88:91],v[132:135],v[28:31]
ex2 v180,v180
mm v[28:31],a[92:95],v[136:139],v[28:31]
ex2 v181,v181
mm v[92:95],a[92:95],v[152:155],v[92:95]
s_waitcnt vmcnt(12) lgkmcnt(0)
s_barrier
mm v[92:95],a[88:91],v[148:151],v[92:95]
bl v232,s[8:11],s40 offen offset:0 sc1 lds
v_smfmac_f32_16x16x64_bf16 a[128:131],a[144:147],v[132:139],v246
dr a[64:67],v232 offset:0
cv v183,v182,v183
v_smfmac_f32_16x16x64_bf16 a[132:135],a[144:147],v[148:155],v246
dr a[68:71],v232 offset:1024
cv v182,v180,v181
mm v[100:103],a[96:99],v[148:151],v[100:103]
ex2 v176,v176
mm v[100:103],a[100:103],v[152:155],v[100:103]
bl v232,s[8:11],s40 offen offset:1024 sc1 lds
mm v[36:39],a[100:103],v[136:139],v[36:39]
dr a[72:75],v232 offset:2048
mm v[36:39],a[96:99],v[132:135],v[36:39]
dr a[76:79],v232 offset:3072
mm v[44:47],a[104:107],v[132:135],v[44:47]
ex2 v177,v177
mm v[44:47],a[108:111],v[136:139],v[44:47]
bl v232,s[8:11],s40 offen offset:2048 sc1 lds
mm v[108:111],a[108:111],v[152:155],v[108:111]
dr a[80:83],v232 offset:4096
cv v180,v176,v177
mm v[108:111],a[104:107],v[148:151],v[108:111]
EB_13:
s_cbranch_execz EB_R
dr a[84:87],v232 offset:5120
mm v[116:119],a[112:115],v[148:151],v[116:119]
ex2 v178,v178
mm v[116:119],a[116:119],v[152:155],v[116:119]
s_mov_b32 s30,s31
dr a[88:91],v232 offset:6144
s_add_u32 s31,s31,65536
mm v[52:55],a[116:119],v[136:139],v[52:55]
dr a[92:95],v232 offset:7168
mm v[52:55],a[112:115],v[132:135],v[52:55]
bl v232,s[8:11],s40 offen offset:3072 sc1 lds
mm v[60:63],a[120:123],v[132:135],v[60:63]
dr a[96:99],v232 offset:8192
mm v[60:63],a[124:127],v[136:139],v[60:63]
dr a[100:103],v232 offset:9216
)" R"(mm v[124:127],a[124:127],v[152:155],v[124:127]
ex2 v179,v179
dr a[104:107],v232 offset:10240
ex2 v166,v166
ex2 v167,v167
ex2 v164,v164
ex2 v165,v165
ex2 v160,v160
ex2 v161,v161
ex2 v162,v162
ex2 v163,v163
mm v[124:127],a[120:123],v[148:151],v[124:127]
dr a[108:111],v232 offset:11264
cv v181,v178,v179
cv v167,v166,v167
cv v166,v164,v165
cv v164,v160,v161
cv v165,v162,v163
s_cmp_eq_u32 s26,5
s_cbranch_scc1 D
B5:
s_waitcnt lgkmcnt(6)
mm v[128:131],a[64:67],v[0:3],v[192:195]
s_add_u32 s41,s39,s58
dr a[112:115],v232 offset:12288
s_add_u32 m0,s34,90112
mm v[128:131],a[68:71],v[8:11],v[128:131]
s_add_u32 s40,s30,40960
dr a[116:119],v232 offset:13312
mm v[128:131],a[72:75],v[16:19],v[128:131]
bs a[12:15],v233,s[8:11],s41 offen offset:1024 sc1
s_add_u32 s41,s35,57344
ex2 v184,v184
ex2 v185,v185
mm v[128:131],a[76:79],v[24:27],v[128:131]
dr a[120:123],v232 offset:14336
cv v184,v184,v185
mm v[144:147],a[76:79],v[56:59],v[196:199]
dr a[124:127],v232 offset:15360
mm v[144:147],a[72:75],v[48:51],v[144:147]
ex2 v186,v186
mm v[144:147],a[68:71],v[40:43],v[144:147]
ex2 v187,v187
mm v[144:147],a[64:67],v[32:35],v[144:147]
ex2 v188,v188
mm v[148:151],a[80:83],v[32:35],v[196:199]
bl v232,s[8:11],s41 offen offset:0 sc1 lds
mm v[148:151],a[84:87],v[40:43],v[148:151]
s_waitcnt lgkmcnt(2)
ex2 v189,v189
mm v[148:151],a[88:91],v[48:51],v[148:151]
s_add_u32 m0,s33,0
ex2 v190,v190
mm v[148:151],a[92:95],v[56:59],v[148:151]
s_add_u32 s41,s35,57344
ex2 v191,v191
mm v[132:135],a[92:95],v[24:27],v[192:195]
s_waitcnt vmcnt(10) lgkmcnt(0)
s_barrier
mm v[132:135],a[88:91],v[16:19],v[132:135]
bl v232,s[8:11],s31 offen offset:0 sc1 lds
mm v[132:135],a[84:87],v[8:11],v[132:135]
dr a[64:67],v232 offset:49152
cv v185,v186,v187
)" R"(mm v[132:135],a[80:83],v[0:3],v[132:135]
dr a[68:71],v232 offset:57344
cv v186,v188,v189
mm v[136:139],a[96:99],v[0:3],v[192:195]
ex2 v168,v168
mm v[136:139],a[100:103],v[8:11],v[136:139]
bl v232,s[8:11],s31 offen offset:1024 sc1 lds
mm v[136:139],a[104:107],v[16:19],v[136:139]
dr a[72:75],v232 offset:50176
cv v187,v190,v191
mm v[136:139],a[108:111],v[24:27],v[136:139]
dr a[76:79],v232 offset:58368
mm v[152:155],a[108:111],v[56:59],v[196:199]
ex2 v169,v169
mm v[152:155],a[104:107],v[48:51],v[152:155]
bl v232,s[8:11],s31 offen offset:2048 sc1 lds
mm v[152:155],a[100:103],v[40:43],v[152:155]
dr a[80:83],v232 offset:51200
cv v168,v168,v169
mm v[152:155],a[96:99],v[32:35],v[152:155]
ex2 v170,v170
mm v[156:159],a[112:115],v[32:35],v[196:199]
ex2 v171,v171
mm v[156:159],a[116:119],v[40:43],v[156:159]
bl v232,s[8:11],s31 offen offset:3072 sc1 lds
mm v[156:159],a[120:123],v[48:51],v[156:159]
s_add_u32 m0,s34,90112
ex2 v172,v172
mm v[156:159],a[124:127],v[56:59],v[156:159]
ex2 v173,v173
mm v[140:143],a[124:127],v[24:27],v[192:195]
ex2 v174,v174
mm v[140:143],a[120:123],v[16:19],v[140:143]
ex2 v175,v175
mm v[140:143],a[116:119],v[8:11],v[140:143]
dr a[84:87],v232 offset:59392
cv v169,v170,v171
mm v[140:143],a[112:115],v[0:3],v[140:143]
s_waitcnt lgkmcnt(1)
dr a[88:91],v232 offset:52224
cv v170,v172,v173
mm v[68:71],a[64:67],v[180:183],v[68:71]
dr a[92:95],v232 offset:60416
cv v171,v174,v175
mm v[68:71],a[68:71],v[184:187],v[68:71]
dr a[96:99],v232 offset:53248
mm v[4:7],a[68:71],v[168:171],v[4:7]
dr a[100:103],v232 offset:61440
mm v[4:7],a[64:67],v[164:167],v[4:7]
dr a[104:107],v232 offset:54272
mm v[12:15],a[72:75],v[164:167],v[12:15]
bl v232,s[8:11],s41 offen offset:1024 sc1 lds
mm v[12:15],a[76:79],v[168:171],v[12:15]
s_add_u32 m0,s33,49152
dr a[108:111],v232 offset:62464
s_add_u32 s41,s39,s59
mm v[76:79],a[76:79],v[184:187],v[76:79]
dr a[112:115],v232 offset:55296
mm v[76:79],a[72:75],v[180:183],v[76:79]
dr a[116:119],v232 offset:63488
mm v[84:87],a[80:83],v[180:183],v[84:87]
s_waitcnt lgkmcnt(5)
dr a[120:123],v232 offset:56320
mm v[84:87],a[84:87],v[184:187],v[84:87]
)" R"(dr a[124:127],v232 offset:64512
mm v[20:23],a[84:87],v[168:171],v[20:23]
ex2 v150,v150
mm v[20:23],a[80:83],v[164:167],v[20:23]
ex2 v151,v151
mm v[28:31],a[88:91],v[164:167],v[28:31]
ex2 v148,v148
mm v[28:31],a[92:95],v[168:171],v[28:31]
ex2 v149,v149
mm v[92:95],a[92:95],v[184:187],v[92:95]
s_waitcnt vmcnt(11) lgkmcnt(0)
s_barrier
mm v[92:95],a[88:91],v[180:183],v[92:95]
bl v232,s[8:11],s40 offen offset:0 sc1 lds
v_smfmac_f32_16x16x64_bf16 a[128:131],a[144:147],v[164:171],v246
dr a[64:67],v235 offset:0
cv v151,v150,v151
v_smfmac_f32_16x16x64_bf16 a[132:135],a[144:147],v[180:187],v246
dr a[68:71],v235 offset:1024
cv v150,v148,v149
mm v[100:103],a[96:99],v[180:183],v[100:103]
ex2 v144,v144
mm v[100:103],a[100:103],v[184:187],v[100:103]
bl v232,s[8:11],s40 offen offset:1024 sc1 lds
mm v[36:39],a[100:103],v[168:171],v[36:39]
dr a[72:75],v235 offset:2048
mm v[36:39],a[96:99],v[164:167],v[36:39]
dr a[76:79],v235 offset:3072
mm v[44:47],a[104:107],v[164:167],v[44:47]
ex2 v145,v145
mm v[44:47],a[108:111],v[168:171],v[44:47]
bl v232,s[8:11],s40 offen offset:2048 sc1 lds
mm v[108:111],a[108:111],v[184:187],v[108:111]
dr a[80:83],v235 offset:4096
cv v148,v144,v145
mm v[108:111],a[104:107],v[180:183],v[108:111]
dr a[84:87],v235 offset:5120
mm v[116:119],a[112:115],v[180:183],v[116:119]
ex2 v146,v146
mm v[116:119],a[116:119],v[184:187],v[116:119]
bl v232,s[8:11],s40 offen offset:3072 sc1 lds
mm v[52:55],a[116:119],v[168:171],v[52:55]
s_add_u32 m0,s34,98304
dr a[88:91],v235 offset:6144
s_add_u32 s40,s31,32768
mm v[52:55],a[112:115],v[164:167],v[52:55]
dr a[92:95],v235 offset:7168
mm v[60:63],a[120:123],v[164:167],v[60:63]
ex2 v147,v147
mm v[60:63],a[124:127],v[168:171],v[60:63]
bs a[180:183],v232,s[8:11],s41 offen offset:0 sc1
dr a[96:99],v235 offset:8192
s_add_u32 s41,s35,65536
cv v149,v146,v147
ex2 v152,v152
mm v[124:127],a[124:127],v[184:187],v[124:127]
dr a[100:103],v235 offset:9216
mm v[124:127],a[120:123],v[180:183],v[124:127]
s_waitcnt lgkmcnt(4)
ex2 v153,v153
mm v[160:163],a[64:67],v[0:3],v[192:195]
dr a[104:107],v235 offset:10240
cv v152,v152,v153
mm v[160:163],a[68:71],v[8:11],v[160:163]
)" R"(dr a[108:111],v235 offset:11264
mm v[160:163],a[72:75],v[16:19],v[160:163]
bl v232,s[8:11],s41 offen offset:0 sc1 lds
mm v[160:163],a[76:79],v[24:27],v[160:163]
s_add_u32 m0,s33,131072
dr a[112:115],v235 offset:12288
s_add_u32 s41,s35,65536
mm v[176:179],a[76:79],v[56:59],v[196:199]
dr a[116:119],v235 offset:13312
mm v[176:179],a[72:75],v[48:51],v[176:179]
dr a[120:123],v235 offset:14336
mm v[176:179],a[68:71],v[40:43],v[176:179]
dr a[124:127],v235 offset:15360
mm v[176:179],a[64:67],v[32:35],v[176:179]
ex2 v154,v154
mm v[180:183],a[80:83],v[32:35],v[196:199]
ex2 v155,v155
mm v[180:183],a[84:87],v[40:43],v[180:183]
s_waitcnt lgkmcnt(4)
ex2 v156,v156
mm v[180:183],a[88:91],v[48:51],v[180:183]
ex2 v157,v157
mm v[180:183],a[92:95],v[56:59],v[180:183]
ex2 v158,v158
mm v[164:167],a[92:95],v[24:27],v[192:195]
s_waitcnt vmcnt(13) lgkmcnt(0)
s_barrier
mm v[164:167],a[88:91],v[16:19],v[164:167]
bl v232,s[8:11],s40 offen offset:0 sc1 lds
mm v[164:167],a[84:87],v[8:11],v[164:167]
dr a[64:67],v235 offset:16384
cv v153,v154,v155
mm v[164:167],a[80:83],v[0:3],v[164:167]
dr a[68:71],v235 offset:24576
cv v154,v156,v157
mm v[168:171],a[96:99],v[0:3],v[192:195]
ex2 v159,v159
mm v[168:171],a[100:103],v[8:11],v[168:171]
bl v232,s[8:11],s40 offen offset:1024 sc1 lds
mm v[168:171],a[104:107],v[16:19],v[168:171]
dr a[72:75],v235 offset:17408
cv v155,v158,v159
mm v[168:171],a[108:111],v[24:27],v[168:171]
dr a[76:79],v235 offset:25600
mm v[184:187],a[108:111],v[56:59],v[196:199]
ex2 v136,v136
mm v[184:187],a[104:107],v[48:51],v[184:187]
bl v232,s[8:11],s40 offen offset:2048 sc1 lds
mm v[184:187],a[100:103],v[40:43],v[184:187]
dr a[80:83],v235 offset:18432
mm v[184:187],a[96:99],v[32:35],v[184:187]
ex2 v137,v137
mm v[188:191],a[112:115],v[32:35],v[196:199]
ex2 v138,v138
mm v[188:191],a[116:119],v[40:43],v[188:191]
bl v232,s[8:11],s40 offen offset:3072 sc1 lds
mm v[188:191],a[120:123],v[48:51],v[188:191]
s_add_u32 m0,s34,98304
ex2 v139,v139
mm v[188:191],a[124:127],v[56:59],v[188:191]
s_add_u32 s40,s31,8192
ex2 v140,v140
mm v[172:175],a[124:127],v[24:27],v[192:195]
ex2 v141,v141
)" R"(mm v[172:175],a[120:123],v[16:19],v[172:175]
ex2 v142,v142
mm v[172:175],a[116:119],v[8:11],v[172:175]
ex2 v143,v143
mm v[172:175],a[112:115],v[0:3],v[172:175]
s_waitcnt lgkmcnt(0)
dr a[84:87],v235 offset:26624
cv v136,v136,v137
bl v232,s[8:11],s41 offen offset:1024 sc1 lds
mm v[68:71],a[64:67],v[148:151],v[68:71]
s_add_u32 m0,s33,147456
dr a[88:91],v235 offset:19456
cv v137,v138,v139
ex2 v134,v134
ex2 v135,v135
ex2 v132,v132
ex2 v133,v133
ex2 v128,v128
ex2 v129,v129
ex2 v130,v130
ex2 v131,v131
cv v138,v140,v141
cv v139,v142,v143
mm v[68:71],a[68:71],v[152:155],v[68:71]
dr a[92:95],v235 offset:27648
cv v135,v134,v135
cv v134,v132,v133
cv v132,v128,v129
cv v133,v130,v131
mm v[4:7],a[68:71],v[136:139],v[4:7]
dr a[96:99],v235 offset:20480
mm v[4:7],a[64:67],v[132:135],v[4:7]
dr a[100:103],v235 offset:28672
mm v[12:15],a[72:75],v[132:135],v[12:15]
dr a[104:107],v235 offset:21504
mm v[12:15],a[76:79],v[136:139],v[12:15]
dr a[108:111],v235 offset:29696
mm v[76:79],a[76:79],v[152:155],v[76:79]
dr a[112:115],v235 offset:22528
mm v[76:79],a[72:75],v[148:151],v[76:79]
dr a[116:119],v235 offset:30720
mm v[84:87],a[80:83],v[148:151],v[84:87]
s_waitcnt lgkmcnt(6)
dr a[120:123],v235 offset:23552
mm v[84:87],a[84:87],v[152:155],v[84:87]
dr a[124:127],v235 offset:31744
mm v[20:23],a[84:87],v[136:139],v[20:23]
ex2 v182,v182
mm v[20:23],a[80:83],v[132:135],v[20:23]
ex2 v183,v183
mm v[28:31],a[88:91],v[132:135],v[28:31]
ex2 v180,v180
mm v[28:31],a[92:95],v[136:139],v[28:31]
ex2 v181,v181
mm v[92:95],a[92:95],v[152:155],v[92:95]
s_waitcnt vmcnt(12) lgkmcnt(0)
s_barrier
mm v[92:95],a[88:91],v[148:151],v[92:95]
bl v232,s[8:11],s40 offen offset:0 sc1 lds
v_smfmac_f32_16x16x64_bf16 a[128:131],a[144:147],v[132:139],v246
dr a[64:67],v232 offset:0
cv v183,v182,v183
v_smfmac_f32_16x16x64_bf16 a[132:135],a[144:147],v[148:155],v246
dr a[68:71],v232 offset:1024
)" R"(cv v182,v180,v181
mm v[100:103],a[96:99],v[148:151],v[100:103]
ex2 v176,v176
mm v[100:103],a[100:103],v[152:155],v[100:103]
bl v232,s[8:11],s40 offen offset:1024 sc1 lds
mm v[36:39],a[100:103],v[136:139],v[36:39]
dr a[72:75],v232 offset:2048
mm v[36:39],a[96:99],v[132:135],v[36:39]
dr a[76:79],v232 offset:3072
mm v[44:47],a[104:107],v[132:135],v[44:47]
ex2 v177,v177
mm v[44:47],a[108:111],v[136:139],v[44:47]
bl v232,s[8:11],s40 offen offset:2048 sc1 lds
mm v[108:111],a[108:111],v[152:155],v[108:111]
dr a[80:83],v232 offset:4096
cv v180,v176,v177
mm v[108:111],a[104:107],v[148:151],v[108:111]
dr a[84:87],v232 offset:5120
mm v[116:119],a[112:115],v[148:151],v[116:119]
ex2 v178,v178
mm v[116:119],a[116:119],v[152:155],v[116:119]
s_mov_b32 s30,s31
dr a[88:91],v232 offset:6144
s_add_u32 s31,s31,65536
mm v[52:55],a[116:119],v[136:139],v[52:55]
dr a[92:95],v232 offset:7168
mm v[52:55],a[112:115],v[132:135],v[52:55]
bl v232,s[8:11],s40 offen offset:3072 sc1 lds
mm v[60:63],a[120:123],v[132:135],v[60:63]
dr a[96:99],v232 offset:8192
mm v[60:63],a[124:127],v[136:139],v[60:63]
dr a[100:103],v232 offset:9216
mm v[124:127],a[124:127],v[152:155],v[124:127]
ex2 v179,v179
dr a[104:107],v232 offset:10240
ex2 v166,v166
ex2 v167,v167
ex2 v164,v164
ex2 v165,v165
ex2 v160,v160
ex2 v161,v161
ex2 v162,v162
ex2 v163,v163
mm v[124:127],a[120:123],v[148:151],v[124:127]
dr a[108:111],v232 offset:11264
cv v181,v178,v179
cv v167,v166,v167
cv v166,v164,v165
cv v164,v160,v161
cv v165,v162,v163
s_cmp_eq_u32 s26,6
s_cbranch_scc1 D
B6:
s_waitcnt lgkmcnt(6)
mm v[128:131],a[64:67],v[0:3],v[192:195]
s_add_u32 s41,s39,s59
dr a[112:115],v232 offset:12288
s_add_u32 m0,s34,106496
mm v[128:131],a[68:71],v[8:11],v[128:131]
s_mov_b32 s2,EF_0-EF_G
dr a[116:119],v232 offset:13312
s_getpc_b64 s[0:1]
EF_G:
mm v[128:131],a[72:75],v[16:19],v[128:131]
)" R"(s_cmp_eq_u32 s24,1
s_cselect_b32 s2,EF_1-EF_G,s2
s_add_u32 s40,s30,40960
mm v[128:131],a[76:79],v[24:27],v[128:131]
s_cmp_eq_u32 s24,2
s_cselect_b32 s2,EF_2-EF_G,s2
dr a[120:123],v232 offset:14336
mm v[144:147],a[76:79],v[56:59],v[196:199]
s_cmp_eq_u32 s24,3
s_cselect_b32 s2,EF_3-EF_G,s2
dr a[124:127],v232 offset:15360
bs a[184:187],v232,s[8:11],s41 offen offset:1024 sc1
s_add_u32 s41,s35,73728
s_add_u32 s0,s0,s2
s_addc_u32 s1,s1,0
ex2 v184,v184
mm v[144:147],a[72:75],v[48:51],v[144:147]
ex2 v185,v185
mm v[144:147],a[68:71],v[40:43],v[144:147]
ex2 v186,v186
mm v[144:147],a[64:67],v[32:35],v[144:147]
ex2 v187,v187
mm v[148:151],a[80:83],v[32:35],v[196:199]
bl v232,s[8:11],s41 offen offset:0 sc1 lds
mm v[148:151],a[84:87],v[40:43],v[148:151]
s_waitcnt lgkmcnt(2)
ex2 v188,v188
mm v[148:151],a[88:91],v[48:51],v[148:151]
s_add_u32 m0,s33,0
ex2 v189,v189
mm v[148:151],a[92:95],v[56:59],v[148:151]
s_add_u32 s41,s39,s59
ex2 v190,v190
s_mov_b64 exec,0
s_setpc_b64 s[0:1]
EF_R:
s_mov_b64 exec,-1
mm v[132:135],a[92:95],v[24:27],v[192:195]
s_waitcnt vmcnt(10) lgkmcnt(0)
s_barrier
mm v[132:135],a[88:91],v[16:19],v[132:135]
bl v232,s[8:11],s31 offen offset:0 sc1 lds
mm v[132:135],a[84:87],v[8:11],v[132:135]
dr a[64:67],v232 offset:49152
cv v184,v184,v185
mm v[132:135],a[80:83],v[0:3],v[132:135]
dr a[68:71],v232 offset:57344
cv v185,v186,v187
mm v[136:139],a[96:99],v[0:3],v[192:195]
ex2 v191,v191
mm v[136:139],a[100:103],v[8:11],v[136:139]
bl v232,s[8:11],s31 offen offset:1024 sc1 lds
mm v[136:139],a[104:107],v[16:19],v[136:139]
dr a[72:75],v232 offset:50176
cv v186,v188,v189
mm v[136:139],a[108:111],v[24:27],v[136:139]
dr a[76:79],v232 offset:58368
cv v187,v190,v191
mm v[152:155],a[108:111],v[56:59],v[196:199]
ex2 v168,v168
mm v[152:155],a[104:107],v[48:51],v[152:155]
bl v232,s[8:11],s31 offen offset:2048 sc1 lds
mm v[152:155],a[100:103],v[40:43],v[152:155]
dr a[80:83],v232 offset:51200
)" R"(mm v[152:155],a[96:99],v[32:35],v[152:155]
ex2 v169,v169
mm v[156:159],a[112:115],v[32:35],v[196:199]
ex2 v170,v170
mm v[156:159],a[116:119],v[40:43],v[156:159]
bl v232,s[8:11],s31 offen offset:3072 sc1 lds
mm v[156:159],a[120:123],v[48:51],v[156:159]
s_add_u32 m0,s34,106496
ex2 v171,v171
mm v[156:159],a[124:127],v[56:59],v[156:159]
ex2 v172,v172
mm v[140:143],a[124:127],v[24:27],v[192:195]
ex2 v173,v173
mm v[140:143],a[120:123],v[16:19],v[140:143]
ex2 v174,v174
mm v[140:143],a[116:119],v[8:11],v[140:143]
ex2 v175,v175
mm v[140:143],a[112:115],v[0:3],v[140:143]
s_waitcnt lgkmcnt(0)
dr a[84:87],v232 offset:59392
cv v168,v168,v169
mm v[68:71],a[64:67],v[180:183],v[68:71]
dr a[88:91],v232 offset:52224
cv v169,v170,v171
cv v170,v172,v173
cv v171,v174,v175
mm v[68:71],a[68:71],v[184:187],v[68:71]
dr a[92:95],v232 offset:60416
mm v[4:7],a[68:71],v[168:171],v[4:7]
dr a[96:99],v232 offset:53248
mm v[4:7],a[64:67],v[164:167],v[4:7]
dr a[100:103],v232 offset:61440
mm v[12:15],a[72:75],v[164:167],v[12:15]
dr a[104:107],v232 offset:54272
mm v[12:15],a[76:79],v[168:171],v[12:15]
dr a[108:111],v232 offset:62464
bs a[188:191],v232,s[8:11],s41 offen offset:2048 sc1
dr a[112:115],v232 offset:55296
s_add_u32 s41,s35,73728
ex2 v150,v150
ex2 v151,v151
mm v[76:79],a[76:79],v[184:187],v[76:79]
dr a[116:119],v232 offset:63488
cv v151,v150,v151
mm v[76:79],a[72:75],v[180:183],v[76:79]
dr a[120:123],v232 offset:56320
mm v[84:87],a[80:83],v[180:183],v[84:87]
s_waitcnt lgkmcnt(7)
dr a[124:127],v232 offset:64512
mm v[84:87],a[84:87],v[184:187],v[84:87]
bl v232,s[8:11],s41 offen offset:1024 sc1 lds
mm v[20:23],a[84:87],v[168:171],v[20:23]
s_add_u32 m0,s33,49152
ex2 v148,v148
mm v[20:23],a[80:83],v[164:167],v[20:23]
s_add_u32 s41,s35,81920
ex2 v149,v149
mm v[28:31],a[88:91],v[164:167],v[28:31]
ex2 v144,v144
mm v[28:31],a[92:95],v[168:171],v[28:31]
ex2 v145,v145
mm v[92:95],a[92:95],v[184:187],v[92:95]
s_waitcnt vmcnt(12) lgkmcnt(0)
s_barrier
)" R"(mm v[92:95],a[88:91],v[180:183],v[92:95]
bl v232,s[8:11],s40 offen offset:0 sc1 lds
v_smfmac_f32_16x16x64_bf16 a[128:131],a[144:147],v[164:171],v246
dr a[64:67],v235 offset:0
cv v150,v148,v149
v_smfmac_f32_16x16x64_bf16 a[132:135],a[144:147],v[180:187],v246
dr a[68:71],v235 offset:1024
cv v148,v144,v145
mm v[100:103],a[96:99],v[180:183],v[100:103]
ex2 v146,v146
mm v[100:103],a[100:103],v[184:187],v[100:103]
bl v232,s[8:11],s40 offen offset:1024 sc1 lds
mm v[36:39],a[100:103],v[168:171],v[36:39]
dr a[72:75],v235 offset:2048
mm v[36:39],a[96:99],v[164:167],v[36:39]
dr a[76:79],v235 offset:3072
mm v[44:47],a[104:107],v[164:167],v[44:47]
ex2 v147,v147
mm v[44:47],a[108:111],v[168:171],v[44:47]
bl v232,s[8:11],s40 offen offset:2048 sc1 lds
mm v[108:111],a[108:111],v[184:187],v[108:111]
dr a[80:83],v235 offset:4096
cv v149,v146,v147
mm v[108:111],a[104:107],v[180:183],v[108:111]
dr a[84:87],v235 offset:5120
mm v[116:119],a[112:115],v[180:183],v[116:119]
ex2 v152,v152
mm v[116:119],a[116:119],v[184:187],v[116:119]
bl v232,s[8:11],s40 offen offset:3072 sc1 lds
mm v[52:55],a[116:119],v[168:171],v[52:55]
s_add_u32 m0,s34,114688
dr a[88:91],v235 offset:6144
s_add_u32 s40,s31,32768
mm v[52:55],a[112:115],v[164:167],v[52:55]
dr a[92:95],v235 offset:7168
mm v[60:63],a[120:123],v[164:167],v[60:63]
ex2 v153,v153
mm v[60:63],a[124:127],v[168:171],v[60:63]
bl v232,s[8:11],s41 offen offset:0 sc1 lds
mm v[124:127],a[124:127],v[184:187],v[124:127]
s_add_u32 s41,s39,s59
dr a[96:99],v235 offset:8192
s_add_u32 m0,s33,131072
mm v[124:127],a[120:123],v[180:183],v[124:127]
s_waitcnt lgkmcnt(3)
dr a[100:103],v235 offset:9216
cv v152,v152,v153
mm v[160:163],a[64:67],v[0:3],v[192:195]
ex2 v154,v154
mm v[160:163],a[68:71],v[8:11],v[160:163]
dr a[104:107],v235 offset:10240
mm v[160:163],a[72:75],v[16:19],v[160:163]
dr a[108:111],v235 offset:11264
mm v[160:163],a[76:79],v[24:27],v[160:163]
dr a[112:115],v235 offset:12288
mm v[176:179],a[76:79],v[56:59],v[196:199]
bs a[192:195],v232,s[8:11],s41 offen offset:3072 sc1
dr a[116:119],v235 offset:13312
ex2 v155,v155
ex2 v156,v156
cv v153,v154,v155
mm v[176:179],a[72:75],v[48:51],v[176:179]
EB_14:
s_cbranch_execz EB_R
)" R"(s_add_u32 s41,s35,81920
dr a[120:123],v235 offset:14336
mm v[176:179],a[68:71],v[40:43],v[176:179]
dr a[124:127],v235 offset:15360
mm v[176:179],a[64:67],v[32:35],v[176:179]
ex2 v157,v157
mm v[180:183],a[80:83],v[32:35],v[196:199]
ex2 v158,v158
mm v[180:183],a[84:87],v[40:43],v[180:183]
s_waitcnt lgkmcnt(4)
ex2 v159,v159
mm v[180:183],a[88:91],v[48:51],v[180:183]
ex2 v136,v136
mm v[180:183],a[92:95],v[56:59],v[180:183]
ex2 v137,v137
mm v[164:167],a[92:95],v[24:27],v[192:195]
s_waitcnt vmcnt(14) lgkmcnt(0)
s_barrier
mm v[164:167],a[88:91],v[16:19],v[164:167]
bl v232,s[8:11],s40 offen offset:0 sc1 lds
mm v[164:167],a[84:87],v[8:11],v[164:167]
dr a[64:67],v235 offset:16384
cv v154,v156,v157
mm v[164:167],a[80:83],v[0:3],v[164:167]
dr a[68:71],v235 offset:24576
cv v155,v158,v159
mm v[168:171],a[96:99],v[0:3],v[192:195]
ex2 v138,v138
mm v[168:171],a[100:103],v[8:11],v[168:171]
bl v232,s[8:11],s40 offen offset:1024 sc1 lds
mm v[168:171],a[104:107],v[16:19],v[168:171]
dr a[72:75],v235 offset:17408
cv v136,v136,v137
mm v[168:171],a[108:111],v[24:27],v[168:171]
dr a[76:79],v235 offset:25600
mm v[184:187],a[108:111],v[56:59],v[196:199]
ex2 v139,v139
mm v[184:187],a[104:107],v[48:51],v[184:187]
bl v232,s[8:11],s40 offen offset:2048 sc1 lds
mm v[184:187],a[100:103],v[40:43],v[184:187]
dr a[80:83],v235 offset:18432
cv v137,v138,v139
mm v[184:187],a[96:99],v[32:35],v[184:187]
ex2 v140,v140
mm v[188:191],a[112:115],v[32:35],v[196:199]
ex2 v141,v141
mm v[188:191],a[116:119],v[40:43],v[188:191]
bl v232,s[8:11],s40 offen offset:3072 sc1 lds
mm v[188:191],a[120:123],v[48:51],v[188:191]
s_add_u32 m0,s34,114688
ex2 v142,v142
mm v[188:191],a[124:127],v[56:59],v[188:191]
s_add_u32 s40,s31,8192
ex2 v143,v143
mm v[172:175],a[124:127],v[24:27],v[192:195]
dr a[84:87],v235 offset:26624
cv v138,v140,v141
mm v[172:175],a[120:123],v[16:19],v[172:175]
bl v232,s[8:11],s41 offen offset:1024 sc1 lds
mm v[172:175],a[116:119],v[8:11],v[172:175]
s_add_u32 m0,s33,147456
dr a[88:91],v235 offset:19456
cv v139,v142,v143
mm v[172:175],a[112:115],v[0:3],v[172:175]
)" R"(s_waitcnt lgkmcnt(2)
ex2 v134,v134
mm v[68:71],a[64:67],v[148:151],v[68:71]
s_lshr_b32 s46,s39,15
ex2 v135,v135
ex2 v132,v132
ex2 v133,v133
ex2 v128,v128
ex2 v129,v129
ex2 v130,v130
ex2 v131,v131
mm v[68:71],a[68:71],v[152:155],v[68:71]
s_lshl_b32 s46,s46,2
dr a[92:95],v235 offset:27648
cv v135,v134,v135
cv v134,v132,v133
cv v132,v128,v129
cv v133,v130,v131
mm v[4:7],a[68:71],v[136:139],v[4:7]
s_add_u32 s46,s46,s24
dr a[96:99],v235 offset:20480
s_lshl_b32 s46,s46,3
mm v[4:7],a[64:67],v[132:135],v[4:7]
s_add_u32 s46,s46,0x4018000
dr a[100:103],v235 offset:28672
mm v[12:15],a[72:75],v[132:135],v[12:15]
dr a[104:107],v235 offset:21504
mm v[12:15],a[76:79],v[136:139],v[12:15]
dr a[108:111],v235 offset:29696
mm v[76:79],a[76:79],v[152:155],v[76:79]
dr a[112:115],v235 offset:22528
mm v[76:79],a[72:75],v[148:151],v[76:79]
dr a[116:119],v235 offset:30720
mm v[84:87],a[80:83],v[148:151],v[84:87]
s_waitcnt lgkmcnt(6)
dr a[120:123],v235 offset:23552
mm v[84:87],a[84:87],v[152:155],v[84:87]
dr a[124:127],v235 offset:31744
mm v[20:23],a[84:87],v[136:139],v[20:23]
ex2 v182,v182
mm v[20:23],a[80:83],v[132:135],v[20:23]
ex2 v183,v183
mm v[28:31],a[88:91],v[132:135],v[28:31]
ex2 v180,v180
mm v[28:31],a[92:95],v[136:139],v[28:31]
ex2 v181,v181
mm v[92:95],a[92:95],v[152:155],v[92:95]
s_waitcnt vmcnt(13) lgkmcnt(0)
s_barrier
mm v[92:95],a[88:91],v[148:151],v[92:95]
bl v232,s[8:11],s40 offen offset:0 sc1 lds
v_smfmac_f32_16x16x64_bf16 a[128:131],a[144:147],v[132:139],v246
dr a[64:67],v232 offset:0
cv v183,v182,v183
v_smfmac_f32_16x16x64_bf16 a[132:135],a[144:147],v[148:155],v246
dr a[68:71],v232 offset:1024
cv v182,v180,v181
mm v[100:103],a[96:99],v[148:151],v[100:103]
ex2 v176,v176
mm v[100:103],a[100:103],v[152:155],v[100:103]
bl v232,s[8:11],s40 offen offset:1024 sc1 lds
mm v[36:39],a[100:103],v[136:139],v[36:39]
dr a[72:75],v232 offset:2048
mm v[36:39],a[96:99],v[132:135],v[36:39]
)" R"(dr a[76:79],v232 offset:3072
mm v[44:47],a[104:107],v[132:135],v[44:47]
ex2 v177,v177
mm v[44:47],a[108:111],v[136:139],v[44:47]
bl v232,s[8:11],s40 offen offset:2048 sc1 lds
mm v[108:111],a[108:111],v[152:155],v[108:111]
dr a[80:83],v232 offset:4096
cv v180,v176,v177
mm v[108:111],a[104:107],v[148:151],v[108:111]
dr a[84:87],v232 offset:5120
mm v[116:119],a[112:115],v[148:151],v[116:119]
ex2 v178,v178
mm v[116:119],a[116:119],v[152:155],v[116:119]
s_mov_b32 s30,s31
dr a[88:91],v232 offset:6144
s_add_u32 s31,s31,65536
mm v[52:55],a[116:119],v[136:139],v[52:55]
dr a[92:95],v232 offset:7168
mm v[52:55],a[112:115],v[132:135],v[52:55]
bl v232,s[8:11],s40 offen offset:3072 sc1 lds
mm v[60:63],a[120:123],v[132:135],v[60:63]
dr a[96:99],v232 offset:8192
mm v[60:63],a[124:127],v[136:139],v[60:63]
dr a[100:103],v232 offset:9216
mm v[124:127],a[124:127],v[152:155],v[124:127]
ex2 v179,v179
dr a[104:107],v232 offset:10240
ex2 v166,v166
ex2 v167,v167
ex2 v164,v164
ex2 v165,v165
ex2 v160,v160
ex2 v161,v161
ex2 v162,v162
ex2 v163,v163
mm v[124:127],a[120:123],v[148:151],v[124:127]
dr a[108:111],v232 offset:11264
cv v181,v178,v179
s_waitcnt vmcnt(9)
buffer_store_dwordx2 v[252:253],v254,s[8:11],s46 offen sc1
cv v167,v166,v167
cv v166,v164,v165
cv v164,v160,v161
cv v165,v162,v163
D:
EA_D:
s_cbranch_execz EA_R
EB_D:
s_cbranch_execz EB_R
s_waitcnt lgkmcnt(6)
mm v[128:131],a[64:67],v[0:3],v[208:211]
s_add_u32 s40,s30,40960
dr a[112:115],v232 offset:12288
s_add_u32 m0,s33,49152
mm v[128:131],a[68:71],v[8:11],v[128:131]
s_mov_b32 s31,s32
dr a[116:119],v232 offset:13312
mm v[128:131],a[72:75],v[16:19],v[128:131]
ex2 v184,v184
mm v[128:131],a[76:79],v[24:27],v[128:131]
dr a[120:123],v232 offset:14336
mm v[144:147],a[76:79],v[56:59],v[196:199]
dr a[124:127],v232 offset:15360
mm v[144:147],a[72:75],v[48:51],v[144:147]
)" R"(ex2 v185,v185
mm v[144:147],a[68:71],v[40:43],v[144:147]
ex2 v186,v186
mm v[144:147],a[64:67],v[32:35],v[144:147]
ex2 v187,v187
mm v[148:151],a[80:83],v[32:35],v[196:199]
ex2 v188,v188
mm v[148:151],a[84:87],v[40:43],v[148:151]
s_waitcnt lgkmcnt(2)
ex2 v189,v189
mm v[148:151],a[88:91],v[48:51],v[148:151]
ex2 v190,v190
mm v[148:151],a[92:95],v[56:59],v[148:151]
ex2 v191,v191
mm v[132:135],a[92:95],v[24:27],v[212:215]
s_waitcnt vmcnt(8) lgkmcnt(0)
s_barrier
mm v[132:135],a[88:91],v[16:19],v[132:135]
dr a[64:67],v232 offset:49152
cv v184,v184,v185
mm v[132:135],a[84:87],v[8:11],v[132:135]
dr a[68:71],v232 offset:57344
cv v185,v186,v187
mm v[132:135],a[80:83],v[0:3],v[132:135]
ex2 v168,v168
mm v[136:139],a[96:99],v[0:3],v[216:219]
dr a[72:75],v232 offset:50176
cv v186,v188,v189
mm v[136:139],a[100:103],v[8:11],v[136:139]
dr a[76:79],v232 offset:58368
cv v187,v190,v191
mm v[136:139],a[104:107],v[16:19],v[136:139]
ex2 v169,v169
mm v[136:139],a[108:111],v[24:27],v[136:139]
dr a[80:83],v232 offset:51200
cv v168,v168,v169
mm v[152:155],a[108:111],v[56:59],v[196:199]
ex2 v170,v170
mm v[152:155],a[104:107],v[48:51],v[152:155]
ex2 v171,v171
mm v[152:155],a[100:103],v[40:43],v[152:155]
ex2 v172,v172
mm v[152:155],a[96:99],v[32:35],v[152:155]
ex2 v173,v173
mm v[156:159],a[112:115],v[32:35],v[196:199]
ex2 v174,v174
mm v[156:159],a[116:119],v[40:43],v[156:159]
ex2 v175,v175
mm v[156:159],a[120:123],v[48:51],v[156:159]
dr a[84:87],v232 offset:59392
cv v169,v170,v171
mm v[156:159],a[124:127],v[56:59],v[156:159]
dr a[88:91],v232 offset:52224
cv v170,v172,v173
mm v[140:143],a[124:127],v[24:27],v[220:223]
ex2 v150,v150
mm v[140:143],a[120:123],v[16:19],v[140:143]
dr a[92:95],v232 offset:60416
cv v171,v174,v175
mm v[140:143],a[116:119],v[8:11],v[140:143]
dr a[96:99],v232 offset:53248
mm v[140:143],a[112:115],v[0:3],v[140:143]
s_waitcnt lgkmcnt(4)
ex2 v151,v151
)" R"(mm v[68:71],a[64:67],v[180:183],v[68:71]
s_bfe_i64 s[48:49],s[36:37],0x10008
dr a[100:103],v232 offset:61440
s_bfe_i64 s[50:51],s[36:37],0x1000c
mm v[68:71],a[68:71],v[184:187],v[68:71]
s_and_b64 s[52:53],s[12:13],s[48:49]
dr a[104:107],v232 offset:54272
s_or_b64 s[52:53],s[52:53],s[50:51]
mm v[4:7],a[68:71],v[168:171],v[4:7]
s_and_b64 s[86:87],s[14:15],s[48:49]
ex2 v148,v148
mm v[4:7],a[64:67],v[164:167],v[4:7]
s_or_b64 s[86:87],s[86:87],s[50:51]
dr a[108:111],v232 offset:62464
v_cndmask_b32_e64 v208,v236,v196,s[52:53]
mm v[12:15],a[72:75],v[164:167],v[12:15]
s_and_b64 s[52:53],s[16:17],s[48:49]
dr a[112:115],v232 offset:55296
v_cndmask_b32_e64 v209,v236,v196,s[86:87]
mm v[12:15],a[76:79],v[168:171],v[12:15]
s_or_b64 s[52:53],s[52:53],s[50:51]
ex2 v149,v149
mm v[76:79],a[76:79],v[184:187],v[76:79]
s_and_b64 s[86:87],s[18:19],s[48:49]
dr a[116:119],v232 offset:63488
v_cndmask_b32_e64 v210,v236,v196,s[52:53]
mm v[76:79],a[72:75],v[180:183],v[76:79]
s_or_b64 s[86:87],s[86:87],s[50:51]
dr a[120:123],v232 offset:56320
v_cndmask_b32_e64 v211,v236,v196,s[86:87]
mm v[84:87],a[80:83],v[180:183],v[84:87]
s_waitcnt lgkmcnt(4)
dr a[124:127],v232 offset:64512
s_bfe_i64 s[48:49],s[36:37],0x10009
mm v[84:87],a[84:87],v[184:187],v[84:87]
s_bfe_i64 s[50:51],s[36:37],0x1000d
ex2 v144,v144
mm v[20:23],a[84:87],v[168:171],v[20:23]
s_and_b64 s[52:53],s[12:13],s[48:49]
ex2 v145,v145
mm v[20:23],a[80:83],v[164:167],v[20:23]
s_or_b64 s[52:53],s[52:53],s[50:51]
ex2 v146,v146
mm v[28:31],a[88:91],v[164:167],v[28:31]
s_and_b64 s[86:87],s[14:15],s[48:49]
ex2 v147,v147
mm v[28:31],a[92:95],v[168:171],v[28:31]
s_or_b64 s[86:87],s[86:87],s[50:51]
ex2 v152,v152
mm v[92:95],a[92:95],v[184:187],v[92:95]
s_waitcnt vmcnt(4) lgkmcnt(0)
s_barrier
mm v[92:95],a[88:91],v[180:183],v[92:95]
bl v232,s[8:11],s40 offen offset:0 sc1 lds
v_smfmac_f32_16x16x64_bf16 a[128:131],a[144:147],v[164:171],v246
dr a[64:67],v235 offset:0
v_cndmask_b32_e64 v212,v236,v196,s[52:53]
v_smfmac_f32_16x16x64_bf16 a[132:135],a[144:147],v[180:187],v246
s_and_b64 s[52:53],s[16:17],s[48:49]
dr a[68:71],v235 offset:1024
v_cndmask_b32_e64 v213,v236,v196,s[86:87]
mm v[100:103],a[96:99],v[180:183],v[100:103]
s_or_b64 s[52:53],s[52:53],s[50:51]
ex2 v153,v153
)" R"(mm v[100:103],a[100:103],v[184:187],v[100:103]
s_and_b64 s[86:87],s[18:19],s[48:49]
bl v232,s[8:11],s40 offen offset:1024 sc1 lds
mm v[36:39],a[100:103],v[168:171],v[36:39]
s_or_b64 s[86:87],s[86:87],s[50:51]
dr a[72:75],v235 offset:2048
v_cndmask_b32_e64 v214,v236,v196,s[52:53]
mm v[36:39],a[96:99],v[164:167],v[36:39]
s_bfe_i64 s[48:49],s[36:37],0x1000a
dr a[76:79],v235 offset:3072
v_cndmask_b32_e64 v215,v236,v196,s[86:87]
mm v[44:47],a[104:107],v[164:167],v[44:47]
s_bfe_i64 s[50:51],s[36:37],0x1000e
ex2 v154,v154
mm v[44:47],a[108:111],v[168:171],v[44:47]
s_and_b64 s[52:53],s[12:13],s[48:49]
dr a[80:83],v235 offset:4096
s_or_b64 s[52:53],s[52:53],s[50:51]
mm v[108:111],a[108:111],v[184:187],v[108:111]
s_and_b64 s[86:87],s[14:15],s[48:49]
dr a[84:87],v235 offset:5120
v_cndmask_b32_e64 v216,v236,v196,s[52:53]
mm v[108:111],a[104:107],v[180:183],v[108:111]
s_or_b64 s[86:87],s[86:87],s[50:51]
bl v232,s[8:11],s40 offen offset:2048 sc1 lds
mm v[116:119],a[112:115],v[180:183],v[116:119]
s_and_b64 s[52:53],s[16:17],s[48:49]
dr a[88:91],v235 offset:6144
v_cndmask_b32_e64 v217,v236,v196,s[86:87]
mm v[116:119],a[116:119],v[184:187],v[116:119]
s_or_b64 s[52:53],s[52:53],s[50:51]
dr a[92:95],v235 offset:7168
v_cndmask_b32_e64 v218,v236,v196,s[52:53]
mm v[52:55],a[116:119],v[168:171],v[52:55]
s_and_b64 s[86:87],s[18:19],s[48:49]
dr a[96:99],v235 offset:8192
s_or_b64 s[86:87],s[86:87],s[50:51]
mm v[52:55],a[112:115],v[164:167],v[52:55]
s_bfe_i64 s[48:49],s[36:37],0x1000b
dr a[100:103],v235 offset:9216
v_cndmask_b32_e64 v219,v236,v196,s[86:87]
bl v232,s[8:11],s40 offen offset:3072 sc1 lds
mm v[60:63],a[120:123],v[164:167],v[60:63]
s_bfe_i64 s[50:51],s[36:37],0x1000f
dr a[104:107],v235 offset:10240
s_and_b64 s[52:53],s[12:13],s[48:49]
mm v[60:63],a[124:127],v[168:171],v[60:63]
s_or_b64 s[52:53],s[52:53],s[50:51]
dr a[108:111],v235 offset:11264
v_cndmask_b32_e64 v220,v236,v196,s[52:53]
mm v[124:127],a[124:127],v[184:187],v[124:127]
s_and_b64 s[86:87],s[14:15],s[48:49]
dr a[112:115],v235 offset:12288
s_or_b64 s[86:87],s[86:87],s[50:51]
mm v[124:127],a[120:123],v[180:183],v[124:127]
s_waitcnt lgkmcnt(6)
dr a[116:119],v235 offset:13312
v_cndmask_b32_e64 v221,v236,v196,s[86:87]
mm v[176:179],a[64:67],v[32:35],v[208:211]
s_and_b64 s[52:53],s[16:17],s[48:49]
dr a[120:123],v235 offset:14336
s_or_b64 s[52:53],s[52:53],s[50:51]
mm v[176:179],a[68:71],v[40:43],v[176:179]
s_and_b64 s[86:87],s[18:19],s[48:49]
)" R"(dr a[124:127],v235 offset:15360
v_cndmask_b32_e64 v222,v236,v196,s[52:53]
mm v[176:179],a[72:75],v[48:51],v[176:179]
s_or_b64 s[86:87],s[86:87],s[50:51]
ex2 v155,v155
mm v[176:179],a[76:79],v[56:59],v[176:179]
ex2 v156,v156
mm v[180:183],a[80:83],v[32:35],v[212:215]
ex2 v157,v157
mm v[180:183],a[84:87],v[40:43],v[180:183]
ex2 v158,v158
mm v[180:183],a[88:91],v[48:51],v[180:183]
s_waitcnt lgkmcnt(2)
ex2 v159,v159
s_waitcnt vmcnt(4) lgkmcnt(0)
s_barrier
mm v[180:183],a[92:95],v[56:59],v[180:183]
dr a[64:67],v235 offset:16384
v_cndmask_b32_e64 v223,v236,v196,s[86:87]
mm v[184:187],a[96:99],v[32:35],v[216:219]
dr a[68:71],v235 offset:24576
cv v151,v150,v151
mm v[184:187],a[100:103],v[40:43],v[184:187]
ex2 v136,v136
mm v[184:187],a[104:107],v[48:51],v[184:187]
dr a[72:75],v235 offset:17408
cv v150,v148,v149
mm v[184:187],a[108:111],v[56:59],v[184:187]
dr a[76:79],v235 offset:25600
cv v148,v144,v145
mm v[188:191],a[112:115],v[32:35],v[220:223]
cv v149,v146,v147
cv v152,v152,v153
mm v[188:191],a[116:119],v[40:43],v[188:191]
dr a[80:83],v235 offset:18432
cv v153,v154,v155
mm v[188:191],a[120:123],v[48:51],v[188:191]
dr a[84:87],v235 offset:26624
cv v154,v156,v157
mm v[188:191],a[124:127],v[56:59],v[188:191]
s_waitcnt lgkmcnt(4)
dr a[88:91],v235 offset:19456
cv v155,v158,v159
ex2 v137,v137
ex2 v138,v138
ex2 v139,v139
ex2 v140,v140
ex2 v141,v141
ex2 v142,v142
ex2 v143,v143
mm v[68:71],a[64:67],v[148:151],v[68:71]
dr a[92:95],v235 offset:27648
cv v136,v136,v137
ex2 v134,v134
ex2 v135,v135
ex2 v132,v132
ex2 v133,v133
ex2 v128,v128
ex2 v129,v129
ex2 v130,v130
ex2 v131,v131
cv v137,v138,v139
cv v138,v140,v141
cv v139,v142,v143
)" R"(mm v[68:71],a[68:71],v[152:155],v[68:71]
dr a[96:99],v235 offset:20480
cv v135,v134,v135
cv v134,v132,v133
cv v132,v128,v129
cv v133,v130,v131
mm v[4:7],a[68:71],v[136:139],v[4:7]
dr a[100:103],v235 offset:28672
mm v[4:7],a[64:67],v[132:135],v[4:7]
s_waitcnt lgkmcnt(6)
dr a[104:107],v235 offset:21504
mm v[12:15],a[72:75],v[132:135],v[12:15]
dr a[108:111],v235 offset:29696
mm v[12:15],a[76:79],v[136:139],v[12:15]
dr a[112:115],v235 offset:22528
mm v[76:79],a[76:79],v[152:155],v[76:79]
dr a[116:119],v235 offset:30720
mm v[76:79],a[72:75],v[148:151],v[76:79]
s_waitcnt lgkmcnt(6)
dr a[120:123],v235 offset:23552
mm v[84:87],a[80:83],v[148:151],v[84:87]
dr a[124:127],v235 offset:31744
mm v[84:87],a[84:87],v[152:155],v[84:87]
ex2 v182,v182
mm v[20:23],a[84:87],v[136:139],v[20:23]
ex2 v183,v183
mm v[20:23],a[80:83],v[132:135],v[20:23]
ex2 v180,v180
mm v[28:31],a[88:91],v[132:135],v[28:31]
ex2 v181,v181
mm v[28:31],a[92:95],v[136:139],v[28:31]
ex2 v176,v176
mm v[92:95],a[92:95],v[152:155],v[92:95]
s_waitcnt lgkmcnt(0)
s_barrier
mm v[92:95],a[88:91],v[148:151],v[92:95]
dr a[64:67],v232 offset:0
cv v183,v182,v183
v_smfmac_f32_16x16x64_bf16 a[128:131],a[144:147],v[132:139],v246
dr a[68:71],v232 offset:1024
cv v182,v180,v181
v_smfmac_f32_16x16x64_bf16 a[132:135],a[144:147],v[148:155],v246
ex2 v177,v177
mm v[100:103],a[96:99],v[148:151],v[100:103]
dr a[72:75],v232 offset:2048
cv v180,v176,v177
mm v[100:103],a[100:103],v[152:155],v[100:103]
dr a[76:79],v232 offset:3072
mm v[36:39],a[100:103],v[136:139],v[36:39]
ex2 v178,v178
mm v[36:39],a[96:99],v[132:135],v[36:39]
dr a[80:83],v232 offset:4096
mm v[44:47],a[104:107],v[132:135],v[44:47]
dr a[84:87],v232 offset:5120
mm v[44:47],a[108:111],v[136:139],v[44:47]
ex2 v179,v179
mm v[108:111],a[108:111],v[152:155],v[108:111]
dr a[88:91],v232 offset:6144
cv v181,v178,v179
mm v[108:111],a[104:107],v[148:151],v[108:111]
dr a[92:95],v232 offset:7168
mm v[116:119],a[112:115],v[148:151],v[116:119]
mm v[116:119],a[116:119],v[152:155],v[116:119]
dr a[96:99],v232 offset:8192
)" R"(mm v[52:55],a[116:119],v[136:139],v[52:55]
dr a[100:103],v232 offset:9216
mm v[52:55],a[112:115],v[132:135],v[52:55]
mm v[60:63],a[120:123],v[132:135],v[60:63]
dr a[104:107],v232 offset:10240
mm v[60:63],a[124:127],v[136:139],v[60:63]
dr a[108:111],v232 offset:11264
mm v[124:127],a[124:127],v[152:155],v[124:127]
mm v[124:127],a[120:123],v[148:151],v[124:127]
T:
EA_T:
s_cbranch_execz EA_R
s_waitcnt lgkmcnt(6)
mm v[128:131],a[64:67],v[64:67],v[200:203]
s_lshl_b32 s43,s26,7
dr a[112:115],v232 offset:12288
s_lshl_b32 s101,s24,5
mm v[128:131],a[68:71],v[72:75],v[128:131]
s_add_u32 s43,s43,s101
dr a[116:119],v232 offset:13312
s_add_u32 s43,s43,s25
mm v[128:131],a[72:75],v[80:83],v[128:131]
s_lshl_b32 s42,s43,13
ard v244,a128
ard v208,a129
v_add_f32 v244,v244,v208
v_cmp_ngt_f32 vcc,0x5f800000,v244
s_cbranch_vccnz FX00
FX00_D:
mm v[128:131],a[76:79],v[88:91],v[128:131]
s_add_u32 s42,s42,0x2000000
dr a[120:123],v232 offset:14336
s_lshl_b32 s43,s43,3
mm v[144:147],a[76:79],v[120:123],v[204:207]
s_add_u32 s43,s43,0x4014000
dr a[124:127],v232 offset:15360
s_add_u32 m0,s33,0
mm v[144:147],a[72:75],v[112:115],v[144:147]
s_mov_b32 s3,s30
v_rcp_f32 v244,v244
mm v[144:147],a[68:71],v[104:107],v[144:147]
ex2 v184,v184
mm v[144:147],a[64:67],v[96:99],v[144:147]
ex2 v185,v185
mm v[148:151],a[80:83],v[96:99],v[204:207]
v_mul_f32 v4,v244,v4
v_mul_f32 v5,v244,v5
mm v[148:151],a[84:87],v[104:107],v[148:151]
s_waitcnt lgkmcnt(2)
v_mul_f32 v6,v244,v6
v_mul_f32 v7,v244,v7
mm v[148:151],a[88:91],v[112:115],v[148:151]
v_mul_f32 v12,v244,v12
v_mul_f32 v13,v244,v13
v_mul_f32 v14,v244,v14
v_mul_f32 v15,v244,v15
cv v0,v4,v5
cv v1,v6,v7
v_and_b32 v0,-2,v0
cv v2,v12,v13
cv v3,v14,v15
bs v[0:3],v232,s[8:11],s42 offen offset:0 sc1
ex2 v186,v186
ex2 v187,v187
)" R"(ex2 v188,v188
mm v[148:151],a[92:95],v[120:123],v[148:151]
ex2 v189,v189
mm v[132:135],a[92:95],v[88:91],v[200:203]
s_waitcnt vmcnt(1) lgkmcnt(0)
s_barrier
mm v[132:135],a[88:91],v[80:83],v[132:135]
bl v232,s[8:11],s31 offen offset:0 sc1 lds
mm v[132:135],a[84:87],v[72:75],v[132:135]
dr a[64:67],v232 offset:49152
cv v184,v184,v185
mm v[132:135],a[80:83],v[64:67],v[132:135]
dr a[68:71],v232 offset:57344
cv v185,v186,v187
mm v[136:139],a[96:99],v[64:67],v[200:203]
ex2 v190,v190
mm v[136:139],a[100:103],v[72:75],v[136:139]
bl v232,s[8:11],s31 offen offset:1024 sc1 lds
mm v[136:139],a[104:107],v[80:83],v[136:139]
dr a[72:75],v232 offset:50176
cv v186,v188,v189
mm v[136:139],a[108:111],v[88:91],v[136:139]
dr a[76:79],v232 offset:58368
v_mul_f32 v20,v244,v20
mm v[152:155],a[108:111],v[120:123],v[204:207]
ex2 v191,v191
mm v[152:155],a[104:107],v[112:115],v[152:155]
dr a[80:83],v232 offset:51200
cv v187,v190,v191
mm v[152:155],a[100:103],v[104:107],v[152:155]
dr a[84:87],v232 offset:59392
v_mul_f32 v21,v244,v21
mm v[152:155],a[96:99],v[96:99],v[152:155]
bl v232,s[8:11],s31 offen offset:2048 sc1 lds
mm v[156:159],a[112:115],v[96:99],v[204:207]
dr a[88:91],v232 offset:52224
v_mul_f32 v22,v244,v22
mm v[156:159],a[116:119],v[104:107],v[156:159]
EA_15:
s_cbranch_execz EA_R
EB_15:
s_cbranch_execz EB_R
dr a[92:95],v232 offset:60416
v_mul_f32 v23,v244,v23
mm v[156:159],a[120:123],v[112:115],v[156:159]
dr a[96:99],v232 offset:53248
v_mul_f32 v28,v244,v28
mm v[156:159],a[124:127],v[120:123],v[156:159]
dr a[100:103],v232 offset:61440
v_mul_f32 v29,v244,v29
bl v232,s[8:11],s31 offen offset:3072 sc1 lds
mm v[140:143],a[124:127],v[88:91],v[200:203]
dr a[104:107],v232 offset:54272
v_mul_f32 v30,v244,v30
mm v[140:143],a[120:123],v[80:83],v[140:143]
dr a[108:111],v232 offset:62464
v_mul_f32 v31,v244,v31
mm v[140:143],a[116:119],v[72:75],v[140:143]
cv v8,v20,v21
cv v9,v22,v23
mm v[140:143],a[112:115],v[64:67],v[140:143]
s_waitcnt lgkmcnt(5)
dr a[112:115],v232 offset:55296
v_and_b32 v8,-2,v8
)" R"(dr a[116:119],v232 offset:63488
cv v10,v28,v29
cv v11,v30,v31
bs v[8:11],v232,s[8:11],s42 offen offset:1024 sc1
dr a[120:123],v232 offset:56320
v_mul_f32 v36,v244,v36
v_mul_f32 v37,v244,v37
v_mul_f32 v38,v244,v38
v_mul_f32 v39,v244,v39
v_mul_f32 v44,v244,v44
mm v[68:71],a[64:67],v[180:183],v[68:71]
s_add_u32 s40,s31,32768
dr a[124:127],v232 offset:64512
v_mul_f32 v45,v244,v45
mm v[68:71],a[68:71],v[184:187],v[68:71]
s_add_u32 m0,s33,131072
v_mul_f32 v46,v244,v46
v_mul_f32 v47,v244,v47
mm v[76:79],a[72:75],v[180:183],v[76:79]
cv v16,v36,v37
cv v17,v38,v39
mm v[76:79],a[76:79],v[184:187],v[76:79]
v_and_b32 v16,-2,v16
cv v18,v44,v45
cv v19,v46,v47
bs v[16:19],v232,s[8:11],s42 offen offset:2048 sc1
v_mul_f32 v52,v244,v52
v_mul_f32 v53,v244,v53
v_mul_f32 v54,v244,v54
v_mul_f32 v55,v244,v55
v_mul_f32 v60,v244,v60
v_mul_f32 v61,v244,v61
mm v[84:87],a[80:83],v[180:183],v[84:87]
v_mul_f32 v62,v244,v62
v_mul_f32 v63,v244,v63
mm v[84:87],a[84:87],v[184:187],v[84:87]
cv v24,v52,v53
cv v25,v54,v55
mm v[92:95],a[88:91],v[180:183],v[92:95]
s_waitcnt lgkmcnt(1)
v_and_b32 v24,-2,v24
cv v26,v60,v61
cv v27,v62,v63
bs v[24:27],v232,s[8:11],s42 offen offset:3072 sc1
s_waitcnt lgkmcnt(0)
s_barrier
ex2 v150,v150
aw a136,0
mm v[92:95],a[92:95],v[184:187],v[92:95]
dr a[64:67],v235 offset:0
aw a137,0
v_smfmac_f32_16x16x64_bf16 a[132:135],a[144:147],v[180:187],v246
dr a[68:71],v235 offset:1024
aw a140,0
mm v[100:103],a[96:99],v[180:183],v[100:103]
ex2 v151,v151
mm v[100:103],a[100:103],v[184:187],v[100:103]
dr a[72:75],v235 offset:2048
cv v151,v150,v151
mm v[108:111],a[104:107],v[180:183],v[108:111]
dr a[76:79],v235 offset:3072
aw a141,0
mm v[108:111],a[108:111],v[184:187],v[108:111]
dr a[80:83],v235 offset:4096
)" R"(mm v[116:119],a[112:115],v[180:183],v[116:119]
dr a[84:87],v235 offset:5120
mm v[116:119],a[116:119],v[184:187],v[116:119]
dr a[88:91],v235 offset:6144
mm v[124:127],a[120:123],v[180:183],v[124:127]
dr a[92:95],v235 offset:7168
mm v[124:127],a[124:127],v[184:187],v[124:127]
s_waitcnt lgkmcnt(6)
dr a[96:99],v235 offset:8192
mm v[160:163],a[64:67],v[64:67],v[200:203]
dr a[100:103],v235 offset:9216
mm v[160:163],a[68:71],v[72:75],v[160:163]
s_waitcnt lgkmcnt(6)
dr a[104:107],v235 offset:10240
mm v[160:163],a[72:75],v[80:83],v[160:163]
dr a[108:111],v235 offset:11264
mm v[160:163],a[76:79],v[88:91],v[160:163]
dr a[112:115],v235 offset:12288
mm v[176:179],a[76:79],v[120:123],v[204:207]
dr a[116:119],v235 offset:13312
mm v[176:179],a[72:75],v[112:115],v[176:179]
dr a[120:123],v235 offset:14336
mm v[176:179],a[68:71],v[104:107],v[176:179]
ard v245,a132
ard v208,a133
v_add_f32 v245,v245,v208
v_cmp_ngt_f32 vcc,0x5f800000,v245
s_cbranch_vccnz FX01
FX01_D:
mm v[176:179],a[64:67],v[96:99],v[176:179]
s_waitcnt lgkmcnt(5)
dr a[124:127],v235 offset:15360
mm v[180:183],a[80:83],v[96:99],v[204:207]
v_rcp_f32 v245,v245
mm v[180:183],a[84:87],v[104:107],v[180:183]
v_mul_f32 v68,v245,v68
v_mul_f32 v69,v245,v69
v_mul_f32 v70,v245,v70
v_mul_f32 v71,v245,v71
v_mul_f32 v76,v245,v76
v_mul_f32 v77,v245,v77
v_mul_f32 v78,v245,v78
v_mul_f32 v79,v245,v79
cv v32,v68,v69
cv v33,v70,v71
v_and_b32 v32,-2,v32
cv v34,v76,v77
cv v35,v78,v79
bs v[32:35],v233,s[8:11],s42 offen offset:0 sc1
ex2 v148,v148
ex2 v149,v149
ex2 v144,v144
mm v[180:183],a[88:91],v[112:115],v[180:183]
ex2 v145,v145
mm v[180:183],a[92:95],v[120:123],v[180:183]
ex2 v146,v146
mm v[164:167],a[92:95],v[88:91],v[200:203]
s_waitcnt lgkmcnt(0)
s_barrier
mm v[164:167],a[88:91],v[80:83],v[164:167]
bl v232,s[8:11],s40 offen offset:0 sc1 lds
mm v[164:167],a[84:87],v[72:75],v[164:167]
dr a[64:67],v235 offset:16384
cv v150,v148,v149
)" R"(mm v[164:167],a[80:83],v[64:67],v[164:167]
dr a[68:71],v235 offset:24576
cv v148,v144,v145
mm v[168:171],a[96:99],v[64:67],v[200:203]
ex2 v147,v147
mm v[168:171],a[100:103],v[72:75],v[168:171]
bl v232,s[8:11],s40 offen offset:1024 sc1 lds
mm v[168:171],a[104:107],v[80:83],v[168:171]
dr a[72:75],v235 offset:17408
cv v149,v146,v147
mm v[168:171],a[108:111],v[88:91],v[168:171]
dr a[76:79],v235 offset:25600
v_mul_f32 v84,v245,v84
mm v[184:187],a[108:111],v[120:123],v[204:207]
ex2 v152,v152
mm v[184:187],a[104:107],v[112:115],v[184:187]
bl v232,s[8:11],s40 offen offset:2048 sc1 lds
mm v[184:187],a[100:103],v[104:107],v[184:187]
ex2 v153,v153
mm v[184:187],a[96:99],v[96:99],v[184:187]
ex2 v154,v154
mm v[188:191],a[112:115],v[96:99],v[204:207]
ex2 v155,v155
mm v[188:191],a[116:119],v[104:107],v[188:191]
bl v232,s[8:11],s40 offen offset:3072 sc1 lds
mm v[188:191],a[120:123],v[112:115],v[188:191]
s_add_u32 s40,s31,8192
ex2 v156,v156
mm v[188:191],a[124:127],v[120:123],v[188:191]
s_add_u32 m0,s33,147456
ex2 v157,v157
mm v[172:175],a[124:127],v[88:91],v[200:203]
ex2 v158,v158
mm v[172:175],a[120:123],v[80:83],v[172:175]
ex2 v159,v159
mm v[172:175],a[116:119],v[72:75],v[172:175]
dr a[80:83],v235 offset:18432
cv v152,v152,v153
mm v[172:175],a[112:115],v[64:67],v[172:175]
s_waitcnt lgkmcnt(1)
dr a[84:87],v235 offset:26624
cv v153,v154,v155
cv v154,v156,v157
cv v155,v158,v159
ex2 v136,v136
ex2 v137,v137
ex2 v138,v138
ex2 v139,v139
ex2 v140,v140
ex2 v141,v141
ex2 v142,v142
ex2 v143,v143
v_mul_f32 v85,v245,v85
v_mul_f32 v86,v245,v86
v_mul_f32 v87,v245,v87
v_mul_f32 v92,v245,v92
v_mul_f32 v93,v245,v93
v_mul_f32 v94,v245,v94
v_mul_f32 v95,v245,v95
cv v40,v84,v85
cv v41,v86,v87
v_and_b32 v40,-2,v40
cv v42,v92,v93
cv v43,v94,v95
)" R"(bs v[40:43],v233,s[8:11],s42 offen offset:1024 sc1
ex2 v134,v134
ex2 v135,v135
ex2 v132,v132
mm v[68:71],a[64:67],v[148:151],0
s_getpc_b64 s[60:61]
CT3:
dr a[88:91],v235 offset:19456
cv v136,v136,v137
ex2 v133,v133
ex2 v128,v128
ex2 v129,v129
ex2 v130,v130
ex2 v131,v131
cv v137,v138,v139
cv v138,v140,v141
cv v139,v142,v143
mm v[68:71],a[68:71],v[152:155],v[68:71]
s_lshr_b32 s62,s29,3
dr a[92:95],v235 offset:27648
cv v135,v134,v135
cv v134,v132,v133
cv v132,v128,v129
cv v133,v130,v131
mm v[4:7],a[68:71],v[136:139],0
s_lshl_b32 s62,s62,2
dr a[96:99],v235 offset:20480
v_mul_f32 v100,v245,v100
mm v[4:7],a[64:67],v[132:135],v[4:7]
s_add_u32 s62,s62,s24
dr a[100:103],v235 offset:28672
v_mul_f32 v101,v245,v101
v_mul_f32 v102,v245,v102
v_mul_f32 v103,v245,v103
v_mul_f32 v108,v245,v108
v_mul_f32 v109,v245,v109
v_mul_f32 v110,v245,v110
v_mul_f32 v111,v245,v111
cv v48,v100,v101
cv v49,v102,v103
v_and_b32 v48,-2,v48
cv v50,v108,v109
cv v51,v110,v111
s_lshl_b32 s62,s62,7
s_add_u32 s62,s62,U-CT3
v_min_u32 v250,16,v232
v_lshlrev_b32 v250,10,v250
v_add_u32 v250,s62,v250
v_min_u32 v250,THEEND-CT3-4,v250
global_load_dword v255,v250,s[60:61]
bs v[48:51],v233,s[8:11],s42 offen offset:2048 sc1
dr a[104:107],v235 offset:21504
v_mul_f32 v116,v245,v116
v_mul_f32 v117,v245,v117
v_mul_f32 v118,v245,v118
v_mul_f32 v119,v245,v119
v_mul_f32 v124,v245,v124
mm v[12:15],a[72:75],v[132:135],0
s_getpc_b64 s[44:45]
TJ:
dr a[108:111],v235 offset:29696
v_mul_f32 v125,v245,v125
mm v[12:15],a[76:79],v[136:139],v[12:15]
s_mov_b32 s46,U-TJ
)" R"(dr a[112:115],v235 offset:22528
v_mul_f32 v126,v245,v126
mm v[76:79],a[76:79],v[152:155],0
s_cmp_eq_u32 s26,0
s_cselect_b32 s46,R10-TJ,s46
dr a[116:119],v235 offset:30720
mm v[76:79],a[72:75],v[148:151],v[76:79]
s_waitcnt lgkmcnt(7)
dr a[120:123],v235 offset:23552
v_mul_f32 v127,v245,v127
cv v56,v116,v117
cv v57,v118,v119
v_and_b32 v56,-2,v56
cv v58,v124,v125
cv v59,v126,v127
bs v[56:59],v233,s[8:11],s42 offen offset:3072 sc1
ex2 v182,v182
ex2 v183,v183
ex2 v180,v180
mm v[84:87],a[80:83],v[148:151],0
s_cmp_eq_u32 s26,1
s_cselect_b32 s46,R11-TJ,s46
dr a[124:127],v235 offset:31744
mm v[84:87],a[84:87],v[152:155],v[84:87]
s_cmp_eq_u32 s26,2
s_cselect_b32 s46,R12-TJ,s46
cv v183,v182,v183
mm v[20:23],a[84:87],v[136:139],0
s_cmp_eq_u32 s26,3
s_cselect_b32 s46,R13-TJ,s46
mm v[20:23],a[80:83],v[132:135],v[20:23]
s_cmp_eq_u32 s26,4
s_cselect_b32 s46,R14-TJ,s46
mm v[28:31],a[88:91],v[132:135],0
s_waitcnt lgkmcnt(3)
ex2 v181,v181
mm v[28:31],a[92:95],v[136:139],v[28:31]
s_cmp_eq_u32 s26,5
s_cselect_b32 s46,R15-TJ,s46
cv v182,v180,v181
mm v[92:95],a[92:95],v[152:155],0
s_cmp_eq_u32 s26,6
s_cselect_b32 s46,R16-TJ,s46
s_waitcnt vmcnt(12) lgkmcnt(0)
s_barrier
mm v[92:95],a[88:91],v[148:151],v[92:95]
s_add_u32 s44,s44,s46
s_addc_u32 s45,s45,0
v_smfmac_f32_16x16x64_bf16 a[136:139],a[144:147],v[132:139],v246
bl v232,s[8:11],s40 offen offset:0 sc1 lds
v_smfmac_f32_16x16x64_bf16 a[140:143],a[144:147],v[148:155],v246
dr a[64:67],v232 offset:0
mm v[100:103],a[96:99],v[148:151],0
dr a[68:71],v232 offset:1024
mm v[100:103],a[100:103],v[152:155],v[100:103]
ex2 v176,v176
mm v[36:39],a[100:103],v[136:139],0
bl v232,s[8:11],s40 offen offset:1024 sc1 lds
mm v[36:39],a[96:99],v[132:135],v[36:39]
dr a[72:75],v232 offset:2048
mm v[44:47],a[104:107],v[132:135],0
dr a[76:79],v232 offset:3072
mm v[44:47],a[108:111],v[136:139],v[44:47]
ex2 v177,v177
)" R"(mm v[108:111],a[108:111],v[152:155],0
bl v232,s[8:11],s40 offen offset:2048 sc1 lds
mm v[108:111],a[104:107],v[148:151],v[108:111]
dr a[80:83],v232 offset:4096
cv v180,v176,v177
mm v[116:119],a[112:115],v[148:151],0
dr a[84:87],v232 offset:5120
mm v[116:119],a[116:119],v[152:155],v[116:119]
s_mov_b32 s30,s31
dr a[88:91],v232 offset:6144
s_add_u32 s31,s31,65536
mm v[52:55],a[116:119],v[136:139],0
dr a[92:95],v232 offset:7168
mm v[52:55],a[112:115],v[132:135],v[52:55]
bl v232,s[8:11],s40 offen offset:3072 sc1 lds
mm v[60:63],a[120:123],v[132:135],0
s_cmp_eq_u32 s31,s3
s_cselect_b32 s40,65536,0
dr a[96:99],v232 offset:8192
mm v[60:63],a[124:127],v[136:139],v[60:63]
s_add_u32 s31,s31,s40
dr a[100:103],v232 offset:9216
mm v[124:127],a[124:127],v[152:155],0
ex2 v178,v178
dr a[104:107],v232 offset:10240
ex2 v179,v179
ex2 v166,v166
ex2 v167,v167
ex2 v164,v164
ex2 v165,v165
ex2 v160,v160
ex2 v161,v161
ex2 v162,v162
ex2 v163,v163
mm v[124:127],a[120:123],v[148:151],v[124:127]
dr a[108:111],v232 offset:11264
cv v181,v178,v179
cv v167,v166,v167
cv v166,v164,v165
cv v164,v160,v161
cv v165,v162,v163
s_setpc_b64 s[44:45]
TE:
s_nop 0
s_nop 0
s_nop 0
s_nop 0
s_nop 0
s_nop 0
s_nop 0
s_nop 0
s_nop 0
s_nop 0
s_nop 0
s_nop 0
s_nop 0
s_nop 0
s_nop 0
s_nop 0
s_nop 0
s_nop 0
s_nop 0
s_nop 0
s_nop 0
)" R"(s_nop 0
s_nop 0
s_nop 0
s_nop 0
s_nop 0
s_nop 0
s_nop 0
s_nop 0
s_nop 0
s_nop 0
s_nop 0
s_nop 0
s_nop 0
s_nop 0
s_nop 0
s_nop 0
s_nop 0
s_nop 0
s_nop 0
s_nop 0
s_nop 0
s_nop 0
s_nop 0
s_nop 0
s_nop 0
s_nop 0
s_nop 0
s_nop 0
s_nop 0
s_nop 0
s_nop 0
s_nop 0
s_nop 0
s_nop 0
s_nop 0
s_nop 0
s_nop 0
s_nop 0
s_nop 0
s_nop 0
s_nop 0
s_nop 0
s_nop 0
s_nop 0
s_nop 0
s_nop 0
s_nop 0
s_nop 0
s_nop 0
s_nop 0
s_nop 0
s_nop 0
s_nop 0
s_nop 0
s_nop 0
s_nop 0
s_nop 0
s_nop 0
s_nop 0
s_nop 0
s_nop 0
s_nop 0
s_nop 0
s_nop 0
)" R"(s_nop 0
s_nop 0
s_nop 0
s_nop 0
s_nop 0
s_nop 0
s_nop 0
s_nop 0
s_nop 0
s_nop 0
s_nop 0
s_nop 0
s_nop 0
s_nop 0
s_nop 0
s_nop 0
s_nop 0
s_nop 0
s_nop 0
s_nop 0
s_nop 0
s_nop 0
s_nop 0
s_nop 0
s_nop 0
s_nop 0
s_nop 0
s_nop 0
s_nop 0
s_nop 0
s_nop 0
s_nop 0
s_nop 0
s_nop 0
s_nop 0
s_nop 0
s_nop 0
s_nop 0
s_nop 0
s_nop 0
s_nop 0
s_nop 0
s_nop 0
s_nop 0
s_nop 0
s_nop 0
s_nop 0
s_nop 0
s_nop 0
s_nop 0
s_nop 0
s_nop 0
s_nop 0
s_nop 0
s_nop 0
s_nop 0
s_nop 0
s_nop 0
s_nop 0
s_nop 0
s_nop 0
s_nop 0
s_nop 0
s_nop 0
)" R"(s_nop 0
s_nop 0
s_nop 0
s_nop 0
s_nop 0
s_nop 0
s_nop 0
s_nop 0
s_nop 0
s_nop 0
s_nop 0
s_nop 0
s_nop 0
s_nop 0
s_nop 0
s_nop 0
s_nop 0
s_nop 0
s_nop 0
s_nop 0
s_nop 0
s_nop 0
s_nop 0
s_nop 0
s_nop 0
s_nop 0
s_nop 0
s_nop 0
s_nop 0
s_nop 0
s_nop 0
s_nop 0
s_nop 0
s_nop 0
s_nop 0
s_nop 0
s_nop 0
s_nop 0
s_nop 0
s_nop 0
s_nop 0
s_nop 0
s_nop 0
s_nop 0
s_nop 0
s_nop 0
s_nop 0
s_nop 0
s_nop 0
s_nop 0
s_nop 0
s_nop 0
s_nop 0
s_nop 0
s_nop 0
s_nop 0
s_nop 0
s_nop 0
s_nop 0
s_nop 0
s_nop 0
s_nop 0
s_nop 0
s_nop 0
)" R"(s_nop 0
s_nop 0
s_nop 0
s_nop 0
s_nop 0
s_nop 0
s_nop 0
s_nop 0
s_nop 0
s_nop 0
s_nop 0
s_nop 0
s_nop 0
s_nop 0
s_nop 0
s_nop 0
s_nop 0
s_nop 0
s_nop 0
s_nop 0
s_nop 0
s_nop 0
s_nop 0
s_nop 0
s_nop 0
s_nop 0
s_nop 0
s_nop 0
s_nop 0
s_nop 0
s_nop 0
s_nop 0
s_nop 0
s_nop 0
s_nop 0
s_nop 0
s_nop 0
s_nop 0
s_nop 0
s_nop 0
s_nop 0
s_nop 0
s_nop 0
s_nop 0
s_nop 0
s_nop 0
s_nop 0
s_nop 0
s_nop 0
s_nop 0
s_nop 0
s_nop 0
s_nop 0
s_nop 0
s_nop 0
s_nop 0
s_nop 0
s_nop 0
s_nop 0
s_nop 0
s_nop 0
s_nop 0
s_nop 0
s_nop 0
)" R"(s_nop 0
s_nop 0
s_nop 0
s_nop 0
s_nop 0
s_nop 0
s_nop 0
s_nop 0
s_nop 0
s_nop 0
s_nop 0
s_nop 0
s_nop 0
s_nop 0
s_nop 0
s_nop 0
s_nop 0
s_nop 0
s_nop 0
s_nop 0
s_nop 0
s_nop 0
s_nop 0
s_nop 0
s_nop 0
s_nop 0
s_nop 0
s_nop 0
s_nop 0
s_nop 0
s_nop 0
s_nop 0
s_nop 0
s_nop 0
s_nop 0
s_nop 0
s_nop 0
s_nop 0
s_nop 0
s_nop 0
s_nop 0
s_nop 0
s_nop 0
s_nop 0
s_nop 0
s_nop 0
s_nop 0
s_nop 0
s_nop 0
s_nop 0
s_nop 0
s_nop 0
s_nop 0
s_nop 0
s_nop 0
s_nop 0
s_nop 0
s_nop 0
s_nop 0
s_nop 0
s_nop 0
s_nop 0
s_nop 0
s_nop 0
)" R"(s_nop 0
s_nop 0
s_nop 0
s_nop 0
s_nop 0
s_nop 0
s_nop 0
s_nop 0
s_nop 0
s_nop 0
s_nop 0
s_nop 0
s_nop 0
s_nop 0
s_nop 0
s_nop 0
s_nop 0
s_nop 0
s_nop 0
s_nop 0
s_nop 0
s_nop 0
s_nop 0
s_nop 0
s_nop 0
s_nop 0
s_nop 0
s_nop 0
s_nop 0
s_nop 0
s_nop 0
s_nop 0
s_nop 0
s_nop 0
s_nop 0
s_nop 0
s_nop 0
s_nop 0
s_nop 0
s_nop 0
s_nop 0
s_nop 0
s_nop 0
s_nop 0
s_nop 0
s_nop 0
s_nop 0
s_nop 0
s_nop 0
s_nop 0
s_nop 0
s_nop 0
s_nop 0
s_nop 0
s_nop 0
s_nop 0
s_nop 0
s_nop 0
s_nop 0
s_nop 0
s_nop 0
s_nop 0
s_nop 0
s_nop 0
)" R"(s_nop 0
s_nop 0
s_nop 0
s_nop 0
s_nop 0
s_nop 0
s_nop 0
s_nop 0
s_nop 0
s_nop 0
s_nop 0
s_nop 0
s_nop 0
s_nop 0
s_nop 0
s_nop 0
s_nop 0
s_nop 0
s_nop 0
s_nop 0
s_nop 0
s_nop 0
s_nop 0
s_nop 0
s_nop 0
s_nop 0
s_nop 0
s_nop 0
s_nop 0
s_nop 0
s_nop 0
s_nop 0
s_nop 0
s_nop 0
s_nop 0
s_nop 0
s_nop 0
s_nop 0
s_nop 0
s_nop 0
s_nop 0
s_nop 0
s_nop 0
s_nop 0
s_nop 0
s_nop 0
s_nop 0
s_nop 0
s_nop 0
s_nop 0
s_nop 0
s_nop 0
s_nop 0
s_nop 0
s_nop 0
s_nop 0
s_nop 0
s_nop 0
s_nop 0
s_nop 0
s_nop 0
s_nop 0
s_nop 0
s_nop 0
)" R"(s_nop 0
s_nop 0
s_nop 0
s_nop 0
s_nop 0
s_nop 0
s_nop 0
s_nop 0
s_nop 0
s_nop 0
s_nop 0
s_nop 0
s_nop 0
s_nop 0
s_nop 0
s_nop 0
s_nop 0
s_nop 0
s_nop 0
s_nop 0
s_nop 0
s_nop 0
s_nop 0
s_nop 0
s_nop 0
s_nop 0
s_nop 0
s_nop 0
s_nop 0
s_nop 0
s_nop 0
s_nop 0
s_nop 0
s_nop 0
s_nop 0
s_nop 0
s_nop 0
s_nop 0
s_nop 0
s_nop 0
s_nop 0
s_nop 0
s_nop 0
s_nop 0
s_nop 0
s_nop 0
s_nop 0
s_nop 0
s_nop 0
s_nop 0
s_nop 0
s_nop 0
s_nop 0
s_nop 0
s_nop 0
s_nop 0
s_nop 0
s_nop 0
s_nop 0
s_nop 0
s_nop 0
s_nop 0
s_nop 0
s_nop 0
)" R"(s_nop 0
s_nop 0
s_nop 0
s_nop 0
s_nop 0
s_nop 0
s_nop 0
s_nop 0
s_nop 0
s_nop 0
s_nop 0
R10:
EB_R10:
s_cbranch_execz EB_R
s_waitcnt lgkmcnt(6)
mm v[128:131],a[64:67],v[64:67],v[200:203]
s_add_u32 s41,s38,s58
dr a[112:115],v232 offset:12288
s_add_u32 m0,s34,16384
mm v[128:131],a[68:71],v[72:75],v[128:131]
s_add_u32 s40,s30,40960
dr a[116:119],v232 offset:13312
mm v[128:131],a[72:75],v[80:83],v[128:131]
bs a[16:19],v232,s[8:11],s41 offen offset:0 sc1
s_add_u32 s41,s35,0
ex2 v184,v184
ex2 v185,v185
mm v[128:131],a[76:79],v[88:91],v[128:131]
dr a[120:123],v232 offset:14336
cv v184,v184,v185
mm v[144:147],a[76:79],v[120:123],v[204:207]
dr a[124:127],v232 offset:15360
mm v[144:147],a[72:75],v[112:115],v[144:147]
ex2 v186,v186
mm v[144:147],a[68:71],v[104:107],v[144:147]
ex2 v187,v187
mm v[144:147],a[64:67],v[96:99],v[144:147]
ex2 v188,v188
mm v[148:151],a[80:83],v[96:99],v[204:207]
bl v232,s[8:11],s41 offen offset:0 sc1 lds
mm v[148:151],a[84:87],v[104:107],v[148:151]
s_waitcnt lgkmcnt(2)
ex2 v189,v189
mm v[148:151],a[88:91],v[112:115],v[148:151]
s_add_u32 m0,s33,0
ex2 v190,v190
mm v[148:151],a[92:95],v[120:123],v[148:151]
s_add_u32 s41,s38,s58
ex2 v191,v191
mm v[132:135],a[92:95],v[88:91],v[200:203]
s_waitcnt vmcnt(10) lgkmcnt(0)
s_barrier
mm v[132:135],a[88:91],v[80:83],v[132:135]
bl v232,s[8:11],s31 offen offset:0 sc1 lds
mm v[132:135],a[84:87],v[72:75],v[132:135]
dr a[64:67],v232 offset:49152
cv v185,v186,v187
mm v[132:135],a[80:83],v[64:67],v[132:135]
dr a[68:71],v232 offset:57344
cv v186,v188,v189
mm v[136:139],a[96:99],v[64:67],v[200:203]
ex2 v168,v168
mm v[136:139],a[100:103],v[72:75],v[136:139]
bl v232,s[8:11],s31 offen offset:1024 sc1 lds
)" R"(mm v[136:139],a[104:107],v[80:83],v[136:139]
dr a[72:75],v232 offset:50176
cv v187,v190,v191
mm v[136:139],a[108:111],v[88:91],v[136:139]
dr a[76:79],v232 offset:58368
mm v[152:155],a[108:111],v[120:123],v[204:207]
ex2 v169,v169
mm v[152:155],a[104:107],v[112:115],v[152:155]
bl v232,s[8:11],s31 offen offset:2048 sc1 lds
mm v[152:155],a[100:103],v[104:107],v[152:155]
dr a[80:83],v232 offset:51200
cv v168,v168,v169
mm v[152:155],a[96:99],v[96:99],v[152:155]
ex2 v170,v170
mm v[156:159],a[112:115],v[96:99],v[204:207]
ex2 v171,v171
mm v[156:159],a[116:119],v[104:107],v[156:159]
bl v232,s[8:11],s31 offen offset:3072 sc1 lds
mm v[156:159],a[120:123],v[112:115],v[156:159]
s_add_u32 m0,s33,49152
ex2 v172,v172
mm v[156:159],a[124:127],v[120:123],v[156:159]
ex2 v173,v173
mm v[140:143],a[124:127],v[88:91],v[200:203]
ex2 v174,v174
mm v[140:143],a[120:123],v[80:83],v[140:143]
ex2 v175,v175
mm v[140:143],a[116:119],v[72:75],v[140:143]
dr a[84:87],v232 offset:59392
cv v169,v170,v171
mm v[140:143],a[112:115],v[64:67],v[140:143]
s_waitcnt lgkmcnt(1)
dr a[88:91],v232 offset:52224
cv v170,v172,v173
mm v[68:71],a[64:67],v[180:183],v[68:71]
dr a[92:95],v232 offset:60416
cv v171,v174,v175
mm v[68:71],a[68:71],v[184:187],v[68:71]
dr a[96:99],v232 offset:53248
mm v[4:7],a[68:71],v[168:171],v[4:7]
dr a[100:103],v232 offset:61440
mm v[4:7],a[64:67],v[164:167],v[4:7]
dr a[104:107],v232 offset:54272
mm v[12:15],a[72:75],v[164:167],v[12:15]
dr a[108:111],v232 offset:62464
mm v[12:15],a[76:79],v[168:171],v[12:15]
dr a[112:115],v232 offset:55296
mm v[76:79],a[76:79],v[184:187],v[76:79]
dr a[116:119],v232 offset:63488
mm v[76:79],a[72:75],v[180:183],v[76:79]
dr a[120:123],v232 offset:56320
mm v[84:87],a[80:83],v[180:183],v[84:87]
s_waitcnt lgkmcnt(6)
dr a[124:127],v232 offset:64512
mm v[84:87],a[84:87],v[184:187],v[84:87]
ex2 v150,v150
mm v[20:23],a[84:87],v[168:171],v[20:23]
ex2 v151,v151
mm v[20:23],a[80:83],v[164:167],v[20:23]
ex2 v148,v148
mm v[28:31],a[88:91],v[164:167],v[28:31]
ex2 v149,v149
mm v[28:31],a[92:95],v[168:171],v[28:31]
ex2 v144,v144
)" R"(mm v[92:95],a[92:95],v[184:187],v[92:95]
s_waitcnt vmcnt(10) lgkmcnt(0)
s_barrier
mm v[92:95],a[88:91],v[180:183],v[92:95]
bl v232,s[8:11],s40 offen offset:0 sc1 lds
v_smfmac_f32_16x16x64_bf16 a[136:139],a[144:147],v[164:171],v246
dr a[64:67],v235 offset:0
cv v151,v150,v151
v_smfmac_f32_16x16x64_bf16 a[140:143],a[144:147],v[180:187],v246
dr a[68:71],v235 offset:1024
cv v150,v148,v149
mm v[100:103],a[96:99],v[180:183],v[100:103]
ex2 v145,v145
mm v[100:103],a[100:103],v[184:187],v[100:103]
bl v232,s[8:11],s40 offen offset:1024 sc1 lds
mm v[36:39],a[100:103],v[168:171],v[36:39]
dr a[72:75],v235 offset:2048
cv v148,v144,v145
mm v[36:39],a[96:99],v[164:167],v[36:39]
dr a[76:79],v235 offset:3072
mm v[44:47],a[104:107],v[164:167],v[44:47]
ex2 v146,v146
mm v[44:47],a[108:111],v[168:171],v[44:47]
bl v232,s[8:11],s40 offen offset:2048 sc1 lds
mm v[108:111],a[108:111],v[184:187],v[108:111]
dr a[80:83],v235 offset:4096
mm v[108:111],a[104:107],v[180:183],v[108:111]
dr a[84:87],v235 offset:5120
mm v[116:119],a[112:115],v[180:183],v[116:119]
ex2 v147,v147
mm v[116:119],a[116:119],v[184:187],v[116:119]
bl v232,s[8:11],s40 offen offset:3072 sc1 lds
mm v[52:55],a[116:119],v[168:171],v[52:55]
s_add_u32 m0,s34,16384
dr a[88:91],v235 offset:6144
s_add_u32 s40,s31,32768
mm v[52:55],a[112:115],v[164:167],v[52:55]
dr a[92:95],v235 offset:7168
cv v149,v146,v147
mm v[60:63],a[120:123],v[164:167],v[60:63]
ex2 v152,v152
mm v[60:63],a[124:127],v[168:171],v[60:63]
bs a[20:23],v232,s[8:11],s41 offen offset:1024 sc1
dr a[96:99],v235 offset:8192
s_add_u32 s41,s35,0
ex2 v153,v153
ex2 v154,v154
mm v[124:127],a[124:127],v[184:187],v[124:127]
dr a[100:103],v235 offset:9216
cv v152,v152,v153
mm v[124:127],a[120:123],v[180:183],v[124:127]
s_waitcnt lgkmcnt(4)
ex2 v155,v155
mm v[160:163],a[64:67],v[64:67],v[200:203]
dr a[104:107],v235 offset:10240
cv v153,v154,v155
mm v[160:163],a[68:71],v[72:75],v[160:163]
dr a[108:111],v235 offset:11264
mm v[160:163],a[72:75],v[80:83],v[160:163]
bl v232,s[8:11],s41 offen offset:1024 sc1 lds
mm v[160:163],a[76:79],v[88:91],v[160:163]
s_add_u32 m0,s33,131072
dr a[112:115],v235 offset:12288
mm v[176:179],a[76:79],v[120:123],v[204:207]
)" R"(dr a[116:119],v235 offset:13312
mm v[176:179],a[72:75],v[112:115],v[176:179]
dr a[120:123],v235 offset:14336
mm v[176:179],a[68:71],v[104:107],v[176:179]
dr a[124:127],v235 offset:15360
mm v[176:179],a[64:67],v[96:99],v[176:179]
ex2 v156,v156
mm v[180:183],a[80:83],v[96:99],v[204:207]
ex2 v157,v157
mm v[180:183],a[84:87],v[104:107],v[180:183]
s_waitcnt lgkmcnt(4)
ex2 v158,v158
mm v[180:183],a[88:91],v[112:115],v[180:183]
ex2 v159,v159
mm v[180:183],a[92:95],v[120:123],v[180:183]
ex2 v136,v136
mm v[164:167],a[92:95],v[88:91],v[200:203]
s_waitcnt vmcnt(12) lgkmcnt(0)
s_barrier
mm v[164:167],a[88:91],v[80:83],v[164:167]
bl v232,s[8:11],s40 offen offset:0 sc1 lds
mm v[164:167],a[84:87],v[72:75],v[164:167]
dr a[64:67],v235 offset:16384
cv v154,v156,v157
mm v[164:167],a[80:83],v[64:67],v[164:167]
dr a[68:71],v235 offset:24576
cv v155,v158,v159
mm v[168:171],a[96:99],v[64:67],v[200:203]
ex2 v137,v137
mm v[168:171],a[100:103],v[72:75],v[168:171]
bl v232,s[8:11],s40 offen offset:1024 sc1 lds
mm v[168:171],a[104:107],v[80:83],v[168:171]
dr a[72:75],v235 offset:17408
cv v136,v136,v137
mm v[168:171],a[108:111],v[88:91],v[168:171]
dr a[76:79],v235 offset:25600
mm v[184:187],a[108:111],v[120:123],v[204:207]
ex2 v138,v138
mm v[184:187],a[104:107],v[112:115],v[184:187]
bl v232,s[8:11],s40 offen offset:2048 sc1 lds
mm v[184:187],a[100:103],v[104:107],v[184:187]
dr a[80:83],v235 offset:18432
mm v[184:187],a[96:99],v[96:99],v[184:187]
ex2 v139,v139
mm v[188:191],a[112:115],v[96:99],v[204:207]
ex2 v140,v140
mm v[188:191],a[116:119],v[104:107],v[188:191]
bl v232,s[8:11],s40 offen offset:3072 sc1 lds
mm v[188:191],a[120:123],v[112:115],v[188:191]
s_add_u32 s40,s31,8192
ex2 v141,v141
mm v[188:191],a[124:127],v[120:123],v[188:191]
s_add_u32 m0,s33,147456
ex2 v142,v142
mm v[172:175],a[124:127],v[88:91],v[200:203]
ex2 v143,v143
mm v[172:175],a[120:123],v[80:83],v[172:175]
dr a[84:87],v235 offset:26624
cv v137,v138,v139
mm v[172:175],a[116:119],v[72:75],v[172:175]
dr a[88:91],v235 offset:19456
cv v138,v140,v141
mm v[172:175],a[112:115],v[64:67],v[172:175]
s_waitcnt lgkmcnt(2)
)" R"(cv v139,v142,v143
mm v[68:71],a[64:67],v[148:151],v[68:71]
ex2 v134,v134
ex2 v135,v135
ex2 v132,v132
ex2 v133,v133
ex2 v128,v128
ex2 v129,v129
ex2 v130,v130
ex2 v131,v131
mm v[68:71],a[68:71],v[152:155],v[68:71]
dr a[92:95],v235 offset:27648
cv v135,v134,v135
cv v134,v132,v133
cv v132,v128,v129
cv v133,v130,v131
mm v[4:7],a[68:71],v[136:139],v[4:7]
dr a[96:99],v235 offset:20480
mm v[4:7],a[64:67],v[132:135],v[4:7]
dr a[100:103],v235 offset:28672
mm v[12:15],a[72:75],v[132:135],v[12:15]
dr a[104:107],v235 offset:21504
mm v[12:15],a[76:79],v[136:139],v[12:15]
dr a[108:111],v235 offset:29696
mm v[76:79],a[76:79],v[152:155],v[76:79]
dr a[112:115],v235 offset:22528
mm v[76:79],a[72:75],v[148:151],v[76:79]
dr a[116:119],v235 offset:30720
mm v[84:87],a[80:83],v[148:151],v[84:87]
s_waitcnt lgkmcnt(6)
dr a[120:123],v235 offset:23552
mm v[84:87],a[84:87],v[152:155],v[84:87]
dr a[124:127],v235 offset:31744
mm v[20:23],a[84:87],v[136:139],v[20:23]
ex2 v182,v182
mm v[20:23],a[80:83],v[132:135],v[20:23]
ex2 v183,v183
mm v[28:31],a[88:91],v[132:135],v[28:31]
ex2 v180,v180
mm v[28:31],a[92:95],v[136:139],v[28:31]
ex2 v181,v181
mm v[92:95],a[92:95],v[152:155],v[92:95]
s_waitcnt vmcnt(10) lgkmcnt(0)
s_barrier
mm v[92:95],a[88:91],v[148:151],v[92:95]
bl v232,s[8:11],s40 offen offset:0 sc1 lds
v_smfmac_f32_16x16x64_bf16 a[136:139],a[144:147],v[132:139],v246
dr a[64:67],v232 offset:0
cv v183,v182,v183
v_smfmac_f32_16x16x64_bf16 a[140:143],a[144:147],v[148:155],v246
dr a[68:71],v232 offset:1024
cv v182,v180,v181
mm v[100:103],a[96:99],v[148:151],v[100:103]
ex2 v176,v176
mm v[100:103],a[100:103],v[152:155],v[100:103]
bl v232,s[8:11],s40 offen offset:1024 sc1 lds
mm v[36:39],a[100:103],v[136:139],v[36:39]
dr a[72:75],v232 offset:2048
mm v[36:39],a[96:99],v[132:135],v[36:39]
dr a[76:79],v232 offset:3072
mm v[44:47],a[104:107],v[132:135],v[44:47]
ex2 v177,v177
mm v[44:47],a[108:111],v[136:139],v[44:47]
bl v232,s[8:11],s40 offen offset:2048 sc1 lds
)" R"(mm v[108:111],a[108:111],v[152:155],v[108:111]
dr a[80:83],v232 offset:4096
cv v180,v176,v177
mm v[108:111],a[104:107],v[148:151],v[108:111]
dr a[84:87],v232 offset:5120
mm v[116:119],a[112:115],v[148:151],v[116:119]
ex2 v178,v178
mm v[116:119],a[116:119],v[152:155],v[116:119]
s_mov_b32 s30,s31
dr a[88:91],v232 offset:6144
s_add_u32 s31,s31,65536
mm v[52:55],a[116:119],v[136:139],v[52:55]
dr a[92:95],v232 offset:7168
mm v[52:55],a[112:115],v[132:135],v[52:55]
bl v232,s[8:11],s40 offen offset:3072 sc1 lds
mm v[60:63],a[120:123],v[132:135],v[60:63]
s_cmp_eq_u32 s31,s3
s_cselect_b32 s40,65536,0
dr a[96:99],v232 offset:8192
mm v[60:63],a[124:127],v[136:139],v[60:63]
s_add_u32 s31,s31,s40
dr a[100:103],v232 offset:9216
mm v[124:127],a[124:127],v[152:155],v[124:127]
ex2 v179,v179
dr a[104:107],v232 offset:10240
ex2 v166,v166
ex2 v167,v167
ex2 v164,v164
ex2 v165,v165
ex2 v160,v160
ex2 v161,v161
ex2 v162,v162
ex2 v163,v163
mm v[124:127],a[120:123],v[148:151],v[124:127]
dr a[108:111],v232 offset:11264
cv v181,v178,v179
cv v167,v166,v167
cv v166,v164,v165
cv v164,v160,v161
cv v165,v162,v163
R11:
EB_R11:
s_cbranch_execz EB_R
s_waitcnt lgkmcnt(6)
mm v[128:131],a[64:67],v[64:67],v[200:203]
s_add_u32 s41,s38,s58
dr a[112:115],v232 offset:12288
s_add_u32 m0,s34,24576
mm v[128:131],a[68:71],v[72:75],v[128:131]
s_add_u32 s40,s30,40960
dr a[116:119],v232 offset:13312
mm v[128:131],a[72:75],v[80:83],v[128:131]
bs a[24:27],v233,s[8:11],s41 offen offset:0 sc1
s_add_u32 s41,s35,8192
ex2 v184,v184
ex2 v185,v185
mm v[128:131],a[76:79],v[88:91],v[128:131]
dr a[120:123],v232 offset:14336
cv v184,v184,v185
mm v[144:147],a[76:79],v[120:123],v[204:207]
dr a[124:127],v232 offset:15360
mm v[144:147],a[72:75],v[112:115],v[144:147]
ex2 v186,v186
mm v[144:147],a[68:71],v[104:107],v[144:147]
)" R"(ex2 v187,v187
mm v[144:147],a[64:67],v[96:99],v[144:147]
ex2 v188,v188
mm v[148:151],a[80:83],v[96:99],v[204:207]
bl v232,s[8:11],s41 offen offset:0 sc1 lds
mm v[148:151],a[84:87],v[104:107],v[148:151]
s_waitcnt lgkmcnt(2)
ex2 v189,v189
mm v[148:151],a[88:91],v[112:115],v[148:151]
s_add_u32 m0,s33,0
ex2 v190,v190
mm v[148:151],a[92:95],v[120:123],v[148:151]
s_add_u32 s41,s35,8192
ex2 v191,v191
mm v[132:135],a[92:95],v[88:91],v[200:203]
s_waitcnt vmcnt(10) lgkmcnt(0)
s_barrier
mm v[132:135],a[88:91],v[80:83],v[132:135]
bl v232,s[8:11],s31 offen offset:0 sc1 lds
mm v[132:135],a[84:87],v[72:75],v[132:135]
dr a[64:67],v232 offset:49152
cv v185,v186,v187
mm v[132:135],a[80:83],v[64:67],v[132:135]
dr a[68:71],v232 offset:57344
cv v186,v188,v189
mm v[136:139],a[96:99],v[64:67],v[200:203]
ex2 v168,v168
mm v[136:139],a[100:103],v[72:75],v[136:139]
bl v232,s[8:11],s31 offen offset:1024 sc1 lds
mm v[136:139],a[104:107],v[80:83],v[136:139]
dr a[72:75],v232 offset:50176
cv v187,v190,v191
mm v[136:139],a[108:111],v[88:91],v[136:139]
dr a[76:79],v232 offset:58368
mm v[152:155],a[108:111],v[120:123],v[204:207]
ex2 v169,v169
mm v[152:155],a[104:107],v[112:115],v[152:155]
bl v232,s[8:11],s31 offen offset:2048 sc1 lds
mm v[152:155],a[100:103],v[104:107],v[152:155]
dr a[80:83],v232 offset:51200
cv v168,v168,v169
mm v[152:155],a[96:99],v[96:99],v[152:155]
ex2 v170,v170
mm v[156:159],a[112:115],v[96:99],v[204:207]
ex2 v171,v171
mm v[156:159],a[116:119],v[104:107],v[156:159]
bl v232,s[8:11],s31 offen offset:3072 sc1 lds
mm v[156:159],a[120:123],v[112:115],v[156:159]
s_add_u32 m0,s34,24576
ex2 v172,v172
mm v[156:159],a[124:127],v[120:123],v[156:159]
ex2 v173,v173
mm v[140:143],a[124:127],v[88:91],v[200:203]
ex2 v174,v174
mm v[140:143],a[120:123],v[80:83],v[140:143]
ex2 v175,v175
mm v[140:143],a[116:119],v[72:75],v[140:143]
dr a[84:87],v232 offset:59392
cv v169,v170,v171
mm v[140:143],a[112:115],v[64:67],v[140:143]
s_waitcnt lgkmcnt(1)
dr a[88:91],v232 offset:52224
cv v170,v172,v173
mm v[68:71],a[64:67],v[180:183],v[68:71]
)" R"(dr a[92:95],v232 offset:60416
cv v171,v174,v175
mm v[68:71],a[68:71],v[184:187],v[68:71]
dr a[96:99],v232 offset:53248
mm v[4:7],a[68:71],v[168:171],v[4:7]
dr a[100:103],v232 offset:61440
mm v[4:7],a[64:67],v[164:167],v[4:7]
dr a[104:107],v232 offset:54272
mm v[12:15],a[72:75],v[164:167],v[12:15]
bl v232,s[8:11],s41 offen offset:1024 sc1 lds
mm v[12:15],a[76:79],v[168:171],v[12:15]
s_add_u32 m0,s33,49152
dr a[108:111],v232 offset:62464
s_add_u32 s41,s38,s58
mm v[76:79],a[76:79],v[184:187],v[76:79]
dr a[112:115],v232 offset:55296
mm v[76:79],a[72:75],v[180:183],v[76:79]
dr a[116:119],v232 offset:63488
mm v[84:87],a[80:83],v[180:183],v[84:87]
s_waitcnt lgkmcnt(5)
dr a[120:123],v232 offset:56320
mm v[84:87],a[84:87],v[184:187],v[84:87]
dr a[124:127],v232 offset:64512
mm v[20:23],a[84:87],v[168:171],v[20:23]
ex2 v150,v150
mm v[20:23],a[80:83],v[164:167],v[20:23]
ex2 v151,v151
mm v[28:31],a[88:91],v[164:167],v[28:31]
ex2 v148,v148
mm v[28:31],a[92:95],v[168:171],v[28:31]
ex2 v149,v149
mm v[92:95],a[92:95],v[184:187],v[92:95]
s_waitcnt vmcnt(11) lgkmcnt(0)
s_barrier
mm v[92:95],a[88:91],v[180:183],v[92:95]
bl v232,s[8:11],s40 offen offset:0 sc1 lds
v_smfmac_f32_16x16x64_bf16 a[136:139],a[144:147],v[164:171],v246
dr a[64:67],v235 offset:0
cv v151,v150,v151
v_smfmac_f32_16x16x64_bf16 a[140:143],a[144:147],v[180:187],v246
dr a[68:71],v235 offset:1024
cv v150,v148,v149
mm v[100:103],a[96:99],v[180:183],v[100:103]
ex2 v144,v144
mm v[100:103],a[100:103],v[184:187],v[100:103]
bl v232,s[8:11],s40 offen offset:1024 sc1 lds
mm v[36:39],a[100:103],v[168:171],v[36:39]
dr a[72:75],v235 offset:2048
mm v[36:39],a[96:99],v[164:167],v[36:39]
dr a[76:79],v235 offset:3072
mm v[44:47],a[104:107],v[164:167],v[44:47]
ex2 v145,v145
mm v[44:47],a[108:111],v[168:171],v[44:47]
bl v232,s[8:11],s40 offen offset:2048 sc1 lds
mm v[108:111],a[108:111],v[184:187],v[108:111]
dr a[80:83],v235 offset:4096
cv v148,v144,v145
mm v[108:111],a[104:107],v[180:183],v[108:111]
dr a[84:87],v235 offset:5120
mm v[116:119],a[112:115],v[180:183],v[116:119]
ex2 v146,v146
mm v[116:119],a[116:119],v[184:187],v[116:119]
bl v232,s[8:11],s40 offen offset:3072 sc1 lds
mm v[52:55],a[116:119],v[168:171],v[52:55]
)" R"(s_add_u32 m0,s34,32768
dr a[88:91],v235 offset:6144
s_add_u32 s40,s31,32768
mm v[52:55],a[112:115],v[164:167],v[52:55]
dr a[92:95],v235 offset:7168
mm v[60:63],a[120:123],v[164:167],v[60:63]
ex2 v147,v147
mm v[60:63],a[124:127],v[168:171],v[60:63]
bs a[28:31],v233,s[8:11],s41 offen offset:1024 sc1
dr a[96:99],v235 offset:8192
s_add_u32 s41,s35,16384
cv v149,v146,v147
ex2 v152,v152
mm v[124:127],a[124:127],v[184:187],v[124:127]
EB_17:
s_cbranch_execz EB_R
dr a[100:103],v235 offset:9216
mm v[124:127],a[120:123],v[180:183],v[124:127]
s_waitcnt lgkmcnt(4)
ex2 v153,v153
mm v[160:163],a[64:67],v[64:67],v[200:203]
dr a[104:107],v235 offset:10240
cv v152,v152,v153
mm v[160:163],a[68:71],v[72:75],v[160:163]
dr a[108:111],v235 offset:11264
mm v[160:163],a[72:75],v[80:83],v[160:163]
bl v232,s[8:11],s41 offen offset:0 sc1 lds
mm v[160:163],a[76:79],v[88:91],v[160:163]
s_add_u32 m0,s33,131072
dr a[112:115],v235 offset:12288
s_add_u32 s41,s35,16384
mm v[176:179],a[76:79],v[120:123],v[204:207]
dr a[116:119],v235 offset:13312
mm v[176:179],a[72:75],v[112:115],v[176:179]
dr a[120:123],v235 offset:14336
mm v[176:179],a[68:71],v[104:107],v[176:179]
dr a[124:127],v235 offset:15360
mm v[176:179],a[64:67],v[96:99],v[176:179]
ex2 v154,v154
mm v[180:183],a[80:83],v[96:99],v[204:207]
ex2 v155,v155
mm v[180:183],a[84:87],v[104:107],v[180:183]
s_waitcnt lgkmcnt(4)
ex2 v156,v156
mm v[180:183],a[88:91],v[112:115],v[180:183]
ex2 v157,v157
mm v[180:183],a[92:95],v[120:123],v[180:183]
ex2 v158,v158
mm v[164:167],a[92:95],v[88:91],v[200:203]
s_waitcnt vmcnt(13) lgkmcnt(0)
s_barrier
mm v[164:167],a[88:91],v[80:83],v[164:167]
bl v232,s[8:11],s40 offen offset:0 sc1 lds
mm v[164:167],a[84:87],v[72:75],v[164:167]
dr a[64:67],v235 offset:16384
cv v153,v154,v155
mm v[164:167],a[80:83],v[64:67],v[164:167]
dr a[68:71],v235 offset:24576
cv v154,v156,v157
mm v[168:171],a[96:99],v[64:67],v[200:203]
ex2 v159,v159
mm v[168:171],a[100:103],v[72:75],v[168:171]
bl v232,s[8:11],s40 offen offset:1024 sc1 lds
mm v[168:171],a[104:107],v[80:83],v[168:171]
)" R"(dr a[72:75],v235 offset:17408
cv v155,v158,v159
mm v[168:171],a[108:111],v[88:91],v[168:171]
dr a[76:79],v235 offset:25600
mm v[184:187],a[108:111],v[120:123],v[204:207]
ex2 v136,v136
mm v[184:187],a[104:107],v[112:115],v[184:187]
bl v232,s[8:11],s40 offen offset:2048 sc1 lds
mm v[184:187],a[100:103],v[104:107],v[184:187]
dr a[80:83],v235 offset:18432
mm v[184:187],a[96:99],v[96:99],v[184:187]
ex2 v137,v137
mm v[188:191],a[112:115],v[96:99],v[204:207]
ex2 v138,v138
mm v[188:191],a[116:119],v[104:107],v[188:191]
bl v232,s[8:11],s40 offen offset:3072 sc1 lds
mm v[188:191],a[120:123],v[112:115],v[188:191]
s_add_u32 m0,s34,32768
ex2 v139,v139
mm v[188:191],a[124:127],v[120:123],v[188:191]
s_add_u32 s40,s31,8192
ex2 v140,v140
mm v[172:175],a[124:127],v[88:91],v[200:203]
ex2 v141,v141
mm v[172:175],a[120:123],v[80:83],v[172:175]
ex2 v142,v142
mm v[172:175],a[116:119],v[72:75],v[172:175]
ex2 v143,v143
mm v[172:175],a[112:115],v[64:67],v[172:175]
s_waitcnt lgkmcnt(0)
dr a[84:87],v235 offset:26624
cv v136,v136,v137
bl v232,s[8:11],s41 offen offset:1024 sc1 lds
mm v[68:71],a[64:67],v[148:151],v[68:71]
s_add_u32 m0,s33,147456
dr a[88:91],v235 offset:19456
cv v137,v138,v139
ex2 v134,v134
ex2 v135,v135
ex2 v132,v132
ex2 v133,v133
ex2 v128,v128
ex2 v129,v129
ex2 v130,v130
ex2 v131,v131
cv v138,v140,v141
cv v139,v142,v143
mm v[68:71],a[68:71],v[152:155],v[68:71]
dr a[92:95],v235 offset:27648
cv v135,v134,v135
cv v134,v132,v133
cv v132,v128,v129
cv v133,v130,v131
mm v[4:7],a[68:71],v[136:139],v[4:7]
dr a[96:99],v235 offset:20480
mm v[4:7],a[64:67],v[132:135],v[4:7]
dr a[100:103],v235 offset:28672
mm v[12:15],a[72:75],v[132:135],v[12:15]
dr a[104:107],v235 offset:21504
mm v[12:15],a[76:79],v[136:139],v[12:15]
dr a[108:111],v235 offset:29696
mm v[76:79],a[76:79],v[152:155],v[76:79]
dr a[112:115],v235 offset:22528
mm v[76:79],a[72:75],v[148:151],v[76:79]
)" R"(dr a[116:119],v235 offset:30720
mm v[84:87],a[80:83],v[148:151],v[84:87]
s_waitcnt lgkmcnt(6)
dr a[120:123],v235 offset:23552
mm v[84:87],a[84:87],v[152:155],v[84:87]
dr a[124:127],v235 offset:31744
mm v[20:23],a[84:87],v[136:139],v[20:23]
ex2 v182,v182
mm v[20:23],a[80:83],v[132:135],v[20:23]
ex2 v183,v183
mm v[28:31],a[88:91],v[132:135],v[28:31]
ex2 v180,v180
mm v[28:31],a[92:95],v[136:139],v[28:31]
ex2 v181,v181
mm v[92:95],a[92:95],v[152:155],v[92:95]
s_waitcnt vmcnt(12) lgkmcnt(0)
s_barrier
mm v[92:95],a[88:91],v[148:151],v[92:95]
bl v232,s[8:11],s40 offen offset:0 sc1 lds
v_smfmac_f32_16x16x64_bf16 a[136:139],a[144:147],v[132:139],v246
dr a[64:67],v232 offset:0
cv v183,v182,v183
v_smfmac_f32_16x16x64_bf16 a[140:143],a[144:147],v[148:155],v246
dr a[68:71],v232 offset:1024
cv v182,v180,v181
mm v[100:103],a[96:99],v[148:151],v[100:103]
ex2 v176,v176
mm v[100:103],a[100:103],v[152:155],v[100:103]
bl v232,s[8:11],s40 offen offset:1024 sc1 lds
mm v[36:39],a[100:103],v[136:139],v[36:39]
dr a[72:75],v232 offset:2048
mm v[36:39],a[96:99],v[132:135],v[36:39]
dr a[76:79],v232 offset:3072
mm v[44:47],a[104:107],v[132:135],v[44:47]
ex2 v177,v177
mm v[44:47],a[108:111],v[136:139],v[44:47]
bl v232,s[8:11],s40 offen offset:2048 sc1 lds
mm v[108:111],a[108:111],v[152:155],v[108:111]
dr a[80:83],v232 offset:4096
cv v180,v176,v177
mm v[108:111],a[104:107],v[148:151],v[108:111]
dr a[84:87],v232 offset:5120
mm v[116:119],a[112:115],v[148:151],v[116:119]
ex2 v178,v178
mm v[116:119],a[116:119],v[152:155],v[116:119]
s_mov_b32 s30,s31
dr a[88:91],v232 offset:6144
s_add_u32 s31,s31,65536
mm v[52:55],a[116:119],v[136:139],v[52:55]
dr a[92:95],v232 offset:7168
mm v[52:55],a[112:115],v[132:135],v[52:55]
bl v232,s[8:11],s40 offen offset:3072 sc1 lds
mm v[60:63],a[120:123],v[132:135],v[60:63]
s_cmp_eq_u32 s31,s3
s_cselect_b32 s40,65536,0
dr a[96:99],v232 offset:8192
mm v[60:63],a[124:127],v[136:139],v[60:63]
s_add_u32 s31,s31,s40
dr a[100:103],v232 offset:9216
mm v[124:127],a[124:127],v[152:155],v[124:127]
ex2 v179,v179
dr a[104:107],v232 offset:10240
ex2 v166,v166
ex2 v167,v167
)" R"(ex2 v164,v164
ex2 v165,v165
ex2 v160,v160
ex2 v161,v161
ex2 v162,v162
ex2 v163,v163
mm v[124:127],a[120:123],v[148:151],v[124:127]
dr a[108:111],v232 offset:11264
cv v181,v178,v179
cv v167,v166,v167
cv v166,v164,v165
cv v164,v160,v161
cv v165,v162,v163
R12:
EB_R12:
s_cbranch_execz EB_R
s_waitcnt lgkmcnt(6)
mm v[128:131],a[64:67],v[64:67],v[200:203]
s_add_u32 s41,s38,s59
dr a[112:115],v232 offset:12288
s_add_u32 m0,s34,40960
mm v[128:131],a[68:71],v[72:75],v[128:131]
s_add_u32 s40,s30,40960
dr a[116:119],v232 offset:13312
v_bfe_u32 v250,v232,7,1
mm v[128:131],a[72:75],v[80:83],v[128:131]
s_lshr_b32 s61,s29,5
bs a[196:199],v232,s[8:11],s41 offen offset:0 sc1
s_add_u32 s41,s35,24576
ex2 v184,v184
ex2 v185,v185
v_lshrrev_b32 v251,8,v232
mm v[128:131],a[76:79],v[88:91],v[128:131]
s_and_b32 s63,s29,4
dr a[120:123],v232 offset:14336
cv v184,v184,v185
mm v[144:147],a[76:79],v[120:123],v[204:207]
s_lshl_b32 s63,s63,1
dr a[124:127],v232 offset:15360
s_add_u32 s61,s61,s63
mm v[144:147],a[72:75],v[112:115],v[144:147]
s_bfe_u32 s62,s29,0x20003
ex2 v186,v186
mm v[144:147],a[68:71],v[104:107],v[144:147]
s_and_b32 s63,s29,3
ex2 v187,v187
mm v[144:147],a[64:67],v[96:99],v[144:147]
s_lshl_b32 s63,s63,2
ex2 v188,v188
mm v[148:151],a[80:83],v[96:99],v[204:207]
s_add_u32 s62,s62,s63
bl v232,s[8:11],s41 offen offset:0 sc1 lds
mm v[148:151],a[84:87],v[104:107],v[148:151]
s_waitcnt lgkmcnt(2)
ex2 v189,v189
mm v[148:151],a[88:91],v[112:115],v[148:151]
s_add_u32 m0,s33,0
ex2 v190,v190
mm v[148:151],a[92:95],v[120:123],v[148:151]
s_add_u32 s41,s35,24576
ex2 v191,v191
mm v[132:135],a[92:95],v[88:91],v[200:203]
s_lshl_b32 s85,s61,2
ex2 v168,v168
)" R"(s_waitcnt vmcnt(10) lgkmcnt(0)
s_barrier
mm v[132:135],a[88:91],v[80:83],v[132:135]
s_add_u32 s85,s85,s24
bl v232,s[8:11],s31 offen offset:0 sc1 lds
mm v[132:135],a[84:87],v[72:75],v[132:135]
s_lshl_b32 s85,s85,4
dr a[64:67],v232 offset:49152
cv v185,v186,v187
mm v[132:135],a[80:83],v[64:67],v[132:135]
s_add_u32 s85,s85,s62
dr a[68:71],v232 offset:57344
cv v186,v188,v189
mm v[136:139],a[96:99],v[64:67],v[200:203]
s_lshl_b32 s85,s85,3
ex2 v169,v169
mm v[136:139],a[100:103],v[72:75],v[136:139]
s_add_u32 s85,s85,0x400a000
bl v232,s[8:11],s31 offen offset:1024 sc1 lds
mm v[136:139],a[104:107],v[80:83],v[136:139]
s_lshl_b32 s61,s61,1
dr a[72:75],v232 offset:50176
cv v187,v190,v191
mm v[136:139],a[108:111],v[88:91],v[136:139]
s_add_u32 s61,s61,s27
dr a[76:79],v232 offset:58368
cv v168,v168,v169
mm v[152:155],a[108:111],v[120:123],v[204:207]
s_mul_i32 s61,s61,20
ex2 v170,v170
mm v[152:155],a[104:107],v[112:115],v[152:155]
s_add_u32 s82,s61,s62
bl v232,s[8:11],s31 offen offset:2048 sc1 lds
mm v[152:155],a[100:103],v[104:107],v[152:155]
s_lshr_b32 s63,s62,2
dr a[80:83],v232 offset:51200
s_add_u32 s83,s61,s63
mm v[152:155],a[96:99],v[96:99],v[152:155]
s_add_u32 s83,s83,16
ex2 v171,v171
mm v[156:159],a[112:115],v[96:99],v[204:207]
s_lshl_b32 s61,s28,11
ex2 v172,v172
mm v[156:159],a[116:119],v[104:107],v[156:159]
s_add_u32 s61,s61,0x3800000
bl v232,s[8:11],s31 offen offset:3072 sc1 lds
mm v[156:159],a[120:123],v[112:115],v[156:159]
s_add_u32 m0,s34,40960
ex2 v173,v173
mm v[156:159],a[124:127],v[120:123],v[156:159]
s_lshl_b32 s82,s82,12
ex2 v174,v174
mm v[140:143],a[124:127],v[88:91],v[200:203]
s_add_u32 s82,s82,s61
ex2 v175,v175
mm v[140:143],a[120:123],v[80:83],v[140:143]
s_lshl_b32 s83,s83,12
dr a[84:87],v232 offset:59392
cv v169,v170,v171
mm v[140:143],a[116:119],v[72:75],v[140:143]
s_add_u32 s83,s83,s61
dr a[88:91],v232 offset:52224
cv v170,v172,v173
mm v[140:143],a[112:115],v[64:67],v[140:143]
)" R"(s_waitcnt lgkmcnt(2)
cv v171,v174,v175
s_and_b32 s63,s62,3
mm v[68:71],a[64:67],v[180:183],v[68:71]
s_lshl_b32 s63,s63,8
dr a[92:95],v232 offset:60416
s_add_u32 s83,s83,s63
mm v[68:71],a[68:71],v[184:187],v[68:71]
s_lshl_b32 s81,s62,12
dr a[96:99],v232 offset:53248
s_add_u32 s81,s81,0x3f00000
mm v[4:7],a[68:71],v[168:171],v[4:7]
s_lshl_b32 s84,s62,7
dr a[100:103],v232 offset:61440
s_add_u32 s84,s84,0x4008000
mm v[4:7],a[64:67],v[164:167],v[4:7]
dr a[104:107],v232 offset:54272
v_lshrrev_b32 v249,2,v232
mm v[12:15],a[72:75],v[164:167],v[12:15]
bl v232,s[8:11],s41 offen offset:1024 sc1 lds
mm v[12:15],a[76:79],v[168:171],v[12:15]
s_add_u32 m0,s33,49152
dr a[108:111],v232 offset:62464
s_add_u32 s41,s38,s59
mm v[76:79],a[76:79],v[184:187],v[76:79]
dr a[112:115],v232 offset:55296
mm v[76:79],a[72:75],v[180:183],v[76:79]
dr a[116:119],v232 offset:63488
mm v[84:87],a[80:83],v[180:183],v[84:87]
s_waitcnt lgkmcnt(5)
dr a[120:123],v232 offset:56320
mm v[84:87],a[84:87],v[184:187],v[84:87]
dr a[124:127],v232 offset:64512
mm v[20:23],a[84:87],v[168:171],v[20:23]
ex2 v150,v150
mm v[20:23],a[80:83],v[164:167],v[20:23]
ex2 v151,v151
mm v[28:31],a[88:91],v[164:167],v[28:31]
ex2 v148,v148
mm v[28:31],a[92:95],v[168:171],v[28:31]
ex2 v149,v149
mm v[92:95],a[92:95],v[184:187],v[92:95]
s_waitcnt vmcnt(11) lgkmcnt(0)
s_barrier
mm v[92:95],a[88:91],v[180:183],v[92:95]
bl v232,s[8:11],s40 offen offset:0 sc1 lds
v_smfmac_f32_16x16x64_bf16 a[136:139],a[144:147],v[164:171],v246
dr a[64:67],v235 offset:0
cv v151,v150,v151
v_smfmac_f32_16x16x64_bf16 a[140:143],a[144:147],v[180:187],v246
dr a[68:71],v235 offset:1024
cv v150,v148,v149
mm v[100:103],a[96:99],v[180:183],v[100:103]
ex2 v144,v144
mm v[100:103],a[100:103],v[184:187],v[100:103]
bl v232,s[8:11],s40 offen offset:1024 sc1 lds
mm v[36:39],a[100:103],v[168:171],v[36:39]
dr a[72:75],v235 offset:2048
mm v[36:39],a[96:99],v[164:167],v[36:39]
dr a[76:79],v235 offset:3072
mm v[44:47],a[104:107],v[164:167],v[44:47]
ex2 v145,v145
mm v[44:47],a[108:111],v[168:171],v[44:47]
bl v232,s[8:11],s40 offen offset:2048 sc1 lds
)" R"(mm v[108:111],a[108:111],v[184:187],v[108:111]
dr a[80:83],v235 offset:4096
cv v148,v144,v145
mm v[108:111],a[104:107],v[180:183],v[108:111]
dr a[84:87],v235 offset:5120
mm v[116:119],a[112:115],v[180:183],v[116:119]
ex2 v146,v146
mm v[116:119],a[116:119],v[184:187],v[116:119]
bl v232,s[8:11],s40 offen offset:3072 sc1 lds
mm v[52:55],a[116:119],v[168:171],v[52:55]
s_add_u32 m0,s34,65536
dr a[88:91],v235 offset:6144
s_add_u32 s40,s31,32768
mm v[52:55],a[112:115],v[164:167],v[52:55]
dr a[92:95],v235 offset:7168
mm v[60:63],a[120:123],v[164:167],v[60:63]
ex2 v147,v147
mm v[60:63],a[124:127],v[168:171],v[60:63]
bs a[200:203],v232,s[8:11],s41 offen offset:1024 sc1
dr a[96:99],v235 offset:8192
s_add_u32 s41,s35,32768
cv v149,v146,v147
ex2 v152,v152
mm v[124:127],a[124:127],v[184:187],v[124:127]
dr a[100:103],v235 offset:9216
mm v[124:127],a[120:123],v[180:183],v[124:127]
s_waitcnt lgkmcnt(4)
ex2 v153,v153
mm v[160:163],a[64:67],v[64:67],v[200:203]
dr a[104:107],v235 offset:10240
cv v152,v152,v153
mm v[160:163],a[68:71],v[72:75],v[160:163]
dr a[108:111],v235 offset:11264
mm v[160:163],a[72:75],v[80:83],v[160:163]
bl v232,s[8:11],s41 offen offset:0 sc1 lds
mm v[160:163],a[76:79],v[88:91],v[160:163]
s_add_u32 m0,s33,131072
dr a[112:115],v235 offset:12288
s_add_u32 s41,s35,32768
mm v[176:179],a[76:79],v[120:123],v[204:207]
dr a[116:119],v235 offset:13312
mm v[176:179],a[72:75],v[112:115],v[176:179]
dr a[120:123],v235 offset:14336
mm v[176:179],a[68:71],v[104:107],v[176:179]
dr a[124:127],v235 offset:15360
mm v[176:179],a[64:67],v[96:99],v[176:179]
ex2 v154,v154
mm v[180:183],a[80:83],v[96:99],v[204:207]
ex2 v155,v155
mm v[180:183],a[84:87],v[104:107],v[180:183]
s_waitcnt lgkmcnt(4)
ex2 v156,v156
mm v[180:183],a[88:91],v[112:115],v[180:183]
ex2 v157,v157
mm v[180:183],a[92:95],v[120:123],v[180:183]
ex2 v158,v158
mm v[164:167],a[92:95],v[88:91],v[200:203]
s_waitcnt vmcnt(13) lgkmcnt(0)
s_barrier
mm v[164:167],a[88:91],v[80:83],v[164:167]
bl v232,s[8:11],s40 offen offset:0 sc1 lds
mm v[164:167],a[84:87],v[72:75],v[164:167]
dr a[64:67],v235 offset:16384
cv v153,v154,v155
)" R"(mm v[164:167],a[80:83],v[64:67],v[164:167]
dr a[68:71],v235 offset:24576
cv v154,v156,v157
mm v[168:171],a[96:99],v[64:67],v[200:203]
ex2 v159,v159
mm v[168:171],a[100:103],v[72:75],v[168:171]
bl v232,s[8:11],s40 offen offset:1024 sc1 lds
mm v[168:171],a[104:107],v[80:83],v[168:171]
dr a[72:75],v235 offset:17408
cv v155,v158,v159
mm v[168:171],a[108:111],v[88:91],v[168:171]
dr a[76:79],v235 offset:25600
mm v[184:187],a[108:111],v[120:123],v[204:207]
ex2 v136,v136
mm v[184:187],a[104:107],v[112:115],v[184:187]
bl v232,s[8:11],s40 offen offset:2048 sc1 lds
mm v[184:187],a[100:103],v[104:107],v[184:187]
dr a[80:83],v235 offset:18432
mm v[184:187],a[96:99],v[96:99],v[184:187]
ex2 v137,v137
mm v[188:191],a[112:115],v[96:99],v[204:207]
ex2 v138,v138
mm v[188:191],a[116:119],v[104:107],v[188:191]
bl v232,s[8:11],s40 offen offset:3072 sc1 lds
mm v[188:191],a[120:123],v[112:115],v[188:191]
s_add_u32 m0,s34,65536
ex2 v139,v139
mm v[188:191],a[124:127],v[120:123],v[188:191]
s_add_u32 s40,s31,8192
ex2 v140,v140
mm v[172:175],a[124:127],v[88:91],v[200:203]
ex2 v141,v141
mm v[172:175],a[120:123],v[80:83],v[172:175]
ex2 v142,v142
mm v[172:175],a[116:119],v[72:75],v[172:175]
ex2 v143,v143
mm v[172:175],a[112:115],v[64:67],v[172:175]
s_waitcnt lgkmcnt(0)
dr a[84:87],v235 offset:26624
cv v136,v136,v137
bl v232,s[8:11],s41 offen offset:1024 sc1 lds
mm v[68:71],a[64:67],v[148:151],v[68:71]
s_add_u32 m0,s33,147456
dr a[88:91],v235 offset:19456
cv v137,v138,v139
ex2 v134,v134
ex2 v135,v135
ex2 v132,v132
ex2 v133,v133
ex2 v128,v128
ex2 v129,v129
ex2 v130,v130
ex2 v131,v131
cv v138,v140,v141
cv v139,v142,v143
mm v[68:71],a[68:71],v[152:155],v[68:71]
dr a[92:95],v235 offset:27648
cv v135,v134,v135
cv v134,v132,v133
cv v132,v128,v129
cv v133,v130,v131
mm v[4:7],a[68:71],v[136:139],v[4:7]
dr a[96:99],v235 offset:20480
mm v[4:7],a[64:67],v[132:135],v[4:7]
)" R"(dr a[100:103],v235 offset:28672
mm v[12:15],a[72:75],v[132:135],v[12:15]
dr a[104:107],v235 offset:21504
mm v[12:15],a[76:79],v[136:139],v[12:15]
dr a[108:111],v235 offset:29696
mm v[76:79],a[76:79],v[152:155],v[76:79]
dr a[112:115],v235 offset:22528
mm v[76:79],a[72:75],v[148:151],v[76:79]
dr a[116:119],v235 offset:30720
mm v[84:87],a[80:83],v[148:151],v[84:87]
s_waitcnt lgkmcnt(6)
dr a[120:123],v235 offset:23552
mm v[84:87],a[84:87],v[152:155],v[84:87]
dr a[124:127],v235 offset:31744
mm v[20:23],a[84:87],v[136:139],v[20:23]
v_cmp_eq_u32 vcc,v250,v251
v_and_b32 v250,0xf0,v232
v_mov_b32 v251,0x1000
v_cndmask_b32 v239,v251,v250,vcc
mm v[20:23],a[80:83],v[132:135],v[20:23]
ex2 v182,v182
mm v[28:31],a[88:91],v[132:135],v[28:31]
ex2 v183,v183
mm v[28:31],a[92:95],v[136:139],v[28:31]
ex2 v180,v180
mm v[92:95],a[92:95],v[152:155],v[92:95]
s_waitcnt vmcnt(12) lgkmcnt(0)
s_barrier
mm v[92:95],a[88:91],v[148:151],v[92:95]
bl v232,s[8:11],s40 offen offset:0 sc1 lds
v_smfmac_f32_16x16x64_bf16 a[136:139],a[144:147],v[132:139],v246
dr a[64:67],v232 offset:0
v_add_u32 v239,0x1e000,v239
v_smfmac_f32_16x16x64_bf16 a[140:143],a[144:147],v[148:155],v246
dr a[68:71],v232 offset:1024
v_xor_b32 v250,v232,v249
mm v[100:103],a[96:99],v[148:151],v[100:103]
ex2 v181,v181
mm v[100:103],a[100:103],v[152:155],v[100:103]
bl v232,s[8:11],s40 offen offset:1024 sc1 lds
mm v[36:39],a[100:103],v[136:139],v[36:39]
dr a[72:75],v232 offset:2048
v_and_b32 v250,0xc0,v250
mm v[36:39],a[96:99],v[132:135],v[36:39]
dr a[76:79],v232 offset:3072
v_lshl_add_u32 v250,v250,2,v250
mm v[44:47],a[104:107],v[132:135],v[44:47]
ex2 v176,v176
mm v[44:47],a[108:111],v[136:139],v[44:47]
bl v232,s[8:11],s40 offen offset:2048 sc1 lds
mm v[108:111],a[108:111],v[152:155],v[108:111]
dr a[80:83],v232 offset:4096
v_xor_b32 v243,v250,v232
mm v[108:111],a[104:107],v[148:151],v[108:111]
dr a[84:87],v232 offset:5120
cv v183,v182,v183
mm v[116:119],a[112:115],v[148:151],v[116:119]
ex2 v177,v177
mm v[116:119],a[116:119],v[152:155],v[116:119]
s_mov_b32 s30,s31
dr a[88:91],v232 offset:6144
cv v182,v180,v181
mm v[52:55],a[116:119],v[136:139],v[52:55]
s_add_u32 s31,s31,65536
)" R"(dr a[92:95],v232 offset:7168
cv v180,v176,v177
mm v[52:55],a[112:115],v[132:135],v[52:55]
bl v232,s[8:11],s40 offen offset:3072 sc1 lds
mm v[60:63],a[120:123],v[132:135],v[60:63]
s_cmp_eq_u32 s31,s3
s_cselect_b32 s40,65536,0
dr a[96:99],v232 offset:8192
mm v[60:63],a[124:127],v[136:139],v[60:63]
s_add_u32 s31,s31,s40
dr a[100:103],v232 offset:9216
mm v[124:127],a[124:127],v[152:155],v[124:127]
ex2 v178,v178
dr a[104:107],v232 offset:10240
ex2 v179,v179
ex2 v166,v166
ex2 v167,v167
ex2 v164,v164
ex2 v165,v165
ex2 v160,v160
ex2 v161,v161
ex2 v162,v162
ex2 v163,v163
mm v[124:127],a[120:123],v[148:151],v[124:127]
dr a[108:111],v232 offset:11264
cv v181,v178,v179
cv v167,v166,v167
cv v166,v164,v165
cv v164,v160,v161
cv v165,v162,v163
R13:
EB_R13:
s_cbranch_execz EB_R
s_waitcnt lgkmcnt(6)
mm v[128:131],a[64:67],v[64:67],v[200:203]
s_add_u32 s41,s38,s59
dr a[112:115],v232 offset:12288
s_add_u32 m0,s34,73728
mm v[128:131],a[68:71],v[72:75],v[128:131]
EB_18:
s_cbranch_execz EB_R
s_add_u32 s40,s30,40960
dr a[116:119],v232 offset:13312
mm v[128:131],a[72:75],v[80:83],v[128:131]
bs a[204:207],v232,s[8:11],s41 offen offset:2048 sc1
s_add_u32 s41,s35,40960
ex2 v184,v184
ex2 v185,v185
mm v[128:131],a[76:79],v[88:91],v[128:131]
dr a[120:123],v232 offset:14336
cv v184,v184,v185
mm v[144:147],a[76:79],v[120:123],v[204:207]
dr a[124:127],v232 offset:15360
mm v[144:147],a[72:75],v[112:115],v[144:147]
ex2 v186,v186
mm v[144:147],a[68:71],v[104:107],v[144:147]
ex2 v187,v187
mm v[144:147],a[64:67],v[96:99],v[144:147]
ex2 v188,v188
mm v[148:151],a[80:83],v[96:99],v[204:207]
bl v232,s[8:11],s41 offen offset:0 sc1 lds
mm v[148:151],a[84:87],v[104:107],v[148:151]
s_waitcnt lgkmcnt(2)
ex2 v189,v189
)" R"(mm v[148:151],a[88:91],v[112:115],v[148:151]
s_add_u32 m0,s33,0
ex2 v190,v190
mm v[148:151],a[92:95],v[120:123],v[148:151]
s_add_u32 s41,s38,s59
ex2 v191,v191
mm v[132:135],a[92:95],v[88:91],v[200:203]
s_waitcnt vmcnt(10) lgkmcnt(0)
s_barrier
mm v[132:135],a[88:91],v[80:83],v[132:135]
bl v232,s[8:11],s31 offen offset:0 sc1 lds
mm v[132:135],a[84:87],v[72:75],v[132:135]
dr a[64:67],v232 offset:49152
cv v185,v186,v187
mm v[132:135],a[80:83],v[64:67],v[132:135]
dr a[68:71],v232 offset:57344
cv v186,v188,v189
mm v[136:139],a[96:99],v[64:67],v[200:203]
ex2 v168,v168
mm v[136:139],a[100:103],v[72:75],v[136:139]
bl v232,s[8:11],s31 offen offset:1024 sc1 lds
mm v[136:139],a[104:107],v[80:83],v[136:139]
dr a[72:75],v232 offset:50176
cv v187,v190,v191
mm v[136:139],a[108:111],v[88:91],v[136:139]
dr a[76:79],v232 offset:58368
mm v[152:155],a[108:111],v[120:123],v[204:207]
ex2 v169,v169
mm v[152:155],a[104:107],v[112:115],v[152:155]
bl v232,s[8:11],s31 offen offset:2048 sc1 lds
mm v[152:155],a[100:103],v[104:107],v[152:155]
dr a[80:83],v232 offset:51200
cv v168,v168,v169
mm v[152:155],a[96:99],v[96:99],v[152:155]
ex2 v170,v170
mm v[156:159],a[112:115],v[96:99],v[204:207]
ex2 v171,v171
mm v[156:159],a[116:119],v[104:107],v[156:159]
bl v232,s[8:11],s31 offen offset:3072 sc1 lds
mm v[156:159],a[120:123],v[112:115],v[156:159]
s_add_u32 m0,s33,49152
ex2 v172,v172
mm v[156:159],a[124:127],v[120:123],v[156:159]
ex2 v173,v173
mm v[140:143],a[124:127],v[88:91],v[200:203]
ex2 v174,v174
mm v[140:143],a[120:123],v[80:83],v[140:143]
ex2 v175,v175
mm v[140:143],a[116:119],v[72:75],v[140:143]
dr a[84:87],v232 offset:59392
cv v169,v170,v171
mm v[140:143],a[112:115],v[64:67],v[140:143]
s_waitcnt lgkmcnt(1)
dr a[88:91],v232 offset:52224
cv v170,v172,v173
mm v[68:71],a[64:67],v[180:183],v[68:71]
dr a[92:95],v232 offset:60416
cv v171,v174,v175
mm v[68:71],a[68:71],v[184:187],v[68:71]
dr a[96:99],v232 offset:53248
mm v[4:7],a[68:71],v[168:171],v[4:7]
dr a[100:103],v232 offset:61440
mm v[4:7],a[64:67],v[164:167],v[4:7]
dr a[104:107],v232 offset:54272
)" R"(mm v[12:15],a[72:75],v[164:167],v[12:15]
dr a[108:111],v232 offset:62464
mm v[12:15],a[76:79],v[168:171],v[12:15]
bs a[208:211],v232,s[8:11],s41 offen offset:3072 sc1
dr a[112:115],v232 offset:55296
s_add_u32 s41,s35,40960
ex2 v150,v150
ex2 v151,v151
mm v[76:79],a[76:79],v[184:187],v[76:79]
dr a[116:119],v232 offset:63488
cv v151,v150,v151
mm v[76:79],a[72:75],v[180:183],v[76:79]
dr a[120:123],v232 offset:56320
mm v[84:87],a[80:83],v[180:183],v[84:87]
s_waitcnt lgkmcnt(6)
dr a[124:127],v232 offset:64512
mm v[84:87],a[84:87],v[184:187],v[84:87]
ex2 v148,v148
mm v[20:23],a[84:87],v[168:171],v[20:23]
ex2 v149,v149
mm v[20:23],a[80:83],v[164:167],v[20:23]
ex2 v144,v144
mm v[28:31],a[88:91],v[164:167],v[28:31]
ex2 v145,v145
mm v[28:31],a[92:95],v[168:171],v[28:31]
ex2 v146,v146
mm v[92:95],a[92:95],v[184:187],v[92:95]
s_waitcnt vmcnt(11) lgkmcnt(0)
s_barrier
mm v[92:95],a[88:91],v[180:183],v[92:95]
bl v232,s[8:11],s40 offen offset:0 sc1 lds
v_smfmac_f32_16x16x64_bf16 a[136:139],a[144:147],v[164:171],v246
dr a[64:67],v235 offset:0
cv v150,v148,v149
v_smfmac_f32_16x16x64_bf16 a[140:143],a[144:147],v[180:187],v246
dr a[68:71],v235 offset:1024
cv v148,v144,v145
mm v[100:103],a[96:99],v[180:183],v[100:103]
ex2 v147,v147
mm v[100:103],a[100:103],v[184:187],v[100:103]
bl v232,s[8:11],s40 offen offset:1024 sc1 lds
mm v[36:39],a[100:103],v[168:171],v[36:39]
dr a[72:75],v235 offset:2048
cv v149,v146,v147
mm v[36:39],a[96:99],v[164:167],v[36:39]
dr a[76:79],v235 offset:3072
mm v[44:47],a[104:107],v[164:167],v[44:47]
ex2 v152,v152
mm v[44:47],a[108:111],v[168:171],v[44:47]
bl v232,s[8:11],s40 offen offset:2048 sc1 lds
mm v[108:111],a[108:111],v[184:187],v[108:111]
dr a[80:83],v235 offset:4096
mm v[108:111],a[104:107],v[180:183],v[108:111]
dr a[84:87],v235 offset:5120
mm v[116:119],a[112:115],v[180:183],v[116:119]
ex2 v153,v153
mm v[116:119],a[116:119],v[184:187],v[116:119]
bl v232,s[8:11],s40 offen offset:3072 sc1 lds
mm v[52:55],a[116:119],v[168:171],v[52:55]
s_add_u32 m0,s34,73728
dr a[88:91],v235 offset:6144
s_add_u32 s40,s31,32768
mm v[52:55],a[112:115],v[164:167],v[52:55]
dr a[92:95],v235 offset:7168
)" R"(cv v152,v152,v153
mm v[60:63],a[120:123],v[164:167],v[60:63]
ex2 v154,v154
mm v[60:63],a[124:127],v[168:171],v[60:63]
bl v232,s[8:11],s41 offen offset:1024 sc1 lds
mm v[124:127],a[124:127],v[184:187],v[124:127]
s_add_u32 s41,s39,s58
dr a[96:99],v235 offset:8192
s_add_u32 m0,s33,131072
mm v[124:127],a[120:123],v[180:183],v[124:127]
s_waitcnt lgkmcnt(3)
dr a[100:103],v235 offset:9216
mm v[160:163],a[64:67],v[64:67],v[200:203]
ex2 v155,v155
mm v[160:163],a[68:71],v[72:75],v[160:163]
dr a[104:107],v235 offset:10240
cv v153,v154,v155
mm v[160:163],a[72:75],v[80:83],v[160:163]
dr a[108:111],v235 offset:11264
mm v[160:163],a[76:79],v[88:91],v[160:163]
dr a[112:115],v235 offset:12288
mm v[176:179],a[76:79],v[120:123],v[204:207]
bs a[0:3],v232,s[8:11],s41 offen offset:0 sc1
dr a[116:119],v235 offset:13312
ex2 v156,v156
ex2 v157,v157
mm v[176:179],a[72:75],v[112:115],v[176:179]
dr a[120:123],v235 offset:14336
cv v154,v156,v157
mm v[176:179],a[68:71],v[104:107],v[176:179]
dr a[124:127],v235 offset:15360
mm v[176:179],a[64:67],v[96:99],v[176:179]
ex2 v158,v158
mm v[180:183],a[80:83],v[96:99],v[204:207]
ex2 v159,v159
mm v[180:183],a[84:87],v[104:107],v[180:183]
s_waitcnt lgkmcnt(4)
ex2 v136,v136
mm v[180:183],a[88:91],v[112:115],v[180:183]
ex2 v137,v137
mm v[180:183],a[92:95],v[120:123],v[180:183]
ex2 v138,v138
mm v[164:167],a[92:95],v[88:91],v[200:203]
s_waitcnt vmcnt(13) lgkmcnt(0)
s_barrier
mm v[164:167],a[88:91],v[80:83],v[164:167]
bl v232,s[8:11],s40 offen offset:0 sc1 lds
mm v[164:167],a[84:87],v[72:75],v[164:167]
dr a[64:67],v235 offset:16384
cv v155,v158,v159
mm v[164:167],a[80:83],v[64:67],v[164:167]
dr a[68:71],v235 offset:24576
cv v136,v136,v137
mm v[168:171],a[96:99],v[64:67],v[200:203]
ex2 v139,v139
mm v[168:171],a[100:103],v[72:75],v[168:171]
bl v232,s[8:11],s40 offen offset:1024 sc1 lds
mm v[168:171],a[104:107],v[80:83],v[168:171]
dr a[72:75],v235 offset:17408
cv v137,v138,v139
mm v[168:171],a[108:111],v[88:91],v[168:171]
dr a[76:79],v235 offset:25600
mm v[184:187],a[108:111],v[120:123],v[204:207]
ex2 v140,v140
)" R"(mm v[184:187],a[104:107],v[112:115],v[184:187]
bl v232,s[8:11],s40 offen offset:2048 sc1 lds
mm v[184:187],a[100:103],v[104:107],v[184:187]
dr a[80:83],v235 offset:18432
mm v[184:187],a[96:99],v[96:99],v[184:187]
ex2 v141,v141
mm v[188:191],a[112:115],v[96:99],v[204:207]
ex2 v142,v142
mm v[188:191],a[116:119],v[104:107],v[188:191]
bl v232,s[8:11],s40 offen offset:3072 sc1 lds
mm v[188:191],a[120:123],v[112:115],v[188:191]
s_add_u32 s40,s31,8192
ex2 v143,v143
mm v[188:191],a[124:127],v[120:123],v[188:191]
s_add_u32 m0,s33,147456
dr a[84:87],v235 offset:26624
cv v138,v140,v141
mm v[172:175],a[124:127],v[88:91],v[200:203]
ex2 v134,v134
mm v[172:175],a[120:123],v[80:83],v[172:175]
ex2 v135,v135
mm v[172:175],a[116:119],v[72:75],v[172:175]
dr a[88:91],v235 offset:19456
cv v139,v142,v143
mm v[172:175],a[112:115],v[64:67],v[172:175]
s_waitcnt lgkmcnt(2)
ex2 v132,v132
mm v[68:71],a[64:67],v[148:151],v[68:71]
s_lshr_b32 s46,s38,15
ex2 v133,v133
ex2 v128,v128
ex2 v129,v129
ex2 v130,v130
ex2 v131,v131
mm v[68:71],a[68:71],v[152:155],v[68:71]
s_lshl_b32 s46,s46,2
dr a[92:95],v235 offset:27648
cv v135,v134,v135
cv v134,v132,v133
cv v132,v128,v129
cv v133,v130,v131
mm v[4:7],a[68:71],v[136:139],v[4:7]
s_add_u32 s46,s46,s24
dr a[96:99],v235 offset:20480
s_lshl_b32 s46,s46,3
mm v[4:7],a[64:67],v[132:135],v[4:7]
s_add_u32 s46,s46,0x4018000
dr a[100:103],v235 offset:28672
mm v[12:15],a[72:75],v[132:135],v[12:15]
dr a[104:107],v235 offset:21504
mm v[12:15],a[76:79],v[136:139],v[12:15]
dr a[108:111],v235 offset:29696
mm v[76:79],a[76:79],v[152:155],v[76:79]
dr a[112:115],v235 offset:22528
mm v[76:79],a[72:75],v[148:151],v[76:79]
dr a[116:119],v235 offset:30720
mm v[84:87],a[80:83],v[148:151],v[84:87]
s_waitcnt lgkmcnt(6)
dr a[120:123],v235 offset:23552
mm v[84:87],a[84:87],v[152:155],v[84:87]
dr a[124:127],v235 offset:31744
mm v[20:23],a[84:87],v[136:139],v[20:23]
ex2 v182,v182
mm v[20:23],a[80:83],v[132:135],v[20:23]
)" R"(ex2 v183,v183
mm v[28:31],a[88:91],v[132:135],v[28:31]
ex2 v180,v180
mm v[28:31],a[92:95],v[136:139],v[28:31]
ex2 v181,v181
mm v[92:95],a[92:95],v[152:155],v[92:95]
s_waitcnt vmcnt(11) lgkmcnt(0)
s_barrier
mm v[92:95],a[88:91],v[148:151],v[92:95]
bl v232,s[8:11],s40 offen offset:0 sc1 lds
v_smfmac_f32_16x16x64_bf16 a[136:139],a[144:147],v[132:139],v246
dr a[64:67],v232 offset:0
cv v183,v182,v183
v_smfmac_f32_16x16x64_bf16 a[140:143],a[144:147],v[148:155],v246
dr a[68:71],v232 offset:1024
cv v182,v180,v181
mm v[100:103],a[96:99],v[148:151],v[100:103]
ex2 v176,v176
mm v[100:103],a[100:103],v[152:155],v[100:103]
bl v232,s[8:11],s40 offen offset:1024 sc1 lds
mm v[36:39],a[100:103],v[136:139],v[36:39]
dr a[72:75],v232 offset:2048
mm v[36:39],a[96:99],v[132:135],v[36:39]
dr a[76:79],v232 offset:3072
mm v[44:47],a[104:107],v[132:135],v[44:47]
ex2 v177,v177
mm v[44:47],a[108:111],v[136:139],v[44:47]
bl v232,s[8:11],s40 offen offset:2048 sc1 lds
mm v[108:111],a[108:111],v[152:155],v[108:111]
dr a[80:83],v232 offset:4096
cv v180,v176,v177
mm v[108:111],a[104:107],v[148:151],v[108:111]
dr a[84:87],v232 offset:5120
mm v[116:119],a[112:115],v[148:151],v[116:119]
ex2 v178,v178
mm v[116:119],a[116:119],v[152:155],v[116:119]
s_mov_b32 s30,s31
dr a[88:91],v232 offset:6144
s_add_u32 s31,s31,65536
mm v[52:55],a[116:119],v[136:139],v[52:55]
dr a[92:95],v232 offset:7168
mm v[52:55],a[112:115],v[132:135],v[52:55]
bl v232,s[8:11],s40 offen offset:3072 sc1 lds
mm v[60:63],a[120:123],v[132:135],v[60:63]
s_cmp_eq_u32 s31,s3
s_cselect_b32 s40,65536,0
dr a[96:99],v232 offset:8192
mm v[60:63],a[124:127],v[136:139],v[60:63]
s_add_u32 s31,s31,s40
dr a[100:103],v232 offset:9216
mm v[124:127],a[124:127],v[152:155],v[124:127]
ex2 v179,v179
dr a[104:107],v232 offset:10240
ex2 v166,v166
ex2 v167,v167
ex2 v164,v164
ex2 v165,v165
ex2 v160,v160
ex2 v161,v161
ex2 v162,v162
ex2 v163,v163
mm v[124:127],a[120:123],v[148:151],v[124:127]
dr a[108:111],v232 offset:11264
cv v181,v178,v179
)" R"(s_waitcnt vmcnt(14)
buffer_store_dwordx2 v[252:253],v254,s[8:11],s46 offen sc1
cv v167,v166,v167
cv v166,v164,v165
cv v164,v160,v161
cv v165,v162,v163
R14:
EB_R14:
s_cbranch_execz EB_R
s_waitcnt lgkmcnt(6)
mm v[128:131],a[64:67],v[64:67],v[200:203]
s_mov_b32 s46,s84
dr a[112:115],v232 offset:12288
v_lshrrev_b32 v242,1,v232
mm v[128:131],a[68:71],v[72:75],v[128:131]
s_add_u32 s41,s39,s58
dr a[116:119],v232 offset:13312
v_and_b32 v242,120,v242
mm v[128:131],a[72:75],v[80:83],v[128:131]
s_add_u32 m0,s34,81920
buffer_load_dwordx2 v[240:241],v242,s[8:11],s46 offen sc1
mm v[128:131],a[76:79],v[88:91],v[128:131]
s_add_u32 s40,s30,40960
bs a[4:7],v232,s[8:11],s41 offen offset:1024 sc1
dr a[120:123],v232 offset:14336
s_add_u32 s41,s35,49152
ex2 v184,v184
ex2 v185,v185
mm v[144:147],a[76:79],v[120:123],v[204:207]
dr a[124:127],v232 offset:15360
cv v184,v184,v185
mm v[144:147],a[72:75],v[112:115],v[144:147]
ex2 v186,v186
mm v[144:147],a[68:71],v[104:107],v[144:147]
ex2 v187,v187
mm v[144:147],a[64:67],v[96:99],v[144:147]
ex2 v188,v188
mm v[148:151],a[80:83],v[96:99],v[204:207]
bl v232,s[8:11],s41 offen offset:0 sc1 lds
mm v[148:151],a[84:87],v[104:107],v[148:151]
s_waitcnt lgkmcnt(2)
ex2 v189,v189
mm v[148:151],a[88:91],v[112:115],v[148:151]
s_add_u32 m0,s33,0
ex2 v190,v190
mm v[148:151],a[92:95],v[120:123],v[148:151]
s_add_u32 s41,s39,s58
ex2 v191,v191
mm v[132:135],a[92:95],v[88:91],v[200:203]
s_waitcnt vmcnt(11) lgkmcnt(0)
s_barrier
mm v[132:135],a[88:91],v[80:83],v[132:135]
bl v232,s[8:11],s31 offen offset:0 sc1 lds
mm v[132:135],a[84:87],v[72:75],v[132:135]
dr a[64:67],v232 offset:49152
cv v185,v186,v187
mm v[132:135],a[80:83],v[64:67],v[132:135]
dr a[68:71],v232 offset:57344
cv v186,v188,v189
mm v[136:139],a[96:99],v[64:67],v[200:203]
ex2 v168,v168
mm v[136:139],a[100:103],v[72:75],v[136:139]
bl v232,s[8:11],s31 offen offset:1024 sc1 lds
mm v[136:139],a[104:107],v[80:83],v[136:139]
)" R"(dr a[72:75],v232 offset:50176
cv v187,v190,v191
mm v[136:139],a[108:111],v[88:91],v[136:139]
dr a[76:79],v232 offset:58368
mm v[152:155],a[108:111],v[120:123],v[204:207]
ex2 v169,v169
mm v[152:155],a[104:107],v[112:115],v[152:155]
bl v232,s[8:11],s31 offen offset:2048 sc1 lds
mm v[152:155],a[100:103],v[104:107],v[152:155]
dr a[80:83],v232 offset:51200
cv v168,v168,v169
mm v[152:155],a[96:99],v[96:99],v[152:155]
ex2 v170,v170
mm v[156:159],a[112:115],v[96:99],v[204:207]
ex2 v171,v171
mm v[156:159],a[116:119],v[104:107],v[156:159]
bl v232,s[8:11],s31 offen offset:3072 sc1 lds
mm v[156:159],a[120:123],v[112:115],v[156:159]
s_add_u32 m0,s33,49152
ex2 v172,v172
mm v[156:159],a[124:127],v[120:123],v[156:159]
ex2 v173,v173
mm v[140:143],a[124:127],v[88:91],v[200:203]
ex2 v174,v174
mm v[140:143],a[120:123],v[80:83],v[140:143]
ex2 v175,v175
mm v[140:143],a[116:119],v[72:75],v[140:143]
dr a[84:87],v232 offset:59392
cv v169,v170,v171
mm v[140:143],a[112:115],v[64:67],v[140:143]
s_waitcnt lgkmcnt(1)
dr a[88:91],v232 offset:52224
cv v170,v172,v173
mm v[68:71],a[64:67],v[180:183],v[68:71]
dr a[92:95],v232 offset:60416
cv v171,v174,v175
mm v[68:71],a[68:71],v[184:187],v[68:71]
dr a[96:99],v232 offset:53248
mm v[4:7],a[68:71],v[168:171],v[4:7]
dr a[100:103],v232 offset:61440
mm v[4:7],a[64:67],v[164:167],v[4:7]
dr a[104:107],v232 offset:54272
mm v[12:15],a[72:75],v[164:167],v[12:15]
dr a[108:111],v232 offset:62464
mm v[12:15],a[76:79],v[168:171],v[12:15]
dr a[112:115],v232 offset:55296
mm v[76:79],a[76:79],v[184:187],v[76:79]
dr a[116:119],v232 offset:63488
mm v[76:79],a[72:75],v[180:183],v[76:79]
dr a[120:123],v232 offset:56320
mm v[84:87],a[80:83],v[180:183],v[84:87]
s_waitcnt lgkmcnt(6)
dr a[124:127],v232 offset:64512
mm v[84:87],a[84:87],v[184:187],v[84:87]
ex2 v150,v150
mm v[20:23],a[84:87],v[168:171],v[20:23]
ex2 v151,v151
mm v[20:23],a[80:83],v[164:167],v[20:23]
ex2 v148,v148
mm v[28:31],a[88:91],v[164:167],v[28:31]
ex2 v149,v149
mm v[28:31],a[92:95],v[168:171],v[28:31]
ex2 v144,v144
mm v[92:95],a[92:95],v[184:187],v[92:95]
)" R"(s_waitcnt vmcnt(11) lgkmcnt(0)
s_barrier
mm v[92:95],a[88:91],v[180:183],v[92:95]
bl v232,s[8:11],s40 offen offset:0 sc1 lds
v_smfmac_f32_16x16x64_bf16 a[136:139],a[144:147],v[164:171],v246
dr a[64:67],v235 offset:0
cv v151,v150,v151
v_smfmac_f32_16x16x64_bf16 a[140:143],a[144:147],v[180:187],v246
dr a[68:71],v235 offset:1024
cv v150,v148,v149
mm v[100:103],a[96:99],v[180:183],v[100:103]
ex2 v145,v145
mm v[100:103],a[100:103],v[184:187],v[100:103]
bl v232,s[8:11],s40 offen offset:1024 sc1 lds
mm v[36:39],a[100:103],v[168:171],v[36:39]
dr a[72:75],v235 offset:2048
cv v148,v144,v145
mm v[36:39],a[96:99],v[164:167],v[36:39]
dr a[76:79],v235 offset:3072
mm v[44:47],a[104:107],v[164:167],v[44:47]
ex2 v146,v146
mm v[44:47],a[108:111],v[168:171],v[44:47]
bl v232,s[8:11],s40 offen offset:2048 sc1 lds
mm v[108:111],a[108:111],v[184:187],v[108:111]
dr a[80:83],v235 offset:4096
mm v[108:111],a[104:107],v[180:183],v[108:111]
dr a[84:87],v235 offset:5120
mm v[116:119],a[112:115],v[180:183],v[116:119]
ex2 v147,v147
mm v[116:119],a[116:119],v[184:187],v[116:119]
bl v232,s[8:11],s40 offen offset:3072 sc1 lds
mm v[52:55],a[116:119],v[168:171],v[52:55]
s_add_u32 m0,s34,81920
dr a[88:91],v235 offset:6144
s_add_u32 s40,s31,32768
mm v[52:55],a[112:115],v[164:167],v[52:55]
dr a[92:95],v235 offset:7168
cv v149,v146,v147
mm v[60:63],a[120:123],v[164:167],v[60:63]
ex2 v152,v152
mm v[60:63],a[124:127],v[168:171],v[60:63]
bs a[8:11],v233,s[8:11],s41 offen offset:0 sc1
dr a[96:99],v235 offset:8192
s_add_u32 s41,s35,49152
ex2 v153,v153
ex2 v154,v154
mm v[124:127],a[124:127],v[184:187],v[124:127]
dr a[100:103],v235 offset:9216
cv v152,v152,v153
mm v[124:127],a[120:123],v[180:183],v[124:127]
s_waitcnt lgkmcnt(4)
ex2 v155,v155
mm v[160:163],a[64:67],v[64:67],v[200:203]
dr a[104:107],v235 offset:10240
cv v153,v154,v155
mm v[160:163],a[68:71],v[72:75],v[160:163]
s_waitcnt vmcnt(11)
v_cmp_ne_u64 vcc,s[54:55],v[240:241]
s_cbranch_vccnz FA21_S
FA21_D:
mm v[160:163],a[72:75],v[80:83],v[160:163]
dr a[108:111],v235 offset:11264
mm v[160:163],a[76:79],v[88:91],v[160:163]
bl v232,s[8:11],s41 offen offset:1024 sc1 lds
)" R"(mm v[176:179],a[76:79],v[120:123],v[204:207]
s_lshl_b32 s41,s24,10
dr a[112:115],v235 offset:12288
s_add_u32 m0,s41,122880
mm v[176:179],a[72:75],v[112:115],v[176:179]
s_add_u32 s41,s41,s81
dr a[116:119],v235 offset:13312
mm v[176:179],a[68:71],v[104:107],v[176:179]
dr a[120:123],v235 offset:14336
mm v[176:179],a[64:67],v[96:99],v[176:179]
dr a[124:127],v235 offset:15360
bl v232,s[8:11],s41 offen sc1 lds
mm v[180:183],a[80:83],v[96:99],v[204:207]
s_lshl_b32 s41,s24,10
ex2 v156,v156
mm v[180:183],a[84:87],v[104:107],v[180:183]
s_waitcnt lgkmcnt(4)
s_add_u32 m0,s41,126976
s_mov_b32 s41,0x80000000
mm v[180:183],a[88:91],v[112:115],v[180:183]
ex2 v157,v157
mm v[180:183],a[92:95],v[120:123],v[180:183]
bl v232,s[8:11],s41 offen sc1 lds
mm v[164:167],a[92:95],v[88:91],v[200:203]
s_add_u32 m0,s33,131072
ex2 v158,v158
s_waitcnt lgkmcnt(0)
s_barrier
mm v[164:167],a[88:91],v[80:83],v[164:167]
dr a[64:67],v235 offset:16384
cv v154,v156,v157
mm v[164:167],a[84:87],v[72:75],v[164:167]
dr a[68:71],v235 offset:24576
mm v[164:167],a[80:83],v[64:67],v[164:167]
bl v232,s[8:11],s40 offen offset:0 sc1 lds
mm v[168:171],a[96:99],v[64:67],v[200:203]
dr a[72:75],v235 offset:17408
mm v[168:171],a[100:103],v[72:75],v[168:171]
dr a[76:79],v235 offset:25600
mm v[168:171],a[104:107],v[80:83],v[168:171]
ex2 v159,v159
mm v[168:171],a[108:111],v[88:91],v[168:171]
bl v232,s[8:11],s40 offen offset:1024 sc1 lds
mm v[184:187],a[108:111],v[120:123],v[204:207]
dr a[80:83],v235 offset:18432
cv v155,v158,v159
mm v[184:187],a[104:107],v[112:115],v[184:187]
ex2 v136,v136
mm v[184:187],a[100:103],v[104:107],v[184:187]
ex2 v137,v137
mm v[184:187],a[96:99],v[96:99],v[184:187]
bl v232,s[8:11],s40 offen offset:2048 sc1 lds
mm v[188:191],a[112:115],v[96:99],v[204:207]
EB_19:
s_cbranch_execz EB_R
ex2 v138,v138
mm v[188:191],a[116:119],v[104:107],v[188:191]
ex2 v139,v139
mm v[188:191],a[120:123],v[112:115],v[188:191]
ex2 v140,v140
mm v[188:191],a[124:127],v[120:123],v[188:191]
bl v232,s[8:11],s40 offen offset:3072 sc1 lds
mm v[172:175],a[124:127],v[88:91],v[200:203]
s_add_u32 s40,s31,8192
)" R"(ex2 v141,v141
mm v[172:175],a[120:123],v[80:83],v[172:175]
s_add_u32 m0,s33,147456
ex2 v142,v142
mm v[172:175],a[116:119],v[72:75],v[172:175]
ex2 v143,v143
mm v[172:175],a[112:115],v[64:67],v[172:175]
s_waitcnt lgkmcnt(0)
dr a[84:87],v235 offset:26624
cv v136,v136,v137
mm v[68:71],a[64:67],v[148:151],v[68:71]
dr a[88:91],v235 offset:19456
cv v137,v138,v139
ex2 v134,v134
ex2 v135,v135
ex2 v132,v132
ex2 v133,v133
ex2 v128,v128
ex2 v129,v129
ex2 v130,v130
ex2 v131,v131
cv v138,v140,v141
cv v139,v142,v143
mm v[68:71],a[68:71],v[152:155],v[68:71]
dr a[92:95],v235 offset:27648
cv v135,v134,v135
cv v134,v132,v133
cv v132,v128,v129
cv v133,v130,v131
mm v[4:7],a[68:71],v[136:139],v[4:7]
dr a[96:99],v235 offset:20480
mm v[4:7],a[64:67],v[132:135],v[4:7]
dr a[100:103],v235 offset:28672
mm v[12:15],a[72:75],v[132:135],v[12:15]
dr a[104:107],v235 offset:21504
mm v[12:15],a[76:79],v[136:139],v[12:15]
dr a[108:111],v235 offset:29696
mm v[76:79],a[76:79],v[152:155],v[76:79]
dr a[112:115],v235 offset:22528
mm v[76:79],a[72:75],v[148:151],v[76:79]
dr a[116:119],v235 offset:30720
mm v[84:87],a[80:83],v[148:151],v[84:87]
s_waitcnt lgkmcnt(6)
dr a[120:123],v235 offset:23552
mm v[84:87],a[84:87],v[152:155],v[84:87]
dr a[124:127],v235 offset:31744
mm v[20:23],a[84:87],v[136:139],v[20:23]
ex2 v182,v182
mm v[20:23],a[80:83],v[132:135],v[20:23]
ex2 v183,v183
mm v[28:31],a[88:91],v[132:135],v[28:31]
ex2 v180,v180
mm v[28:31],a[92:95],v[136:139],v[28:31]
ex2 v181,v181
mm v[92:95],a[92:95],v[152:155],v[92:95]
s_waitcnt vmcnt(12) lgkmcnt(0)
s_barrier
mm v[92:95],a[88:91],v[148:151],v[92:95]
bl v232,s[8:11],s40 offen offset:0 sc1 lds
v_smfmac_f32_16x16x64_bf16 a[136:139],a[144:147],v[132:139],v246
dr a[64:67],v232 offset:0
cv v183,v182,v183
v_smfmac_f32_16x16x64_bf16 a[140:143],a[144:147],v[148:155],v246
dr a[68:71],v232 offset:1024
)" R"(cv v182,v180,v181
mm v[100:103],a[96:99],v[148:151],v[100:103]
ex2 v176,v176
mm v[100:103],a[100:103],v[152:155],v[100:103]
bl v232,s[8:11],s40 offen offset:1024 sc1 lds
mm v[36:39],a[100:103],v[136:139],v[36:39]
dr a[72:75],v232 offset:2048
mm v[36:39],a[96:99],v[132:135],v[36:39]
dr a[76:79],v232 offset:3072
mm v[44:47],a[104:107],v[132:135],v[44:47]
ex2 v177,v177
mm v[44:47],a[108:111],v[136:139],v[44:47]
bl v232,s[8:11],s40 offen offset:2048 sc1 lds
mm v[108:111],a[108:111],v[152:155],v[108:111]
dr a[80:83],v232 offset:4096
cv v180,v176,v177
mm v[108:111],a[104:107],v[148:151],v[108:111]
dr a[84:87],v232 offset:5120
mm v[116:119],a[112:115],v[148:151],v[116:119]
ex2 v178,v178
mm v[116:119],a[116:119],v[152:155],v[116:119]
s_mov_b32 s30,s31
dr a[88:91],v232 offset:6144
s_add_u32 s31,s31,65536
mm v[52:55],a[116:119],v[136:139],v[52:55]
dr a[92:95],v232 offset:7168
mm v[52:55],a[112:115],v[132:135],v[52:55]
bl v232,s[8:11],s40 offen offset:3072 sc1 lds
mm v[60:63],a[120:123],v[132:135],v[60:63]
s_cmp_eq_u32 s31,s3
s_cselect_b32 s40,65536,0
dr a[96:99],v232 offset:8192
mm v[60:63],a[124:127],v[136:139],v[60:63]
s_add_u32 s31,s31,s40
dr a[100:103],v232 offset:9216
mm v[124:127],a[124:127],v[152:155],v[124:127]
ex2 v179,v179
dr a[104:107],v232 offset:10240
ex2 v166,v166
ex2 v167,v167
ex2 v164,v164
ex2 v165,v165
ex2 v160,v160
ex2 v161,v161
ex2 v162,v162
ex2 v163,v163
mm v[124:127],a[120:123],v[148:151],v[124:127]
dr a[108:111],v232 offset:11264
cv v181,v178,v179
cv v167,v166,v167
cv v166,v164,v165
cv v164,v160,v161
cv v165,v162,v163
R15:
EB_R15:
s_cbranch_execz EB_R
s_waitcnt lgkmcnt(6)
mm v[128:131],a[64:67],v[64:67],v[200:203]
s_add_u32 s41,s39,s58
dr a[112:115],v232 offset:12288
s_add_u32 m0,s34,90112
mm v[128:131],a[68:71],v[72:75],v[128:131]
s_lshl_b32 s46,s25,10
dr a[116:119],v232 offset:13312
)" R"(v_lshrrev_b32 v242,1,v232
mm v[128:131],a[72:75],v[80:83],v[128:131]
s_add_u32 s46,s46,0x4018200
bs a[12:15],v233,s[8:11],s41 offen offset:1024 sc1
s_add_u32 s41,s35,57344
ex2 v184,v184
ex2 v185,v185
v_and_b32 v242,248,v242
mm v[128:131],a[76:79],v[88:91],v[128:131]
s_add_u32 s40,s30,40960
dr a[120:123],v232 offset:14336
cv v184,v184,v185
mm v[144:147],a[76:79],v[120:123],v[204:207]
dr a[124:127],v232 offset:15360
mm v[144:147],a[72:75],v[112:115],v[144:147]
ex2 v186,v186
mm v[144:147],a[68:71],v[104:107],v[144:147]
ex2 v187,v187
mm v[144:147],a[64:67],v[96:99],v[144:147]
ex2 v188,v188
mm v[148:151],a[80:83],v[96:99],v[204:207]
bl v232,s[8:11],s41 offen offset:0 sc1 lds
mm v[148:151],a[84:87],v[104:107],v[148:151]
s_waitcnt lgkmcnt(2)
ex2 v189,v189
mm v[148:151],a[88:91],v[112:115],v[148:151]
s_add_u32 m0,s33,0
ex2 v190,v190
mm v[148:151],a[92:95],v[120:123],v[148:151]
s_add_u32 s41,s35,57344
ex2 v191,v191
mm v[132:135],a[92:95],v[88:91],v[200:203]
s_waitcnt vmcnt(10) lgkmcnt(0)
s_barrier
mm v[132:135],a[88:91],v[80:83],v[132:135]
bl v232,s[8:11],s31 offen offset:0 sc1 lds
mm v[132:135],a[84:87],v[72:75],v[132:135]
dr a[64:67],v232 offset:49152
cv v185,v186,v187
mm v[132:135],a[80:83],v[64:67],v[132:135]
dr a[68:71],v232 offset:57344
cv v186,v188,v189
mm v[136:139],a[96:99],v[64:67],v[200:203]
ex2 v168,v168
mm v[136:139],a[100:103],v[72:75],v[136:139]
bl v232,s[8:11],s31 offen offset:1024 sc1 lds
mm v[136:139],a[104:107],v[80:83],v[136:139]
dr a[72:75],v232 offset:50176
cv v187,v190,v191
mm v[136:139],a[108:111],v[88:91],v[136:139]
dr a[76:79],v232 offset:58368
mm v[152:155],a[108:111],v[120:123],v[204:207]
ex2 v169,v169
mm v[152:155],a[104:107],v[112:115],v[152:155]
bl v232,s[8:11],s31 offen offset:2048 sc1 lds
mm v[152:155],a[100:103],v[104:107],v[152:155]
dr a[80:83],v232 offset:51200
cv v168,v168,v169
mm v[152:155],a[96:99],v[96:99],v[152:155]
ex2 v170,v170
mm v[156:159],a[112:115],v[96:99],v[204:207]
ex2 v171,v171
mm v[156:159],a[116:119],v[104:107],v[156:159]
bl v232,s[8:11],s31 offen offset:3072 sc1 lds
)" R"(mm v[156:159],a[120:123],v[112:115],v[156:159]
s_add_u32 m0,s34,90112
ex2 v172,v172
mm v[156:159],a[124:127],v[120:123],v[156:159]
ex2 v173,v173
mm v[140:143],a[124:127],v[88:91],v[200:203]
ex2 v174,v174
mm v[140:143],a[120:123],v[80:83],v[140:143]
ex2 v175,v175
mm v[140:143],a[116:119],v[72:75],v[140:143]
dr a[84:87],v232 offset:59392
cv v169,v170,v171
mm v[140:143],a[112:115],v[64:67],v[140:143]
s_waitcnt lgkmcnt(1)
dr a[88:91],v232 offset:52224
cv v170,v172,v173
mm v[68:71],a[64:67],v[180:183],v[68:71]
dr a[92:95],v232 offset:60416
cv v171,v174,v175
mm v[68:71],a[68:71],v[184:187],v[68:71]
dr a[96:99],v232 offset:53248
mm v[4:7],a[68:71],v[168:171],v[4:7]
buffer_load_dwordx2 v[240:241],v242,s[8:11],s46 offen sc1
mm v[4:7],a[64:67],v[164:167],v[4:7]
dr a[100:103],v232 offset:61440
mm v[12:15],a[72:75],v[164:167],v[12:15]
dr a[104:107],v232 offset:54272
bl v232,s[8:11],s41 offen offset:1024 sc1 lds
mm v[12:15],a[76:79],v[168:171],v[12:15]
s_add_u32 m0,s33,49152
dr a[108:111],v232 offset:62464
s_add_u32 s41,s39,s59
mm v[76:79],a[76:79],v[184:187],v[76:79]
dr a[112:115],v232 offset:55296
mm v[76:79],a[72:75],v[180:183],v[76:79]
dr a[116:119],v232 offset:63488
mm v[84:87],a[80:83],v[180:183],v[84:87]
s_waitcnt lgkmcnt(5)
dr a[120:123],v232 offset:56320
mm v[84:87],a[84:87],v[184:187],v[84:87]
dr a[124:127],v232 offset:64512
mm v[20:23],a[84:87],v[168:171],v[20:23]
ex2 v150,v150
mm v[20:23],a[80:83],v[164:167],v[20:23]
ex2 v151,v151
mm v[28:31],a[88:91],v[164:167],v[28:31]
ex2 v148,v148
mm v[28:31],a[92:95],v[168:171],v[28:31]
ex2 v149,v149
mm v[92:95],a[92:95],v[184:187],v[92:95]
s_waitcnt vmcnt(12) lgkmcnt(0)
s_barrier
mm v[92:95],a[88:91],v[180:183],v[92:95]
bl v232,s[8:11],s40 offen offset:0 sc1 lds
v_smfmac_f32_16x16x64_bf16 a[136:139],a[144:147],v[164:171],v246
dr a[64:67],v235 offset:0
cv v151,v150,v151
v_smfmac_f32_16x16x64_bf16 a[140:143],a[144:147],v[180:187],v246
dr a[68:71],v235 offset:1024
cv v150,v148,v149
mm v[100:103],a[96:99],v[180:183],v[100:103]
ex2 v144,v144
mm v[100:103],a[100:103],v[184:187],v[100:103]
bl v232,s[8:11],s40 offen offset:1024 sc1 lds
)" R"(mm v[36:39],a[100:103],v[168:171],v[36:39]
dr a[72:75],v235 offset:2048
mm v[36:39],a[96:99],v[164:167],v[36:39]
dr a[76:79],v235 offset:3072
mm v[44:47],a[104:107],v[164:167],v[44:47]
ex2 v145,v145
mm v[44:47],a[108:111],v[168:171],v[44:47]
bl v232,s[8:11],s40 offen offset:2048 sc1 lds
mm v[108:111],a[108:111],v[184:187],v[108:111]
dr a[80:83],v235 offset:4096
cv v148,v144,v145
mm v[108:111],a[104:107],v[180:183],v[108:111]
dr a[84:87],v235 offset:5120
mm v[116:119],a[112:115],v[180:183],v[116:119]
ex2 v146,v146
mm v[116:119],a[116:119],v[184:187],v[116:119]
bl v232,s[8:11],s40 offen offset:3072 sc1 lds
mm v[52:55],a[116:119],v[168:171],v[52:55]
s_add_u32 m0,s34,98304
dr a[88:91],v235 offset:6144
s_add_u32 s40,s31,32768
mm v[52:55],a[112:115],v[164:167],v[52:55]
dr a[92:95],v235 offset:7168
mm v[60:63],a[120:123],v[164:167],v[60:63]
ex2 v147,v147
mm v[60:63],a[124:127],v[168:171],v[60:63]
bs a[180:183],v232,s[8:11],s41 offen offset:0 sc1
dr a[96:99],v235 offset:8192
s_add_u32 s41,s35,65536
cv v149,v146,v147
ex2 v152,v152
mm v[124:127],a[124:127],v[184:187],v[124:127]
dr a[100:103],v235 offset:9216
mm v[124:127],a[120:123],v[180:183],v[124:127]
s_waitcnt lgkmcnt(4)
ex2 v153,v153
mm v[160:163],a[64:67],v[64:67],v[200:203]
dr a[104:107],v235 offset:10240
cv v152,v152,v153
mm v[160:163],a[68:71],v[72:75],v[160:163]
dr a[108:111],v235 offset:11264
mm v[160:163],a[72:75],v[80:83],v[160:163]
bl v232,s[8:11],s41 offen offset:0 sc1 lds
mm v[160:163],a[76:79],v[88:91],v[160:163]
s_add_u32 m0,s33,131072
dr a[112:115],v235 offset:12288
s_add_u32 s41,s35,65536
mm v[176:179],a[76:79],v[120:123],v[204:207]
dr a[116:119],v235 offset:13312
mm v[176:179],a[72:75],v[112:115],v[176:179]
dr a[120:123],v235 offset:14336
mm v[176:179],a[68:71],v[104:107],v[176:179]
dr a[124:127],v235 offset:15360
mm v[176:179],a[64:67],v[96:99],v[176:179]
ex2 v154,v154
mm v[180:183],a[80:83],v[96:99],v[204:207]
ex2 v155,v155
mm v[180:183],a[84:87],v[104:107],v[180:183]
s_waitcnt lgkmcnt(4)
ex2 v156,v156
mm v[180:183],a[88:91],v[112:115],v[180:183]
ex2 v157,v157
mm v[180:183],a[92:95],v[120:123],v[180:183]
ex2 v158,v158
)" R"(mm v[164:167],a[92:95],v[88:91],v[200:203]
s_waitcnt vmcnt(14) lgkmcnt(0)
s_barrier
mm v[164:167],a[88:91],v[80:83],v[164:167]
bl v232,s[8:11],s40 offen offset:0 sc1 lds
mm v[164:167],a[84:87],v[72:75],v[164:167]
dr a[64:67],v235 offset:16384
cv v153,v154,v155
mm v[164:167],a[80:83],v[64:67],v[164:167]
dr a[68:71],v235 offset:24576
cv v154,v156,v157
mm v[168:171],a[96:99],v[64:67],v[200:203]
ex2 v159,v159
mm v[168:171],a[100:103],v[72:75],v[168:171]
bl v232,s[8:11],s40 offen offset:1024 sc1 lds
mm v[168:171],a[104:107],v[80:83],v[168:171]
dr a[72:75],v235 offset:17408
cv v155,v158,v159
mm v[168:171],a[108:111],v[88:91],v[168:171]
dr a[76:79],v235 offset:25600
mm v[184:187],a[108:111],v[120:123],v[204:207]
ex2 v136,v136
mm v[184:187],a[104:107],v[112:115],v[184:187]
bl v232,s[8:11],s40 offen offset:2048 sc1 lds
mm v[184:187],a[100:103],v[104:107],v[184:187]
dr a[80:83],v235 offset:18432
mm v[184:187],a[96:99],v[96:99],v[184:187]
ex2 v137,v137
mm v[188:191],a[112:115],v[96:99],v[204:207]
ex2 v138,v138
mm v[188:191],a[116:119],v[104:107],v[188:191]
bl v232,s[8:11],s40 offen offset:3072 sc1 lds
mm v[188:191],a[120:123],v[112:115],v[188:191]
s_add_u32 m0,s34,98304
ex2 v139,v139
mm v[188:191],a[124:127],v[120:123],v[188:191]
s_add_u32 s40,s31,8192
ex2 v140,v140
mm v[172:175],a[124:127],v[88:91],v[200:203]
s_waitcnt vmcnt(11)
v_cmp_ne_u64 vcc,s[54:55],v[240:241]
s_cbranch_vccnz FK2_S
FK2_D:
mm v[172:175],a[120:123],v[80:83],v[172:175]
ex2 v141,v141
mm v[172:175],a[116:119],v[72:75],v[172:175]
ex2 v142,v142
mm v[172:175],a[112:115],v[64:67],v[172:175]
s_waitcnt lgkmcnt(0)
ex2 v143,v143
dr a[84:87],v235 offset:26624
bl v232,s[8:11],s41 offen offset:1024 sc1 lds
mm v[68:71],a[64:67],v[148:151],v[68:71]
s_add_u32 m0,s33,147456
dr a[88:91],v235 offset:19456
cv v136,v136,v137
ex2 v134,v134
ex2 v135,v135
ex2 v132,v132
ex2 v133,v133
ex2 v128,v128
ex2 v129,v129
ex2 v130,v130
ex2 v131,v131
)" R"(cv v137,v138,v139
cv v138,v140,v141
cv v139,v142,v143
mm v[68:71],a[68:71],v[152:155],v[68:71]
dr a[92:95],v235 offset:27648
cv v135,v134,v135
cv v134,v132,v133
cv v132,v128,v129
cv v133,v130,v131
mm v[4:7],a[68:71],v[136:139],v[4:7]
dr a[96:99],v235 offset:20480
mm v[4:7],a[64:67],v[132:135],v[4:7]
dr a[100:103],v235 offset:28672
mm v[12:15],a[72:75],v[132:135],v[12:15]
dr a[104:107],v235 offset:21504
mm v[12:15],a[76:79],v[136:139],v[12:15]
dr a[108:111],v235 offset:29696
mm v[76:79],a[76:79],v[152:155],v[76:79]
dr a[112:115],v235 offset:22528
mm v[76:79],a[72:75],v[148:151],v[76:79]
dr a[116:119],v235 offset:30720
mm v[84:87],a[80:83],v[148:151],v[84:87]
s_waitcnt lgkmcnt(6)
dr a[120:123],v235 offset:23552
mm v[84:87],a[84:87],v[152:155],v[84:87]
dr a[124:127],v235 offset:31744
mm v[20:23],a[84:87],v[136:139],v[20:23]
ex2 v182,v182
mm v[20:23],a[80:83],v[132:135],v[20:23]
ex2 v183,v183
mm v[28:31],a[88:91],v[132:135],v[28:31]
ex2 v180,v180
mm v[28:31],a[92:95],v[136:139],v[28:31]
ex2 v181,v181
mm v[92:95],a[92:95],v[152:155],v[92:95]
s_waitcnt lgkmcnt(0)
s_barrier
mm v[92:95],a[88:91],v[148:151],v[92:95]
bl v232,s[8:11],s40 offen offset:0 sc1 lds
v_smfmac_f32_16x16x64_bf16 a[136:139],a[144:147],v[132:139],v246
dr a[64:67],v232 offset:0
cv v183,v182,v183
v_smfmac_f32_16x16x64_bf16 a[140:143],a[144:147],v[148:155],v246
dr a[68:71],v232 offset:1024
cv v182,v180,v181
mm v[100:103],a[96:99],v[148:151],v[100:103]
ex2 v176,v176
mm v[100:103],a[100:103],v[152:155],v[100:103]
bl v232,s[8:11],s40 offen offset:1024 sc1 lds
mm v[36:39],a[100:103],v[136:139],v[36:39]
dr a[72:75],v232 offset:2048
mm v[36:39],a[96:99],v[132:135],v[36:39]
dr a[76:79],v232 offset:3072
mm v[44:47],a[104:107],v[132:135],v[44:47]
ex2 v177,v177
mm v[44:47],a[108:111],v[136:139],v[44:47]
bl v232,s[8:11],s40 offen offset:2048 sc1 lds
mm v[108:111],a[108:111],v[152:155],v[108:111]
dr a[80:83],v232 offset:4096
cv v180,v176,v177
mm v[108:111],a[104:107],v[148:151],v[108:111]
dr a[84:87],v232 offset:5120
mm v[116:119],a[112:115],v[148:151],v[116:119]
ex2 v178,v178
)" R"(mm v[116:119],a[116:119],v[152:155],v[116:119]
s_mov_b32 s30,s31
dr a[88:91],v232 offset:6144
s_add_u32 s31,s31,65536
mm v[52:55],a[116:119],v[136:139],v[52:55]
dr a[92:95],v232 offset:7168
mm v[52:55],a[112:115],v[132:135],v[52:55]
bl v232,s[8:11],s40 offen offset:3072 sc1 lds
mm v[60:63],a[120:123],v[132:135],v[60:63]
s_cmp_eq_u32 s31,s3
s_cselect_b32 s40,65536,0
dr a[96:99],v232 offset:8192
mm v[60:63],a[124:127],v[136:139],v[60:63]
s_add_u32 s31,s31,s40
dr a[100:103],v232 offset:9216
mm v[124:127],a[124:127],v[152:155],v[124:127]
ex2 v179,v179
dr a[104:107],v232 offset:10240
ex2 v166,v166
ex2 v167,v167
ex2 v164,v164
ex2 v165,v165
ex2 v160,v160
ex2 v161,v161
ex2 v162,v162
ex2 v163,v163
mm v[124:127],a[120:123],v[148:151],v[124:127]
dr a[108:111],v232 offset:11264
cv v181,v178,v179
cv v167,v166,v167
cv v166,v164,v165
cv v164,v160,v161
cv v165,v162,v163
R16:
EB_R16:
s_cbranch_execz EB_R
s_waitcnt lgkmcnt(6)
mm v[128:131],a[64:67],v[64:67],v[200:203]
s_add_u32 s41,s39,s59
dr a[112:115],v232 offset:12288
s_add_u32 m0,s34,106496
mm v[128:131],a[68:71],v[72:75],v[128:131]
s_mov_b32 s2,EC_0-EC_G
dr a[116:119],v232 offset:13312
s_getpc_b64 s[0:1]
EC_G:
mm v[128:131],a[72:75],v[80:83],v[128:131]
s_cmp_eq_u32 s24,1
s_cselect_b32 s2,EC_1-EC_G,s2
s_add_u32 s40,s30,40960
mm v[128:131],a[76:79],v[88:91],v[128:131]
s_cmp_eq_u32 s24,2
s_cselect_b32 s2,EC_2-EC_G,s2
dr a[120:123],v232 offset:14336
mm v[144:147],a[76:79],v[120:123],v[204:207]
s_cmp_eq_u32 s24,3
s_cselect_b32 s2,EC_3-EC_G,s2
dr a[124:127],v232 offset:15360
bs a[184:187],v232,s[8:11],s41 offen offset:1024 sc1
s_add_u32 s41,s35,73728
s_add_u32 s0,s0,s2
s_addc_u32 s1,s1,0
ex2 v184,v184
mm v[144:147],a[72:75],v[112:115],v[144:147]
)" R"(ex2 v185,v185
mm v[144:147],a[68:71],v[104:107],v[144:147]
ex2 v186,v186
mm v[144:147],a[64:67],v[96:99],v[144:147]
ex2 v187,v187
mm v[148:151],a[80:83],v[96:99],v[204:207]
bl v232,s[8:11],s41 offen offset:0 sc1 lds
mm v[148:151],a[84:87],v[104:107],v[148:151]
s_waitcnt lgkmcnt(2)
ex2 v188,v188
mm v[148:151],a[88:91],v[112:115],v[148:151]
s_add_u32 m0,s33,0
ex2 v189,v189
mm v[148:151],a[92:95],v[120:123],v[148:151]
s_add_u32 s41,s39,s59
ex2 v190,v190
s_mov_b64 exec,0
s_setpc_b64 s[0:1]
EC_R:
s_mov_b64 exec,-1
mm v[132:135],a[92:95],v[88:91],v[200:203]
s_waitcnt vmcnt(10) lgkmcnt(0)
s_barrier
mm v[132:135],a[88:91],v[80:83],v[132:135]
bl v232,s[8:11],s31 offen offset:0 sc1 lds
mm v[132:135],a[84:87],v[72:75],v[132:135]
dr a[64:67],v232 offset:49152
cv v184,v184,v185
mm v[132:135],a[80:83],v[64:67],v[132:135]
dr a[68:71],v232 offset:57344
cv v185,v186,v187
mm v[136:139],a[96:99],v[64:67],v[200:203]
ex2 v191,v191
mm v[136:139],a[100:103],v[72:75],v[136:139]
bl v232,s[8:11],s31 offen offset:1024 sc1 lds
mm v[136:139],a[104:107],v[80:83],v[136:139]
dr a[72:75],v232 offset:50176
cv v186,v188,v189
mm v[136:139],a[108:111],v[88:91],v[136:139]
dr a[76:79],v232 offset:58368
cv v187,v190,v191
mm v[152:155],a[108:111],v[120:123],v[204:207]
ex2 v168,v168
mm v[152:155],a[104:107],v[112:115],v[152:155]
bl v232,s[8:11],s31 offen offset:2048 sc1 lds
mm v[152:155],a[100:103],v[104:107],v[152:155]
dr a[80:83],v232 offset:51200
mm v[152:155],a[96:99],v[96:99],v[152:155]
ex2 v169,v169
mm v[156:159],a[112:115],v[96:99],v[204:207]
ex2 v170,v170
mm v[156:159],a[116:119],v[104:107],v[156:159]
bl v232,s[8:11],s31 offen offset:3072 sc1 lds
mm v[156:159],a[120:123],v[112:115],v[156:159]
s_add_u32 m0,s34,106496
ex2 v171,v171
mm v[156:159],a[124:127],v[120:123],v[156:159]
ex2 v172,v172
mm v[140:143],a[124:127],v[88:91],v[200:203]
ex2 v173,v173
mm v[140:143],a[120:123],v[80:83],v[140:143]
ex2 v174,v174
mm v[140:143],a[116:119],v[72:75],v[140:143]
ex2 v175,v175
)" R"(mm v[140:143],a[112:115],v[64:67],v[140:143]
s_waitcnt lgkmcnt(0)
dr a[84:87],v232 offset:59392
cv v168,v168,v169
mm v[68:71],a[64:67],v[180:183],v[68:71]
dr a[88:91],v232 offset:52224
cv v169,v170,v171
cv v170,v172,v173
cv v171,v174,v175
mm v[68:71],a[68:71],v[184:187],v[68:71]
dr a[92:95],v232 offset:60416
mm v[4:7],a[68:71],v[168:171],v[4:7]
dr a[96:99],v232 offset:53248
mm v[4:7],a[64:67],v[164:167],v[4:7]
dr a[100:103],v232 offset:61440
mm v[12:15],a[72:75],v[164:167],v[12:15]
EF_0:
s_cbranch_execz EF_R
EC_0:
s_cbranch_execz EC_R
dr a[104:107],v232 offset:54272
mm v[12:15],a[76:79],v[168:171],v[12:15]
dr a[108:111],v232 offset:62464
bs a[188:191],v232,s[8:11],s41 offen offset:2048 sc1
dr a[112:115],v232 offset:55296
s_add_u32 s41,s35,73728
ex2 v150,v150
ex2 v151,v151
mm v[76:79],a[76:79],v[184:187],v[76:79]
dr a[116:119],v232 offset:63488
cv v151,v150,v151
mm v[76:79],a[72:75],v[180:183],v[76:79]
dr a[120:123],v232 offset:56320
mm v[84:87],a[80:83],v[180:183],v[84:87]
s_waitcnt lgkmcnt(7)
dr a[124:127],v232 offset:64512
mm v[84:87],a[84:87],v[184:187],v[84:87]
bl v232,s[8:11],s41 offen offset:1024 sc1 lds
mm v[20:23],a[84:87],v[168:171],v[20:23]
s_add_u32 m0,s33,49152
ex2 v148,v148
mm v[20:23],a[80:83],v[164:167],v[20:23]
s_add_u32 s41,s35,81920
ex2 v149,v149
mm v[28:31],a[88:91],v[164:167],v[28:31]
ex2 v144,v144
mm v[28:31],a[92:95],v[168:171],v[28:31]
ex2 v145,v145
mm v[92:95],a[92:95],v[184:187],v[92:95]
s_waitcnt vmcnt(12) lgkmcnt(0)
s_barrier
mm v[92:95],a[88:91],v[180:183],v[92:95]
bl v232,s[8:11],s40 offen offset:0 sc1 lds
v_smfmac_f32_16x16x64_bf16 a[136:139],a[144:147],v[164:171],v246
dr a[64:67],v235 offset:0
cv v150,v148,v149
v_smfmac_f32_16x16x64_bf16 a[140:143],a[144:147],v[180:187],v246
dr a[68:71],v235 offset:1024
cv v148,v144,v145
mm v[100:103],a[96:99],v[180:183],v[100:103]
ex2 v146,v146
mm v[100:103],a[100:103],v[184:187],v[100:103]
bl v232,s[8:11],s40 offen offset:1024 sc1 lds
mm v[36:39],a[100:103],v[168:171],v[36:39]
)" R"(dr a[72:75],v235 offset:2048
mm v[36:39],a[96:99],v[164:167],v[36:39]
dr a[76:79],v235 offset:3072
mm v[44:47],a[104:107],v[164:167],v[44:47]
ex2 v147,v147
mm v[44:47],a[108:111],v[168:171],v[44:47]
bl v232,s[8:11],s40 offen offset:2048 sc1 lds
mm v[108:111],a[108:111],v[184:187],v[108:111]
dr a[80:83],v235 offset:4096
cv v149,v146,v147
mm v[108:111],a[104:107],v[180:183],v[108:111]
dr a[84:87],v235 offset:5120
mm v[116:119],a[112:115],v[180:183],v[116:119]
ex2 v152,v152
mm v[116:119],a[116:119],v[184:187],v[116:119]
bl v232,s[8:11],s40 offen offset:3072 sc1 lds
mm v[52:55],a[116:119],v[168:171],v[52:55]
s_add_u32 m0,s34,114688
dr a[88:91],v235 offset:6144
s_add_u32 s40,s31,32768
mm v[52:55],a[112:115],v[164:167],v[52:55]
dr a[92:95],v235 offset:7168
mm v[60:63],a[120:123],v[164:167],v[60:63]
ex2 v153,v153
mm v[60:63],a[124:127],v[168:171],v[60:63]
bl v232,s[8:11],s41 offen offset:0 sc1 lds
mm v[124:127],a[124:127],v[184:187],v[124:127]
s_add_u32 s41,s39,s59
dr a[96:99],v235 offset:8192
s_add_u32 m0,s33,131072
mm v[124:127],a[120:123],v[180:183],v[124:127]
s_waitcnt lgkmcnt(3)
dr a[100:103],v235 offset:9216
cv v152,v152,v153
mm v[160:163],a[64:67],v[64:67],v[200:203]
ex2 v154,v154
mm v[160:163],a[68:71],v[72:75],v[160:163]
dr a[104:107],v235 offset:10240
mm v[160:163],a[72:75],v[80:83],v[160:163]
dr a[108:111],v235 offset:11264
mm v[160:163],a[76:79],v[88:91],v[160:163]
dr a[112:115],v235 offset:12288
mm v[176:179],a[76:79],v[120:123],v[204:207]
bs a[192:195],v232,s[8:11],s41 offen offset:3072 sc1
dr a[116:119],v235 offset:13312
ex2 v155,v155
ex2 v156,v156
cv v153,v154,v155
mm v[176:179],a[72:75],v[112:115],v[176:179]
s_add_u32 s41,s35,81920
dr a[120:123],v235 offset:14336
mm v[176:179],a[68:71],v[104:107],v[176:179]
dr a[124:127],v235 offset:15360
mm v[176:179],a[64:67],v[96:99],v[176:179]
ex2 v157,v157
mm v[180:183],a[80:83],v[96:99],v[204:207]
ex2 v158,v158
mm v[180:183],a[84:87],v[104:107],v[180:183]
s_waitcnt lgkmcnt(4)
ex2 v159,v159
mm v[180:183],a[88:91],v[112:115],v[180:183]
ex2 v136,v136
mm v[180:183],a[92:95],v[120:123],v[180:183]
ex2 v137,v137
)" R"(mm v[164:167],a[92:95],v[88:91],v[200:203]
s_waitcnt vmcnt(14) lgkmcnt(0)
s_barrier
mm v[164:167],a[88:91],v[80:83],v[164:167]
bl v232,s[8:11],s40 offen offset:0 sc1 lds
mm v[164:167],a[84:87],v[72:75],v[164:167]
dr a[64:67],v235 offset:16384
cv v154,v156,v157
mm v[164:167],a[80:83],v[64:67],v[164:167]
dr a[68:71],v235 offset:24576
cv v155,v158,v159
mm v[168:171],a[96:99],v[64:67],v[200:203]
ex2 v138,v138
mm v[168:171],a[100:103],v[72:75],v[168:171]
bl v232,s[8:11],s40 offen offset:1024 sc1 lds
mm v[168:171],a[104:107],v[80:83],v[168:171]
dr a[72:75],v235 offset:17408
cv v136,v136,v137
mm v[168:171],a[108:111],v[88:91],v[168:171]
dr a[76:79],v235 offset:25600
mm v[184:187],a[108:111],v[120:123],v[204:207]
ex2 v139,v139
mm v[184:187],a[104:107],v[112:115],v[184:187]
bl v232,s[8:11],s40 offen offset:2048 sc1 lds
mm v[184:187],a[100:103],v[104:107],v[184:187]
dr a[80:83],v235 offset:18432
cv v137,v138,v139
mm v[184:187],a[96:99],v[96:99],v[184:187]
ex2 v140,v140
mm v[188:191],a[112:115],v[96:99],v[204:207]
ex2 v141,v141
mm v[188:191],a[116:119],v[104:107],v[188:191]
bl v232,s[8:11],s40 offen offset:3072 sc1 lds
mm v[188:191],a[120:123],v[112:115],v[188:191]
s_add_u32 m0,s34,114688
ex2 v142,v142
mm v[188:191],a[124:127],v[120:123],v[188:191]
s_add_u32 s40,s31,8192
ex2 v143,v143
mm v[172:175],a[124:127],v[88:91],v[200:203]
dr a[84:87],v235 offset:26624
cv v138,v140,v141
mm v[172:175],a[120:123],v[80:83],v[172:175]
bl v232,s[8:11],s41 offen offset:1024 sc1 lds
mm v[172:175],a[116:119],v[72:75],v[172:175]
s_add_u32 m0,s33,147456
dr a[88:91],v235 offset:19456
cv v139,v142,v143
mm v[172:175],a[112:115],v[64:67],v[172:175]
s_waitcnt lgkmcnt(2)
ex2 v134,v134
mm v[68:71],a[64:67],v[148:151],v[68:71]
s_lshr_b32 s46,s39,15
ex2 v135,v135
ex2 v132,v132
ex2 v133,v133
ex2 v128,v128
ex2 v129,v129
ex2 v130,v130
ex2 v131,v131
mm v[68:71],a[68:71],v[152:155],v[68:71]
s_lshl_b32 s46,s46,2
dr a[92:95],v235 offset:27648
cv v135,v134,v135
)" R"(cv v134,v132,v133
cv v132,v128,v129
cv v133,v130,v131
mm v[4:7],a[68:71],v[136:139],v[4:7]
s_add_u32 s46,s46,s24
dr a[96:99],v235 offset:20480
s_lshl_b32 s46,s46,3
mm v[4:7],a[64:67],v[132:135],v[4:7]
s_add_u32 s46,s46,0x4018000
dr a[100:103],v235 offset:28672
mm v[12:15],a[72:75],v[132:135],v[12:15]
dr a[104:107],v235 offset:21504
mm v[12:15],a[76:79],v[136:139],v[12:15]
dr a[108:111],v235 offset:29696
mm v[76:79],a[76:79],v[152:155],v[76:79]
dr a[112:115],v235 offset:22528
mm v[76:79],a[72:75],v[148:151],v[76:79]
dr a[116:119],v235 offset:30720
mm v[84:87],a[80:83],v[148:151],v[84:87]
s_waitcnt lgkmcnt(6)
dr a[120:123],v235 offset:23552
mm v[84:87],a[84:87],v[152:155],v[84:87]
dr a[124:127],v235 offset:31744
mm v[20:23],a[84:87],v[136:139],v[20:23]
ex2 v182,v182
mm v[20:23],a[80:83],v[132:135],v[20:23]
ex2 v183,v183
mm v[28:31],a[88:91],v[132:135],v[28:31]
ex2 v180,v180
mm v[28:31],a[92:95],v[136:139],v[28:31]
ex2 v181,v181
mm v[92:95],a[92:95],v[152:155],v[92:95]
s_waitcnt vmcnt(13) lgkmcnt(0)
s_barrier
mm v[92:95],a[88:91],v[148:151],v[92:95]
bl v232,s[8:11],s40 offen offset:0 sc1 lds
v_smfmac_f32_16x16x64_bf16 a[136:139],a[144:147],v[132:139],v246
dr a[64:67],v232 offset:0
cv v183,v182,v183
v_smfmac_f32_16x16x64_bf16 a[140:143],a[144:147],v[148:155],v246
dr a[68:71],v232 offset:1024
cv v182,v180,v181
mm v[100:103],a[96:99],v[148:151],v[100:103]
ex2 v176,v176
mm v[100:103],a[100:103],v[152:155],v[100:103]
bl v232,s[8:11],s40 offen offset:1024 sc1 lds
mm v[36:39],a[100:103],v[136:139],v[36:39]
dr a[72:75],v232 offset:2048
mm v[36:39],a[96:99],v[132:135],v[36:39]
dr a[76:79],v232 offset:3072
mm v[44:47],a[104:107],v[132:135],v[44:47]
ex2 v177,v177
mm v[44:47],a[108:111],v[136:139],v[44:47]
bl v232,s[8:11],s40 offen offset:2048 sc1 lds
mm v[108:111],a[108:111],v[152:155],v[108:111]
dr a[80:83],v232 offset:4096
cv v180,v176,v177
mm v[108:111],a[104:107],v[148:151],v[108:111]
dr a[84:87],v232 offset:5120
mm v[116:119],a[112:115],v[148:151],v[116:119]
ex2 v178,v178
mm v[116:119],a[116:119],v[152:155],v[116:119]
s_mov_b32 s30,s31
dr a[88:91],v232 offset:6144
)" R"(s_add_u32 s31,s31,65536
mm v[52:55],a[116:119],v[136:139],v[52:55]
dr a[92:95],v232 offset:7168
mm v[52:55],a[112:115],v[132:135],v[52:55]
bl v232,s[8:11],s40 offen offset:3072 sc1 lds
mm v[60:63],a[120:123],v[132:135],v[60:63]
s_cmp_eq_u32 s31,s3
s_cselect_b32 s40,65536,0
dr a[96:99],v232 offset:8192
mm v[60:63],a[124:127],v[136:139],v[60:63]
s_add_u32 s31,s31,s40
dr a[100:103],v232 offset:9216
mm v[124:127],a[124:127],v[152:155],v[124:127]
ex2 v179,v179
dr a[104:107],v232 offset:10240
ex2 v166,v166
ex2 v167,v167
ex2 v164,v164
ex2 v165,v165
ex2 v160,v160
ex2 v161,v161
ex2 v162,v162
ex2 v163,v163
mm v[124:127],a[120:123],v[148:151],v[124:127]
dr a[108:111],v232 offset:11264
cv v181,v178,v179
s_waitcnt vmcnt(9)
buffer_store_dwordx2 v[252:253],v254,s[8:11],s46 offen sc1
cv v167,v166,v167
cv v166,v164,v165
cv v164,v160,v161
cv v165,v162,v163
U:
EB_U:
s_cbranch_execz EB_R
s_waitcnt lgkmcnt(6)
mm v[128:131],a[64:67],v[64:67],v[200:203]
s_add_u32 m0,s33,0
dr a[112:115],v232 offset:12288
s_add_u32 s75,s57,1
mm v[128:131],a[68:71],v[72:75],v[128:131]
s_and_b32 s75,s75,3
dr a[116:119],v232 offset:13312
s_sub_u32 s75,s75,s57
mm v[128:131],a[72:75],v[80:83],v[128:131]
s_lshl_b32 s75,s75,13
ex2 v184,v184
mm v[128:131],a[76:79],v[88:91],v[128:131]
s_add_u32 s75,s75,s42
dr a[120:123],v232 offset:14336
s_add_u32 s76,s57,2
mm v[144:147],a[76:79],v[120:123],v[204:207]
s_and_b32 s76,s76,3
dr a[124:127],v232 offset:15360
s_sub_u32 s76,s76,s57
mm v[144:147],a[72:75],v[112:115],v[144:147]
s_lshl_b32 s76,s76,13
ex2 v185,v185
mm v[144:147],a[68:71],v[104:107],v[144:147]
s_add_u32 s76,s76,s42
ex2 v186,v186
mm v[144:147],a[64:67],v[96:99],v[144:147]
s_add_u32 s77,s57,3
dr a[64:67],v239 offset:0
)" R"(cv v184,v184,v185
mm v[148:151],a[80:83],v[96:99],v[204:207]
s_and_b32 s77,s77,3
dr a[68:71],v239 offset:256
s_sub_u32 s77,s77,s57
mm v[148:151],a[84:87],v[104:107],v[148:151]
s_waitcnt lgkmcnt(4)
ex2 v187,v187
mm v[148:151],a[88:91],v[112:115],v[148:151]
s_lshl_b32 s77,s77,13
dr a[72:75],v239 offset:512
cv v185,v186,v187
mm v[148:151],a[92:95],v[120:123],v[148:151]
s_add_u32 s40,s30,40960
dr a[76:79],v239 offset:768
s_add_u32 s77,s77,s42
mm v[132:135],a[92:95],v[88:91],v[200:203]
s_lshl_b32 s73,s26,2
ex2 v188,v188
mm v[132:135],a[88:91],v[80:83],v[132:135]
s_add_u32 s73,s73,s57
ex2 v189,v189
mm v[132:135],a[84:87],v[72:75],v[132:135]
s_mul_i32 s64,s73,327680
ex2 v190,v190
mm v[132:135],a[80:83],v[64:67],v[132:135]
s_mul_i32 s101,s56,5120
dr a[80:83],v239 offset:1024
cv v186,v188,v189
mm v[136:139],a[96:99],v[64:67],v[200:203]
s_add_u32 s64,s64,s101
dr a[84:87],v239 offset:1280
s_mul_i32 s101,s24,40960
mm v[136:139],a[100:103],v[72:75],v[136:139]
s_add_u32 s64,s64,s101
ex2 v191,v191
mm v[136:139],a[104:107],v[80:83],v[136:139]
s_add_u32 s64,s64,0x4600000
dr a[88:91],v239 offset:1536
cv v187,v190,v191
mm v[136:139],a[108:111],v[88:91],v[136:139]
s_mul_i32 s65,s24,-81920
dr a[92:95],v239 offset:1792
s_add_u32 s65,s65,s64
mm v[152:155],a[108:111],v[120:123],v[204:207]
s_add_u32 s65,s65,0x46000
ex2 v168,v168
mm v[152:155],a[104:107],v[112:115],v[152:155]
s_lshl_b32 s67,s73,8
ex2 v169,v169
mm v[152:155],a[100:103],v[104:107],v[152:155]
s_lshl_b32 s101,s24,6
ex2 v170,v170
mm v[152:155],a[96:99],v[96:99],v[152:155]
s_add_u32 s67,s67,s101
ex2 v171,v171
mm v[156:159],a[112:115],v[96:99],v[204:207]
s_lshl_b32 s101,s56,3
ex2 v172,v172
mm v[156:159],a[116:119],v[104:107],v[156:159]
s_waitcnt lgkmcnt(1)
ex2 v173,v173
mm v[156:159],a[120:123],v[112:115],v[156:159]
s_add_u32 s67,s67,s101
)" R"(ex2 v174,v174
mm v[156:159],a[124:127],v[120:123],v[156:159]
s_add_u32 s67,s67,0x400c000
ex2 v175,v175
mm v[140:143],a[124:127],v[88:91],v[200:203]
s_sub_u32 s68,7,s56
ex2 v150,v150
s_waitcnt vmcnt(8)
s_barrier
mm v[140:143],a[120:123],v[80:83],v[140:143]
s_min_u32 s68,s68,s56
bl v232,s[8:11],s31 offen offset:0 sc1 lds
mm v[140:143],a[116:119],v[72:75],v[140:143]
s_lshl_b32 s68,s68,6
dr a[96:99],v232 offset:49152
cv v168,v168,v169
mm v[140:143],a[112:115],v[64:67],v[140:143]
s_lshl_b32 s101,s73,8
dr a[100:103],v232 offset:57344
cv v169,v170,v171
mm v[208:211],a[64:67],a[216:219],0
s_add_u32 s68,s68,s101
ex2 v151,v151
mm v[220:223],a[64:67],a[236:239],0
s_add_u32 s68,s68,0x400c000
bl v232,s[8:11],s31 offen offset:1024 sc1 lds
mm v[220:223],a[68:71],a[240:243],v[220:223]
s_mul_i32 s66,s73,327680
dr a[104:107],v232 offset:50176
cv v170,v172,v173
mm v[208:211],a[68:71],a[220:223],v[208:211]
s_mul_i32 s101,s56,40960
dr a[108:111],v232 offset:58368
cv v171,v174,v175
mm v[208:211],a[72:75],a[224:227],v[208:211]
s_add_u32 s66,s66,s101
ex2 v148,v148
mm v[220:223],a[72:75],a[244:247],v[220:223]
s_lshl_b32 s101,s24,8
bl v232,s[8:11],s31 offen offset:2048 sc1 lds
mm v[220:223],a[76:79],a[248:251],v[220:223]
s_add_u32 s66,s66,s101
dr a[112:115],v232 offset:51200
s_add_u32 s66,s66,0x4600000
mm v[208:211],a[76:79],a[228:231],v[208:211]
s_add_u32 s78,s66,20480
dr a[116:119],v232 offset:59392
s_add_u32 s79,s66,4096
mm v[208:211],a[80:83],a[232:235],v[208:211]
s_add_u32 s80,s66,24576
ex2 v149,v149
mm v[220:223],a[80:83],a[252:255],v[220:223]
s_mul_i32 s69,s26,327680
bl v232,s[8:11],s31 offen offset:3072 sc1 lds
mm v[224:227],a[84:87],a[236:239],0
s_add_u32 m0,s33,49152
dr a[120:123],v232 offset:52224
s_mul_i32 s101,s56,40960
mm v[212:215],a[84:87],a[216:219],0
s_add_u32 s69,s69,s101
dr a[124:127],v232 offset:60416
s_mul_i32 s101,s57,320
mm v[212:215],a[88:91],a[220:223],v[212:215]
s_add_u32 s69,s69,s101
)" R"(ex2 v144,v144
mm v[224:227],a[88:91],a[240:243],v[224:227]
s_waitcnt lgkmcnt(3)
dr a[64:67],v232 offset:53248
s_mul_i32 s101,s24,10240
mm v[224:227],a[92:95],a[244:247],v[224:227]
s_add_u32 s69,s69,s101
dr a[68:71],v232 offset:61440
s_add_u32 s69,s69,0x500
mm v[212:215],a[92:95],a[224:227],v[212:215]
s_mul_i32 s70,s25,81920
ex2 v145,v145
mm v[68:71],a[96:99],v[180:183],v[68:71]
s_lshl_b32 s101,s24,10
dr a[72:75],v232 offset:54272
s_add_u32 s70,s70,s101
mm v[68:71],a[100:103],v[184:187],v[68:71]
s_add_u32 s70,s70,0x3800000
dr a[76:79],v232 offset:62464
s_mul_i32 s71,s26,102400
mm v[4:7],a[100:103],v[168:171],v[4:7]
s_add_u32 s71,s71,s34
ex2 v146,v146
mm v[4:7],a[96:99],v[164:167],v[4:7]
s_add_u32 s71,s71,0x3500000
dr a[80:83],v232 offset:55296
s_mul_i32 s72,s25,163840
mm v[12:15],a[104:107],v[164:167],v[12:15]
s_mul_i32 s101,s24,40960
dr a[84:87],v232 offset:63488
s_add_u32 s72,s72,s101
mm v[12:15],a[108:111],v[168:171],v[12:15]
s_mul_i32 s101,s26,160
ex2 v147,v147
mm v[76:79],a[108:111],v[184:187],v[76:79]
s_add_u32 s72,s72,s101
dr a[88:91],v232 offset:56320
cv v151,v150,v151
mm v[76:79],a[104:107],v[180:183],v[76:79]
dr a[92:95],v232 offset:64512
cv v150,v148,v149
mm v[84:87],a[112:115],v[180:183],v[84:87]
s_waitcnt lgkmcnt(4)
ex2 v152,v152
mm v[84:87],a[116:119],v[184:187],v[84:87]
dr a[96:99],v239 offset:2048
cv v148,v144,v145
mm v[20:23],a[116:119],v[168:171],v[20:23]
dr a[100:103],v239 offset:2304
cv v149,v146,v147
mm v[20:23],a[112:115],v[164:167],v[20:23]
ex2 v153,v153
mm v[28:31],a[120:123],v[164:167],v[28:31]
dr a[104:107],v239 offset:2560
cv v152,v152,v153
mm v[28:31],a[124:127],v[168:171],v[28:31]
dr a[108:111],v239 offset:2816
v_lshrrev_b32 v250,8,v232
mm v[92:95],a[124:127],v[184:187],v[92:95]
ex2 v154,v154
mm v[92:95],a[120:123],v[180:183],v[92:95]
dr a[112:115],v239 offset:3072
v_and_b32 v251,0xf0,v232
v_smfmac_f32_16x16x64_bf16 a[136:139],a[144:147],v[164:171],v246
)" R"(dr a[116:119],v239 offset:3328
v_mul_u32_u24 v248,5120,v250
v_smfmac_f32_16x16x64_bf16 a[140:143],a[144:147],v[180:187],v246
ex2 v155,v155
mm v[100:103],a[64:67],v[180:183],v[100:103]
dr a[120:123],v239 offset:3584
cv v153,v154,v155
mm v[100:103],a[68:71],v[184:187],v[100:103]
ex2 v156,v156
mm v[36:39],a[68:71],v[168:171],v[36:39]
ex2 v157,v157
mm v[36:39],a[64:67],v[164:167],v[36:39]
ex2 v158,v158
mm v[44:47],a[72:75],v[164:167],v[44:47]
ex2 v159,v159
mm v[44:47],a[76:79],v[168:171],v[44:47]
ex2 v136,v136
mm v[108:111],a[76:79],v[184:187],v[108:111]
ex2 v137,v137
mm v[108:111],a[72:75],v[180:183],v[108:111]
s_waitcnt lgkmcnt(0)
ex2 v138,v138
mm v[116:119],a[80:83],v[180:183],v[116:119]
ex2 v139,v139
mm v[116:119],a[84:87],v[184:187],v[116:119]
ex2 v140,v140
mm v[52:55],a[84:87],v[168:171],v[52:55]
s_waitcnt vmcnt(8)
s_barrier
mm v[52:55],a[80:83],v[164:167],v[52:55]
bl v232,s[8:11],s40 offen offset:0 sc1 lds
mm v[60:63],a[88:91],v[164:167],v[60:63]
dr a[124:127],v235 offset:0
cv v154,v156,v157
mm v[60:63],a[92:95],v[168:171],v[60:63]
dr a[64:67],v235 offset:1024
cv v155,v158,v159
mm v[124:127],a[92:95],v[184:187],v[124:127]
ex2 v141,v141
mm v[124:127],a[88:91],v[180:183],v[124:127]
bl v232,s[8:11],s40 offen offset:1024 sc1 lds
mm v[212:215],a[96:99],a[228:231],v[212:215]
s_and_b32 s46,s43,-32
dr a[68:71],v235 offset:2048
cv v136,v136,v137
mm v[224:227],a[96:99],a[248:251],v[224:227]
dr a[72:75],v235 offset:3072
cv v137,v138,v139
mm v[224:227],a[100:103],a[252:255],v[224:227]
ex2 v142,v142
mm v[212:215],a[100:103],a[232:235],v[212:215]
bl v232,s[8:11],s40 offen offset:2048 sc1 lds
mm v[216:219],a[104:107],a[216:219],0
dr a[76:79],v235 offset:4096
cv v138,v140,v141
mm v[228:231],a[104:107],a[236:239],0
dr a[80:83],v235 offset:5120
v_add_u32 v248,v251,v248
mm v[228:231],a[108:111],a[240:243],v[228:231]
ex2 v143,v143
mm v[216:219],a[108:111],a[220:223],v[216:219]
bl v232,s[8:11],s40 offen offset:3072 sc1 lds
mm v[216:219],a[112:115],a[224:227],v[216:219]
s_add_u32 s40,s31,32768
)" R"(dr a[84:87],v235 offset:6144
s_add_u32 m0,s33,131072
mm v[228:231],a[112:115],a[244:247],v[228:231]
dr a[88:91],v235 offset:7168
cv v139,v142,v143
mm v[228:231],a[116:119],a[248:251],v[228:231]
ex2 v134,v134
mm v[216:219],a[116:119],a[228:231],v[216:219]
dr a[92:95],v235 offset:8192
v_add_u32 v239,0xa00000,v248
mm v[216:219],a[120:123],a[232:235],v[216:219]
dr a[96:99],v235 offset:9216
v_mov_b32 v192,0
mm v[228:231],a[120:123],a[252:255],v[228:231]
s_waitcnt lgkmcnt(4)
ex2 v135,v135
mm v[160:163],a[124:127],v[64:67],v[200:203]
dr a[100:103],v235 offset:10240
cv v135,v134,v135
mm v[160:163],a[64:67],v[72:75],v[160:163]
dr a[104:107],v235 offset:11264
v_add_f32 v218,0x358637bd,v218
mm v[160:163],a[68:71],v[80:83],v[160:163]
v_rcp_f32 v218,v218
mm v[160:163],a[72:75],v[88:91],v[160:163]
dr a[108:111],v235 offset:12288
v_mul_f32 v208,v208,v218
mm v[176:179],a[72:75],v[120:123],v[204:207]
dr a[112:115],v235 offset:13312
v_mul_f32 v209,v209,v218
mm v[176:179],a[68:71],v[112:115],v[176:179]
dr a[116:119],v235 offset:14336
v_mul_f32 v210,v210,v218
mm v[176:179],a[64:67],v[104:107],v[176:179]
dr a[120:123],v235 offset:15360
v_mul_f32 v211,v211,v218
mm v[176:179],a[124:127],v[96:99],v[176:179]
ex2 v132,v132
mm v[180:183],a[76:79],v[96:99],v[204:207]
ex2 v133,v133
mm v[180:183],a[80:83],v[104:107],v[180:183]
s_waitcnt lgkmcnt(4)
ex2 v128,v128
mm v[180:183],a[84:87],v[112:115],v[180:183]
EF_1:
s_cbranch_execz EF_R
EC_1:
s_cbranch_execz EC_R
ex2 v129,v129
mm v[180:183],a[88:91],v[120:123],v[180:183]
ex2 v130,v130
mm v[164:167],a[88:91],v[88:91],v[200:203]
s_waitcnt vmcnt(8) lgkmcnt(0)
s_barrier
mm v[164:167],a[84:87],v[80:83],v[164:167]
buffer_store_dwordx2 v[252:253],v254,s[8:11],s43 offen sc1
dr a[124:127],v235 offset:16384
ex2 v131,v131
mm v[164:167],a[80:83],v[72:75],v[164:167]
bl v232,s[8:11],s40 offen offset:0 sc1 lds
mm v[164:167],a[76:79],v[64:67],v[164:167]
dr a[64:67],v235 offset:24576
v_mul_f32 v212,v212,v218
mm v[168:171],a[92:95],v[64:67],v[200:203]
)" R"(dr a[68:71],v235 offset:17408
v_mul_f32 v213,v213,v218
mm v[168:171],a[96:99],v[72:75],v[168:171]
ex2 v182,v182
mm v[168:171],a[100:103],v[80:83],v[168:171]
bl v232,s[8:11],s40 offen offset:1024 sc1 lds
mm v[168:171],a[104:107],v[88:91],v[168:171]
dr a[72:75],v235 offset:25600
v_mul_f32 v214,v214,v218
mm v[184:187],a[104:107],v[120:123],v[204:207]
dr a[76:79],v235 offset:18432
v_mul_f32 v215,v215,v218
mm v[184:187],a[100:103],v[112:115],v[184:187]
ex2 v183,v183
mm v[184:187],a[96:99],v[104:107],v[184:187]
bl v232,s[8:11],s40 offen offset:2048 sc1 lds
mm v[184:187],a[92:95],v[96:99],v[184:187]
dr a[80:83],v235 offset:26624
cv v208,v208,v209
mm v[188:191],a[108:111],v[96:99],v[204:207]
dr a[84:87],v235 offset:19456
cv v209,v210,v211
mm v[188:191],a[112:115],v[104:107],v[188:191]
ex2 v180,v180
mm v[188:191],a[116:119],v[112:115],v[188:191]
bl v232,s[8:11],s40 offen offset:3072 sc1 lds
mm v[188:191],a[120:123],v[120:123],v[188:191]
s_add_u32 s40,s31,8192
dr a[88:91],v235 offset:27648
cv v210,v212,v213
mm v[172:175],a[120:123],v[88:91],v[200:203]
s_add_u32 m0,s33,147456
dr a[92:95],v235 offset:20480
cv v211,v214,v215
mm v[172:175],a[116:119],v[80:83],v[172:175]
ex2 v181,v181
mm v[172:175],a[112:115],v[72:75],v[172:175]
bs v[208:211],v232,s[8:11],s82 offen offset:0 sc1
cv v134,v132,v133
cv v132,v128,v129
cv v133,v130,v131
dr a[96:99],v235 offset:28672
mm v[172:175],a[108:111],v[64:67],v[172:175]
s_waitcnt lgkmcnt(3)
dr a[100:103],v235 offset:21504
v_mov_b32 v193,0
mm v[68:71],a[124:127],v[148:151],v[68:71]
ex2 v176,v176
mm v[68:71],a[64:67],v[152:155],v[68:71]
dr a[104:107],v235 offset:29696
v_mov_b32 v194,0
mm v[4:7],a[64:67],v[136:139],v[4:7]
dr a[108:111],v235 offset:22528
v_mov_b32 v195,0
mm v[4:7],a[124:127],v[132:135],v[4:7]
bs v[192:195],v239,s[8:11],s66 offen offset:0 sc1
v_mul_f32 v216,v216,v218
v_mul_f32 v217,v217,v218
cv v212,v216,v217
v_lshrrev_b32 v242,1,v232
v_and_b32 v242,24,v242
v_add_f32 v230,0x358637bd,v230
mm v[12:15],a[68:71],v[132:135],v[12:15]
dr a[112:115],v235 offset:30720
)" R"(cv v183,v182,v183
mm v[12:15],a[72:75],v[136:139],v[12:15]
dr a[116:119],v235 offset:23552
cv v182,v180,v181
mm v[76:79],a[72:75],v[152:155],v[76:79]
v_rcp_f32 v230,v230
mm v[76:79],a[68:71],v[148:151],v[76:79]
dr a[120:123],v235 offset:31744
v_mul_f32 v220,v220,v230
mm v[84:87],a[76:79],v[148:151],v[84:87]
buffer_store_dword v212,v249,s[8:11],s83 offen offset:0 sc1
v_mul_f32 v221,v221,v230
mm v[84:87],a[80:83],v[152:155],v[84:87]
ex2 v177,v177
mm v[20:23],a[80:83],v[136:139],v[20:23]
ex2 v178,v178
mm v[20:23],a[76:79],v[132:135],v[20:23]
ex2 v179,v179
mm v[28:31],a[84:87],v[132:135],v[28:31]
s_waitcnt lgkmcnt(1)
ex2 v184,v184
mm v[28:31],a[88:91],v[136:139],v[28:31]
ex2 v185,v185
mm v[92:95],a[88:91],v[152:155],v[92:95]
s_waitcnt vmcnt(12) lgkmcnt(0)
s_barrier
mm v[92:95],a[84:87],v[148:151],v[92:95]
bl v232,s[8:11],s40 offen offset:0 sc1 lds
v_smfmac_f32_16x16x64_bf16 a[136:139],a[144:147],v[132:139],v246
dr a[124:127],v232 offset:0
v_mul_f32 v222,v222,v230
v_smfmac_f32_16x16x64_bf16 a[140:143],a[144:147],v[148:155],v246
dr a[64:67],v232 offset:1024
v_mul_f32 v223,v223,v230
mm v[100:103],a[92:95],v[148:151],v[100:103]
ex2 v186,v186
mm v[100:103],a[96:99],v[152:155],v[100:103]
bl v232,s[8:11],s40 offen offset:1024 sc1 lds
mm v[36:39],a[96:99],v[136:139],v[36:39]
dr a[68:71],v232 offset:2048
v_mul_f32 v224,v224,v230
mm v[36:39],a[92:95],v[132:135],v[36:39]
dr a[72:75],v232 offset:3072
v_mul_f32 v225,v225,v230
mm v[44:47],a[100:103],v[132:135],v[44:47]
ex2 v187,v187
mm v[44:47],a[104:107],v[136:139],v[44:47]
bl v232,s[8:11],s40 offen offset:2048 sc1 lds
mm v[108:111],a[104:107],v[152:155],v[108:111]
dr a[76:79],v232 offset:4096
v_mul_f32 v226,v226,v230
mm v[108:111],a[100:103],v[148:151],v[108:111]
dr a[80:83],v232 offset:5120
v_mul_f32 v227,v227,v230
mm v[116:119],a[108:111],v[148:151],v[116:119]
ex2 v188,v188
mm v[116:119],a[112:115],v[152:155],v[116:119]
s_mov_b32 s30,s31
bl v232,s[8:11],s40 offen offset:3072 sc1 lds
mm v[52:55],a[112:115],v[136:139],v[52:55]
s_add_u32 s31,s31,65536
dr a[84:87],v232 offset:6144
cv v220,v220,v221
mm v[52:55],a[108:111],v[132:135],v[52:55]
)" R"(s_cmp_eq_u32 s31,s3
s_cselect_b32 s40,65536,0
dr a[88:91],v232 offset:7168
mm v[60:63],a[116:119],v[132:135],v[60:63]
s_add_u32 s31,s31,s40
ex2 v189,v189
mm v[60:63],a[120:123],v[136:139],v[60:63]
s_add_u32 m0,s33,0
dr a[92:95],v232 offset:8192
cv v221,v222,v223
mm v[124:127],a[120:123],v[152:155],v[124:127]
s_add_u32 s40,s30,40960
dr a[96:99],v232 offset:9216
cv v222,v224,v225
mm v[124:127],a[116:119],v[148:151],v[124:127]
s_waitcnt lgkmcnt(4)
ex2 v190,v190
mm v[128:131],a[124:127],v[64:67],v[200:203]
dr a[100:103],v232 offset:10240
cv v223,v226,v227
mm v[128:131],a[64:67],v[72:75],v[128:131]
buffer_load_dwordx2 v[240:241],v242,s[8:11],s46 offen sc1
mm v[128:131],a[68:71],v[80:83],v[128:131]
dr a[104:107],v232 offset:11264
v_mul_f32 v228,v228,v230
mm v[128:131],a[72:75],v[88:91],v[128:131]
bs v[220:223],v232,s[8:11],s82 offen offset:1024 sc1
dr a[108:111],v232 offset:12288
v_mul_f32 v229,v229,v230
cv v224,v228,v229
cv v180,v176,v177
cv v181,v178,v179
cv v184,v184,v185
mm v[144:147],a[72:75],v[120:123],v[204:207]
dr a[112:115],v232 offset:13312
cv v185,v186,v187
mm v[144:147],a[68:71],v[112:115],v[144:147]
dr a[116:119],v232 offset:14336
cv v186,v188,v189
mm v[144:147],a[64:67],v[104:107],v[144:147]
buffer_store_dword v224,v249,s[8:11],s83 offen offset:1024 sc1
dr a[120:123],v232 offset:15360
mm v[144:147],a[124:127],v[96:99],v[144:147]
ex2 v191,v191
mm v[148:151],a[76:79],v[96:99],v[204:207]
ex2 v168,v168
mm v[148:151],a[80:83],v[104:107],v[148:151]
s_waitcnt lgkmcnt(4)
ex2 v169,v169
mm v[148:151],a[84:87],v[112:115],v[148:151]
ex2 v170,v170
mm v[148:151],a[88:91],v[120:123],v[148:151]
ex2 v171,v171
mm v[132:135],a[88:91],v[88:91],v[200:203]
s_waitcnt vmcnt(15) lgkmcnt(0)
s_barrier
mm v[132:135],a[84:87],v[80:83],v[132:135]
bl v232,s[8:11],s31 offen offset:0 sc1 lds
mm v[132:135],a[80:83],v[72:75],v[132:135]
dr a[124:127],v232 offset:49152
cv v187,v190,v191
mm v[132:135],a[76:79],v[64:67],v[132:135]
dr a[64:67],v232 offset:57344
cv v168,v168,v169
)" R"(mm v[136:139],a[92:95],v[64:67],v[200:203]
ex2 v172,v172
mm v[136:139],a[96:99],v[72:75],v[136:139]
bl v232,s[8:11],s31 offen offset:1024 sc1 lds
mm v[136:139],a[100:103],v[80:83],v[136:139]
dr a[68:71],v232 offset:50176
cv v169,v170,v171
mm v[136:139],a[104:107],v[88:91],v[136:139]
dr a[72:75],v232 offset:58368
v_lshrrev_b32 v247,1,v248
mm v[152:155],a[104:107],v[120:123],v[204:207]
ex2 v173,v173
mm v[152:155],a[100:103],v[112:115],v[152:155]
bl v232,s[8:11],s31 offen offset:2048 sc1 lds
mm v[152:155],a[96:99],v[104:107],v[152:155]
dr a[76:79],v232 offset:51200
cv v170,v172,v173
mm v[152:155],a[92:95],v[96:99],v[152:155]
ex2 v174,v174
mm v[156:159],a[108:111],v[96:99],v[204:207]
ex2 v175,v175
mm v[156:159],a[112:115],v[104:107],v[156:159]
bl v232,s[8:11],s31 offen offset:3072 sc1 lds
mm v[156:159],a[116:119],v[112:115],v[156:159]
s_add_u32 m0,s33,49152
dr a[80:83],v232 offset:59392
cv v171,v174,v175
mm v[156:159],a[120:123],v[120:123],v[156:159]
ex2 v166,v166
mm v[140:143],a[120:123],v[88:91],v[200:203]
ex2 v167,v167
mm v[140:143],a[116:119],v[80:83],v[140:143]
bs v[192:195],v239,s[8:11],s78 offen offset:0 sc1
ex2 v164,v164
ex2 v165,v165
ex2 v160,v160
mm v[140:143],a[112:115],v[72:75],v[140:143]
ex2 v161,v161
mm v[140:143],a[108:111],v[64:67],v[140:143]
s_waitcnt lgkmcnt(1)
ex2 v162,v162
mm v[68:71],a[124:127],v[180:183],v[68:71]
ex2 v163,v163
dr a[84:87],v232 offset:52224
mm v[68:71],a[64:67],v[184:187],v[68:71]
dr a[88:91],v232 offset:60416
cv v167,v166,v167
cv v166,v164,v165
cv v164,v160,v161
cv v165,v162,v163
mm v[4:7],a[64:67],v[168:171],v[4:7]
dr a[92:95],v232 offset:53248
mm v[4:7],a[124:127],v[164:167],v[4:7]
dr a[96:99],v232 offset:61440
mm v[12:15],a[68:71],v[164:167],v[12:15]
dr a[100:103],v232 offset:54272
mm v[12:15],a[72:75],v[168:171],v[12:15]
dr a[104:107],v232 offset:62464
mm v[76:79],a[72:75],v[184:187],v[76:79]
dr a[108:111],v232 offset:55296
mm v[76:79],a[68:71],v[180:183],v[76:79]
dr a[112:115],v232 offset:63488
mm v[84:87],a[76:79],v[180:183],v[84:87]
s_waitcnt lgkmcnt(6)
)" R"(dr a[116:119],v232 offset:56320
mm v[84:87],a[80:83],v[184:187],v[84:87]
dr a[120:123],v232 offset:64512
mm v[20:23],a[80:83],v[168:171],v[20:23]
ex2 v150,v150
mm v[20:23],a[76:79],v[164:167],v[20:23]
ex2 v151,v151
mm v[28:31],a[84:87],v[164:167],v[28:31]
ex2 v148,v148
mm v[28:31],a[88:91],v[168:171],v[28:31]
ex2 v149,v149
mm v[92:95],a[88:91],v[184:187],v[92:95]
s_waitcnt vmcnt(15) lgkmcnt(0)
s_barrier
mm v[92:95],a[84:87],v[180:183],v[92:95]
bl v232,s[8:11],s40 offen offset:0 sc1 lds
v_smfmac_f32_16x16x64_bf16 a[136:139],a[144:147],v[164:171],v246
dr a[124:127],v235 offset:0
cv v151,v150,v151
v_smfmac_f32_16x16x64_bf16 a[140:143],a[144:147],v[180:187],v246
dr a[64:67],v235 offset:1024
cv v150,v148,v149
mm v[100:103],a[92:95],v[180:183],v[100:103]
ex2 v144,v144
mm v[100:103],a[96:99],v[184:187],v[100:103]
bl v232,s[8:11],s40 offen offset:1024 sc1 lds
mm v[36:39],a[96:99],v[168:171],v[36:39]
dr a[68:71],v235 offset:2048
mm v[36:39],a[92:95],v[164:167],v[36:39]
dr a[72:75],v235 offset:3072
mm v[44:47],a[100:103],v[164:167],v[44:47]
ex2 v145,v145
mm v[44:47],a[104:107],v[168:171],v[44:47]
bl v232,s[8:11],s40 offen offset:2048 sc1 lds
mm v[108:111],a[104:107],v[184:187],v[108:111]
dr a[76:79],v235 offset:4096
cv v148,v144,v145
mm v[108:111],a[100:103],v[180:183],v[108:111]
dr a[80:83],v235 offset:5120
mm v[116:119],a[108:111],v[180:183],v[116:119]
ex2 v146,v146
mm v[116:119],a[112:115],v[184:187],v[116:119]
bl v232,s[8:11],s40 offen offset:3072 sc1 lds
mm v[52:55],a[112:115],v[168:171],v[52:55]
s_add_u32 s40,s31,32768
dr a[84:87],v235 offset:6144
s_add_u32 m0,s33,131072
mm v[52:55],a[108:111],v[164:167],v[52:55]
dr a[88:91],v235 offset:7168
mm v[60:63],a[116:119],v[164:167],v[60:63]
ex2 v147,v147
mm v[60:63],a[120:123],v[168:171],v[60:63]
dr a[92:95],v235 offset:8192
cv v149,v146,v147
mm v[124:127],a[120:123],v[184:187],v[124:127]
dr a[96:99],v235 offset:9216
mm v[124:127],a[116:119],v[180:183],v[124:127]
s_waitcnt lgkmcnt(4)
ex2 v152,v152
mm v[160:163],a[124:127],v[64:67],v[200:203]
dr a[100:103],v235 offset:10240
mm v[160:163],a[64:67],v[72:75],v[160:163]
s_waitcnt vmcnt(11)
v_cmp_ne_u64 vcc,s[54:55],v[240:241]
)" R"(s_cbranch_vccnz FS_S
FS_D:
mm v[160:163],a[68:71],v[80:83],v[160:163]
s_lshl_b32 s46,s25,10
dr a[104:107],v235 offset:11264
v_lshrrev_b32 v242,1,v232
mm v[160:163],a[72:75],v[88:91],v[160:163]
s_add_u32 s46,s46,0x4018200
dr a[108:111],v235 offset:12288
v_and_b32 v242,504,v242
mm v[176:179],a[72:75],v[120:123],v[204:207]
dr a[112:115],v235 offset:13312
mm v[176:179],a[68:71],v[112:115],v[176:179]
bl a[0:3],v232,s[8:11],s75 offen offset:0 sc1
mm v[176:179],a[64:67],v[104:107],v[176:179]
dr a[116:119],v235 offset:14336
mm v[176:179],a[124:127],v[96:99],v[176:179]
dr a[120:123],v235 offset:15360
mm v[180:183],a[76:79],v[96:99],v[204:207]
ex2 v153,v153
mm v[180:183],a[80:83],v[104:107],v[180:183]
s_waitcnt lgkmcnt(4)
bs v[192:195],v239,s[8:11],s66 offen offset:1024 sc1
ex2 v154,v154
ex2 v155,v155
ex2 v156,v156
mm v[180:183],a[84:87],v[112:115],v[180:183]
ex2 v157,v157
mm v[180:183],a[88:91],v[120:123],v[180:183]
ex2 v158,v158
mm v[164:167],a[88:91],v[88:91],v[200:203]
s_waitcnt vmcnt(11) lgkmcnt(0)
s_barrier
mm v[164:167],a[84:87],v[80:83],v[164:167]
buffer_store_dwordx2 v[252:253],v254,s[8:11],s85 offen sc1
dr a[124:127],v235 offset:16384
ex2 v159,v159
mm v[164:167],a[80:83],v[72:75],v[164:167]
bl v232,s[8:11],s40 offen offset:0 sc1 lds
mm v[164:167],a[76:79],v[64:67],v[164:167]
dr a[64:67],v235 offset:24576
cv v152,v152,v153
mm v[168:171],a[92:95],v[64:67],v[200:203]
dr a[68:71],v235 offset:17408
cv v153,v154,v155
mm v[168:171],a[96:99],v[72:75],v[168:171]
ex2 v136,v136
mm v[168:171],a[100:103],v[80:83],v[168:171]
bl v232,s[8:11],s40 offen offset:1024 sc1 lds
mm v[168:171],a[104:107],v[88:91],v[168:171]
dr a[72:75],v235 offset:25600
cv v154,v156,v157
mm v[184:187],a[104:107],v[120:123],v[204:207]
dr a[76:79],v235 offset:18432
cv v155,v158,v159
mm v[184:187],a[100:103],v[112:115],v[184:187]
ex2 v137,v137
mm v[184:187],a[96:99],v[104:107],v[184:187]
bl v232,s[8:11],s40 offen offset:2048 sc1 lds
mm v[184:187],a[92:95],v[96:99],v[184:187]
ex2 v138,v138
mm v[188:191],a[108:111],v[96:99],v[204:207]
ex2 v139,v139
mm v[188:191],a[112:115],v[104:107],v[188:191]
)" R"(ex2 v140,v140
mm v[188:191],a[116:119],v[112:115],v[188:191]
bl v232,s[8:11],s40 offen offset:3072 sc1 lds
mm v[188:191],a[120:123],v[120:123],v[188:191]
s_add_u32 s40,s31,8192
ex2 v141,v141
mm v[172:175],a[120:123],v[88:91],v[200:203]
s_add_u32 m0,s33,147456
ex2 v142,v142
mm v[172:175],a[116:119],v[80:83],v[172:175]
ex2 v143,v143
mm v[172:175],a[112:115],v[72:75],v[172:175]
dr a[80:83],v235 offset:26624
cv v136,v136,v137
mm v[172:175],a[108:111],v[64:67],v[172:175]
s_waitcnt lgkmcnt(1)
bl a[4:7],v233,s[8:11],s75 offen offset:0 sc1
mm v[68:71],a[124:127],v[148:151],v[68:71]
dr a[84:87],v235 offset:19456
cv v137,v138,v139
ex2 v134,v134
ex2 v135,v135
ex2 v132,v132
ex2 v133,v133
ex2 v128,v128
ex2 v129,v129
ex2 v130,v130
ex2 v131,v131
cv v138,v140,v141
cv v139,v142,v143
mm v[68:71],a[64:67],v[152:155],v[68:71]
dr a[88:91],v235 offset:27648
cv v135,v134,v135
cv v134,v132,v133
cv v132,v128,v129
cv v133,v130,v131
mm v[4:7],a[64:67],v[136:139],v[4:7]
dr a[92:95],v235 offset:20480
mm v[4:7],a[124:127],v[132:135],v[4:7]
dr a[96:99],v235 offset:28672
mm v[12:15],a[68:71],v[132:135],v[12:15]
dr a[100:103],v235 offset:21504
mm v[12:15],a[72:75],v[136:139],v[12:15]
dr a[104:107],v235 offset:29696
mm v[76:79],a[72:75],v[152:155],v[76:79]
dr a[108:111],v235 offset:22528
mm v[76:79],a[68:71],v[148:151],v[76:79]
dr a[112:115],v235 offset:30720
mm v[84:87],a[76:79],v[148:151],v[84:87]
s_waitcnt lgkmcnt(6)
dr a[116:119],v235 offset:23552
mm v[84:87],a[80:83],v[152:155],v[84:87]
dr a[120:123],v235 offset:31744
bl a[8:11],v232,s[8:11],s75 offen offset:1024 sc1
mm v[20:23],a[80:83],v[136:139],v[20:23]
ex2 v182,v182
mm v[20:23],a[76:79],v[132:135],v[20:23]
ex2 v183,v183
mm v[28:31],a[84:87],v[132:135],v[28:31]
ex2 v180,v180
mm v[28:31],a[88:91],v[136:139],v[28:31]
ex2 v181,v181
mm v[92:95],a[88:91],v[152:155],v[92:95]
s_waitcnt vmcnt(14) lgkmcnt(0)
)" R"(s_barrier
mm v[92:95],a[84:87],v[148:151],v[92:95]
bl v232,s[8:11],s40 offen offset:0 sc1 lds
v_smfmac_f32_16x16x64_bf16 a[136:139],a[144:147],v[132:139],v246
dr a[124:127],v232 offset:0
cv v183,v182,v183
v_smfmac_f32_16x16x64_bf16 a[140:143],a[144:147],v[148:155],v246
dr a[64:67],v232 offset:1024
cv v182,v180,v181
mm v[100:103],a[92:95],v[148:151],v[100:103]
ex2 v176,v176
mm v[100:103],a[96:99],v[152:155],v[100:103]
bl v232,s[8:11],s40 offen offset:1024 sc1 lds
mm v[36:39],a[96:99],v[136:139],v[36:39]
dr a[68:71],v232 offset:2048
mm v[36:39],a[92:95],v[132:135],v[36:39]
dr a[72:75],v232 offset:3072
mm v[44:47],a[100:103],v[132:135],v[44:47]
ex2 v177,v177
mm v[44:47],a[104:107],v[136:139],v[44:47]
bl v232,s[8:11],s40 offen offset:2048 sc1 lds
mm v[108:111],a[104:107],v[152:155],v[108:111]
dr a[76:79],v232 offset:4096
cv v180,v176,v177
mm v[108:111],a[100:103],v[148:151],v[108:111]
dr a[80:83],v232 offset:5120
mm v[116:119],a[108:111],v[148:151],v[116:119]
ex2 v178,v178
mm v[116:119],a[112:115],v[152:155],v[116:119]
s_mov_b32 s30,s31
bl v232,s[8:11],s40 offen offset:3072 sc1 lds
mm v[52:55],a[112:115],v[136:139],v[52:55]
s_add_u32 s31,s31,65536
dr a[84:87],v232 offset:6144
s_add_u32 m0,s33,0
mm v[52:55],a[108:111],v[132:135],v[52:55]
s_cmp_eq_u32 s31,s3
s_cselect_b32 s40,65536,0
dr a[88:91],v232 offset:7168
mm v[60:63],a[116:119],v[132:135],v[60:63]
s_add_u32 s31,s31,s40
ex2 v179,v179
mm v[60:63],a[120:123],v[136:139],v[60:63]
s_add_u32 s40,s30,40960
bl a[12:15],v233,s[8:11],s75 offen offset:1024 sc1
mm v[124:127],a[120:123],v[152:155],v[124:127]
dr a[92:95],v232 offset:8192
cv v181,v178,v179
mm v[124:127],a[116:119],v[148:151],v[124:127]
s_waitcnt lgkmcnt(3)
dr a[96:99],v232 offset:9216
mm v[128:131],a[124:127],v[64:67],v[200:203]
ex2 v184,v184
mm v[128:131],a[64:67],v[72:75],v[128:131]
dr a[100:103],v232 offset:10240
mm v[128:131],a[68:71],v[80:83],v[128:131]
dr a[104:107],v232 offset:11264
mm v[128:131],a[72:75],v[88:91],v[128:131]
buffer_load_dwordx2 v[240:241],v242,s[8:11],s46 offen sc1
mm v[144:147],a[72:75],v[120:123],v[204:207]
dr a[108:111],v232 offset:12288
bs v[192:195],v239,s[8:11],s78 offen offset:1024 sc1
dr a[112:115],v232 offset:13312
ex2 v185,v185
)" R"(ex2 v186,v186
cv v184,v184,v185
mm v[144:147],a[68:71],v[112:115],v[144:147]
dr a[116:119],v232 offset:14336
mm v[144:147],a[64:67],v[104:107],v[144:147]
dr a[120:123],v232 offset:15360
mm v[144:147],a[124:127],v[96:99],v[144:147]
ex2 v187,v187
mm v[148:151],a[76:79],v[96:99],v[204:207]
ex2 v188,v188
mm v[148:151],a[80:83],v[104:107],v[148:151]
s_waitcnt lgkmcnt(4)
bl a[16:19],v232,s[8:11],s75 offen offset:2048 sc1
mm v[148:151],a[84:87],v[112:115],v[148:151]
ex2 v189,v189
mm v[148:151],a[88:91],v[120:123],v[148:151]
ex2 v190,v190
mm v[132:135],a[88:91],v[88:91],v[200:203]
s_waitcnt vmcnt(17) lgkmcnt(0)
s_barrier
mm v[132:135],a[84:87],v[80:83],v[132:135]
bl v232,s[8:11],s31 offen offset:0 sc1 lds
mm v[132:135],a[80:83],v[72:75],v[132:135]
dr a[124:127],v232 offset:49152
cv v185,v186,v187
mm v[132:135],a[76:79],v[64:67],v[132:135]
dr a[64:67],v232 offset:57344
cv v186,v188,v189
mm v[136:139],a[92:95],v[64:67],v[200:203]
ex2 v191,v191
mm v[136:139],a[96:99],v[72:75],v[136:139]
bl v232,s[8:11],s31 offen offset:1024 sc1 lds
mm v[136:139],a[100:103],v[80:83],v[136:139]
dr a[68:71],v232 offset:50176
cv v187,v190,v191
mm v[136:139],a[104:107],v[88:91],v[136:139]
dr a[72:75],v232 offset:58368
mm v[152:155],a[104:107],v[120:123],v[204:207]
ex2 v168,v168
mm v[152:155],a[100:103],v[112:115],v[152:155]
bl v232,s[8:11],s31 offen offset:2048 sc1 lds
mm v[152:155],a[96:99],v[104:107],v[152:155]
dr a[76:79],v232 offset:51200
mm v[152:155],a[92:95],v[96:99],v[152:155]
ex2 v169,v169
mm v[156:159],a[108:111],v[96:99],v[204:207]
ex2 v170,v170
mm v[156:159],a[112:115],v[104:107],v[156:159]
bl v232,s[8:11],s31 offen offset:3072 sc1 lds
mm v[156:159],a[116:119],v[112:115],v[156:159]
s_add_u32 m0,s33,49152
ex2 v171,v171
mm v[156:159],a[120:123],v[120:123],v[156:159]
ex2 v172,v172
mm v[140:143],a[120:123],v[88:91],v[200:203]
ex2 v173,v173
mm v[140:143],a[116:119],v[80:83],v[140:143]
ex2 v174,v174
mm v[140:143],a[112:115],v[72:75],v[140:143]
ex2 v175,v175
mm v[140:143],a[108:111],v[64:67],v[140:143]
EF_2:
s_cbranch_execz EF_R
EC_2:
)" R"(s_cbranch_execz EC_R
s_waitcnt lgkmcnt(0)
dr a[80:83],v232 offset:59392
cv v168,v168,v169
bl a[20:23],v233,s[8:11],s75 offen offset:2048 sc1
mm v[68:71],a[124:127],v[180:183],v[68:71]
dr a[84:87],v232 offset:52224
cv v169,v170,v171
ex2 v166,v166
ex2 v167,v167
ex2 v164,v164
ex2 v165,v165
ex2 v160,v160
ex2 v161,v161
ex2 v162,v162
ex2 v163,v163
cv v170,v172,v173
cv v171,v174,v175
mm v[68:71],a[64:67],v[184:187],v[68:71]
dr a[88:91],v232 offset:60416
cv v167,v166,v167
cv v166,v164,v165
cv v164,v160,v161
cv v165,v162,v163
mm v[4:7],a[64:67],v[168:171],v[4:7]
dr a[92:95],v232 offset:53248
mm v[4:7],a[124:127],v[164:167],v[4:7]
dr a[96:99],v232 offset:61440
mm v[12:15],a[68:71],v[164:167],v[12:15]
dr a[100:103],v232 offset:54272
bs v[192:195],v239,s[8:11],s66 offen offset:2048 sc1
dr a[104:107],v232 offset:62464
ex2 v150,v150
ex2 v151,v151
mm v[12:15],a[72:75],v[168:171],v[12:15]
dr a[108:111],v232 offset:55296
cv v151,v150,v151
mm v[76:79],a[72:75],v[184:187],v[76:79]
dr a[112:115],v232 offset:63488
mm v[76:79],a[68:71],v[180:183],v[76:79]
dr a[116:119],v232 offset:56320
mm v[84:87],a[76:79],v[180:183],v[84:87]
s_waitcnt lgkmcnt(7)
dr a[120:123],v232 offset:64512
mm v[84:87],a[80:83],v[184:187],v[84:87]
ex2 v148,v148
mm v[20:23],a[80:83],v[168:171],v[20:23]
ex2 v149,v149
mm v[20:23],a[76:79],v[164:167],v[20:23]
ex2 v144,v144
mm v[28:31],a[84:87],v[164:167],v[28:31]
ex2 v145,v145
mm v[28:31],a[88:91],v[168:171],v[28:31]
ex2 v146,v146
mm v[92:95],a[88:91],v[184:187],v[92:95]
s_waitcnt vmcnt(16) lgkmcnt(0)
s_barrier
mm v[92:95],a[84:87],v[180:183],v[92:95]
bl v232,s[8:11],s40 offen offset:0 sc1 lds
v_smfmac_f32_16x16x64_bf16 a[136:139],a[144:147],v[164:171],v246
dr a[124:127],v235 offset:0
cv v150,v148,v149
v_smfmac_f32_16x16x64_bf16 a[140:143],a[144:147],v[180:187],v246
dr a[64:67],v235 offset:1024
)" R"(cv v148,v144,v145
mm v[100:103],a[92:95],v[180:183],v[100:103]
ex2 v147,v147
mm v[100:103],a[96:99],v[184:187],v[100:103]
bl v232,s[8:11],s40 offen offset:1024 sc1 lds
mm v[36:39],a[96:99],v[168:171],v[36:39]
dr a[68:71],v235 offset:2048
cv v149,v146,v147
mm v[36:39],a[92:95],v[164:167],v[36:39]
dr a[72:75],v235 offset:3072
mm v[44:47],a[100:103],v[164:167],v[44:47]
ex2 v152,v152
mm v[44:47],a[104:107],v[168:171],v[44:47]
bl v232,s[8:11],s40 offen offset:2048 sc1 lds
mm v[108:111],a[104:107],v[184:187],v[108:111]
dr a[76:79],v235 offset:4096
mm v[108:111],a[100:103],v[180:183],v[108:111]
dr a[80:83],v235 offset:5120
mm v[116:119],a[108:111],v[180:183],v[116:119]
ex2 v153,v153
mm v[116:119],a[112:115],v[184:187],v[116:119]
bl v232,s[8:11],s40 offen offset:3072 sc1 lds
mm v[52:55],a[112:115],v[168:171],v[52:55]
s_add_u32 s40,s31,32768
dr a[84:87],v235 offset:6144
s_add_u32 m0,s33,131072
mm v[52:55],a[108:111],v[164:167],v[52:55]
s_lshl_b32 s101,s27,7
dr a[88:91],v235 offset:7168
cv v152,v152,v153
mm v[60:63],a[116:119],v[164:167],v[60:63]
ex2 v154,v154
mm v[60:63],a[120:123],v[168:171],v[60:63]
bl a[24:27],v232,s[8:11],s75 offen offset:3072 sc1
mm v[124:127],a[120:123],v[184:187],v[124:127]
dr a[92:95],v235 offset:8192
mm v[124:127],a[116:119],v[180:183],v[124:127]
s_waitcnt lgkmcnt(3)
dr a[96:99],v235 offset:9216
mm v[160:163],a[124:127],v[64:67],v[200:203]
ex2 v155,v155
mm v[160:163],a[64:67],v[72:75],v[160:163]
dr a[100:103],v235 offset:10240
cv v153,v154,v155
mm v[160:163],a[68:71],v[80:83],v[160:163]
dr a[104:107],v235 offset:11264
mm v[160:163],a[72:75],v[88:91],v[160:163]
bl a[28:31],v233,s[8:11],s75 offen offset:3072 sc1
mm v[176:179],a[72:75],v[120:123],v[204:207]
dr a[108:111],v235 offset:12288
mm v[176:179],a[68:71],v[112:115],v[176:179]
dr a[112:115],v235 offset:13312
mm v[176:179],a[64:67],v[104:107],v[176:179]
dr a[116:119],v235 offset:14336
mm v[176:179],a[124:127],v[96:99],v[176:179]
dr a[120:123],v235 offset:15360
mm v[180:183],a[76:79],v[96:99],v[204:207]
ex2 v156,v156
mm v[180:183],a[80:83],v[104:107],v[180:183]
s_waitcnt lgkmcnt(4)
ex2 v157,v157
mm v[180:183],a[84:87],v[112:115],v[180:183]
ex2 v158,v158
mm v[180:183],a[88:91],v[120:123],v[180:183]
)" R"(ex2 v159,v159
mm v[164:167],a[88:91],v[88:91],v[200:203]
s_waitcnt vmcnt(16) lgkmcnt(0)
s_barrier
mm v[164:167],a[84:87],v[80:83],v[164:167]
bl v232,s[8:11],s40 offen offset:0 sc1 lds
mm v[164:167],a[80:83],v[72:75],v[164:167]
dr a[124:127],v235 offset:16384
cv v154,v156,v157
mm v[164:167],a[76:79],v[64:67],v[164:167]
dr a[64:67],v235 offset:24576
cv v155,v158,v159
mm v[168:171],a[92:95],v[64:67],v[200:203]
ex2 v136,v136
mm v[168:171],a[96:99],v[72:75],v[168:171]
bl v232,s[8:11],s40 offen offset:1024 sc1 lds
mm v[168:171],a[100:103],v[80:83],v[168:171]
dr a[68:71],v235 offset:17408
mm v[168:171],a[104:107],v[88:91],v[168:171]
dr a[72:75],v235 offset:25600
mm v[184:187],a[104:107],v[120:123],v[204:207]
ex2 v137,v137
mm v[184:187],a[100:103],v[112:115],v[184:187]
bl v232,s[8:11],s40 offen offset:2048 sc1 lds
mm v[184:187],a[96:99],v[104:107],v[184:187]
dr a[76:79],v235 offset:18432
cv v136,v136,v137
mm v[184:187],a[92:95],v[96:99],v[184:187]
ex2 v138,v138
mm v[188:191],a[108:111],v[96:99],v[204:207]
ex2 v139,v139
mm v[188:191],a[112:115],v[104:107],v[188:191]
bl v232,s[8:11],s40 offen offset:3072 sc1 lds
mm v[188:191],a[116:119],v[112:115],v[188:191]
s_add_u32 s40,s31,8192
ex2 v140,v140
mm v[188:191],a[120:123],v[120:123],v[188:191]
s_add_u32 m0,s33,147456
ex2 v141,v141
mm v[172:175],a[120:123],v[88:91],v[200:203]
ex2 v142,v142
mm v[172:175],a[116:119],v[80:83],v[172:175]
ex2 v143,v143
mm v[172:175],a[112:115],v[72:75],v[172:175]
dr a[80:83],v235 offset:26624
cv v137,v138,v139
mm v[172:175],a[108:111],v[64:67],v[172:175]
s_waitcnt lgkmcnt(1)
bl a[32:35],v232,s[8:11],s76 offen offset:0 sc1
mm v[68:71],a[124:127],v[148:151],v[68:71]
dr a[84:87],v235 offset:19456
cv v138,v140,v141
ex2 v134,v134
ex2 v135,v135
ex2 v132,v132
ex2 v133,v133
ex2 v128,v128
ex2 v129,v129
ex2 v130,v130
ex2 v131,v131
cv v139,v142,v143
mm v[68:71],a[64:67],v[152:155],v[68:71]
dr a[88:91],v235 offset:27648
cv v135,v134,v135
)" R"(cv v134,v132,v133
cv v132,v128,v129
cv v133,v130,v131
mm v[4:7],a[64:67],v[136:139],v[4:7]
dr a[92:95],v235 offset:20480
s_waitcnt vmcnt(19)
v_cmp_ne_u64 vcc,s[54:55],v[240:241]
s_cbranch_vccnz FK3_S
FK3_D:
mm v[4:7],a[124:127],v[132:135],v[4:7]
s_lshl_b32 s46,s25,8
dr a[96:99],v235 offset:28672
v_lshrrev_b32 v242,1,v232
bl a[36:39],v233,s[8:11],s76 offen offset:0 sc1
mm v[12:15],a[68:71],v[132:135],v[12:15]
s_add_u32 s46,s46,s101
dr a[100:103],v235 offset:21504
v_and_b32 v242,120,v242
mm v[12:15],a[72:75],v[136:139],v[12:15]
s_add_u32 s46,s46,0x400a000
dr a[104:107],v235 offset:29696
mm v[76:79],a[72:75],v[152:155],v[76:79]
dr a[108:111],v235 offset:22528
mm v[76:79],a[68:71],v[148:151],v[76:79]
dr a[112:115],v235 offset:30720
bs v[192:195],v239,s[8:11],s78 offen offset:2048 sc1
dr a[116:119],v235 offset:23552
ex2 v182,v182
ex2 v183,v183
mm v[84:87],a[76:79],v[148:151],v[84:87]
s_waitcnt lgkmcnt(7)
dr a[120:123],v235 offset:31744
cv v183,v182,v183
mm v[84:87],a[80:83],v[152:155],v[84:87]
ex2 v180,v180
mm v[20:23],a[80:83],v[136:139],v[20:23]
ex2 v181,v181
mm v[20:23],a[76:79],v[132:135],v[20:23]
ex2 v176,v176
mm v[28:31],a[84:87],v[132:135],v[28:31]
ex2 v177,v177
mm v[28:31],a[88:91],v[136:139],v[28:31]
ex2 v178,v178
mm v[92:95],a[88:91],v[152:155],v[92:95]
s_waitcnt vmcnt(15) lgkmcnt(0)
s_barrier
mm v[92:95],a[84:87],v[148:151],v[92:95]
s_mov_b32 s2,ED_0-ED_G
bl v232,s[8:11],s40 offen offset:0 sc1 lds
v_smfmac_f32_16x16x64_bf16 a[136:139],a[144:147],v[132:139],v246
s_cmp_eq_u32 s24,1
s_cselect_b32 s2,ED_1-ED_G,s2
dr a[124:127],v232 offset:0
v_smfmac_f32_16x16x64_bf16 a[140:143],a[144:147],v[148:155],v246
s_cmp_eq_u32 s24,2
s_cselect_b32 s2,ED_2-ED_G,s2
dr a[64:67],v232 offset:1024
mm v[100:103],a[92:95],v[148:151],v[100:103]
s_cmp_eq_u32 s24,3
s_cselect_b32 s2,ED_3-ED_G,s2
s_getpc_b64 s[0:1]
ED_G:
mm v[100:103],a[96:99],v[152:155],v[100:103]
s_add_u32 s0,s0,s2
)" R"(s_addc_u32 s1,s1,0
dr a[68:71],v232 offset:2048
mm v[36:39],a[96:99],v[136:139],v[36:39]
s_getpc_b64 s[60:61]
CT4:
bl v232,s[8:11],s40 offen offset:1024 sc1 lds
mm v[36:39],a[92:95],v[132:135],v[36:39]
s_lshr_b32 s62,s29,3
dr a[72:75],v232 offset:3072
cv v182,v180,v181
mm v[44:47],a[100:103],v[132:135],v[44:47]
s_lshl_b32 s62,s62,2
dr a[76:79],v232 offset:4096
cv v180,v176,v177
mm v[44:47],a[104:107],v[136:139],v[44:47]
s_add_u32 s62,s62,s24
ex2 v179,v179
mm v[108:111],a[104:107],v[152:155],v[108:111]
s_lshl_b32 s62,s62,7
bl v232,s[8:11],s40 offen offset:2048 sc1 lds
mm v[108:111],a[100:103],v[148:151],v[108:111]
s_add_u32 s62,s62,UMID-CT4
dr a[80:83],v232 offset:5120
cv v181,v178,v179
mm v[116:119],a[108:111],v[148:151],v[116:119]
dr a[84:87],v232 offset:6144
v_min_u32 v250,16,v232
mm v[116:119],a[112:115],v[152:155],v[116:119]
s_mov_b32 s30,s31
ex2 v184,v184
mm v[52:55],a[112:115],v[136:139],v[52:55]
s_add_u32 s31,s31,65536
bl v232,s[8:11],s40 offen offset:3072 sc1 lds
mm v[52:55],a[108:111],v[132:135],v[52:55]
s_cmp_eq_u32 s31,s3
s_cselect_b32 s40,65536,0
dr a[88:91],v232 offset:7168
mm v[60:63],a[116:119],v[132:135],v[60:63]
s_add_u32 s31,s31,s40
dr a[92:95],v232 offset:8192
s_add_u32 m0,s33,0
mm v[60:63],a[120:123],v[136:139],v[60:63]
s_add_u32 s40,s30,40960
ex2 v185,v185
mm v[124:127],a[120:123],v[152:155],v[124:127]
bl a[40:43],v232,s[8:11],s76 offen offset:1024 sc1
mm v[124:127],a[116:119],v[148:151],v[124:127]
s_waitcnt lgkmcnt(2)
dr a[96:99],v232 offset:9216
cv v184,v184,v185
mm v[128:131],a[124:127],v[64:67],v[200:203]
dr a[100:103],v232 offset:10240
v_lshlrev_b32 v250,10,v250
mm v[128:131],a[64:67],v[72:75],v[128:131]
ex2 v186,v186
mm v[128:131],a[68:71],v[80:83],v[128:131]
dr a[104:107],v232 offset:11264
v_add_u32 v250,s62,v250
mm v[128:131],a[72:75],v[88:91],v[128:131]
dr a[108:111],v232 offset:12288
v_min_u32 v250,THEEND-CT4-4,v250
mm v[144:147],a[72:75],v[120:123],v[204:207]
dr a[112:115],v232 offset:13312
mm v[144:147],a[68:71],v[112:115],v[144:147]
)" R"(bl a[44:47],v233,s[8:11],s76 offen offset:1024 sc1
mm v[144:147],a[64:67],v[104:107],v[144:147]
dr a[116:119],v232 offset:14336
mm v[144:147],a[124:127],v[96:99],v[144:147]
dr a[120:123],v232 offset:15360
mm v[148:151],a[76:79],v[96:99],v[204:207]
ex2 v187,v187
mm v[148:151],a[80:83],v[104:107],v[148:151]
buffer_load_dwordx2 v[240:241],v242,s[8:11],s46 offen sc1
mm v[148:151],a[84:87],v[112:115],v[148:151]
s_waitcnt lgkmcnt(3)
bs v[192:195],v239,s[8:11],s66 offen offset:3072 sc1
ex2 v188,v188
ex2 v189,v189
ex2 v190,v190
mm v[148:151],a[88:91],v[120:123],v[148:151]
s_mov_b64 exec,0
s_setpc_b64 s[0:1]
ED_R:
s_mov_b64 exec,-1
mm v[132:135],a[88:91],v[88:91],v[200:203]
s_waitcnt vmcnt(17) lgkmcnt(0)
s_barrier
mm v[132:135],a[84:87],v[80:83],v[132:135]
bl v232,s[8:11],s31 offen offset:0 sc1 lds
mm v[132:135],a[80:83],v[72:75],v[132:135]
dr a[124:127],v232 offset:49152
cv v185,v186,v187
mm v[132:135],a[76:79],v[64:67],v[132:135]
dr a[64:67],v232 offset:57344
cv v186,v188,v189
mm v[136:139],a[92:95],v[64:67],v[200:203]
ex2 v191,v191
mm v[136:139],a[96:99],v[72:75],v[136:139]
bl v232,s[8:11],s31 offen offset:1024 sc1 lds
mm v[136:139],a[100:103],v[80:83],v[136:139]
dr a[68:71],v232 offset:50176
cv v187,v190,v191
mm v[136:139],a[104:107],v[88:91],v[136:139]
dr a[72:75],v232 offset:58368
mm v[152:155],a[104:107],v[120:123],v[204:207]
ex2 v168,v168
mm v[152:155],a[100:103],v[112:115],v[152:155]
bl v232,s[8:11],s31 offen offset:2048 sc1 lds
mm v[152:155],a[96:99],v[104:107],v[152:155]
dr a[76:79],v232 offset:51200
mm v[152:155],a[92:95],v[96:99],v[152:155]
ex2 v169,v169
mm v[156:159],a[108:111],v[96:99],v[204:207]
ex2 v170,v170
mm v[156:159],a[112:115],v[104:107],v[156:159]
bl v232,s[8:11],s31 offen offset:3072 sc1 lds
mm v[156:159],a[116:119],v[112:115],v[156:159]
s_add_u32 m0,s33,49152
ex2 v171,v171
mm v[156:159],a[120:123],v[120:123],v[156:159]
ex2 v172,v172
mm v[140:143],a[120:123],v[88:91],v[200:203]
ex2 v173,v173
mm v[140:143],a[116:119],v[80:83],v[140:143]
ex2 v174,v174
mm v[140:143],a[112:115],v[72:75],v[140:143]
ex2 v175,v175
mm v[140:143],a[108:111],v[64:67],v[140:143]
)" R"(s_waitcnt lgkmcnt(0)
dr a[80:83],v232 offset:59392
cv v168,v168,v169
bl a[48:51],v232,s[8:11],s76 offen offset:2048 sc1
mm v[68:71],a[124:127],v[180:183],v[68:71]
dr a[84:87],v232 offset:52224
cv v169,v170,v171
ex2 v166,v166
ex2 v167,v167
ex2 v164,v164
ex2 v165,v165
ex2 v160,v160
ex2 v161,v161
ex2 v162,v162
ex2 v163,v163
cv v170,v172,v173
cv v171,v174,v175
mm v[68:71],a[64:67],v[184:187],v[68:71]
dr a[88:91],v232 offset:60416
cv v167,v166,v167
cv v166,v164,v165
cv v164,v160,v161
cv v165,v162,v163
mm v[4:7],a[64:67],v[168:171],v[4:7]
dr a[92:95],v232 offset:53248
mm v[4:7],a[124:127],v[164:167],v[4:7]
dr a[96:99],v232 offset:61440
mm v[12:15],a[68:71],v[164:167],v[12:15]
dr a[100:103],v232 offset:54272
mm v[12:15],a[72:75],v[168:171],v[12:15]
dr a[104:107],v232 offset:62464
mm v[76:79],a[72:75],v[184:187],v[76:79]
dr a[108:111],v232 offset:55296
mm v[76:79],a[68:71],v[180:183],v[76:79]
dr a[112:115],v232 offset:63488
mm v[84:87],a[76:79],v[180:183],v[84:87]
s_waitcnt lgkmcnt(6)
dr a[116:119],v232 offset:56320
mm v[84:87],a[80:83],v[184:187],v[84:87]
dr a[120:123],v232 offset:64512
bl a[52:55],v233,s[8:11],s76 offen offset:2048 sc1
mm v[20:23],a[80:83],v[168:171],v[20:23]
ex2 v150,v150
mm v[20:23],a[76:79],v[164:167],v[20:23]
ex2 v151,v151
mm v[28:31],a[84:87],v[164:167],v[28:31]
ex2 v148,v148
mm v[28:31],a[88:91],v[168:171],v[28:31]
ex2 v149,v149
mm v[92:95],a[88:91],v[184:187],v[92:95]
s_waitcnt vmcnt(17) lgkmcnt(0)
s_barrier
mm v[92:95],a[84:87],v[180:183],v[92:95]
bl v232,s[8:11],s40 offen offset:0 sc1 lds
v_smfmac_f32_16x16x64_bf16 a[136:139],a[144:147],v[164:171],v246
dr a[124:127],v235 offset:0
cv v151,v150,v151
v_smfmac_f32_16x16x64_bf16 a[140:143],a[144:147],v[180:187],v246
dr a[64:67],v235 offset:1024
cv v150,v148,v149
mm v[100:103],a[92:95],v[180:183],v[100:103]
ex2 v144,v144
mm v[100:103],a[96:99],v[184:187],v[100:103]
bl v232,s[8:11],s40 offen offset:1024 sc1 lds
)" R"(mm v[36:39],a[96:99],v[168:171],v[36:39]
dr a[68:71],v235 offset:2048
mm v[36:39],a[92:95],v[164:167],v[36:39]
dr a[72:75],v235 offset:3072
mm v[44:47],a[100:103],v[164:167],v[44:47]
ex2 v145,v145
mm v[44:47],a[104:107],v[168:171],v[44:47]
bl v232,s[8:11],s40 offen offset:2048 sc1 lds
mm v[108:111],a[104:107],v[184:187],v[108:111]
dr a[76:79],v235 offset:4096
cv v148,v144,v145
mm v[108:111],a[100:103],v[180:183],v[108:111]
dr a[80:83],v235 offset:5120
mm v[116:119],a[108:111],v[180:183],v[116:119]
ex2 v146,v146
mm v[116:119],a[112:115],v[184:187],v[116:119]
bl v232,s[8:11],s40 offen offset:3072 sc1 lds
mm v[52:55],a[112:115],v[168:171],v[52:55]
s_add_u32 s40,s31,32768
dr a[84:87],v235 offset:6144
s_add_u32 m0,s33,131072
mm v[52:55],a[108:111],v[164:167],v[52:55]
dr a[88:91],v235 offset:7168
mm v[60:63],a[116:119],v[164:167],v[60:63]
ex2 v147,v147
mm v[60:63],a[120:123],v[168:171],v[60:63]
bl a[56:59],v232,s[8:11],s76 offen offset:3072 sc1
mm v[124:127],a[120:123],v[184:187],v[124:127]
dr a[92:95],v235 offset:8192
cv v149,v146,v147
mm v[124:127],a[116:119],v[180:183],v[124:127]
s_waitcnt lgkmcnt(3)
dr a[96:99],v235 offset:9216
mm v[160:163],a[124:127],v[64:67],v[200:203]
ex2 v152,v152
mm v[160:163],a[64:67],v[72:75],v[160:163]
dr a[100:103],v235 offset:10240
mm v[160:163],a[68:71],v[80:83],v[160:163]
dr a[104:107],v235 offset:11264
mm v[160:163],a[72:75],v[88:91],v[160:163]
global_load_dword v255,v250,s[60:61]
mm v[176:179],a[72:75],v[120:123],v[204:207]
dr a[108:111],v235 offset:12288
bs v[192:195],v239,s[8:11],s78 offen offset:3072 sc1
dr a[112:115],v235 offset:13312
ex2 v153,v153
ex2 v154,v154
cv v152,v152,v153
mm v[176:179],a[68:71],v[112:115],v[176:179]
dr a[116:119],v235 offset:14336
mm v[176:179],a[64:67],v[104:107],v[176:179]
dr a[120:123],v235 offset:15360
mm v[176:179],a[124:127],v[96:99],v[176:179]
ex2 v155,v155
mm v[180:183],a[76:79],v[96:99],v[204:207]
ex2 v156,v156
mm v[180:183],a[80:83],v[104:107],v[180:183]
s_waitcnt lgkmcnt(4)
bl a[60:63],v233,s[8:11],s76 offen offset:3072 sc1
mm v[180:183],a[84:87],v[112:115],v[180:183]
ex2 v157,v157
mm v[180:183],a[88:91],v[120:123],v[180:183]
ex2 v158,v158
mm v[164:167],a[88:91],v[88:91],v[200:203]
)" R"(s_waitcnt vmcnt(18) lgkmcnt(0)
s_barrier
mm v[164:167],a[84:87],v[80:83],v[164:167]
bl v232,s[8:11],s40 offen offset:0 sc1 lds
mm v[164:167],a[80:83],v[72:75],v[164:167]
dr a[124:127],v235 offset:16384
cv v153,v154,v155
mm v[164:167],a[76:79],v[64:67],v[164:167]
dr a[64:67],v235 offset:24576
cv v154,v156,v157
mm v[168:171],a[92:95],v[64:67],v[200:203]
ex2 v159,v159
mm v[168:171],a[96:99],v[72:75],v[168:171]
bl v232,s[8:11],s40 offen offset:1024 sc1 lds
mm v[168:171],a[100:103],v[80:83],v[168:171]
dr a[68:71],v235 offset:17408
cv v155,v158,v159
mm v[168:171],a[104:107],v[88:91],v[168:171]
dr a[72:75],v235 offset:25600
mm v[184:187],a[104:107],v[120:123],v[204:207]
ex2 v136,v136
mm v[184:187],a[100:103],v[112:115],v[184:187]
bl v232,s[8:11],s40 offen offset:2048 sc1 lds
mm v[184:187],a[96:99],v[104:107],v[184:187]
dr a[76:79],v235 offset:18432
mm v[184:187],a[92:95],v[96:99],v[184:187]
ex2 v137,v137
mm v[188:191],a[108:111],v[96:99],v[204:207]
ex2 v138,v138
mm v[188:191],a[112:115],v[104:107],v[188:191]
bl v232,s[8:11],s40 offen offset:3072 sc1 lds
mm v[188:191],a[116:119],v[112:115],v[188:191]
s_add_u32 s40,s31,8192
ex2 v139,v139
mm v[188:191],a[120:123],v[120:123],v[188:191]
s_add_u32 m0,s33,147456
ex2 v140,v140
mm v[172:175],a[120:123],v[88:91],v[200:203]
ex2 v141,v141
mm v[172:175],a[116:119],v[80:83],v[172:175]
ex2 v142,v142
mm v[172:175],a[112:115],v[72:75],v[172:175]
ex2 v143,v143
mm v[172:175],a[108:111],v[64:67],v[172:175]
s_waitcnt lgkmcnt(0)
dr a[80:83],v235 offset:26624
cv v136,v136,v137
bl a[148:151],v232,s[8:11],s77 offen offset:0 sc1
mm v[68:71],a[124:127],v[148:151],v[68:71]
dr a[84:87],v235 offset:19456
cv v137,v138,v139
ex2 v134,v134
ex2 v135,v135
ex2 v132,v132
ex2 v133,v133
ex2 v128,v128
ex2 v129,v129
ex2 v130,v130
ex2 v131,v131
cv v138,v140,v141
cv v139,v142,v143
mm v[68:71],a[64:67],v[152:155],v[68:71]
dr a[88:91],v235 offset:27648
cv v135,v134,v135
)" R"(cv v134,v132,v133
cv v132,v128,v129
cv v133,v130,v131
mm v[4:7],a[64:67],v[136:139],v[4:7]
dr a[92:95],v235 offset:20480
s_waitcnt vmcnt(20)
v_cmp_ne_u64 vcc,s[54:55],v[240:241]
s_cbranch_vccnz FO_S
FO_D:
mm v[4:7],a[124:127],v[132:135],v[4:7]
dr a[96:99],v235 offset:28672
mm v[12:15],a[68:71],v[132:135],v[12:15]
dr a[100:103],v235 offset:21504
mm v[12:15],a[72:75],v[136:139],v[12:15]
dr a[104:107],v235 offset:29696
bl a[128:131],v232,s[8:11],s70 offen sc1
mm v[76:79],a[72:75],v[152:155],v[76:79]
dr a[108:111],v235 offset:22528
mm v[76:79],a[68:71],v[148:151],v[76:79]
dr a[112:115],v235 offset:30720
mm v[84:87],a[76:79],v[148:151],v[84:87]
EF_3:
s_cbranch_execz EF_R
EC_3:
s_cbranch_execz EC_R
s_waitcnt lgkmcnt(6)
dr a[116:119],v235 offset:23552
mm v[84:87],a[80:83],v[152:155],v[84:87]
dr a[120:123],v235 offset:31744
bl a[132:135],v233,s[8:11],s70 offen sc1
mm v[20:23],a[80:83],v[136:139],v[20:23]
s_add_u32 s70,s70,8192
ex2 v182,v182
mm v[20:23],a[76:79],v[132:135],v[20:23]
ex2 v183,v183
mm v[28:31],a[84:87],v[132:135],v[28:31]
ex2 v180,v180
mm v[28:31],a[88:91],v[136:139],v[28:31]
bs v[192:195],v239,s[8:11],s79 offen offset:0 sc1
ex2 v181,v181
ex2 v176,v176
ex2 v177,v177
mm v[92:95],a[88:91],v[152:155],v[92:95]
s_waitcnt vmcnt(18) lgkmcnt(0)
s_barrier
mm v[92:95],a[84:87],v[148:151],v[92:95]
dr a[124:127],v232 offset:0
cv v183,v182,v183
v_smfmac_f32_16x16x64_bf16 a[136:139],a[144:147],v[132:139],v246
bl v232,s[8:11],s40 offen offset:0 sc1 lds
v_smfmac_f32_16x16x64_bf16 a[140:143],a[144:147],v[148:155],v246
dr a[64:67],v232 offset:1024
cv v182,v180,v181
mm v[100:103],a[92:95],v[148:151],v[100:103]
dr a[68:71],v232 offset:2048
cv v180,v176,v177
mm v[100:103],a[96:99],v[152:155],v[100:103]
ex2 v178,v178
mm v[36:39],a[96:99],v[136:139],v[36:39]
bl v232,s[8:11],s40 offen offset:1024 sc1 lds
mm v[36:39],a[92:95],v[132:135],v[36:39]
dr a[72:75],v232 offset:3072
mm v[44:47],a[100:103],v[132:135],v[44:47]
dr a[76:79],v232 offset:4096
)" R"(mm v[44:47],a[104:107],v[136:139],v[44:47]
ex2 v179,v179
mm v[108:111],a[104:107],v[152:155],v[108:111]
bl v232,s[8:11],s40 offen offset:2048 sc1 lds
mm v[108:111],a[100:103],v[148:151],v[108:111]
dr a[80:83],v232 offset:5120
cv v181,v178,v179
mm v[116:119],a[108:111],v[148:151],v[116:119]
dr a[84:87],v232 offset:6144
mm v[116:119],a[112:115],v[152:155],v[116:119]
s_mov_b32 s30,s31
ex2 v184,v184
mm v[52:55],a[112:115],v[136:139],v[52:55]
s_add_u32 s31,s31,65536
bl v232,s[8:11],s40 offen offset:3072 sc1 lds
mm v[52:55],a[108:111],v[132:135],v[52:55]
s_cmp_eq_u32 s31,s3
s_cselect_b32 s40,65536,0
dr a[88:91],v232 offset:7168
mm v[60:63],a[116:119],v[132:135],v[60:63]
s_add_u32 s31,s31,s40
dr a[92:95],v232 offset:8192
s_add_u32 m0,s33,0
mm v[60:63],a[120:123],v[136:139],v[60:63]
s_add_u32 s40,s30,40960
ex2 v185,v185
mm v[124:127],a[120:123],v[152:155],v[124:127]
bl a[152:155],v233,s[8:11],s77 offen offset:0 sc1
mm v[124:127],a[116:119],v[148:151],v[124:127]
s_waitcnt lgkmcnt(2)
dr a[96:99],v232 offset:9216
cv v184,v184,v185
mm v[128:131],a[124:127],v[64:67],v[200:203]
dr a[100:103],v232 offset:10240
mm v[128:131],a[64:67],v[72:75],v[128:131]
ex2 v186,v186
mm v[128:131],a[68:71],v[80:83],v[128:131]
dr a[104:107],v232 offset:11264
mm v[128:131],a[72:75],v[88:91],v[128:131]
bl a[156:159],v232,s[8:11],s77 offen offset:1024 sc1
mm v[144:147],a[72:75],v[120:123],v[204:207]
dr a[108:111],v232 offset:12288
mm v[144:147],a[68:71],v[112:115],v[144:147]
dr a[112:115],v232 offset:13312
mm v[144:147],a[64:67],v[104:107],v[144:147]
dr a[116:119],v232 offset:14336
mm v[144:147],a[124:127],v[96:99],v[144:147]
dr a[120:123],v232 offset:15360
mm v[148:151],a[76:79],v[96:99],v[204:207]
ex2 v187,v187
mm v[148:151],a[80:83],v[104:107],v[148:151]
ex2 v188,v188
mm v[148:151],a[84:87],v[112:115],v[148:151]
s_waitcnt lgkmcnt(4)
ex2 v189,v189
mm v[148:151],a[88:91],v[120:123],v[148:151]
ex2 v190,v190
mm v[132:135],a[88:91],v[88:91],v[200:203]
s_waitcnt vmcnt(18) lgkmcnt(0)
UMID:
s_barrier
mm v[132:135],a[84:87],v[80:83],v[132:135]
bl v232,s[8:11],s31 offen offset:0 sc1 lds
mm v[132:135],a[80:83],v[72:75],v[132:135]
)" R"(dr a[124:127],v232 offset:49152
cv v185,v186,v187
mm v[132:135],a[76:79],v[64:67],v[132:135]
dr a[64:67],v232 offset:57344
cv v186,v188,v189
mm v[136:139],a[92:95],v[64:67],v[200:203]
ex2 v191,v191
mm v[136:139],a[96:99],v[72:75],v[136:139]
bl v232,s[8:11],s31 offen offset:1024 sc1 lds
mm v[136:139],a[100:103],v[80:83],v[136:139]
dr a[68:71],v232 offset:50176
cv v187,v190,v191
mm v[136:139],a[104:107],v[88:91],v[136:139]
dr a[72:75],v232 offset:58368
mm v[152:155],a[104:107],v[120:123],v[204:207]
ex2 v168,v168
mm v[152:155],a[100:103],v[112:115],v[152:155]
bl v232,s[8:11],s31 offen offset:2048 sc1 lds
mm v[152:155],a[96:99],v[104:107],v[152:155]
dr a[76:79],v232 offset:51200
mm v[152:155],a[92:95],v[96:99],v[152:155]
ex2 v169,v169
mm v[156:159],a[108:111],v[96:99],v[204:207]
ex2 v170,v170
mm v[156:159],a[112:115],v[104:107],v[156:159]
bl v232,s[8:11],s31 offen offset:3072 sc1 lds
mm v[156:159],a[116:119],v[112:115],v[156:159]
s_add_u32 m0,s33,49152
ex2 v171,v171
mm v[156:159],a[120:123],v[120:123],v[156:159]
ex2 v172,v172
mm v[140:143],a[120:123],v[88:91],v[200:203]
ex2 v173,v173
mm v[140:143],a[116:119],v[80:83],v[140:143]
ex2 v174,v174
mm v[140:143],a[112:115],v[72:75],v[140:143]
ex2 v175,v175
mm v[140:143],a[108:111],v[64:67],v[140:143]
s_waitcnt lgkmcnt(0)
dr a[80:83],v232 offset:59392
cv v168,v168,v169
bl a[160:163],v233,s[8:11],s77 offen offset:1024 sc1
mm v[68:71],a[124:127],v[180:183],v[68:71]
dr a[84:87],v232 offset:52224
cv v169,v170,v171
ex2 v166,v166
ex2 v167,v167
ex2 v164,v164
ex2 v165,v165
ex2 v160,v160
ex2 v161,v161
ex2 v162,v162
ex2 v163,v163
cv v170,v172,v173
cv v171,v174,v175
mm v[68:71],a[64:67],v[184:187],v[68:71]
dr a[88:91],v232 offset:60416
cv v167,v166,v167
cv v166,v164,v165
cv v164,v160,v161
cv v165,v162,v163
mm v[4:7],a[64:67],v[168:171],v[4:7]
dr a[92:95],v232 offset:53248
mm v[4:7],a[124:127],v[164:167],v[4:7]
)" R"(dr a[96:99],v232 offset:61440
bl a[164:167],v232,s[8:11],s77 offen offset:2048 sc1
mm v[12:15],a[68:71],v[164:167],v[12:15]
dr a[100:103],v232 offset:54272
mm v[12:15],a[72:75],v[168:171],v[12:15]
dr a[104:107],v232 offset:62464
mm v[76:79],a[72:75],v[184:187],v[76:79]
dr a[108:111],v232 offset:55296
mm v[76:79],a[68:71],v[180:183],v[76:79]
dr a[112:115],v232 offset:63488
bl a[248:251],v232,s[8:11],s70 offen sc1
mm v[84:87],a[76:79],v[180:183],v[84:87]
s_waitcnt lgkmcnt(6)
dr a[116:119],v232 offset:56320
mm v[84:87],a[80:83],v[184:187],v[84:87]
dr a[120:123],v232 offset:64512
mm v[20:23],a[80:83],v[168:171],v[20:23]
ex2 v150,v150
mm v[20:23],a[76:79],v[164:167],v[20:23]
bl a[252:255],v233,s[8:11],s70 offen sc1
mm v[28:31],a[84:87],v[164:167],v[28:31]
s_add_u32 s70,s70,8192
ex2 v151,v151
mm v[28:31],a[88:91],v[168:171],v[28:31]
ex2 v148,v148
mm v[92:95],a[88:91],v[184:187],v[92:95]
s_waitcnt vmcnt(18) lgkmcnt(0)
s_barrier
mm v[92:95],a[84:87],v[180:183],v[92:95]
bl v232,s[8:11],s40 offen offset:0 sc1 lds
v_smfmac_f32_16x16x64_bf16 a[136:139],a[144:147],v[164:171],v246
dr a[124:127],v235 offset:0
cv v151,v150,v151
v_smfmac_f32_16x16x64_bf16 a[140:143],a[144:147],v[180:187],v246
dr a[64:67],v235 offset:1024
mm v[100:103],a[92:95],v[180:183],v[100:103]
ex2 v149,v149
mm v[100:103],a[96:99],v[184:187],v[100:103]
bl v232,s[8:11],s40 offen offset:1024 sc1 lds
mm v[36:39],a[96:99],v[168:171],v[36:39]
dr a[68:71],v235 offset:2048
cv v150,v148,v149
mm v[36:39],a[92:95],v[164:167],v[36:39]
dr a[72:75],v235 offset:3072
mm v[44:47],a[100:103],v[164:167],v[44:47]
ex2 v144,v144
mm v[44:47],a[104:107],v[168:171],v[44:47]
bl v232,s[8:11],s40 offen offset:2048 sc1 lds
mm v[108:111],a[104:107],v[184:187],v[108:111]
dr a[76:79],v235 offset:4096
mm v[108:111],a[100:103],v[180:183],v[108:111]
dr a[80:83],v235 offset:5120
mm v[116:119],a[108:111],v[180:183],v[116:119]
ex2 v145,v145
mm v[116:119],a[112:115],v[184:187],v[116:119]
bl v232,s[8:11],s40 offen offset:3072 sc1 lds
mm v[52:55],a[112:115],v[168:171],v[52:55]
s_add_u32 s40,s31,32768
dr a[84:87],v235 offset:6144
s_add_u32 m0,s33,131072
mm v[52:55],a[108:111],v[164:167],v[52:55]
dr a[88:91],v235 offset:7168
cv v148,v144,v145
mm v[60:63],a[116:119],v[164:167],v[60:63]
)" R"(ex2 v146,v146
mm v[60:63],a[120:123],v[168:171],v[60:63]
bl a[168:171],v233,s[8:11],s77 offen offset:2048 sc1
mm v[124:127],a[120:123],v[184:187],v[124:127]
dr a[92:95],v235 offset:8192
mm v[124:127],a[116:119],v[180:183],v[124:127]
s_waitcnt lgkmcnt(3)
dr a[96:99],v235 offset:9216
mm v[160:163],a[124:127],v[64:67],v[200:203]
ex2 v147,v147
mm v[160:163],a[64:67],v[72:75],v[160:163]
dr a[100:103],v235 offset:10240
cv v149,v146,v147
mm v[160:163],a[68:71],v[80:83],v[160:163]
dr a[104:107],v235 offset:11264
mm v[160:163],a[72:75],v[88:91],v[160:163]
dr a[108:111],v235 offset:12288
mm v[176:179],a[72:75],v[120:123],v[204:207]
dr a[112:115],v235 offset:13312
mm v[176:179],a[68:71],v[112:115],v[176:179]
bl a[172:175],v232,s[8:11],s77 offen offset:3072 sc1
mm v[176:179],a[64:67],v[104:107],v[176:179]
dr a[116:119],v235 offset:14336
mm v[176:179],a[124:127],v[96:99],v[176:179]
dr a[120:123],v235 offset:15360
mm v[180:183],a[76:79],v[96:99],v[204:207]
ex2 v152,v152
mm v[180:183],a[80:83],v[104:107],v[180:183]
s_waitcnt lgkmcnt(4)
bl a[180:183],v232,s[8:11],s70 offen sc1
mm v[180:183],a[84:87],v[112:115],v[180:183]
ex2 v153,v153
mm v[180:183],a[88:91],v[120:123],v[180:183]
ex2 v154,v154
mm v[164:167],a[88:91],v[88:91],v[200:203]
s_waitcnt vmcnt(17) lgkmcnt(0)
s_barrier
mm v[164:167],a[84:87],v[80:83],v[164:167]
bl v232,s[8:11],s40 offen offset:0 sc1 lds
mm v[164:167],a[80:83],v[72:75],v[164:167]
dr a[124:127],v235 offset:16384
cv v152,v152,v153
mm v[164:167],a[76:79],v[64:67],v[164:167]
dr a[64:67],v235 offset:24576
mm v[168:171],a[92:95],v[64:67],v[200:203]
ex2 v155,v155
mm v[168:171],a[96:99],v[72:75],v[168:171]
bl v232,s[8:11],s40 offen offset:1024 sc1 lds
mm v[168:171],a[100:103],v[80:83],v[168:171]
dr a[68:71],v235 offset:17408
cv v153,v154,v155
mm v[168:171],a[104:107],v[88:91],v[168:171]
dr a[72:75],v235 offset:25600
mm v[184:187],a[104:107],v[120:123],v[204:207]
ex2 v156,v156
mm v[184:187],a[100:103],v[112:115],v[184:187]
bl v232,s[8:11],s40 offen offset:2048 sc1 lds
mm v[184:187],a[96:99],v[104:107],v[184:187]
ex2 v157,v157
mm v[184:187],a[92:95],v[96:99],v[184:187]
ex2 v158,v158
mm v[188:191],a[108:111],v[96:99],v[204:207]
ex2 v159,v159
mm v[188:191],a[112:115],v[104:107],v[188:191]
)" R"(bl v232,s[8:11],s40 offen offset:3072 sc1 lds
mm v[188:191],a[116:119],v[112:115],v[188:191]
s_add_u32 s40,s31,8192
dr a[76:79],v235 offset:18432
cv v154,v156,v157
mm v[188:191],a[120:123],v[120:123],v[188:191]
s_add_u32 m0,s33,147456
ex2 v136,v136
mm v[172:175],a[120:123],v[88:91],v[200:203]
ex2 v137,v137
mm v[172:175],a[116:119],v[80:83],v[172:175]
dr a[80:83],v235 offset:26624
cv v155,v158,v159
mm v[172:175],a[112:115],v[72:75],v[172:175]
ex2 v138,v138
mm v[172:175],a[108:111],v[64:67],v[172:175]
s_waitcnt lgkmcnt(2)
ex2 v139,v139
bl a[176:179],v233,s[8:11],s77 offen offset:3072 sc1
ex2 v140,v140
ex2 v141,v141
ex2 v142,v142
ex2 v143,v143
mm v[68:71],a[124:127],v[148:151],v[68:71]
dr a[84:87],v235 offset:19456
cv v136,v136,v137
ex2 v134,v134
ex2 v135,v135
ex2 v132,v132
ex2 v133,v133
ex2 v128,v128
ex2 v129,v129
ex2 v130,v130
ex2 v131,v131
cv v137,v138,v139
cv v138,v140,v141
cv v139,v142,v143
mm v[68:71],a[64:67],v[152:155],v[68:71]
dr a[88:91],v235 offset:27648
cv v135,v134,v135
cv v134,v132,v133
cv v132,v128,v129
cv v133,v130,v131
mm v[4:7],a[64:67],v[136:139],v[4:7]
dr a[92:95],v235 offset:20480
mm v[4:7],a[124:127],v[132:135],v[4:7]
dr a[96:99],v235 offset:28672
bl a[184:187],v233,s[8:11],s70 offen sc1
mm v[12:15],a[68:71],v[132:135],v[12:15]
s_add_u32 s70,s70,8192
dr a[100:103],v235 offset:21504
mm v[12:15],a[72:75],v[136:139],v[12:15]
dr a[104:107],v235 offset:29696
mm v[76:79],a[72:75],v[152:155],v[76:79]
dr a[108:111],v235 offset:22528
mm v[76:79],a[68:71],v[148:151],v[76:79]
s_waitcnt lgkmcnt(6)
dr a[112:115],v235 offset:30720
bl a[188:191],v232,s[8:11],s70 offen sc1
mm v[84:87],a[76:79],v[148:151],v[84:87]
dr a[116:119],v235 offset:23552
mm v[84:87],a[80:83],v[152:155],v[84:87]
dr a[120:123],v235 offset:31744
mm v[20:23],a[80:83],v[136:139],v[20:23]
)" R"(ex2 v182,v182
mm v[20:23],a[76:79],v[132:135],v[20:23]
bl a[192:195],v233,s[8:11],s70 offen sc1
mm v[28:31],a[84:87],v[132:135],v[28:31]
s_waitcnt lgkmcnt(4)
ex2 v183,v183
mm v[28:31],a[88:91],v[136:139],v[28:31]
s_add_u32 s70,s70,8192
ex2 v180,v180
mm v[92:95],a[88:91],v[152:155],v[92:95]
s_waitcnt vmcnt(19) lgkmcnt(0)
s_barrier
mm v[92:95],a[84:87],v[148:151],v[92:95]
bl v232,s[8:11],s40 offen offset:0 sc1 lds
v_smfmac_f32_16x16x64_bf16 a[136:139],a[144:147],v[132:139],v246
dr a[124:127],v232 offset:0
cv v183,v182,v183
v_smfmac_f32_16x16x64_bf16 a[140:143],a[144:147],v[148:155],v246
dr a[64:67],v232 offset:1024
mm v[100:103],a[92:95],v[148:151],v[100:103]
ex2 v181,v181
mm v[100:103],a[96:99],v[152:155],v[100:103]
bl v232,s[8:11],s40 offen offset:1024 sc1 lds
mm v[36:39],a[96:99],v[136:139],v[36:39]
dr a[68:71],v232 offset:2048
cv v182,v180,v181
mm v[36:39],a[92:95],v[132:135],v[36:39]
dr a[72:75],v232 offset:3072
mm v[44:47],a[100:103],v[132:135],v[44:47]
ex2 v176,v176
mm v[44:47],a[104:107],v[136:139],v[44:47]
bl v232,s[8:11],s40 offen offset:2048 sc1 lds
mm v[108:111],a[104:107],v[152:155],v[108:111]
dr a[76:79],v232 offset:4096
mm v[108:111],a[100:103],v[148:151],v[108:111]
dr a[80:83],v232 offset:5120
mm v[116:119],a[108:111],v[148:151],v[116:119]
ex2 v177,v177
mm v[116:119],a[112:115],v[152:155],v[116:119]
s_mov_b32 s30,s31
bl v232,s[8:11],s40 offen offset:3072 sc1 lds
mm v[52:55],a[112:115],v[136:139],v[52:55]
s_add_u32 s31,s31,65536
dr a[84:87],v232 offset:6144
s_add_u32 m0,s33,0
mm v[52:55],a[108:111],v[132:135],v[52:55]
s_cmp_eq_u32 s31,s3
s_cselect_b32 s40,65536,0
dr a[88:91],v232 offset:7168
mm v[60:63],a[116:119],v[132:135],v[60:63]
s_add_u32 s31,s31,s40
ex2 v178,v178
mm v[60:63],a[120:123],v[136:139],v[60:63]
bs v[192:195],v239,s[8:11],s80 offen offset:0 sc1
dr a[92:95],v232 offset:8192
ex2 v179,v179
cv v180,v176,v177
cv v181,v178,v179
mm v[124:127],a[120:123],v[152:155],v[124:127]
dr a[96:99],v232 offset:9216
mm v[124:127],a[116:119],v[148:151],v[124:127]
s_waitcnt lgkmcnt(4)
ex2 v184,v184
mm v[128:131],a[124:127],v[64:67],v[200:203]
)" R"(s_bfe_i64 s[48:49],s[36:37],0x10000
dr a[100:103],v232 offset:10240
mm v[128:131],a[64:67],v[72:75],v[128:131]
dr a[104:107],v232 offset:11264
mm v[128:131],a[68:71],v[80:83],v[128:131]
ex2 v185,v185
mm v[128:131],a[72:75],v[88:91],v[128:131]
s_bfe_i64 s[50:51],s[36:37],0x10004
dr a[108:111],v232 offset:12288
cv v184,v184,v185
mm v[144:147],a[72:75],v[120:123],v[204:207]
s_add_u32 s43,s43,8192
dr a[112:115],v232 offset:13312
mm v[144:147],a[68:71],v[112:115],v[144:147]
dr a[116:119],v232 offset:14336
mm v[144:147],a[64:67],v[104:107],v[144:147]
dr a[120:123],v232 offset:15360
mm v[144:147],a[124:127],v[96:99],v[144:147]
s_and_b64 s[52:53],s[12:13],s[48:49]
ex2 v186,v186
mm v[148:151],a[76:79],v[96:99],v[204:207]
ex2 v187,v187
mm v[148:151],a[80:83],v[104:107],v[148:151]
s_waitcnt lgkmcnt(4)
ex2 v188,v188
mm v[148:151],a[84:87],v[112:115],v[148:151]
s_or_b64 s[52:53],s[52:53],s[50:51]
ex2 v189,v189
mm v[148:151],a[88:91],v[120:123],v[148:151]
s_add_u32 s40,s30,40960
ex2 v190,v190
mm v[132:135],a[88:91],v[88:91],v[200:203]
s_waitcnt vmcnt(16) lgkmcnt(0)
s_barrier
mm v[132:135],a[84:87],v[80:83],v[132:135]
bl v232,s[8:11],s31 offen offset:0 sc1 lds
mm v[132:135],a[80:83],v[72:75],v[132:135]
dr a[124:127],v232 offset:49152
cv v185,v186,v187
mm v[132:135],a[76:79],v[64:67],v[132:135]
dr a[64:67],v232 offset:57344
cv v186,v188,v189
mm v[136:139],a[92:95],v[64:67],v[200:203]
ex2 v191,v191
mm v[136:139],a[96:99],v[72:75],v[136:139]
s_and_b64 s[86:87],s[14:15],s[48:49]
bl v232,s[8:11],s31 offen offset:1024 sc1 lds
mm v[136:139],a[100:103],v[80:83],v[136:139]
dr a[68:71],v232 offset:50176
cv v187,v190,v191
mm v[136:139],a[104:107],v[88:91],v[136:139]
dr a[72:75],v232 offset:58368
v_cndmask_b32_e64 v208,v236,v200,s[52:53]
mm v[152:155],a[104:107],v[120:123],v[204:207]
ex2 v168,v168
mm v[152:155],a[100:103],v[112:115],v[152:155]
s_or_b64 s[86:87],s[86:87],s[50:51]
bl v232,s[8:11],s31 offen offset:2048 sc1 lds
mm v[152:155],a[96:99],v[104:107],v[152:155]
dr a[76:79],v232 offset:51200
mm v[152:155],a[92:95],v[96:99],v[152:155]
ex2 v169,v169
mm v[156:159],a[108:111],v[96:99],v[204:207]
ex2 v170,v170
)" R"(mm v[156:159],a[112:115],v[104:107],v[156:159]
s_add_u32 s41,s35,90112
bl v232,s[8:11],s31 offen offset:3072 sc1 lds
mm v[156:159],a[116:119],v[112:115],v[156:159]
s_add_u32 m0,s33,49152
ex2 v171,v171
mm v[156:159],a[120:123],v[120:123],v[156:159]
ex2 v172,v172
mm v[140:143],a[120:123],v[88:91],v[200:203]
s_and_b64 s[52:53],s[16:17],s[48:49]
ex2 v173,v173
mm v[140:143],a[116:119],v[80:83],v[140:143]
ex2 v174,v174
mm v[140:143],a[112:115],v[72:75],v[140:143]
ex2 v175,v175
mm v[140:143],a[108:111],v[64:67],v[140:143]
s_waitcnt lgkmcnt(0)
dr a[80:83],v232 offset:59392
cv v168,v168,v169
bl a[196:199],v232,s[8:11],s70 offen sc1
mm v[68:71],a[124:127],v[180:183],v[68:71]
s_or_b64 s[52:53],s[52:53],s[50:51]
dr a[84:87],v232 offset:52224
cv v169,v170,v171
ex2 v166,v166
ex2 v167,v167
ex2 v164,v164
ex2 v165,v165
ex2 v160,v160
ex2 v161,v161
ex2 v162,v162
ex2 v163,v163
cv v170,v172,v173
cv v171,v174,v175
mm v[68:71],a[64:67],v[184:187],v[68:71]
dr a[88:91],v232 offset:60416
cv v167,v166,v167
cv v166,v164,v165
cv v164,v160,v161
cv v165,v162,v163
mm v[4:7],a[64:67],v[168:171],v[4:7]
dr a[92:95],v232 offset:53248
v_cndmask_b32_e64 v209,v236,v200,s[86:87]
mm v[4:7],a[124:127],v[164:167],v[4:7]
dr a[96:99],v232 offset:61440
v_cndmask_b32_e64 v210,v236,v200,s[52:53]
s_waitcnt vmcnt(13)
buffer_store_dwordx2 v[252:253],v254,s[8:11],s43 offen sc1
dr a[100:103],v232 offset:54272
ex2 v150,v150
mm v[12:15],a[68:71],v[164:167],v[12:15]
bl a[200:203],v233,s[8:11],s70 offen sc1
mm v[12:15],a[72:75],v[168:171],v[12:15]
s_add_u32 s70,s70,8192
dr a[104:107],v232 offset:62464
mm v[76:79],a[72:75],v[184:187],v[76:79]
dr a[108:111],v232 offset:55296
mm v[76:79],a[68:71],v[180:183],v[76:79]
s_and_b64 s[86:87],s[18:19],s[48:49]
dr a[112:115],v232 offset:63488
mm v[84:87],a[76:79],v[180:183],v[84:87]
s_waitcnt lgkmcnt(6)
dr a[116:119],v232 offset:56320
mm v[84:87],a[80:83],v[184:187],v[84:87]
)" R"(dr a[120:123],v232 offset:64512
mm v[20:23],a[80:83],v[168:171],v[20:23]
s_or_b64 s[86:87],s[86:87],s[50:51]
ex2 v151,v151
mm v[20:23],a[76:79],v[164:167],v[20:23]
s_lshl_b32 s73,s56,3
ex2 v148,v148
mm v[28:31],a[84:87],v[164:167],v[28:31]
s_add_u32 s73,s73,0x2000
ex2 v149,v149
mm v[28:31],a[88:91],v[168:171],v[28:31]
s_and_b32 s73,s73,-32
ex2 v144,v144
mm v[92:95],a[88:91],v[184:187],v[92:95]
s_lshl_b32 s73,s73,0
ex2 v145,v145
s_waitcnt lgkmcnt(0)
s_barrier
mm v[92:95],a[84:87],v[180:183],v[92:95]
s_add_u32 s73,s73,s68
bl v232,s[8:11],s40 offen offset:0 sc1 lds
v_smfmac_f32_16x16x64_bf16 a[136:139],a[144:147],v[164:171],v246
s_lshl_b32 s101,s24,3
dr a[124:127],v235 offset:0
v_cndmask_b32_e64 v211,v236,v200,s[86:87]
v_smfmac_f32_16x16x64_bf16 a[140:143],a[144:147],v[180:187],v246
s_add_u32 s73,s73,s101
dr a[64:67],v235 offset:1024
cv v151,v150,v151
mm v[100:103],a[92:95],v[180:183],v[100:103]
s_bfe_i64 s[48:49],s[36:37],0x10001
ex2 v146,v146
mm v[100:103],a[96:99],v[184:187],v[100:103]
bl v232,s[8:11],s40 offen offset:1024 sc1 lds
mm v[36:39],a[96:99],v[168:171],v[36:39]
dr a[68:71],v235 offset:2048
cv v150,v148,v149
mm v[36:39],a[92:95],v[164:167],v[36:39]
ED_0:
s_cbranch_execz ED_R
s_bfe_i64 s[50:51],s[36:37],0x10005
dr a[72:75],v235 offset:3072
cv v148,v144,v145
mm v[44:47],a[100:103],v[164:167],v[44:47]
ex2 v147,v147
mm v[44:47],a[104:107],v[168:171],v[44:47]
bl v232,s[8:11],s40 offen offset:2048 sc1 lds
mm v[108:111],a[104:107],v[184:187],v[108:111]
dr a[76:79],v235 offset:4096
cv v149,v146,v147
mm v[108:111],a[100:103],v[180:183],v[108:111]
s_and_b64 s[52:53],s[12:13],s[48:49]
dr a[80:83],v235 offset:5120
mm v[116:119],a[108:111],v[180:183],v[116:119]
ex2 v152,v152
mm v[116:119],a[112:115],v[184:187],v[116:119]
bl v232,s[8:11],s40 offen offset:3072 sc1 lds
mm v[52:55],a[112:115],v[168:171],v[52:55]
s_add_u32 m0,s34,122880
dr a[84:87],v235 offset:6144
s_add_u32 s40,s31,32768
mm v[52:55],a[108:111],v[164:167],v[52:55]
s_or_b64 s[52:53],s[52:53],s[50:51]
dr a[88:91],v235 offset:7168
)" R"(mm v[60:63],a[116:119],v[164:167],v[60:63]
ex2 v153,v153
mm v[60:63],a[120:123],v[168:171],v[60:63]
bl a[204:207],v232,s[8:11],s70 offen sc1
mm v[124:127],a[120:123],v[184:187],v[124:127]
dr a[92:95],v235 offset:8192
v_cndmask_b32_e64 v212,v236,v200,s[52:53]
mm v[124:127],a[116:119],v[180:183],v[124:127]
s_waitcnt lgkmcnt(3)
dr a[96:99],v235 offset:9216
cv v152,v152,v153
mm v[160:163],a[124:127],v[64:67],v[200:203]
ex2 v154,v154
mm v[160:163],a[64:67],v[72:75],v[160:163]
s_and_b64 s[86:87],s[14:15],s[48:49]
dr a[100:103],v235 offset:10240
mm v[160:163],a[68:71],v[80:83],v[160:163]
dr a[104:107],v235 offset:11264
mm v[160:163],a[72:75],v[88:91],v[160:163]
bl a[208:211],v233,s[8:11],s70 offen sc1
mm v[176:179],a[72:75],v[120:123],v[204:207]
s_add_u32 s70,s70,8192
dr a[108:111],v235 offset:12288
mm v[176:179],a[68:71],v[112:115],v[176:179]
s_or_b64 s[86:87],s[86:87],s[50:51]
dr a[112:115],v235 offset:13312
mm v[176:179],a[64:67],v[104:107],v[176:179]
dr a[116:119],v235 offset:14336
mm v[176:179],a[124:127],v[96:99],v[176:179]
dr a[120:123],v235 offset:15360
bl v232,s[8:11],s41 offen offset:0 sc1 lds
mm v[180:183],a[76:79],v[96:99],v[204:207]
s_add_u32 s41,s35,90112
ex2 v155,v155
mm v[180:183],a[80:83],v[104:107],v[180:183]
s_waitcnt lgkmcnt(4)
s_add_u32 m0,s34,122880
cv v153,v154,v155
mm v[180:183],a[84:87],v[112:115],v[180:183]
ex2 v156,v156
mm v[180:183],a[88:91],v[120:123],v[180:183]
bl v232,s[8:11],s41 offen offset:1024 sc1 lds
mm v[164:167],a[88:91],v[88:91],v[200:203]
s_add_u32 m0,s33,131072
ex2 v157,v157
s_waitcnt vmcnt(16) lgkmcnt(0)
s_barrier
mm v[164:167],a[84:87],v[80:83],v[164:167]
s_and_b64 s[52:53],s[16:17],s[48:49]
dr a[124:127],v235 offset:16384
cv v154,v156,v157
mm v[164:167],a[80:83],v[72:75],v[164:167]
s_and_b32 s46,s43,-32
dr a[64:67],v235 offset:24576
v_cndmask_b32_e64 v213,v236,v200,s[86:87]
mm v[164:167],a[76:79],v[64:67],v[164:167]
bl v232,s[8:11],s40 offen offset:0 sc1 lds
mm v[168:171],a[92:95],v[64:67],v[200:203]
s_or_b64 s[52:53],s[52:53],s[50:51]
dr a[68:71],v235 offset:17408
v_lshrrev_b32 v242,1,v232
mm v[168:171],a[96:99],v[72:75],v[168:171]
dr a[72:75],v235 offset:25600
v_and_b32 v242,24,v242
)" R"(mm v[168:171],a[100:103],v[80:83],v[168:171]
ex2 v158,v158
mm v[168:171],a[104:107],v[88:91],v[168:171]
bl v232,s[8:11],s40 offen offset:1024 sc1 lds
mm v[184:187],a[104:107],v[120:123],v[204:207]
ex2 v159,v159
mm v[184:187],a[100:103],v[112:115],v[184:187]
dr a[76:79],v235 offset:18432
cv v155,v158,v159
mm v[184:187],a[96:99],v[104:107],v[184:187]
ex2 v136,v136
mm v[184:187],a[92:95],v[96:99],v[184:187]
s_and_b64 s[86:87],s[18:19],s[48:49]
bl v232,s[8:11],s40 offen offset:2048 sc1 lds
mm v[188:191],a[108:111],v[96:99],v[204:207]
ex2 v137,v137
mm v[188:191],a[112:115],v[104:107],v[188:191]
ex2 v138,v138
mm v[188:191],a[116:119],v[112:115],v[188:191]
s_or_b64 s[86:87],s[86:87],s[50:51]
ex2 v139,v139
mm v[188:191],a[120:123],v[120:123],v[188:191]
bl v232,s[8:11],s40 offen offset:3072 sc1 lds
mm v[172:175],a[120:123],v[88:91],v[200:203]
s_add_u32 s40,s31,8192
ex2 v140,v140
mm v[172:175],a[116:119],v[80:83],v[172:175]
s_add_u32 m0,s33,147456
ex2 v141,v141
mm v[172:175],a[112:115],v[72:75],v[172:175]
ex2 v142,v142
mm v[172:175],a[108:111],v[64:67],v[172:175]
s_waitcnt lgkmcnt(0)
ex2 v143,v143
dr a[80:83],v235 offset:26624
s_waitcnt vmcnt(19)
buffer_store_dwordx2 v[252:253],v254,s[8:11],s73 offen sc1
ex2 v134,v134
cv v136,v136,v137
mm v[68:71],a[124:127],v[148:151],v[68:71]
dr a[84:87],v235 offset:19456
cv v137,v138,v139
ex2 v135,v135
ex2 v132,v132
ex2 v133,v133
ex2 v128,v128
ex2 v129,v129
ex2 v130,v130
ex2 v131,v131
cv v138,v140,v141
cv v139,v142,v143
mm v[68:71],a[64:67],v[152:155],v[68:71]
dr a[88:91],v235 offset:27648
cv v135,v134,v135
cv v134,v132,v133
cv v132,v128,v129
cv v133,v130,v131
mm v[4:7],a[64:67],v[136:139],v[4:7]
s_bfe_i64 s[48:49],s[36:37],0x10002
dr a[92:95],v235 offset:20480
v_cndmask_b32_e64 v214,v236,v200,s[52:53]
mm v[4:7],a[124:127],v[132:135],v[4:7]
dr a[96:99],v235 offset:28672
v_cndmask_b32_e64 v215,v236,v200,s[86:87]
)" R"(mm v[12:15],a[68:71],v[132:135],v[12:15]
dr a[100:103],v235 offset:21504
mm v[12:15],a[72:75],v[136:139],v[12:15]
s_bfe_i64 s[50:51],s[36:37],0x10006
dr a[104:107],v235 offset:29696
bl a[216:219],v232,s[8:11],s70 offen sc1
mm v[76:79],a[72:75],v[152:155],v[76:79]
dr a[108:111],v235 offset:22528
mm v[76:79],a[68:71],v[148:151],v[76:79]
dr a[112:115],v235 offset:30720
mm v[84:87],a[76:79],v[148:151],v[84:87]
s_waitcnt lgkmcnt(6)
dr a[116:119],v235 offset:23552
mm v[84:87],a[80:83],v[152:155],v[84:87]
s_and_b64 s[52:53],s[12:13],s[48:49]
dr a[120:123],v235 offset:31744
bl a[220:223],v233,s[8:11],s70 offen sc1
mm v[20:23],a[80:83],v[136:139],v[20:23]
s_add_u32 s70,s70,8192
ex2 v182,v182
mm v[20:23],a[76:79],v[132:135],v[20:23]
ex2 v183,v183
mm v[28:31],a[84:87],v[132:135],v[28:31]
s_or_b64 s[52:53],s[52:53],s[50:51]
ex2 v180,v180
mm v[28:31],a[88:91],v[136:139],v[28:31]
ex2 v181,v181
mm v[92:95],a[88:91],v[152:155],v[92:95]
s_waitcnt vmcnt(18) lgkmcnt(0)
s_barrier
mm v[92:95],a[84:87],v[148:151],v[92:95]
bl v232,s[8:11],s40 offen offset:0 sc1 lds
v_smfmac_f32_16x16x64_bf16 a[136:139],a[144:147],v[132:139],v246
dr a[124:127],v232 offset:0
v_cndmask_b32_e64 v216,v236,v200,s[52:53]
v_smfmac_f32_16x16x64_bf16 a[140:143],a[144:147],v[148:155],v246
dr a[64:67],v232 offset:1024
cv v183,v182,v183
mm v[100:103],a[92:95],v[148:151],v[100:103]
ex2 v176,v176
mm v[100:103],a[96:99],v[152:155],v[100:103]
s_and_b64 s[86:87],s[14:15],s[48:49]
bl v232,s[8:11],s40 offen offset:1024 sc1 lds
mm v[36:39],a[96:99],v[136:139],v[36:39]
dr a[68:71],v232 offset:2048
cv v182,v180,v181
mm v[36:39],a[92:95],v[132:135],v[36:39]
dr a[72:75],v232 offset:3072
mm v[44:47],a[100:103],v[132:135],v[44:47]
ex2 v177,v177
mm v[44:47],a[104:107],v[136:139],v[44:47]
s_or_b64 s[86:87],s[86:87],s[50:51]
bl v232,s[8:11],s40 offen offset:2048 sc1 lds
mm v[108:111],a[104:107],v[152:155],v[108:111]
dr a[76:79],v232 offset:4096
cv v180,v176,v177
mm v[108:111],a[100:103],v[148:151],v[108:111]
dr a[80:83],v232 offset:5120
mm v[116:119],a[108:111],v[148:151],v[116:119]
ex2 v178,v178
mm v[116:119],a[112:115],v[152:155],v[116:119]
s_mov_b32 s30,s31
bl v232,s[8:11],s40 offen offset:3072 sc1 lds
mm v[52:55],a[112:115],v[136:139],v[52:55]
)" R"(s_add_u32 s31,s31,65536
dr a[84:87],v232 offset:6144
s_add_u32 m0,s33,0
mm v[52:55],a[108:111],v[132:135],v[52:55]
s_cmp_eq_u32 s31,s3
s_cselect_b32 s40,65536,0
dr a[88:91],v232 offset:7168
mm v[60:63],a[116:119],v[132:135],v[60:63]
s_add_u32 s31,s31,s40
ex2 v179,v179
mm v[60:63],a[120:123],v[136:139],v[60:63]
s_and_b64 s[52:53],s[16:17],s[48:49]
dr a[92:95],v232 offset:8192
v_cndmask_b32_e64 v217,v236,v200,s[86:87]
mm v[124:127],a[120:123],v[152:155],v[124:127]
s_add_u32 s40,s30,40960
dr a[96:99],v232 offset:9216
cv v181,v178,v179
mm v[124:127],a[116:119],v[148:151],v[124:127]
s_waitcnt lgkmcnt(4)
ex2 v184,v184
mm v[128:131],a[124:127],v[64:67],v[200:203]
s_or_b64 s[52:53],s[52:53],s[50:51]
dr a[100:103],v232 offset:10240
mm v[128:131],a[64:67],v[72:75],v[128:131]
buffer_load_dwordx2 v[240:241],v242,s[8:11],s46 offen sc1
mm v[128:131],a[68:71],v[80:83],v[128:131]
dr a[104:107],v232 offset:11264
mm v[128:131],a[72:75],v[88:91],v[128:131]
dr a[108:111],v232 offset:12288
v_cndmask_b32_e64 v218,v236,v200,s[52:53]
mm v[144:147],a[72:75],v[120:123],v[204:207]
dr a[112:115],v232 offset:13312
mm v[144:147],a[68:71],v[112:115],v[144:147]
dr a[116:119],v232 offset:14336
mm v[144:147],a[64:67],v[104:107],v[144:147]
dr a[120:123],v232 offset:15360
mm v[144:147],a[124:127],v[96:99],v[144:147]
s_and_b64 s[86:87],s[18:19],s[48:49]
ex2 v185,v185
mm v[148:151],a[76:79],v[96:99],v[204:207]
ex2 v186,v186
mm v[148:151],a[80:83],v[104:107],v[148:151]
s_waitcnt lgkmcnt(4)
ex2 v187,v187
mm v[148:151],a[84:87],v[112:115],v[148:151]
s_or_b64 s[86:87],s[86:87],s[50:51]
ex2 v188,v188
mm v[148:151],a[88:91],v[120:123],v[148:151]
ex2 v189,v189
mm v[132:135],a[88:91],v[88:91],v[200:203]
s_waitcnt vmcnt(16) lgkmcnt(0)
s_barrier
mm v[132:135],a[84:87],v[80:83],v[132:135]
bl v232,s[8:11],s31 offen offset:0 sc1 lds
mm v[132:135],a[80:83],v[72:75],v[132:135]
dr a[124:127],v232 offset:49152
cv v184,v184,v185
mm v[132:135],a[76:79],v[64:67],v[132:135]
dr a[64:67],v232 offset:57344
cv v185,v186,v187
mm v[136:139],a[92:95],v[64:67],v[200:203]
ex2 v190,v190
mm v[136:139],a[96:99],v[72:75],v[136:139]
)" R"(bl v232,s[8:11],s31 offen offset:1024 sc1 lds
mm v[136:139],a[100:103],v[80:83],v[136:139]
s_bfe_i64 s[48:49],s[36:37],0x10003
dr a[68:71],v232 offset:50176
cv v186,v188,v189
mm v[136:139],a[104:107],v[88:91],v[136:139]
dr a[72:75],v232 offset:58368
v_cndmask_b32_e64 v219,v236,v200,s[86:87]
mm v[152:155],a[104:107],v[120:123],v[204:207]
ex2 v191,v191
mm v[152:155],a[100:103],v[112:115],v[152:155]
s_bfe_i64 s[50:51],s[36:37],0x10007
bl v232,s[8:11],s31 offen offset:2048 sc1 lds
mm v[152:155],a[96:99],v[104:107],v[152:155]
dr a[76:79],v232 offset:51200
cv v187,v190,v191
mm v[152:155],a[92:95],v[96:99],v[152:155]
ex2 v168,v168
mm v[156:159],a[108:111],v[96:99],v[204:207]
ex2 v169,v169
mm v[156:159],a[112:115],v[104:107],v[156:159]
s_and_b64 s[52:53],s[12:13],s[48:49]
bl v232,s[8:11],s31 offen offset:3072 sc1 lds
mm v[156:159],a[116:119],v[112:115],v[156:159]
s_add_u32 m0,s33,49152
ex2 v170,v170
mm v[156:159],a[120:123],v[120:123],v[156:159]
ex2 v171,v171
mm v[140:143],a[120:123],v[88:91],v[200:203]
s_or_b64 s[52:53],s[52:53],s[50:51]
ex2 v172,v172
mm v[140:143],a[116:119],v[80:83],v[140:143]
ex2 v173,v173
mm v[140:143],a[112:115],v[72:75],v[140:143]
ex2 v174,v174
mm v[140:143],a[108:111],v[64:67],v[140:143]
s_waitcnt lgkmcnt(0)
ex2 v175,v175
dr a[80:83],v232 offset:59392
bl a[224:227],v232,s[8:11],s70 offen sc1
mm v[68:71],a[124:127],v[180:183],v[68:71]
dr a[84:87],v232 offset:52224
cv v168,v168,v169
ex2 v166,v166
ex2 v167,v167
ex2 v164,v164
ex2 v165,v165
ex2 v160,v160
ex2 v161,v161
ex2 v162,v162
ex2 v163,v163
cv v169,v170,v171
cv v170,v172,v173
cv v171,v174,v175
mm v[68:71],a[64:67],v[184:187],v[68:71]
dr a[88:91],v232 offset:60416
cv v167,v166,v167
cv v166,v164,v165
cv v164,v160,v161
cv v165,v162,v163
mm v[4:7],a[64:67],v[168:171],v[4:7]
dr a[92:95],v232 offset:53248
v_cndmask_b32_e64 v220,v236,v200,s[52:53]
mm v[4:7],a[124:127],v[164:167],v[4:7]
)" R"(s_and_b64 s[86:87],s[14:15],s[48:49]
dr a[96:99],v232 offset:61440
bl a[228:231],v233,s[8:11],s70 offen sc1
mm v[12:15],a[68:71],v[164:167],v[12:15]
s_add_u32 s70,s70,8192
dr a[100:103],v232 offset:54272
mm v[12:15],a[72:75],v[168:171],v[12:15]
dr a[104:107],v232 offset:62464
mm v[76:79],a[72:75],v[184:187],v[76:79]
dr a[108:111],v232 offset:55296
mm v[76:79],a[68:71],v[180:183],v[76:79]
s_or_b64 s[86:87],s[86:87],s[50:51]
dr a[112:115],v232 offset:63488
mm v[84:87],a[76:79],v[180:183],v[84:87]
s_waitcnt lgkmcnt(6)
dr a[116:119],v232 offset:56320
mm v[84:87],a[80:83],v[184:187],v[84:87]
dr a[120:123],v232 offset:64512
mm v[20:23],a[80:83],v[168:171],v[20:23]
ex2 v150,v150
mm v[20:23],a[76:79],v[164:167],v[20:23]
ex2 v151,v151
mm v[28:31],a[84:87],v[164:167],v[28:31]
ex2 v148,v148
mm v[28:31],a[88:91],v[168:171],v[28:31]
ex2 v149,v149
mm v[92:95],a[88:91],v[184:187],v[92:95]
s_and_b64 s[52:53],s[16:17],s[48:49]
ex2 v144,v144
s_waitcnt vmcnt(14) lgkmcnt(0)
s_barrier
mm v[92:95],a[84:87],v[180:183],v[92:95]
bl v232,s[8:11],s40 offen offset:0 sc1 lds
v_smfmac_f32_16x16x64_bf16 a[136:139],a[144:147],v[164:171],v246
dr a[124:127],v235 offset:0
v_cndmask_b32_e64 v221,v236,v200,s[86:87]
v_smfmac_f32_16x16x64_bf16 a[140:143],a[144:147],v[180:187],v246
dr a[64:67],v235 offset:1024
cv v151,v150,v151
mm v[100:103],a[92:95],v[180:183],v[100:103]
s_or_b64 s[52:53],s[52:53],s[50:51]
ex2 v145,v145
mm v[100:103],a[96:99],v[184:187],v[100:103]
bl v232,s[8:11],s40 offen offset:1024 sc1 lds
mm v[36:39],a[96:99],v[168:171],v[36:39]
dr a[68:71],v235 offset:2048
cv v150,v148,v149
mm v[36:39],a[92:95],v[164:167],v[36:39]
dr a[72:75],v235 offset:3072
v_cndmask_b32_e64 v222,v236,v200,s[52:53]
mm v[44:47],a[100:103],v[164:167],v[44:47]
ex2 v146,v146
mm v[44:47],a[104:107],v[168:171],v[44:47]
bl v232,s[8:11],s40 offen offset:2048 sc1 lds
mm v[108:111],a[104:107],v[184:187],v[108:111]
dr a[76:79],v235 offset:4096
cv v148,v144,v145
mm v[108:111],a[100:103],v[180:183],v[108:111]
s_and_b64 s[86:87],s[18:19],s[48:49]
dr a[80:83],v235 offset:5120
mm v[116:119],a[108:111],v[180:183],v[116:119]
ex2 v147,v147
mm v[116:119],a[112:115],v[184:187],v[116:119]
bl v232,s[8:11],s40 offen offset:3072 sc1 lds
)" R"(mm v[52:55],a[112:115],v[168:171],v[52:55]
s_add_u32 s40,s31,32768
dr a[84:87],v235 offset:6144
s_add_u32 m0,s33,131072
mm v[52:55],a[108:111],v[164:167],v[52:55]
s_or_b64 s[86:87],s[86:87],s[50:51]
dr a[88:91],v235 offset:7168
cv v149,v146,v147
mm v[60:63],a[116:119],v[164:167],v[60:63]
ex2 v152,v152
mm v[60:63],a[120:123],v[168:171],v[60:63]
buffer_load_dword a232,v249,s[8:11],s70 offen offset:0 sc1
mm v[124:127],a[120:123],v[184:187],v[124:127]
dr a[92:95],v235 offset:8192
v_cndmask_b32_e64 v223,v236,v200,s[86:87]
mm v[124:127],a[116:119],v[180:183],v[124:127]
s_waitcnt lgkmcnt(3)
buffer_load_dword a233,v249,s[8:11],s70 offen offset:256 sc1
mm v[160:163],a[124:127],v[64:67],v[200:203]
dr a[96:99],v235 offset:9216
mm v[160:163],a[64:67],v[72:75],v[160:163]
buffer_load_dword a234,v249,s[8:11],s70 offen offset:512 sc1
mm v[160:163],a[68:71],v[80:83],v[160:163]
dr a[100:103],v235 offset:10240
buffer_load_dword a235,v249,s[8:11],s70 offen offset:768 sc1
mm v[160:163],a[72:75],v[88:91],v[160:163]
s_add_u32 s70,s70,4096
dr a[104:107],v235 offset:11264
buffer_load_dword a236,v249,s[8:11],s70 offen offset:0 sc1
mm v[176:179],a[72:75],v[120:123],v[204:207]
dr a[108:111],v235 offset:12288
buffer_load_dword a237,v249,s[8:11],s70 offen offset:256 sc1
mm v[176:179],a[68:71],v[112:115],v[176:179]
dr a[112:115],v235 offset:13312
buffer_load_dword a238,v249,s[8:11],s70 offen offset:512 sc1
mm v[176:179],a[64:67],v[104:107],v[176:179]
dr a[116:119],v235 offset:14336
buffer_load_dword a239,v249,s[8:11],s70 offen offset:768 sc1
mm v[176:179],a[124:127],v[96:99],v[176:179]
s_add_u32 s70,s70,4096
dr a[120:123],v235 offset:15360
mm v[180:183],a[76:79],v[96:99],v[204:207]
ex2 v153,v153
mm v[180:183],a[80:83],v[104:107],v[180:183]
s_waitcnt lgkmcnt(5)
ex2 v154,v154
mm v[180:183],a[84:87],v[112:115],v[180:183]
ex2 v155,v155
mm v[180:183],a[88:91],v[120:123],v[180:183]
ex2 v156,v156
mm v[164:167],a[88:91],v[88:91],v[200:203]
s_waitcnt vmcnt(19) lgkmcnt(0)
s_barrier
mm v[164:167],a[84:87],v[80:83],v[164:167]
bl v232,s[8:11],s40 offen offset:0 sc1 lds
mm v[164:167],a[80:83],v[72:75],v[164:167]
dr a[124:127],v235 offset:16384
cv v152,v152,v153
mm v[164:167],a[76:79],v[64:67],v[164:167]
dr a[64:67],v235 offset:24576
cv v153,v154,v155
mm v[168:171],a[92:95],v[64:67],v[200:203]
ex2 v157,v157
mm v[168:171],a[96:99],v[72:75],v[168:171]
)" R"(bl v232,s[8:11],s40 offen offset:1024 sc1 lds
mm v[168:171],a[100:103],v[80:83],v[168:171]
dr a[68:71],v235 offset:17408
cv v154,v156,v157
mm v[168:171],a[104:107],v[88:91],v[168:171]
dr a[72:75],v235 offset:25600
mm v[184:187],a[104:107],v[120:123],v[204:207]
ex2 v158,v158
mm v[184:187],a[100:103],v[112:115],v[184:187]
bl v232,s[8:11],s40 offen offset:2048 sc1 lds
mm v[184:187],a[96:99],v[104:107],v[184:187]
ex2 v159,v159
mm v[184:187],a[92:95],v[96:99],v[184:187]
dr a[76:79],v235 offset:18432
cv v155,v158,v159
mm v[188:191],a[108:111],v[96:99],v[204:207]
ex2 v136,v136
mm v[188:191],a[112:115],v[104:107],v[188:191]
bl v232,s[8:11],s40 offen offset:3072 sc1 lds
mm v[188:191],a[116:119],v[112:115],v[188:191]
s_add_u32 s40,s31,8192
ex2 v137,v137
mm v[188:191],a[120:123],v[120:123],v[188:191]
s_add_u32 m0,s33,147456
ex2 v138,v138
mm v[172:175],a[120:123],v[88:91],v[200:203]
ex2 v139,v139
mm v[172:175],a[116:119],v[80:83],v[172:175]
ex2 v140,v140
mm v[172:175],a[112:115],v[72:75],v[172:175]
ex2 v141,v141
mm v[172:175],a[108:111],v[64:67],v[172:175]
s_waitcnt lgkmcnt(0)
ex2 v142,v142
dr a[80:83],v235 offset:26624
ex2 v143,v143
mm v[68:71],a[124:127],v[148:151],v[68:71]
dr a[84:87],v235 offset:19456
cv v136,v136,v137
ex2 v134,v134
ex2 v135,v135
ex2 v132,v132
ex2 v133,v133
ex2 v128,v128
ex2 v129,v129
ex2 v130,v130
ex2 v131,v131
cv v137,v138,v139
cv v138,v140,v141
cv v139,v142,v143
mm v[68:71],a[64:67],v[152:155],v[68:71]
dr a[88:91],v235 offset:27648
cv v135,v134,v135
cv v134,v132,v133
cv v132,v128,v129
cv v133,v130,v131
mm v[4:7],a[64:67],v[136:139],v[4:7]
dr a[92:95],v235 offset:20480
s_waitcnt vmcnt(22)
v_cmp_ne_u64 vcc,s[54:55],v[240:241]
s_cbranch_vccnz FT_S
FT_D:
mm v[4:7],a[124:127],v[132:135],v[4:7]
s_and_b32 s46,s67,-64
)" R"(dr a[96:99],v235 offset:28672
v_lshrrev_b32 v242,1,v232
mm v[12:15],a[68:71],v[132:135],v[12:15]
s_add_u32 s46,s46,8192
dr a[100:103],v235 offset:21504
v_and_b32 v242,56,v242
mm v[12:15],a[72:75],v[136:139],v[12:15]
dr a[104:107],v235 offset:29696
buffer_load_dword a240,v249,s[8:11],s70 offen offset:0 sc1
mm v[76:79],a[72:75],v[152:155],v[76:79]
dr a[108:111],v235 offset:22528
buffer_load_dword a241,v249,s[8:11],s70 offen offset:256 sc1
mm v[76:79],a[68:71],v[148:151],v[76:79]
dr a[112:115],v235 offset:30720
buffer_load_dword a242,v249,s[8:11],s70 offen offset:512 sc1
mm v[84:87],a[76:79],v[148:151],v[84:87]
s_waitcnt lgkmcnt(6)
dr a[116:119],v235 offset:23552
buffer_load_dword a243,v249,s[8:11],s70 offen offset:768 sc1
mm v[84:87],a[80:83],v[152:155],v[84:87]
s_add_u32 s70,s70,4096
dr a[120:123],v235 offset:31744
buffer_load_dword a244,v249,s[8:11],s70 offen offset:0 sc1
mm v[20:23],a[80:83],v[136:139],v[20:23]
buffer_load_dword a245,v249,s[8:11],s70 offen offset:256 sc1
mm v[20:23],a[76:79],v[132:135],v[20:23]
buffer_load_dword a246,v249,s[8:11],s70 offen offset:512 sc1
mm v[28:31],a[84:87],v[132:135],v[28:31]
buffer_load_dword a247,v249,s[8:11],s70 offen offset:768 sc1
mm v[28:31],a[88:91],v[136:139],v[28:31]
s_add_u32 s70,s70,4096
ex2 v182,v182
mm v[92:95],a[88:91],v[152:155],v[92:95]
s_waitcnt vmcnt(26) lgkmcnt(0)
s_barrier
mm v[92:95],a[84:87],v[148:151],v[92:95]
bl v232,s[8:11],s40 offen offset:0 sc1 lds
v_smfmac_f32_16x16x64_bf16 a[136:139],a[144:147],v[132:139],v246
dr a[124:127],v232 offset:0
v_smfmac_f32_16x16x64_bf16 a[140:143],a[144:147],v[148:155],v246
dr a[64:67],v232 offset:1024
mm v[100:103],a[92:95],v[148:151],v[100:103]
ex2 v183,v183
mm v[100:103],a[96:99],v[152:155],v[100:103]
bl v232,s[8:11],s40 offen offset:1024 sc1 lds
mm v[36:39],a[96:99],v[136:139],v[36:39]
dr a[68:71],v232 offset:2048
cv v183,v182,v183
mm v[36:39],a[92:95],v[132:135],v[36:39]
dr a[72:75],v232 offset:3072
mm v[44:47],a[100:103],v[132:135],v[44:47]
ED_1:
s_cbranch_execz ED_R
ex2 v180,v180
mm v[44:47],a[104:107],v[136:139],v[44:47]
bl v232,s[8:11],s40 offen offset:2048 sc1 lds
mm v[108:111],a[104:107],v[152:155],v[108:111]
dr a[76:79],v232 offset:4096
mm v[108:111],a[100:103],v[148:151],v[108:111]
dr a[80:83],v232 offset:5120
mm v[116:119],a[108:111],v[148:151],v[116:119]
ex2 v181,v181
mm v[116:119],a[112:115],v[152:155],v[116:119]
s_mov_b32 s30,s31
)" R"(bl v232,s[8:11],s40 offen offset:3072 sc1 lds
mm v[52:55],a[112:115],v[136:139],v[52:55]
s_add_u32 s31,s31,65536
dr a[84:87],v232 offset:6144
s_add_u32 m0,s33,0
mm v[52:55],a[108:111],v[132:135],v[52:55]
s_cmp_eq_u32 s31,s3
s_cselect_b32 s40,65536,0
dr a[88:91],v232 offset:7168
mm v[60:63],a[116:119],v[132:135],v[60:63]
s_add_u32 s31,s31,s40
ex2 v176,v176
mm v[60:63],a[120:123],v[136:139],v[60:63]
s_add_u32 s40,s30,40960
dr a[92:95],v232 offset:8192
cv v182,v180,v181
mm v[124:127],a[120:123],v[152:155],v[124:127]
dr a[96:99],v232 offset:9216
mm v[124:127],a[116:119],v[148:151],v[124:127]
s_waitcnt lgkmcnt(4)
ex2 v177,v177
mm v[128:131],a[124:127],v[64:67],v[208:211]
dr a[100:103],v232 offset:10240
cv v180,v176,v177
mm v[128:131],a[64:67],v[72:75],v[128:131]
dr a[104:107],v232 offset:11264
mm v[128:131],a[68:71],v[80:83],v[128:131]
ex2 v178,v178
mm v[128:131],a[72:75],v[88:91],v[128:131]
dr a[108:111],v232 offset:12288
mm v[144:147],a[72:75],v[120:123],v[204:207]
dr a[112:115],v232 offset:13312
mm v[144:147],a[68:71],v[112:115],v[144:147]
buffer_load_dwordx2 v[240:241],v242,s[8:11],s46 offen sc1
mm v[144:147],a[64:67],v[104:107],v[144:147]
dr a[116:119],v232 offset:14336
mm v[144:147],a[124:127],v[96:99],v[144:147]
dr a[120:123],v232 offset:15360
mm v[148:151],a[76:79],v[96:99],v[204:207]
ex2 v179,v179
mm v[148:151],a[80:83],v[104:107],v[148:151]
s_waitcnt lgkmcnt(4)
ex2 v184,v184
mm v[148:151],a[84:87],v[112:115],v[148:151]
ex2 v185,v185
mm v[148:151],a[88:91],v[120:123],v[148:151]
ex2 v186,v186
mm v[132:135],a[88:91],v[88:91],v[212:215]
s_waitcnt vmcnt(25) lgkmcnt(0)
s_barrier
mm v[132:135],a[84:87],v[80:83],v[132:135]
bl v232,s[8:11],s40 offen offset:0 sc1 lds
mm v[132:135],a[80:83],v[72:75],v[132:135]
dr a[124:127],v232 offset:49152
cv v181,v178,v179
mm v[132:135],a[76:79],v[64:67],v[132:135]
dr a[64:67],v232 offset:57344
cv v184,v184,v185
mm v[136:139],a[92:95],v[64:67],v[216:219]
ex2 v187,v187
mm v[136:139],a[96:99],v[72:75],v[136:139]
s_add_u32 s41,s35,98304
bl v232,s[8:11],s40 offen offset:1024 sc1 lds
mm v[136:139],a[100:103],v[80:83],v[136:139]
)" R"(dr a[68:71],v232 offset:50176
cv v185,v186,v187
mm v[136:139],a[104:107],v[88:91],v[136:139]
dr a[72:75],v232 offset:58368
mm v[152:155],a[104:107],v[120:123],v[204:207]
ex2 v188,v188
mm v[152:155],a[100:103],v[112:115],v[152:155]
bl v232,s[8:11],s40 offen offset:2048 sc1 lds
mm v[152:155],a[96:99],v[104:107],v[152:155]
ex2 v189,v189
mm v[152:155],a[92:95],v[96:99],v[152:155]
ex2 v190,v190
mm v[156:159],a[108:111],v[96:99],v[204:207]
ex2 v191,v191
mm v[156:159],a[112:115],v[104:107],v[156:159]
bl v232,s[8:11],s40 offen offset:3072 sc1 lds
mm v[156:159],a[116:119],v[112:115],v[156:159]
s_add_u32 m0,s34,49152
dr a[76:79],v232 offset:51200
cv v186,v188,v189
mm v[156:159],a[120:123],v[120:123],v[156:159]
ex2 v168,v168
mm v[140:143],a[120:123],v[88:91],v[220:223]
ex2 v169,v169
mm v[140:143],a[116:119],v[80:83],v[140:143]
dr a[80:83],v232 offset:59392
cv v187,v190,v191
mm v[140:143],a[112:115],v[72:75],v[140:143]
ex2 v170,v170
mm v[140:143],a[108:111],v[64:67],v[140:143]
s_waitcnt lgkmcnt(2)
ex2 v171,v171
ex2 v172,v172
ex2 v173,v173
ex2 v174,v174
ex2 v175,v175
mm v[68:71],a[124:127],v[180:183],v[68:71]
s_bfe_i64 s[48:49],s[36:37],0x10008
dr a[84:87],v232 offset:52224
cv v168,v168,v169
ex2 v166,v166
ex2 v167,v167
ex2 v164,v164
ex2 v165,v165
ex2 v160,v160
ex2 v161,v161
ex2 v162,v162
ex2 v163,v163
cv v169,v170,v171
cv v170,v172,v173
cv v171,v174,v175
mm v[68:71],a[64:67],v[184:187],v[68:71]
s_bfe_i64 s[50:51],s[36:37],0x1000c
dr a[88:91],v232 offset:60416
cv v167,v166,v167
cv v166,v164,v165
cv v164,v160,v161
cv v165,v162,v163
mm v[4:7],a[64:67],v[168:171],v[4:7]
s_and_b64 s[52:53],s[12:13],s[48:49]
dr a[92:95],v232 offset:53248
s_or_b64 s[52:53],s[52:53],s[50:51]
mm v[4:7],a[124:127],v[164:167],v[4:7]
s_and_b64 s[86:87],s[14:15],s[48:49]
)" R"(dr a[96:99],v232 offset:61440
v_cndmask_b32_e64 v208,v236,v204,s[52:53]
mm v[12:15],a[68:71],v[164:167],v[12:15]
s_or_b64 s[86:87],s[86:87],s[50:51]
dr a[100:103],v232 offset:54272
v_cndmask_b32_e64 v209,v236,v204,s[86:87]
mm v[12:15],a[72:75],v[168:171],v[12:15]
s_and_b64 s[52:53],s[16:17],s[48:49]
dr a[104:107],v232 offset:62464
s_or_b64 s[52:53],s[52:53],s[50:51]
mm v[76:79],a[72:75],v[184:187],v[76:79]
s_and_b64 s[86:87],s[18:19],s[48:49]
dr a[108:111],v232 offset:55296
v_cndmask_b32_e64 v210,v236,v204,s[52:53]
mm v[76:79],a[68:71],v[180:183],v[76:79]
s_waitcnt lgkmcnt(6)
dr a[112:115],v232 offset:63488
s_or_b64 s[86:87],s[86:87],s[50:51]
mm v[84:87],a[76:79],v[180:183],v[84:87]
s_bfe_i64 s[48:49],s[36:37],0x10009
dr a[116:119],v232 offset:56320
v_cndmask_b32_e64 v211,v236,v204,s[86:87]
mm v[84:87],a[80:83],v[184:187],v[84:87]
s_bfe_i64 s[50:51],s[36:37],0x1000d
dr a[120:123],v232 offset:64512
s_and_b64 s[52:53],s[12:13],s[48:49]
mm v[20:23],a[80:83],v[168:171],v[20:23]
s_or_b64 s[52:53],s[52:53],s[50:51]
ex2 v150,v150
mm v[20:23],a[76:79],v[164:167],v[20:23]
s_and_b64 s[86:87],s[14:15],s[48:49]
ex2 v151,v151
mm v[28:31],a[84:87],v[164:167],v[28:31]
s_waitcnt lgkmcnt(4)
ex2 v148,v148
mm v[28:31],a[88:91],v[168:171],v[28:31]
s_or_b64 s[86:87],s[86:87],s[50:51]
ex2 v149,v149
mm v[92:95],a[88:91],v[184:187],v[92:95]
s_waitcnt vmcnt(17) lgkmcnt(0)
s_barrier
mm v[92:95],a[84:87],v[180:183],v[92:95]
bl v232,s[8:11],s41 offen offset:0 sc1 lds
v_smfmac_f32_16x16x64_bf16 a[136:139],a[144:147],v[164:171],v246
s_add_u32 s41,s35,98304
dr a[124:127],v235 offset:0
s_add_u32 m0,s34,49152
v_smfmac_f32_16x16x64_bf16 a[140:143],a[144:147],v[180:187],v246
dr a[64:67],v235 offset:1024
v_cndmask_b32_e64 v212,v236,v204,s[52:53]
mm v[100:103],a[92:95],v[180:183],v[100:103]
s_and_b64 s[52:53],s[16:17],s[48:49]
ex2 v144,v144
mm v[100:103],a[96:99],v[184:187],v[100:103]
s_or_b64 s[52:53],s[52:53],s[50:51]
bl v232,s[8:11],s41 offen offset:1024 sc1 lds
mm v[36:39],a[96:99],v[168:171],v[36:39]
s_add_u32 s41,s35,106496
dr a[68:71],v235 offset:2048
s_add_u32 m0,s34,57344
mm v[36:39],a[92:95],v[164:167],v[36:39]
dr a[72:75],v235 offset:3072
v_cndmask_b32_e64 v213,v236,v204,s[86:87]
mm v[44:47],a[100:103],v[164:167],v[44:47]
)" R"(s_and_b64 s[86:87],s[18:19],s[48:49]
ex2 v145,v145
mm v[44:47],a[104:107],v[168:171],v[44:47]
s_or_b64 s[86:87],s[86:87],s[50:51]
dr a[76:79],v235 offset:4096
v_cndmask_b32_e64 v214,v236,v204,s[52:53]
mm v[108:111],a[104:107],v[184:187],v[108:111]
s_bfe_i64 s[48:49],s[36:37],0x1000a
dr a[80:83],v235 offset:5120
v_cndmask_b32_e64 v215,v236,v204,s[86:87]
mm v[108:111],a[100:103],v[180:183],v[108:111]
s_bfe_i64 s[50:51],s[36:37],0x1000e
bl v232,s[8:11],s41 offen offset:0 sc1 lds
mm v[116:119],a[108:111],v[180:183],v[116:119]
s_add_u32 s41,s35,106496
dr a[84:87],v235 offset:6144
s_add_u32 m0,s34,57344
mm v[116:119],a[112:115],v[184:187],v[116:119]
s_and_b64 s[52:53],s[12:13],s[48:49]
dr a[88:91],v235 offset:7168
s_or_b64 s[52:53],s[52:53],s[50:51]
mm v[52:55],a[112:115],v[168:171],v[52:55]
s_and_b64 s[86:87],s[14:15],s[48:49]
dr a[92:95],v235 offset:8192
v_cndmask_b32_e64 v216,v236,v204,s[52:53]
mm v[52:55],a[108:111],v[164:167],v[52:55]
s_or_b64 s[86:87],s[86:87],s[50:51]
dr a[96:99],v235 offset:9216
v_cndmask_b32_e64 v217,v236,v204,s[86:87]
bl v232,s[8:11],s41 offen offset:1024 sc1 lds
mm v[60:63],a[116:119],v[164:167],v[60:63]
s_and_b64 s[52:53],s[16:17],s[48:49]
dr a[100:103],v235 offset:10240
s_or_b64 s[52:53],s[52:53],s[50:51]
mm v[60:63],a[120:123],v[168:171],v[60:63]
s_and_b64 s[86:87],s[18:19],s[48:49]
dr a[104:107],v235 offset:11264
v_cndmask_b32_e64 v218,v236,v204,s[52:53]
mm v[124:127],a[120:123],v[184:187],v[124:127]
s_or_b64 s[86:87],s[86:87],s[50:51]
dr a[108:111],v235 offset:12288
v_cndmask_b32_e64 v219,v236,v204,s[86:87]
mm v[124:127],a[116:119],v[180:183],v[124:127]
s_waitcnt lgkmcnt(6)
dr a[112:115],v235 offset:13312
s_add_u32 s41,s35,114688
mm v[176:179],a[124:127],v[96:99],v[208:211]
s_add_u32 m0,s34,131072
dr a[116:119],v235 offset:14336
s_bfe_i64 s[48:49],s[36:37],0x1000b
mm v[176:179],a[64:67],v[104:107],v[176:179]
s_bfe_i64 s[50:51],s[36:37],0x1000f
dr a[120:123],v235 offset:15360
s_and_b64 s[52:53],s[12:13],s[48:49]
mm v[176:179],a[68:71],v[112:115],v[176:179]
s_or_b64 s[52:53],s[52:53],s[50:51]
ex2 v146,v146
mm v[176:179],a[72:75],v[120:123],v[176:179]
s_and_b64 s[86:87],s[14:15],s[48:49]
ex2 v147,v147
mm v[180:183],a[76:79],v[96:99],v[212:215]
s_or_b64 s[86:87],s[86:87],s[50:51]
ex2 v152,v152
mm v[180:183],a[80:83],v[104:107],v[180:183]
)" R"(ex2 v153,v153
mm v[180:183],a[84:87],v[112:115],v[180:183]
s_waitcnt lgkmcnt(2)
ex2 v154,v154
s_waitcnt vmcnt(9) lgkmcnt(0)
s_barrier
mm v[180:183],a[88:91],v[120:123],v[180:183]
dr a[124:127],v235 offset:16384
v_cndmask_b32_e64 v220,v236,v204,s[52:53]
mm v[184:187],a[92:95],v[96:99],v[216:219]
s_and_b64 s[52:53],s[16:17],s[48:49]
dr a[64:67],v235 offset:24576
v_cndmask_b32_e64 v221,v236,v204,s[86:87]
mm v[184:187],a[96:99],v[104:107],v[184:187]
s_or_b64 s[52:53],s[52:53],s[50:51]
bl v232,s[8:11],s41 offen offset:0 sc1 lds
mm v[184:187],a[100:103],v[112:115],v[184:187]
s_and_b64 s[86:87],s[18:19],s[48:49]
dr a[68:71],v235 offset:17408
v_cndmask_b32_e64 v222,v236,v204,s[52:53]
s_or_b64 s[86:87],s[86:87],s[50:51]
v_cndmask_b32_e64 v223,v236,v204,s[86:87]
mm v[184:187],a[104:107],v[120:123],v[184:187]
s_add_u32 s41,s35,114688
dr a[72:75],v235 offset:25600
s_add_u32 m0,s34,131072
mm v[188:191],a[108:111],v[96:99],v[220:223]
cv v151,v150,v151
cv v150,v148,v149
mm v[188:191],a[112:115],v[104:107],v[188:191]
bl v232,s[8:11],s41 offen offset:1024 sc1 lds
mm v[188:191],a[116:119],v[112:115],v[188:191]
s_add_u32 s41,s35,122880
dr a[76:79],v235 offset:18432
cv v148,v144,v145
cv v149,v146,v147
ex2 v155,v155
ex2 v156,v156
ex2 v157,v157
ex2 v158,v158
ex2 v159,v159
mm v[188:191],a[120:123],v[120:123],v[188:191]
s_waitcnt lgkmcnt(3)
dr a[80:83],v235 offset:26624
cv v152,v152,v153
cv v153,v154,v155
cv v154,v156,v157
cv v155,v158,v159
ex2 v136,v136
ex2 v137,v137
ex2 v138,v138
ex2 v139,v139
ex2 v140,v140
ex2 v141,v141
ex2 v142,v142
ex2 v143,v143
mm v[68:71],a[124:127],v[148:151],v[68:71]
s_add_u32 m0,s34,139264
dr a[84:87],v235 offset:19456
cv v136,v136,v137
ex2 v134,v134
ex2 v135,v135
ex2 v132,v132
ex2 v133,v133
)" R"(ex2 v128,v128
ex2 v129,v129
ex2 v130,v130
ex2 v131,v131
cv v137,v138,v139
cv v138,v140,v141
cv v139,v142,v143
mm v[68:71],a[64:67],v[152:155],v[68:71]
dr a[88:91],v235 offset:27648
cv v135,v134,v135
bl v232,s[8:11],s41 offen offset:0 sc1 lds
cv v134,v132,v133
cv v132,v128,v129
cv v133,v130,v131
mm v[4:7],a[64:67],v[136:139],v[4:7]
s_add_u32 s41,s35,122880
dr a[92:95],v235 offset:20480
s_add_u32 m0,s34,139264
mm v[4:7],a[124:127],v[132:135],v[4:7]
s_waitcnt lgkmcnt(5)
dr a[96:99],v235 offset:28672
mm v[12:15],a[68:71],v[132:135],v[12:15]
dr a[100:103],v235 offset:21504
mm v[12:15],a[72:75],v[136:139],v[12:15]
dr a[104:107],v235 offset:29696
bl v232,s[8:11],s41 offen offset:1024 sc1 lds
mm v[76:79],a[72:75],v[152:155],v[76:79]
s_add_u32 s41,s35,131072
dr a[108:111],v235 offset:22528
s_add_u32 m0,s34,147456
mm v[76:79],a[68:71],v[148:151],v[76:79]
s_waitcnt lgkmcnt(6)
dr a[112:115],v235 offset:30720
mm v[84:87],a[76:79],v[148:151],v[84:87]
dr a[116:119],v235 offset:23552
mm v[84:87],a[80:83],v[152:155],v[84:87]
dr a[120:123],v235 offset:31744
mm v[20:23],a[80:83],v[136:139],v[20:23]
ex2 v182,v182
mm v[20:23],a[76:79],v[132:135],v[20:23]
s_waitcnt vmcnt(12)
v_cmp_ne_u64 vcc,s[54:55],v[240:241]
s_cbranch_vccnz FR_S
FR_D:
mm v[28:31],a[84:87],v[132:135],v[28:31]
s_waitcnt lgkmcnt(4)
ex2 v183,v183
mm v[28:31],a[88:91],v[136:139],v[28:31]
ex2 v180,v180
mm v[92:95],a[88:91],v[152:155],v[92:95]
s_waitcnt vmcnt(8) lgkmcnt(0)
s_barrier
mm v[92:95],a[84:87],v[148:151],v[92:95]
bl v232,s[8:11],s41 offen offset:0 sc1 lds
v_smfmac_f32_16x16x64_bf16 a[136:139],a[144:147],v[132:139],v246
s_add_u32 s41,s35,131072
dr a[124:127],v232 offset:0
s_add_u32 m0,s34,147456
v_smfmac_f32_16x16x64_bf16 a[140:143],a[144:147],v[148:155],v246
dr a[64:67],v232 offset:8192
cv v183,v182,v183
mm v[100:103],a[92:95],v[148:151],v[100:103]
ex2 v181,v181
mm v[100:103],a[96:99],v[152:155],v[100:103]
)" R"(bl v232,s[8:11],s41 offen offset:1024 sc1 lds
mm v[36:39],a[96:99],v[136:139],v[36:39]
s_add_u32 s41,s35,139264
dr a[68:71],v232 offset:1024
s_add_u32 m0,s34,155648
mm v[36:39],a[92:95],v[132:135],v[36:39]
dr a[72:75],v232 offset:9216
cv v182,v180,v181
mm v[44:47],a[100:103],v[132:135],v[44:47]
ex2 v176,v176
mm v[44:47],a[104:107],v[136:139],v[44:47]
bl v232,s[8:11],s41 offen offset:0 sc1 lds
mm v[108:111],a[104:107],v[152:155],v[108:111]
s_add_u32 s41,s35,139264
dr a[76:79],v232 offset:2048
s_add_u32 m0,s34,155648
mm v[108:111],a[100:103],v[148:151],v[108:111]
ex2 v177,v177
mm v[116:119],a[108:111],v[148:151],v[116:119]
ex2 v178,v178
mm v[116:119],a[112:115],v[152:155],v[116:119]
bl v232,s[8:11],s41 offen offset:1024 sc1 lds
mm v[52:55],a[112:115],v[136:139],v[52:55]
ex2 v179,v179
mm v[52:55],a[108:111],v[132:135],v[52:55]
dr a[80:83],v232 offset:10240
cv v180,v176,v177
mm v[60:63],a[116:119],v[132:135],v[60:63]
dr a[84:87],v232 offset:3072
cv v181,v178,v179
mm v[60:63],a[120:123],v[136:139],v[60:63]
ex2 v184,v184
mm v[124:127],a[120:123],v[152:155],v[124:127]
ex2 v185,v185
ex2 v186,v186
ex2 v187,v187
ex2 v188,v188
ex2 v189,v189
ex2 v190,v190
ex2 v191,v191
mm v[124:127],a[116:119],v[148:151],v[124:127]
s_waitcnt lgkmcnt(2)
dr a[88:91],v232 offset:11264
cv v184,v184,v185
cv v185,v186,v187
cv v186,v188,v189
cv v187,v190,v191
mm v[68:71],a[124:127],v[180:183],v[68:71]
s_add_u32 s41,s35,147456
dr a[92:95],v232 offset:4096
s_add_u32 m0,s34,0
mm v[68:71],a[64:67],v[184:187],v[68:71]
ard v244,a136
ard v208,a137
v_add_f32 v244,v244,v208
v_cmp_ngt_f32 vcc,0x5f800000,v244
s_cbranch_vccnz FX10
FX10_D:
mm v[76:79],a[68:71],v[180:183],v[76:79]
dr a[96:99],v232 offset:12288
v_rcp_f32 v244,v244
mm v[76:79],a[72:75],v[184:187],v[76:79]
dr a[100:103],v232 offset:5120
v_mul_f32 v4,v244,v4
)" R"(v_mul_f32 v5,v244,v5
v_mul_f32 v6,v244,v6
v_mul_f32 v7,v244,v7
mm v[84:87],a[76:79],v[180:183],v[84:87]
s_waitcnt lgkmcnt(4)
dr a[104:107],v232 offset:13312
v_mul_f32 v12,v244,v12
v_mul_f32 v13,v244,v13
v_mul_f32 v14,v244,v14
v_mul_f32 v15,v244,v15
mm v[84:87],a[80:83],v[184:187],v[84:87]
dr a[108:111],v232 offset:6144
cv v64,v4,v5
cv v65,v6,v7
v_or_b32 v64,1,v64
cv v66,v12,v13
mm v[92:95],a[84:87],v[180:183],v[92:95]
s_waitcnt lgkmcnt(5)
dr a[112:115],v232 offset:14336
cv v67,v14,v15
bs v[64:67],v232,s[8:11],s42 offen offset:0 sc1
v_mul_f32 v20,v244,v20
v_mul_f32 v21,v244,v21
dr a[116:119],v232 offset:7168
v_mul_f32 v22,v244,v22
mm v[92:95],a[88:91],v[184:187],v[92:95]
s_add_u32 s47,s42,0
dr a[120:123],v232 offset:15360
v_mul_f32 v23,v244,v23
v_mul_f32 v28,v244,v28
v_mul_f32 v29,v244,v29
v_smfmac_f32_16x16x64_bf16 a[140:143],a[144:147],v[180:187],v246
s_waitcnt lgkmcnt(7)
dr a[124:127],v232 offset:16384
v_mul_f32 v30,v244,v30
v_mul_f32 v31,v244,v31
cv v72,v20,v21
cv v73,v22,v23
mm v[100:103],a[92:95],v[180:183],v[100:103]
s_waitcnt lgkmcnt(7)
v_or_b32 v72,1,v72
cv v74,v28,v29
cv v75,v30,v31
bs v[72:75],v232,s[8:11],s42 offen offset:1024 sc1
v_mul_f32 v36,v244,v36
v_mul_f32 v37,v244,v37
v_mul_f32 v38,v244,v38
v_mul_f32 v39,v244,v39
v_mul_f32 v44,v244,v44
v_mul_f32 v45,v244,v45
mm v[100:103],a[96:99],v[184:187],v[100:103]
s_waitcnt lgkmcnt(6)
dr a[64:67],v232 offset:18432
v_mul_f32 v46,v244,v46
mm v[108:111],a[100:103],v[180:183],v[108:111]
s_waitcnt lgkmcnt(6)
v_mul_f32 v47,v244,v47
cv v80,v36,v37
mm v[108:111],a[104:107],v[184:187],v[108:111]
s_waitcnt lgkmcnt(5)
dr a[68:71],v232 offset:20480
cv v81,v38,v39
v_or_b32 v80,1,v80
cv v82,v44,v45
)" R"(mm v[116:119],a[108:111],v[180:183],v[116:119]
s_waitcnt lgkmcnt(4)
cv v83,v46,v47
v_mul_f32 v52,v244,v52
bs v[80:83],v232,s[8:11],s42 offen offset:2048 sc1
v_mul_f32 v53,v244,v53
v_mul_f32 v54,v244,v54
v_mul_f32 v55,v244,v55
v_mul_f32 v60,v244,v60
v_mul_f32 v61,v244,v61
v_mul_f32 v62,v244,v62
mm v[116:119],a[112:115],v[184:187],v[116:119]
dr a[72:75],v232 offset:22528
v_mul_f32 v63,v244,v63
mm v[124:127],a[116:119],v[180:183],v[124:127]
s_waitcnt lgkmcnt(3)
cv v88,v52,v53
cv v89,v54,v55
mm v[124:127],a[120:123],v[184:187],v[124:127]
dr a[76:79],v232 offset:23552
v_or_b32 v88,1,v88
cv v90,v60,v61
cv v91,v62,v63
bs v[88:91],v232,s[8:11],s42 offen offset:3072 sc1
v_lshlrev_b32 v251,3,v232
v_and_b32 v251,0xf80,v251
ard v245,a140
ard v208,a141
v_add_f32 v245,v245,v208
v_cmp_ngt_f32 vcc,0x5f800000,v245
s_cbranch_vccnz FX11
FX11_D:
mm v[208:211],a[124:127],v[0:3],0
v_rcp_f32 v245,v245
mm v[216:219],a[124:127],v[32:35],0
s_waitcnt lgkmcnt(3)
dr a[80:83],v232 offset:21504
v_mul_f32 v68,v245,v68
mm v[216:219],a[64:67],v[40:43],v[216:219]
v_mul_f32 v69,v245,v69
v_mul_f32 v70,v245,v70
mm v[208:211],a[64:67],v[8:11],v[208:211]
s_waitcnt lgkmcnt(3)
dr a[84:87],v232 offset:19456
v_mul_f32 v71,v245,v71
mm v[208:211],a[68:71],v[16:19],v[208:211]
v_mul_f32 v76,v245,v76
v_mul_f32 v77,v245,v77
mm v[216:219],a[68:71],v[48:51],v[216:219]
s_waitcnt lgkmcnt(3)
dr a[88:91],v232 offset:17408
v_mul_f32 v78,v245,v78
mm v[216:219],a[72:75],v[56:59],v[216:219]
v_mul_f32 v79,v245,v79
cv v96,v68,v69
mm v[208:211],a[72:75],v[24:27],v[208:211]
s_waitcnt lgkmcnt(3)
dr a[92:95],v232 offset:25600
cv v97,v70,v71
mm v[212:215],a[76:79],v[24:27],0
dr a[96:99],v232 offset:27648
v_or_b32 v96,1,v96
mm v[220:223],a[76:79],v[56:59],0
s_waitcnt lgkmcnt(4)
)" R"(dr a[100:103],v232 offset:29696
cv v98,v76,v77
cv v99,v78,v79
mm v[220:223],a[80:83],v[48:51],v[220:223]
dr a[104:107],v232 offset:31744
v_mul_f32 v84,v245,v84
bs v[96:99],v233,s[8:11],s42 offen offset:0 sc1
v_mul_f32 v85,v245,v85
v_mul_f32 v86,v245,v86
v_mul_f32 v87,v245,v87
v_mul_f32 v92,v245,v92
v_mul_f32 v93,v245,v93
v_mul_f32 v94,v245,v94
mm v[212:215],a[80:83],v[16:19],v[212:215]
s_waitcnt lgkmcnt(5)
dr a[108:111],v232 offset:30720
v_mul_f32 v95,v245,v95
mm v[212:215],a[84:87],v[8:11],v[212:215]
dr a[112:115],v232 offset:28672
cv v104,v84,v85
mm v[220:223],a[84:87],v[40:43],v[220:223]
s_waitcnt lgkmcnt(6)
dr a[116:119],v232 offset:26624
cv v105,v86,v87
mm v[220:223],a[88:91],v[32:35],v[220:223]
dr a[120:123],v232 offset:24576
v_or_b32 v104,1,v104
mm v[212:215],a[88:91],v[0:3],v[212:215]
s_waitcnt lgkmcnt(6)
dr a[124:127],v232 offset:32768
cv v106,v92,v93
mm v[212:215],a[92:95],a[0:3],v[212:215]
dr a[64:67],v232 offset:34816
cv v107,v94,v95
mm v[220:223],a[92:95],a[4:7],v[220:223]
bs v[104:107],v233,s[8:11],s42 offen offset:1024 sc1
dr a[68:71],v232 offset:36864
v_mul_f32 v100,v245,v100
v_mul_f32 v101,v245,v101
v_mul_f32 v102,v245,v102
v_mul_f32 v103,v245,v103
v_mul_f32 v108,v245,v108
mm v[220:223],a[96:99],a[12:15],v[220:223]
ED_2:
s_cbranch_execz ED_R
dr a[72:75],v232 offset:38912
v_mul_f32 v109,v245,v109
mm v[212:215],a[96:99],a[8:11],v[212:215]
s_waitcnt lgkmcnt(6)
dr a[76:79],v232 offset:39936
v_mul_f32 v110,v245,v110
mm v[212:215],a[100:103],a[16:19],v[212:215]
dr a[80:83],v232 offset:37888
v_mul_f32 v111,v245,v111
mm v[220:223],a[100:103],a[20:23],v[220:223]
dr a[84:87],v232 offset:35840
cv v112,v100,v101
mm v[220:223],a[104:107],a[28:31],v[220:223]
dr a[88:91],v232 offset:33792
cv v113,v102,v103
mm v[212:215],a[104:107],a[24:27],v[212:215]
dr a[92:95],v232 offset:41984
v_or_b32 v112,1,v112
mm v[208:211],a[108:111],a[24:27],v[208:211]
)" R"(cv v114,v108,v109
cv v115,v110,v111
bs v[112:115],v233,s[8:11],s42 offen offset:2048 sc1
s_barrier
v_mul_f32 v116,v245,v116
v_mul_f32 v117,v245,v117
v_mul_f32 v118,v245,v118
v_mul_f32 v119,v245,v119
mm v[216:219],a[108:111],a[28:31],v[216:219]
v_mul_f32 v124,v245,v124
v_mul_f32 v125,v245,v125
mm v[216:219],a[112:115],a[20:23],v[216:219]
v_mul_f32 v126,v245,v126
v_mul_f32 v127,v245,v127
mm v[208:211],a[112:115],a[16:19],v[208:211]
s_waitcnt lgkmcnt(3)
cv v120,v116,v117
cv v121,v118,v119
mm v[208:211],a[116:119],a[8:11],v[208:211]
v_or_b32 v120,1,v120
cv v122,v124,v125
mm v[216:219],a[116:119],a[12:15],v[216:219]
cv v123,v126,v127
mm v[216:219],a[120:123],a[4:7],v[216:219]
bs v[120:123],v233,s[8:11],s42 offen offset:3072 sc1
mm v[208:211],a[120:123],a[0:3],v[208:211]
mm v[208:211],a[124:127],a[32:35],v[208:211]
mm v[216:219],a[124:127],a[36:39],v[216:219]
bl v232,s[8:11],s41 offen offset:0 sc1 lds
mm v[216:219],a[64:67],a[44:47],v[216:219]
s_add_u32 s41,s35,147456
dr a[96:99],v232 offset:44032
s_add_u32 m0,s34,0
mm v[208:211],a[64:67],a[40:43],v[208:211]
dr a[100:103],v232 offset:46080
mm v[208:211],a[68:71],a[48:51],v[208:211]
mm v[216:219],a[68:71],a[52:55],v[216:219]
bl v232,s[8:11],s41 offen offset:1024 sc1 lds
mm v[216:219],a[72:75],a[60:63],v[216:219]
s_add_u32 s41,s35,155648
dr a[104:107],v232 offset:48128
s_add_u32 m0,s34,8192
mm v[208:211],a[72:75],a[56:59],v[208:211]
dr a[108:111],v232 offset:47104
mm v[212:215],a[76:79],a[56:59],v[212:215]
mm v[220:223],a[76:79],a[60:63],v[220:223]
bl v232,s[8:11],s41 offen offset:0 sc1 lds
mm v[220:223],a[80:83],a[52:55],v[220:223]
s_add_u32 s41,s35,155648
dr a[112:115],v232 offset:45056
s_add_u32 m0,s34,8192
mm v[212:215],a[80:83],a[48:51],v[212:215]
s_waitcnt lgkmcnt(3)
dr a[116:119],v232 offset:43008
mm v[212:215],a[84:87],a[40:43],v[212:215]
mm v[220:223],a[84:87],a[44:47],v[220:223]
bl v232,s[8:11],s41 offen offset:1024 sc1 lds
mm v[220:223],a[88:91],a[36:39],v[220:223]
dr a[120:123],v232 offset:40960
mm v[212:215],a[88:91],a[32:35],v[212:215]
dr a[124:127],v234 offset:0
mm v[212:215],a[92:95],a[148:151],v[212:215]
mm v[220:223],a[92:95],a[152:155],v[220:223]
dr a[64:67],v234 offset:2048
)" R"(mm v[220:223],a[96:99],a[160:163],v[220:223]
dr a[68:71],v234 offset:4096
mm v[212:215],a[96:99],a[156:159],v[212:215]
mm v[212:215],a[100:103],a[164:167],v[212:215]
dr a[72:75],v234 offset:6144
mm v[220:223],a[100:103],a[168:171],v[220:223]
s_waitcnt lgkmcnt(4)
dr a[76:79],v234 offset:7168
mm v[220:223],a[104:107],a[176:179],v[220:223]
mm v[212:215],a[104:107],a[172:175],v[212:215]
dr a[80:83],v234 offset:5120
mm v[208:211],a[108:111],a[172:175],v[208:211]
buffer_load_dword v255,v251,s[8:11],s47 offen sc1
mm v[216:219],a[108:111],a[176:179],v[216:219]
s_add_u32 s47,s42,4096
dr a[84:87],v234 offset:3072
v_lshlrev_b32 v251,3,v232
mm v[216:219],a[112:115],a[168:171],v[216:219]
dr a[88:91],v234 offset:1024
v_and_b32 v251,0xf80,v251
mm v[208:211],a[112:115],a[164:167],v[208:211]
mm v[208:211],a[116:119],a[156:159],v[208:211]
dr a[92:95],v234 offset:9216
mm v[216:219],a[116:119],a[160:163],v[216:219]
dr a[96:99],v234 offset:11264
mm v[216:219],a[120:123],a[152:155],v[216:219]
mm v[208:211],a[120:123],a[148:151],v[208:211]
s_waitcnt lgkmcnt(4)
dr a[100:103],v234 offset:13312
mm v[224:227],a[124:127],v[0:3],0
dr a[104:107],v234 offset:15360
mm v[192:195],a[124:127],v[32:35],0
mm v[192:195],a[64:67],v[40:43],v[192:195]
dr a[108:111],v234 offset:14336
cv v216,v216,v217
mm v[224:227],a[64:67],v[8:11],v[224:227]
dr a[112:115],v234 offset:12288
cv v208,v208,v209
mm v[224:227],a[68:71],v[16:19],v[224:227]
cv v209,v210,v211
cv v210,v212,v213
mm v[192:195],a[68:71],v[48:51],v[192:195]
dr a[116:119],v234 offset:10240
cv v211,v214,v215
mm v[192:195],a[72:75],v[56:59],v[192:195]
bs v[208:211],v243,s[8:11],s64 offen offset:0 sc1
cv v217,v218,v219
cv v218,v220,v221
cv v219,v222,v223
dr a[120:123],v234 offset:8192
mm v[224:227],a[72:75],v[24:27],v[224:227]
mm v[228:231],a[76:79],v[24:27],0
dr a[124:127],v234 offset:16384
mm v[196:199],a[76:79],v[56:59],0
bs v[216:219],v243,s[8:11],s65 offen offset:0 sc1
dr a[64:67],v234 offset:18432
mm v[196:199],a[80:83],v[48:51],v[196:199]
mm v[228:231],a[80:83],v[16:19],v[228:231]
s_waitcnt lgkmcnt(4)
dr a[68:71],v234 offset:20480
mm v[228:231],a[84:87],v[8:11],v[228:231]
dr a[72:75],v234 offset:22528
mm v[196:199],a[84:87],v[40:43],v[196:199]
mm v[196:199],a[88:91],v[32:35],v[196:199]
)" R"(dr a[76:79],v234 offset:23552
mm v[228:231],a[88:91],v[0:3],v[228:231]
dr a[80:83],v234 offset:21504
mm v[228:231],a[92:95],a[0:3],v[228:231]
mm v[196:199],a[92:95],a[4:7],v[196:199]
dr a[84:87],v234 offset:19456
mm v[196:199],a[96:99],a[12:15],v[196:199]
dr a[88:91],v234 offset:17408
mm v[228:231],a[96:99],a[8:11],v[228:231]
mm v[228:231],a[100:103],a[16:19],v[228:231]
dr a[92:95],v234 offset:25600
mm v[196:199],a[100:103],a[20:23],v[196:199]
dr a[96:99],v234 offset:27648
mm v[196:199],a[104:107],a[28:31],v[196:199]
mm v[228:231],a[104:107],a[24:27],v[228:231]
dr a[100:103],v234 offset:29696
mm v[224:227],a[108:111],a[24:27],v[224:227]
dr a[104:107],v234 offset:31744
mm v[192:195],a[108:111],a[28:31],v[192:195]
mm v[192:195],a[112:115],a[20:23],v[192:195]
dr a[108:111],v234 offset:30720
mm v[224:227],a[112:115],a[16:19],v[224:227]
s_waitcnt lgkmcnt(4)
dr a[112:115],v234 offset:28672
mm v[224:227],a[116:119],a[8:11],v[224:227]
mm v[192:195],a[116:119],a[12:15],v[192:195]
dr a[116:119],v234 offset:26624
mm v[192:195],a[120:123],a[4:7],v[192:195]
mm v[224:227],a[120:123],a[0:3],v[224:227]
dr a[120:123],v234 offset:24576
mm v[224:227],a[124:127],a[32:35],v[224:227]
mm v[192:195],a[124:127],a[36:39],v[192:195]
dr a[124:127],v234 offset:32768
mm v[192:195],a[64:67],a[44:47],v[192:195]
mm v[224:227],a[64:67],a[40:43],v[224:227]
dr a[64:67],v234 offset:34816
mm v[224:227],a[68:71],a[48:51],v[224:227]
mm v[192:195],a[68:71],a[52:55],v[192:195]
dr a[68:71],v234 offset:36864
mm v[192:195],a[72:75],a[60:63],v[192:195]
mm v[224:227],a[72:75],a[56:59],v[224:227]
dr a[72:75],v234 offset:38912
mm v[228:231],a[76:79],a[56:59],v[228:231]
mm v[196:199],a[76:79],a[60:63],v[196:199]
dr a[76:79],v234 offset:39936
mm v[196:199],a[80:83],a[52:55],v[196:199]
mm v[228:231],a[80:83],a[48:51],v[228:231]
dr a[80:83],v234 offset:37888
mm v[228:231],a[84:87],a[40:43],v[228:231]
mm v[196:199],a[84:87],a[44:47],v[196:199]
dr a[84:87],v234 offset:35840
mm v[196:199],a[88:91],a[36:39],v[196:199]
buffer_load_dword v255,v251,s[8:11],s47 offen sc1
mm v[228:231],a[88:91],a[32:35],v[228:231]
dr a[88:91],v234 offset:33792
mm v[228:231],a[92:95],a[148:151],v[228:231]
mm v[196:199],a[92:95],a[152:155],v[196:199]
s_waitcnt lgkmcnt(3)
dr a[92:95],v234 offset:41984
mm v[196:199],a[96:99],a[160:163],v[196:199]
mm v[228:231],a[96:99],a[156:159],v[228:231]
dr a[96:99],v234 offset:44032
mm v[228:231],a[100:103],a[164:167],v[228:231]
mm v[196:199],a[100:103],a[168:171],v[196:199]
)" R"(dr a[100:103],v234 offset:46080
mm v[196:199],a[104:107],a[176:179],v[196:199]
mm v[228:231],a[104:107],a[172:175],v[228:231]
dr a[104:107],v234 offset:48128
mm v[224:227],a[108:111],a[172:175],v[224:227]
mm v[192:195],a[108:111],a[176:179],v[192:195]
dr a[108:111],v234 offset:47104
mm v[192:195],a[112:115],a[168:171],v[192:195]
mm v[224:227],a[112:115],a[164:167],v[224:227]
dr a[112:115],v234 offset:45056
mm v[224:227],a[116:119],a[156:159],v[224:227]
mm v[192:195],a[116:119],a[160:163],v[192:195]
dr a[116:119],v234 offset:43008
mm v[192:195],a[120:123],a[152:155],v[192:195]
mm v[224:227],a[120:123],a[148:151],v[224:227]
dr a[120:123],v234 offset:40960
mm v[208:211],a[124:127],v[0:3],0
mm v[216:219],a[124:127],v[32:35],0
dr a[124:127],v234 offset:49152
mm v[216:219],a[64:67],v[40:43],v[216:219]
mm v[208:211],a[64:67],v[8:11],v[208:211]
dr a[64:67],v234 offset:51200
cv v192,v192,v193
mm v[208:211],a[68:71],v[16:19],v[208:211]
cv v224,v224,v225
cv v225,v226,v227
mm v[216:219],a[68:71],v[48:51],v[216:219]
dr a[68:71],v234 offset:53248
cv v226,v228,v229
mm v[216:219],a[72:75],v[56:59],v[216:219]
cv v227,v230,v231
cv v193,v194,v195
bs v[224:227],v243,s[8:11],s64 offen offset:1024 sc1
cv v194,v196,v197
cv v195,v198,v199
mm v[208:211],a[72:75],v[24:27],v[208:211]
dr a[72:75],v234 offset:55296
mm v[212:215],a[76:79],v[24:27],0
mm v[220:223],a[76:79],v[56:59],0
s_waitcnt lgkmcnt(3)
bs v[192:195],v243,s[8:11],s65 offen offset:1024 sc1
dr a[76:79],v234 offset:56320
mm v[220:223],a[80:83],v[48:51],v[220:223]
mm v[212:215],a[80:83],v[16:19],v[212:215]
dr a[80:83],v234 offset:54272
mm v[212:215],a[84:87],v[8:11],v[212:215]
bl v[128:131],v232,s[8:11],s75 offen offset:0 sc1
mm v[220:223],a[84:87],v[40:43],v[220:223]
dr a[84:87],v234 offset:52224
mm v[220:223],a[88:91],v[32:35],v[220:223]
mm v[212:215],a[88:91],v[0:3],v[212:215]
dr a[88:91],v234 offset:50176
mm v[212:215],a[92:95],a[0:3],v[212:215]
bl v[136:139],v232,s[8:11],s75 offen offset:1024 sc1
mm v[220:223],a[92:95],a[4:7],v[220:223]
dr a[92:95],v234 offset:58368
mm v[220:223],a[96:99],a[12:15],v[220:223]
mm v[212:215],a[96:99],a[8:11],v[212:215]
dr a[96:99],v234 offset:60416
mm v[212:215],a[100:103],a[16:19],v[212:215]
mm v[220:223],a[100:103],a[20:23],v[220:223]
dr a[100:103],v234 offset:62464
mm v[220:223],a[104:107],a[28:31],v[220:223]
bl v[144:147],v232,s[8:11],s75 offen offset:2048 sc1
)" R"(mm v[212:215],a[104:107],a[24:27],v[212:215]
dr a[104:107],v234 offset:64512
mm v[208:211],a[108:111],a[24:27],v[208:211]
mm v[216:219],a[108:111],a[28:31],v[216:219]
dr a[108:111],v234 offset:63488
mm v[216:219],a[112:115],a[20:23],v[216:219]
mm v[208:211],a[112:115],a[16:19],v[208:211]
mm v[208:211],a[116:119],a[8:11],v[208:211]
mm v[216:219],a[116:119],a[12:15],v[216:219]
bl v[152:155],v232,s[8:11],s75 offen offset:3072 sc1
mm v[216:219],a[120:123],a[4:7],v[216:219]
mm v[208:211],a[120:123],a[0:3],v[208:211]
mm v[208:211],a[124:127],a[32:35],v[208:211]
s_waitcnt vmcnt(22)
s_barrier
mm v[216:219],a[124:127],a[36:39],v[216:219]
s_waitcnt lgkmcnt(0)
dr a[112:115],v234 offset:61440
mm v[216:219],a[64:67],a[44:47],v[216:219]
dr a[116:119],v234 offset:59392
mm v[208:211],a[64:67],a[40:43],v[208:211]
mm v[208:211],a[68:71],a[48:51],v[208:211]
bl v[160:163],v232,s[8:11],s76 offen offset:0 sc1
mm v[216:219],a[68:71],a[52:55],v[216:219]
dr a[120:123],v234 offset:57344
mm v[216:219],a[72:75],a[60:63],v[216:219]
dr a[124:127],v232 offset:49152
mm v[208:211],a[72:75],a[56:59],v[208:211]
mm v[212:215],a[76:79],a[56:59],v[212:215]
dr a[64:67],v232 offset:51200
mm v[220:223],a[76:79],a[60:63],v[220:223]
dr a[68:71],v232 offset:53248
mm v[220:223],a[80:83],a[52:55],v[220:223]
mm v[212:215],a[80:83],a[48:51],v[212:215]
bl v[168:171],v232,s[8:11],s76 offen offset:1024 sc1
mm v[212:215],a[84:87],a[40:43],v[212:215]
dr a[72:75],v232 offset:55296
mm v[220:223],a[84:87],a[44:47],v[220:223]
dr a[76:79],v232 offset:56320
mm v[220:223],a[88:91],a[36:39],v[220:223]
mm v[212:215],a[88:91],a[32:35],v[212:215]
dr a[80:83],v232 offset:54272
mm v[212:215],a[92:95],a[148:151],v[212:215]
dr a[84:87],v232 offset:52224
mm v[220:223],a[92:95],a[152:155],v[220:223]
mm v[220:223],a[96:99],a[160:163],v[220:223]
bl v[176:179],v232,s[8:11],s76 offen offset:2048 sc1
mm v[212:215],a[96:99],a[156:159],v[212:215]
dr a[88:91],v232 offset:50176
mm v[212:215],a[100:103],a[164:167],v[212:215]
dr a[92:95],v232 offset:58368
mm v[220:223],a[100:103],a[168:171],v[220:223]
mm v[220:223],a[104:107],a[176:179],v[220:223]
dr a[96:99],v232 offset:60416
mm v[212:215],a[104:107],a[172:175],v[212:215]
dr a[100:103],v232 offset:62464
mm v[208:211],a[108:111],a[172:175],v[208:211]
mm v[216:219],a[108:111],a[176:179],v[216:219]
s_waitcnt lgkmcnt(4)
bl v[184:187],v232,s[8:11],s76 offen offset:3072 sc1
mm v[216:219],a[112:115],a[168:171],v[216:219]
dr a[104:107],v232 offset:64512
mm v[208:211],a[112:115],a[164:167],v[208:211]
dr a[108:111],v232 offset:63488
)" R"(mm v[208:211],a[116:119],a[156:159],v[208:211]
mm v[216:219],a[116:119],a[160:163],v[216:219]
dr a[112:115],v232 offset:61440
mm v[216:219],a[120:123],a[152:155],v[216:219]
dr a[116:119],v232 offset:59392
mm v[208:211],a[120:123],a[148:151],v[208:211]
mm v[224:227],a[124:127],v[0:3],0
bl v[200:203],v232,s[8:11],s77 offen offset:0 sc1
mm v[192:195],a[124:127],v[32:35],0
dr a[120:123],v232 offset:57344
mm v[192:195],a[64:67],v[40:43],v[192:195]
dr a[124:127],v235 offset:0
cv v216,v216,v217
mm v[224:227],a[64:67],v[8:11],v[224:227]
cv v208,v208,v209
cv v209,v210,v211
mm v[224:227],a[68:71],v[16:19],v[224:227]
dr a[64:67],v235 offset:2048
cv v210,v212,v213
mm v[192:195],a[68:71],v[48:51],v[192:195]
dr a[68:71],v235 offset:4096
cv v211,v214,v215
mm v[192:195],a[72:75],v[56:59],v[192:195]
bs v[208:211],v243,s[8:11],s64 offen offset:2048 sc1
cv v217,v218,v219
cv v218,v220,v221
cv v219,v222,v223
mm v[224:227],a[72:75],v[24:27],v[224:227]
dr a[72:75],v235 offset:6144
mm v[228:231],a[76:79],v[24:27],0
mm v[196:199],a[76:79],v[56:59],0
bs v[216:219],v243,s[8:11],s65 offen offset:2048 sc1
dr a[76:79],v235 offset:7168
mm v[196:199],a[80:83],v[48:51],v[196:199]
mm v[228:231],a[80:83],v[16:19],v[228:231]
dr a[80:83],v235 offset:5120
mm v[228:231],a[84:87],v[8:11],v[228:231]
bl v[84:87],v232,s[8:11],s77 offen offset:1024 sc1
mm v[196:199],a[84:87],v[40:43],v[196:199]
s_waitcnt lgkmcnt(3)
dr a[84:87],v235 offset:3072
mm v[196:199],a[88:91],v[32:35],v[196:199]
mm v[228:231],a[88:91],v[0:3],v[228:231]
dr a[88:91],v235 offset:1024
mm v[228:231],a[92:95],a[0:3],v[228:231]
bl v[100:103],v232,s[8:11],s77 offen offset:2048 sc1
mm v[196:199],a[92:95],a[4:7],v[196:199]
dr a[92:95],v235 offset:9216
mm v[196:199],a[96:99],a[12:15],v[196:199]
mm v[228:231],a[96:99],a[8:11],v[228:231]
dr a[96:99],v235 offset:11264
mm v[228:231],a[100:103],a[16:19],v[228:231]
mm v[196:199],a[100:103],a[20:23],v[196:199]
bl v[116:119],v232,s[8:11],s77 offen offset:3072 sc1
mm v[196:199],a[104:107],a[28:31],v[196:199]
dr a[100:103],v235 offset:13312
mm v[228:231],a[104:107],a[24:27],v[228:231]
dr a[104:107],v235 offset:15360
mm v[224:227],a[108:111],a[24:27],v[224:227]
mm v[192:195],a[108:111],a[28:31],v[192:195]
dr a[108:111],v235 offset:14336
mm v[192:195],a[112:115],a[20:23],v[192:195]
mm v[224:227],a[112:115],a[16:19],v[224:227]
dr a[112:115],v235 offset:12288
)" R"(mm v[224:227],a[116:119],a[8:11],v[224:227]
bl v[132:135],v233,s[8:11],s75 offen offset:0 sc1
mm v[192:195],a[116:119],a[12:15],v[192:195]
dr a[116:119],v235 offset:10240
mm v[192:195],a[120:123],a[4:7],v[192:195]
mm v[224:227],a[120:123],a[0:3],v[224:227]
dr a[120:123],v235 offset:8192
mm v[224:227],a[124:127],a[32:35],v[224:227]
mm v[192:195],a[124:127],a[36:39],v[192:195]
dr a[124:127],v235 offset:16384
mm v[192:195],a[64:67],a[44:47],v[192:195]
mm v[224:227],a[64:67],a[40:43],v[224:227]
bl v[140:143],v233,s[8:11],s75 offen offset:1024 sc1
mm v[224:227],a[68:71],a[48:51],v[224:227]
dr a[64:67],v235 offset:18432
mm v[192:195],a[68:71],a[52:55],v[192:195]
s_waitcnt lgkmcnt(3)
dr a[68:71],v235 offset:20480
mm v[192:195],a[72:75],a[60:63],v[192:195]
mm v[224:227],a[72:75],a[56:59],v[224:227]
dr a[72:75],v235 offset:22528
mm v[228:231],a[76:79],a[56:59],v[228:231]
mm v[196:199],a[76:79],a[60:63],v[196:199]
dr a[76:79],v235 offset:23552
mm v[196:199],a[80:83],a[52:55],v[196:199]
bl v[148:151],v233,s[8:11],s75 offen offset:2048 sc1
mm v[228:231],a[80:83],a[48:51],v[228:231]
dr a[80:83],v235 offset:21504
mm v[228:231],a[84:87],a[40:43],v[228:231]
s_mul_i32 s73,s57,25600
s_mul_i32 s101,s24,4352
s_add_u32 s73,s73,s101
mm v[196:199],a[84:87],a[44:47],v[196:199]
s_add_u32 s73,s73,s71
dr a[84:87],v235 offset:19456
mm v[196:199],a[88:91],a[36:39],v[196:199]
mm v[228:231],a[88:91],a[32:35],v[228:231]
dr a[88:91],v235 offset:17408
mm v[228:231],a[92:95],a[148:151],v[228:231]
mm v[196:199],a[92:95],a[152:155],v[196:199]
bl v[156:159],v233,s[8:11],s75 offen offset:3072 sc1
mm v[196:199],a[96:99],a[160:163],v[196:199]
dr a[92:95],v235 offset:25600
mm v[228:231],a[96:99],a[156:159],v[228:231]
dr a[96:99],v235 offset:27648
mm v[228:231],a[100:103],a[164:167],v[228:231]
mm v[196:199],a[100:103],a[168:171],v[196:199]
dr a[100:103],v235 offset:29696
mm v[196:199],a[104:107],a[176:179],v[196:199]
mm v[228:231],a[104:107],a[172:175],v[228:231]
dr a[104:107],v235 offset:31744
mm v[224:227],a[108:111],a[172:175],v[224:227]
bl v[164:167],v233,s[8:11],s76 offen offset:0 sc1
mm v[192:195],a[108:111],a[176:179],v[192:195]
dr a[108:111],v235 offset:30720
mm v[192:195],a[112:115],a[168:171],v[192:195]
mm v[224:227],a[112:115],a[164:167],v[224:227]
mm v[224:227],a[116:119],a[156:159],v[224:227]
mm v[192:195],a[116:119],a[160:163],v[192:195]
s_waitcnt lgkmcnt(2)
mm v[192:195],a[120:123],a[152:155],v[192:195]
mm v[224:227],a[120:123],a[148:151],v[224:227]
bl v[172:175],v233,s[8:11],s76 offen offset:1024 sc1
mm v[208:211],a[124:127],v[0:3],0
)" R"(s_waitcnt vmcnt(26)
s_barrier
mm v[216:219],a[124:127],v[32:35],0
dr a[112:115],v235 offset:28672
mm v[216:219],a[64:67],v[40:43],v[216:219]
dr a[116:119],v235 offset:26624
cv v224,v224,v225
mm v[208:211],a[64:67],v[8:11],v[208:211]
cv v225,v226,v227
cv v226,v228,v229
mm v[208:211],a[68:71],v[16:19],v[208:211]
dr a[120:123],v235 offset:24576
cv v227,v230,v231
mm v[216:219],a[68:71],v[48:51],v[216:219]
bs v[224:227],v243,s[8:11],s64 offen offset:3072 sc1
cv v192,v192,v193
cv v193,v194,v195
cv v194,v196,v197
cv v195,v198,v199
mm v[216:219],a[72:75],v[56:59],v[216:219]
dr a[124:127],v232 offset:0
mm v[208:211],a[72:75],v[24:27],v[208:211]
dr a[64:67],v232 offset:2048
mm v[212:215],a[76:79],v[24:27],0
bs v[192:195],v243,s[8:11],s65 offen offset:3072 sc1
mm v[220:223],a[76:79],v[56:59],0
dr a[68:71],v232 offset:4096
mm v[220:223],a[80:83],v[48:51],v[220:223]
dr a[72:75],v232 offset:6144
mm v[212:215],a[80:83],v[16:19],v[212:215]
mm v[212:215],a[84:87],v[8:11],v[212:215]
s_add_u32 s41,s71,98304
bl v[180:183],v233,s[8:11],s76 offen offset:2048 sc1
mm v[220:223],a[84:87],v[40:43],v[220:223]
s_add_u32 m0,s34,16384
dr a[76:79],v232 offset:7168
mm v[220:223],a[88:91],v[32:35],v[220:223]
dr a[80:83],v232 offset:5120
mm v[212:215],a[88:91],v[0:3],v[212:215]
mm v[212:215],a[92:95],a[0:3],v[212:215]
bl v[188:191],v233,s[8:11],s76 offen offset:3072 sc1
mm v[220:223],a[92:95],a[4:7],v[220:223]
dr a[84:87],v232 offset:3072
mm v[220:223],a[96:99],a[12:15],v[220:223]
dr a[88:91],v232 offset:1024
mm v[212:215],a[96:99],a[8:11],v[212:215]
mm v[212:215],a[100:103],a[16:19],v[212:215]
bl v[204:207],v233,s[8:11],s77 offen offset:0 sc1
mm v[220:223],a[100:103],a[20:23],v[220:223]
s_waitcnt lgkmcnt(2)
dr a[92:95],v232 offset:9216
mm v[220:223],a[104:107],a[28:31],v[220:223]
dr a[96:99],v232 offset:11264
mm v[212:215],a[104:107],a[24:27],v[212:215]
mm v[208:211],a[108:111],a[24:27],v[208:211]
dr a[100:103],v232 offset:13312
mm v[216:219],a[108:111],a[28:31],v[216:219]
dr a[104:107],v232 offset:15360
mm v[216:219],a[112:115],a[20:23],v[216:219]
ED_3:
s_cbranch_execz ED_R
mm v[208:211],a[112:115],a[16:19],v[208:211]
bl v[92:95],v233,s[8:11],s77 offen offset:1024 sc1
mm v[208:211],a[116:119],a[8:11],v[208:211]
)" R"(dr a[108:111],v232 offset:14336
mm v[216:219],a[116:119],a[12:15],v[216:219]
dr a[112:115],v232 offset:12288
mm v[216:219],a[120:123],a[4:7],v[216:219]
mm v[208:211],a[120:123],a[0:3],v[208:211]
dr a[116:119],v232 offset:10240
mm v[208:211],a[124:127],a[32:35],v[208:211]
s_mov_b32 s2,EE_0-EE_G
dr a[120:123],v232 offset:8192
s_getpc_b64 s[0:1]
EE_G:
mm v[216:219],a[124:127],a[36:39],v[216:219]
s_cmp_eq_u32 s24,1
s_cselect_b32 s2,EE_1-EE_G,s2
mm v[216:219],a[64:67],a[44:47],v[216:219]
s_cmp_eq_u32 s24,2
s_cselect_b32 s2,EE_2-EE_G,s2
dr a[124:127],v232 offset:16384
mm v[208:211],a[64:67],a[40:43],v[208:211]
s_cmp_eq_u32 s24,3
s_cselect_b32 s2,EE_3-EE_G,s2
dr a[64:67],v232 offset:18432
mm v[208:211],a[68:71],a[48:51],v[208:211]
s_add_u32 s0,s0,s2
s_addc_u32 s1,s1,0
bl v[108:111],v233,s[8:11],s77 offen offset:2048 sc1
mm v[216:219],a[68:71],a[52:55],v[216:219]
dr a[68:71],v232 offset:20480
mm v[216:219],a[72:75],a[60:63],v[216:219]
mm v[208:211],a[72:75],a[56:59],v[208:211]
dr a[72:75],v232 offset:22528
mm v[212:215],a[76:79],a[56:59],v[212:215]
mm v[220:223],a[76:79],a[60:63],v[220:223]
bl v[124:127],v233,s[8:11],s77 offen offset:3072 sc1
mm v[220:223],a[80:83],a[52:55],v[220:223]
dr a[76:79],v232 offset:23552
mm v[212:215],a[80:83],a[48:51],v[212:215]
s_waitcnt lgkmcnt(3)
dr a[80:83],v232 offset:21504
mm v[212:215],a[84:87],a[40:43],v[212:215]
mm v[220:223],a[84:87],a[44:47],v[220:223]
dr a[84:87],v232 offset:19456
mm v[220:223],a[88:91],a[36:39],v[220:223]
mm v[212:215],a[88:91],a[32:35],v[212:215]
dr a[88:91],v232 offset:17408
mm v[212:215],a[92:95],a[148:151],v[212:215]
mm v[220:223],a[92:95],a[152:155],v[220:223]
dr a[92:95],v232 offset:25600
mm v[220:223],a[96:99],a[160:163],v[220:223]
mm v[212:215],a[96:99],a[156:159],v[212:215]
dr a[96:99],v232 offset:27648
mm v[212:215],a[100:103],a[164:167],v[212:215]
mm v[220:223],a[100:103],a[168:171],v[220:223]
dr a[100:103],v232 offset:29696
mm v[220:223],a[104:107],a[176:179],v[220:223]
mm v[212:215],a[104:107],a[172:175],v[212:215]
dr a[104:107],v232 offset:31744
mm v[208:211],a[108:111],a[172:175],v[208:211]
mm v[216:219],a[108:111],a[176:179],v[216:219]
dr a[108:111],v232 offset:30720
mm v[216:219],a[112:115],a[168:171],v[216:219]
mm v[208:211],a[112:115],a[164:167],v[208:211]
dr a[112:115],v232 offset:28672
mm v[208:211],a[116:119],a[156:159],v[208:211]
)" R"(mm v[216:219],a[116:119],a[160:163],v[216:219]
dr a[116:119],v232 offset:26624
mm v[216:219],a[120:123],a[152:155],v[216:219]
s_mov_b64 exec,0
s_setpc_b64 s[0:1]
EE_R:
s_mov_b64 exec,-1
mm v[208:211],a[120:123],a[148:151],v[208:211]
dr a[120:123],v232 offset:24576
mm v[224:227],a[124:127],v[64:67],0
mm v[192:195],a[124:127],v[96:99],0
dr a[124:127],v232 offset:32768
cv v216,v216,v217
mm v[192:195],a[64:67],v[104:107],v[192:195]
cv v217,v218,v219
cv v218,v220,v221
mm v[224:227],a[64:67],v[72:75],v[224:227]
s_waitcnt lgkmcnt(3)
dr a[64:67],v232 offset:34816
cv v208,v208,v209
mm v[224:227],a[68:71],v[80:83],v[224:227]
cv v209,v210,v211
cv v210,v212,v213
mm v[192:195],a[68:71],v[112:115],v[192:195]
dr a[68:71],v232 offset:36864
cv v211,v214,v215
mm v[192:195],a[72:75],v[120:123],v[192:195]
s_add_u32 s47,s64,4096
bs v[208:211],v243,s[8:11],s47 offen offset:0 sc1
cv v219,v222,v223
mm v[224:227],a[72:75],v[88:91],v[224:227]
dr a[72:75],v232 offset:38912
mm v[228:231],a[76:79],v[88:91],0
mm v[196:199],a[76:79],v[120:123],0
s_add_u32 s47,s65,4096
bs v[216:219],v243,s[8:11],s47 offen offset:0 sc1
s_waitcnt vmcnt(15)
v_bitop3_b32 v250,v128,v132,1 bitop3:0x80
s_waitcnt vmcnt(14)
v_bitop3_b32 v250,v136,v140,v250 bitop3:0x80
s_waitcnt vmcnt(13)
v_bitop3_b32 v250,v144,v148,v250 bitop3:0x80
mm v[196:199],a[80:83],v[112:115],v[196:199]
dr a[76:79],v232 offset:39936
v_add_u32 v243,0xa00000,v243
mm v[228:231],a[80:83],v[80:83],v[228:231]
dr a[80:83],v232 offset:37888
mm v[228:231],a[84:87],v[72:75],v[228:231]
s_waitcnt vmcnt(12)
v_bitop3_b32 v250,v152,v156,v250 bitop3:0x80
s_waitcnt vmcnt(11)
v_bitop3_b32 v250,v160,v164,v250 bitop3:0x80
s_waitcnt vmcnt(10)
v_bitop3_b32 v250,v168,v172,v250 bitop3:0x80
s_waitcnt vmcnt(7)
v_bitop3_b32 v250,v176,v180,v250 bitop3:0x80
s_waitcnt vmcnt(6)
v_bitop3_b32 v250,v184,v188,v250 bitop3:0x80
s_waitcnt vmcnt(5)
v_bitop3_b32 v250,v200,v204,v250 bitop3:0x80
s_waitcnt vmcnt(4)
v_bitop3_b32 v250,v84,v92,v250 bitop3:0x80
s_waitcnt vmcnt(3)
v_bitop3_b32 v250,v100,v108,v250 bitop3:0x80
)" R"(s_waitcnt vmcnt(2)
v_bitop3_b32 v250,v116,v124,v250 bitop3:0x80
mm v[196:199],a[84:87],v[104:107],v[196:199]
dr a[84:87],v232 offset:35840
mm v[196:199],a[88:91],v[96:99],v[196:199]
v_cmp_ne_u32 vcc,1,v250
s_cbranch_vccnz CH1
CH1_D:
mm v[228:231],a[88:91],v[64:67],v[228:231]
dr a[88:91],v232 offset:33792
v_min_u32 v250,784,v232
mm v[228:231],a[92:95],v[128:131],v[228:231]
v_lshlrev_b32 v250,3,v250
mm v[196:199],a[92:95],v[132:135],v[196:199]
buffer_load_dword v255,v250,s[8:11],s73 offen sc1
mm v[196:199],a[96:99],v[140:143],v[196:199]
dr a[92:95],v232 offset:41984
mm v[228:231],a[96:99],v[136:139],v[228:231]
dr a[96:99],v232 offset:44032
mm v[228:231],a[100:103],v[144:147],v[228:231]
s_mov_b32 s52,0x30003
v_lshrrev_b32 v250,8,v232
v_and_b32 v251,0xf0,v232
mm v[196:199],a[100:103],v[148:151],v[196:199]
s_mov_b32 s53,0x30003
dr a[100:103],v232 offset:46080
v_lshl_add_u32 v57,v250,2,v251
mm v[196:199],a[104:107],v[156:159],v[196:199]
s_mov_b32 s86,0xc000c
v_and_b32 v251,0x80,v251
v_lshl_add_u32 v56,v251,23,v247
mm v[228:231],a[104:107],v[152:155],v[228:231]
s_mov_b32 s87,0xc000c
dr a[104:107],v232 offset:48128
v_lshrrev_b32 v250,6,v232
mm v[224:227],a[108:111],v[152:155],v[224:227]
v_and_b32 v251,0x30,v232
v_mul_u32_u24 v246,5120,v250
mm v[192:195],a[108:111],v[156:159],v[192:195]
dr a[108:111],v232 offset:47104
v_add_u32 v246,v251,v246
mm v[192:195],a[112:115],v[148:151],v[192:195]
v_lshrrev_b32 v246,1,v246
v_add_lshl_u32 v58,v250,v251,2
mm v[224:227],a[112:115],v[144:147],v[224:227]
s_waitcnt lgkmcnt(3)
dr a[112:115],v232 offset:45056
v_and_b32 v251,16,v232
mm v[224:227],a[116:119],v[136:139],v[224:227]
v_mov_b32 v48,0x3f80
v_lshlrev_b32 v251,v251,v48
mm v[192:195],a[116:119],v[140:143],v[192:195]
dr a[116:119],v232 offset:43008
v_cndmask_b32_e64 v40,0,v251,s[52:53]
mm v[192:195],a[120:123],v[132:135],v[192:195]
s_mov_b32 s52,0x300030
v_cndmask_b32_e64 v41,0,v251,s[86:87]
s_mov_b32 s53,0x300030
mm v[224:227],a[120:123],v[128:131],v[224:227]
s_mov_b32 s86,0xc000c0
dr a[120:123],v232 offset:40960
v_cndmask_b32_e64 v42,0,v251,s[52:53]
mm v[224:227],a[124:127],v[160:163],v[224:227]
s_mov_b32 s87,0xc000c0
)" R"(v_cndmask_b32_e64 v43,0,v251,s[86:87]
s_mov_b32 s52,0x3000300
mm v[192:195],a[124:127],v[164:167],v[192:195]
s_mov_b32 s53,0x3000300
dr a[124:127],v234 offset:0
v_cndmask_b32_e64 v48,0,v251,s[52:53]
mm v[192:195],a[64:67],v[172:175],v[192:195]
s_mov_b32 s86,0xc000c00
s_mov_b32 s87,0xc000c00
v_cndmask_b32_e64 v49,0,v251,s[86:87]
mm v[224:227],a[64:67],v[168:171],v[224:227]
s_mov_b32 s52,0x30003000
dr a[64:67],v234 offset:2048
s_mov_b32 s53,0x30003000
mm v[224:227],a[68:71],v[176:179],v[224:227]
s_mov_b32 s86,0xc000c000
v_cndmask_b32_e64 v50,0,v251,s[52:53]
s_mov_b32 s87,0xc000c000
mm v[192:195],a[68:71],v[180:183],v[192:195]
dr a[68:71],v234 offset:4096
v_cndmask_b32_e64 v51,0,v251,s[86:87]
mm v[192:195],a[72:75],v[188:191],v[192:195]
mm v[224:227],a[72:75],v[184:187],v[224:227]
dr a[72:75],v234 offset:6144
mm v[228:231],a[76:79],v[184:187],v[228:231]
mm v[196:199],a[76:79],v[188:191],v[196:199]
mm v[196:199],a[80:83],v[180:183],v[196:199]
mm v[228:231],a[80:83],v[176:179],v[228:231]
mm v[228:231],a[84:87],v[168:171],v[228:231]
mm v[196:199],a[84:87],v[172:175],v[196:199]
mm v[196:199],a[88:91],v[164:167],v[196:199]
s_waitcnt lgkmcnt(4)
s_barrier
mm v[228:231],a[88:91],v[160:163],v[228:231]
bl v232,s[8:11],s41 offen offset:0 sc1 lds
mm v[228:231],a[92:95],v[200:203],v[228:231]
s_add_u32 s41,s71,98304
dr a[76:79],v234 offset:7168
s_add_u32 m0,s34,16384
mm v[196:199],a[92:95],v[204:207],v[196:199]
dr a[80:83],v234 offset:5120
mm v[196:199],a[96:99],v[92:95],v[196:199]
mm v[228:231],a[96:99],v[84:87],v[228:231]
bl v232,s[8:11],s41 offen offset:1024 sc1 lds
mm v[228:231],a[100:103],v[100:103],v[228:231]
s_add_u32 s41,s71,0
dr a[84:87],v234 offset:3072
s_add_u32 m0,s34,24576
mm v[196:199],a[100:103],v[108:111],v[196:199]
dr a[88:91],v234 offset:1024
mm v[196:199],a[104:107],v[124:127],v[196:199]
mm v[228:231],a[104:107],v[116:119],v[228:231]
bl v232,s[8:11],s41 offen offset:0 sc1 lds
mm v[224:227],a[108:111],v[116:119],v[224:227]
s_add_u32 s41,s71,0
dr a[92:95],v234 offset:9216
s_add_u32 m0,s34,24576
mm v[192:195],a[108:111],v[124:127],v[192:195]
dr a[96:99],v234 offset:11264
mm v[192:195],a[112:115],v[108:111],v[192:195]
mm v[224:227],a[112:115],v[100:103],v[224:227]
bl v232,s[8:11],s41 offen offset:1024 sc1 lds
mm v[224:227],a[116:119],v[84:87],v[224:227]
s_add_u32 s41,s71,8192
)" R"(dr a[100:103],v234 offset:13312
s_add_u32 m0,s34,32768
mm v[192:195],a[116:119],v[92:95],v[192:195]
dr a[104:107],v234 offset:15360
mm v[192:195],a[120:123],v[204:207],v[192:195]
mm v[224:227],a[120:123],v[200:203],v[224:227]
s_waitcnt lgkmcnt(3)
bl v232,s[8:11],s41 offen offset:0 sc1 lds
mm v[208:211],a[124:127],v[64:67],0
s_add_u32 s41,s71,8192
dr a[108:111],v234 offset:14336
s_add_u32 m0,s34,32768
mm v[216:219],a[124:127],v[96:99],0
dr a[112:115],v234 offset:12288
cv v224,v224,v225
mm v[216:219],a[64:67],v[104:107],v[216:219]
cv v225,v226,v227
v_or_b32 v224,1,v224
mm v[208:211],a[64:67],v[72:75],v[208:211]
dr a[116:119],v234 offset:10240
cv v226,v228,v229
mm v[208:211],a[68:71],v[80:83],v[208:211]
dr a[120:123],v234 offset:8192
cv v227,v230,v231
mm v[216:219],a[68:71],v[112:115],v[216:219]
bs v[224:227],v243,s[8:11],s64 offen offset:0 sc1
cv v192,v192,v193
cv v193,v194,v195
v_or_b32 v192,1,v192
cv v194,v196,v197
cv v195,v198,v199
mm v[216:219],a[72:75],v[120:123],v[216:219]
dr a[124:127],v234 offset:16384
mm v[208:211],a[72:75],v[88:91],v[208:211]
dr a[64:67],v234 offset:18432
mm v[212:215],a[76:79],v[88:91],0
bs v[192:195],v243,s[8:11],s65 offen offset:0 sc1
mm v[220:223],a[76:79],v[120:123],0
dr a[68:71],v234 offset:20480
mm v[220:223],a[80:83],v[112:115],v[220:223]
dr a[72:75],v234 offset:22528
mm v[212:215],a[80:83],v[80:83],v[212:215]
mm v[212:215],a[84:87],v[72:75],v[212:215]
bl v232,s[8:11],s41 offen offset:1024 sc1 lds
mm v[220:223],a[84:87],v[104:107],v[220:223]
s_add_u32 s41,s71,16384
dr a[76:79],v234 offset:23552
s_add_u32 m0,s34,40960
mm v[220:223],a[88:91],v[96:99],v[220:223]
dr a[80:83],v234 offset:21504
mm v[212:215],a[88:91],v[64:67],v[212:215]
mm v[212:215],a[92:95],v[128:131],v[212:215]
bl v232,s[8:11],s41 offen offset:0 sc1 lds
mm v[220:223],a[92:95],v[132:135],v[220:223]
s_waitcnt lgkmcnt(2)
dr a[84:87],v234 offset:19456
s_add_u32 s41,s71,16384
mm v[220:223],a[96:99],v[140:143],v[220:223]
s_add_u32 m0,s34,40960
dr a[88:91],v234 offset:17408
mm v[212:215],a[96:99],v[136:139],v[212:215]
mm v[212:215],a[100:103],v[144:147],v[212:215]
bl v232,s[8:11],s41 offen offset:1024 sc1 lds
mm v[220:223],a[100:103],v[148:151],v[220:223]
)" R"(s_add_u32 s41,s71,24576
dr a[92:95],v234 offset:25600
s_add_u32 m0,s34,65536
mm v[220:223],a[104:107],v[156:159],v[220:223]
dr a[96:99],v234 offset:27648
mm v[212:215],a[104:107],v[152:155],v[212:215]
mm v[208:211],a[108:111],v[152:155],v[208:211]
dr a[100:103],v234 offset:29696
mm v[216:219],a[108:111],v[156:159],v[216:219]
dr a[104:107],v234 offset:31744
mm v[216:219],a[112:115],v[148:151],v[216:219]
mm v[208:211],a[112:115],v[144:147],v[208:211]
dr a[108:111],v234 offset:30720
mm v[208:211],a[116:119],v[136:139],v[208:211]
dr a[112:115],v234 offset:28672
mm v[216:219],a[116:119],v[140:143],v[216:219]
mm v[216:219],a[120:123],v[132:135],v[216:219]
dr a[116:119],v234 offset:26624
mm v[208:211],a[120:123],v[128:131],v[208:211]
dr a[120:123],v234 offset:24576
mm v[208:211],a[124:127],v[160:163],v[208:211]
mm v[216:219],a[124:127],v[164:167],v[216:219]
dr a[124:127],v234 offset:32768
mm v[216:219],a[64:67],v[172:175],v[216:219]
mm v[208:211],a[64:67],v[168:171],v[208:211]
dr a[64:67],v234 offset:34816
mm v[208:211],a[68:71],v[176:179],v[208:211]
s_waitcnt vmcnt(11)
buffer_store_dwordx2 v[252:253],v254,s[8:11],s67 offen sc1
mm v[216:219],a[68:71],v[180:183],v[216:219]
dr a[68:71],v234 offset:36864
mm v[216:219],a[72:75],v[188:191],v[216:219]
mm v[208:211],a[72:75],v[184:187],v[208:211]
s_waitcnt lgkmcnt(3)
dr a[72:75],v234 offset:38912
mm v[212:215],a[76:79],v[184:187],v[212:215]
mm v[220:223],a[76:79],v[188:191],v[220:223]
mm v[220:223],a[80:83],v[180:183],v[220:223]
mm v[212:215],a[80:83],v[176:179],v[212:215]
mm v[212:215],a[84:87],v[168:171],v[212:215]
mm v[220:223],a[84:87],v[172:175],v[220:223]
mm v[220:223],a[88:91],v[164:167],v[220:223]
s_barrier
dr a[76:79],v234 offset:39936
mm v[212:215],a[88:91],v[160:163],v[212:215]
bl v232,s[8:11],s41 offen offset:0 sc1 lds
mm v[212:215],a[92:95],v[200:203],v[212:215]
s_add_u32 s41,s71,24576
dr a[80:83],v234 offset:37888
s_add_u32 m0,s34,65536
mm v[220:223],a[92:95],v[204:207],v[220:223]
dr a[84:87],v234 offset:35840
mm v[220:223],a[96:99],v[92:95],v[220:223]
mm v[212:215],a[96:99],v[84:87],v[212:215]
bl v232,s[8:11],s41 offen offset:1024 sc1 lds
mm v[212:215],a[100:103],v[100:103],v[212:215]
s_add_u32 s41,s71,32768
dr a[88:91],v234 offset:33792
s_add_u32 m0,s34,73728
mm v[220:223],a[100:103],v[108:111],v[220:223]
dr a[92:95],v234 offset:41984
mm v[220:223],a[104:107],v[124:127],v[220:223]
mm v[212:215],a[104:107],v[116:119],v[212:215]
bl v232,s[8:11],s41 offen offset:0 sc1 lds
)" R"(mm v[208:211],a[108:111],v[116:119],v[208:211]
s_add_u32 s41,s71,32768
dr a[96:99],v234 offset:44032
s_add_u32 m0,s34,73728
mm v[216:219],a[108:111],v[124:127],v[216:219]
s_mov_b32 s46,s68
dr a[100:103],v234 offset:46080
v_lshrrev_b32 v242,1,v232
mm v[216:219],a[112:115],v[108:111],v[216:219]
v_and_b32 v242,56,v242
mm v[208:211],a[112:115],v[100:103],v[208:211]
dr a[104:107],v234 offset:48128
mm v[208:211],a[116:119],v[84:87],v[208:211]
bl v[4:7],v239,s[8:11],s66 offen offset:0 sc1
mm v[216:219],a[116:119],v[92:95],v[216:219]
dr a[108:111],v234 offset:47104
mm v[216:219],a[120:123],v[204:207],v[216:219]
dr a[112:115],v234 offset:45056
mm v[208:211],a[120:123],v[200:203],v[208:211]
s_waitcnt lgkmcnt(4)
mm v[224:227],a[124:127],v[64:67],0
bl v[12:15],v239,s[8:11],s78 offen offset:0 sc1
mm v[192:195],a[124:127],v[96:99],0
dr a[116:119],v234 offset:43008
mm v[192:195],a[64:67],v[104:107],v[192:195]
dr a[120:123],v234 offset:40960
cv v216,v216,v217
mm v[224:227],a[64:67],v[72:75],v[224:227]
cv v208,v208,v209
cv v209,v210,v211
mm v[224:227],a[68:71],v[80:83],v[224:227]
dr a[124:127],v234 offset:49152
v_or_b32 v208,1,v208
mm v[192:195],a[68:71],v[112:115],v[192:195]
dr a[64:67],v234 offset:51200
cv v210,v212,v213
mm v[192:195],a[72:75],v[120:123],v[192:195]
cv v211,v214,v215
cv v217,v218,v219
bs v[208:211],v243,s[8:11],s64 offen offset:1024 sc1
v_or_b32 v216,1,v216
cv v218,v220,v221
cv v219,v222,v223
mm v[224:227],a[72:75],v[88:91],v[224:227]
dr a[68:71],v234 offset:53248
mm v[228:231],a[76:79],v[88:91],0
dr a[72:75],v234 offset:55296
mm v[196:199],a[76:79],v[120:123],0
bs v[216:219],v243,s[8:11],s65 offen offset:1024 sc1
mm v[196:199],a[80:83],v[112:115],v[196:199]
dr a[76:79],v234 offset:56320
mm v[228:231],a[80:83],v[80:83],v[228:231]
dr a[80:83],v234 offset:54272
mm v[228:231],a[84:87],v[72:75],v[228:231]
bl v232,s[8:11],s41 offen offset:1024 sc1 lds
mm v[196:199],a[84:87],v[104:107],v[196:199]
s_add_u32 s41,s71,40960
dr a[84:87],v234 offset:52224
s_add_u32 m0,s34,81920
mm v[196:199],a[88:91],v[96:99],v[196:199]
mm v[228:231],a[88:91],v[64:67],v[228:231]
dr a[88:91],v234 offset:50176
mm v[228:231],a[92:95],v[128:131],v[228:231]
bl v232,s[8:11],s41 offen offset:0 sc1 lds
)" R"(mm v[196:199],a[92:95],v[132:135],v[196:199]
s_add_u32 s41,s71,40960
dr a[92:95],v234 offset:58368
s_add_u32 m0,s34,81920
mm v[196:199],a[96:99],v[140:143],v[196:199]
mm v[228:231],a[96:99],v[136:139],v[228:231]
s_waitcnt lgkmcnt(3)
dr a[96:99],v234 offset:60416
mm v[228:231],a[100:103],v[144:147],v[228:231]
bl v232,s[8:11],s41 offen offset:1024 sc1 lds
mm v[196:199],a[100:103],v[148:151],v[196:199]
s_add_u32 s41,s71,49152
dr a[100:103],v234 offset:62464
s_add_u32 m0,s34,90112
mm v[196:199],a[104:107],v[156:159],v[196:199]
mm v[228:231],a[104:107],v[152:155],v[228:231]
dr a[104:107],v234 offset:64512
mm v[224:227],a[108:111],v[152:155],v[224:227]
bl v232,s[8:11],s41 offen offset:0 sc1 lds
mm v[192:195],a[108:111],v[156:159],v[192:195]
s_add_u32 s41,s71,49152
dr a[108:111],v234 offset:63488
s_add_u32 m0,s34,90112
mm v[192:195],a[112:115],v[148:151],v[192:195]
mm v[224:227],a[112:115],v[144:147],v[224:227]
dr a[112:115],v234 offset:61440
mm v[224:227],a[116:119],v[136:139],v[224:227]
bl v232,s[8:11],s41 offen offset:1024 sc1 lds
mm v[192:195],a[116:119],v[140:143],v[192:195]
s_add_u32 s41,s71,57344
dr a[116:119],v234 offset:59392
s_add_u32 m0,s34,98304
mm v[192:195],a[120:123],v[132:135],v[192:195]
mm v[224:227],a[120:123],v[128:131],v[224:227]
dr a[120:123],v234 offset:57344
mm v[224:227],a[124:127],v[160:163],v[224:227]
mm v[192:195],a[124:127],v[164:167],v[192:195]
dr a[124:127],v232 offset:49152
mm v[192:195],a[64:67],v[172:175],v[192:195]
mm v[224:227],a[64:67],v[168:171],v[224:227]
dr a[64:67],v232 offset:51200
mm v[224:227],a[68:71],v[176:179],v[224:227]
mm v[192:195],a[68:71],v[180:183],v[192:195]
dr a[68:71],v232 offset:53248
mm v[192:195],a[72:75],v[188:191],v[192:195]
mm v[224:227],a[72:75],v[184:187],v[224:227]
dr a[72:75],v232 offset:55296
mm v[228:231],a[76:79],v[184:187],v[228:231]
mm v[196:199],a[76:79],v[188:191],v[196:199]
dr a[76:79],v232 offset:56320
mm v[196:199],a[80:83],v[180:183],v[196:199]
mm v[228:231],a[80:83],v[176:179],v[228:231]
s_waitcnt lgkmcnt(3)
dr a[80:83],v232 offset:54272
mm v[228:231],a[84:87],v[168:171],v[228:231]
mm v[196:199],a[84:87],v[172:175],v[196:199]
dr a[84:87],v232 offset:52224
mm v[196:199],a[88:91],v[164:167],v[196:199]
mm v[228:231],a[88:91],v[160:163],v[228:231]
dr a[88:91],v232 offset:50176
mm v[228:231],a[92:95],v[200:203],v[228:231]
mm v[196:199],a[92:95],v[204:207],v[196:199]
dr a[92:95],v232 offset:58368
mm v[196:199],a[96:99],v[92:95],v[196:199]
)" R"(mm v[228:231],a[96:99],v[84:87],v[228:231]
dr a[96:99],v232 offset:60416
mm v[228:231],a[100:103],v[100:103],v[228:231]
mm v[196:199],a[100:103],v[108:111],v[196:199]
dr a[100:103],v232 offset:62464
mm v[196:199],a[104:107],v[124:127],v[196:199]
mm v[228:231],a[104:107],v[116:119],v[228:231]
dr a[104:107],v232 offset:64512
mm v[224:227],a[108:111],v[116:119],v[224:227]
mm v[192:195],a[108:111],v[124:127],v[192:195]
mm v[192:195],a[112:115],v[108:111],v[192:195]
mm v[224:227],a[112:115],v[100:103],v[224:227]
mm v[224:227],a[116:119],v[84:87],v[224:227]
bl v[20:23],v239,s[8:11],s66 offen offset:1024 sc1
mm v[192:195],a[116:119],v[92:95],v[192:195]
mm v[192:195],a[120:123],v[204:207],v[192:195]
s_waitcnt lgkmcnt(4)
s_barrier
mm v[224:227],a[120:123],v[200:203],v[224:227]
dr a[108:111],v232 offset:63488
mm v[208:211],a[124:127],v[64:67],0
bl v[28:31],v239,s[8:11],s78 offen offset:1024 sc1
mm v[216:219],a[124:127],v[96:99],0
dr a[112:115],v232 offset:61440
cv v192,v192,v193
mm v[216:219],a[64:67],v[104:107],v[216:219]
EE_0:
s_cbranch_execz EE_R
dr a[116:119],v232 offset:59392
cv v224,v224,v225
mm v[208:211],a[64:67],v[72:75],v[208:211]
cv v225,v226,v227
v_or_b32 v224,1,v224
mm v[208:211],a[68:71],v[80:83],v[208:211]
dr a[120:123],v232 offset:57344
cv v226,v228,v229
mm v[216:219],a[68:71],v[112:115],v[216:219]
dr a[124:127],v235 offset:0
cv v227,v230,v231
mm v[216:219],a[72:75],v[120:123],v[216:219]
bs v[224:227],v243,s[8:11],s64 offen offset:2048 sc1
cv v193,v194,v195
v_or_b32 v192,1,v192
cv v194,v196,v197
cv v195,v198,v199
mm v[208:211],a[72:75],v[88:91],v[208:211]
dr a[64:67],v235 offset:2048
mm v[212:215],a[76:79],v[88:91],0
dr a[68:71],v235 offset:4096
mm v[220:223],a[76:79],v[120:123],0
bs v[192:195],v243,s[8:11],s65 offen offset:2048 sc1
mm v[220:223],a[80:83],v[112:115],v[220:223]
dr a[72:75],v235 offset:6144
mm v[212:215],a[80:83],v[80:83],v[212:215]
dr a[76:79],v235 offset:7168
mm v[212:215],a[84:87],v[72:75],v[212:215]
bl v232,s[8:11],s41 offen offset:0 sc1 lds
mm v[220:223],a[84:87],v[104:107],v[220:223]
s_add_u32 s41,s71,57344
dr a[80:83],v235 offset:5120
s_add_u32 m0,s34,98304
mm v[220:223],a[88:91],v[96:99],v[220:223]
dr a[84:87],v235 offset:3072
mm v[212:215],a[88:91],v[64:67],v[212:215]
)" R"(s_waitcnt lgkmcnt(4)
mm v[212:215],a[92:95],v[128:131],v[212:215]
bl v232,s[8:11],s41 offen offset:1024 sc1 lds
mm v[220:223],a[92:95],v[132:135],v[220:223]
s_add_u32 s41,s71,65536
dr a[88:91],v235 offset:1024
s_add_u32 m0,s34,106496
mm v[220:223],a[96:99],v[140:143],v[220:223]
dr a[92:95],v235 offset:9216
mm v[212:215],a[96:99],v[136:139],v[212:215]
mm v[212:215],a[100:103],v[144:147],v[212:215]
bl v232,s[8:11],s41 offen offset:0 sc1 lds
mm v[220:223],a[100:103],v[148:151],v[220:223]
s_add_u32 s41,s71,65536
dr a[96:99],v235 offset:11264
s_add_u32 m0,s34,106496
mm v[220:223],a[104:107],v[156:159],v[220:223]
dr a[100:103],v235 offset:13312
mm v[212:215],a[104:107],v[152:155],v[212:215]
mm v[208:211],a[108:111],v[152:155],v[208:211]
bl v232,s[8:11],s41 offen offset:1024 sc1 lds
mm v[216:219],a[108:111],v[156:159],v[216:219]
s_add_u32 s41,s71,73728
dr a[104:107],v235 offset:15360
s_add_u32 m0,s34,114688
mm v[216:219],a[112:115],v[148:151],v[216:219]
dr a[108:111],v235 offset:14336
mm v[208:211],a[112:115],v[144:147],v[208:211]
mm v[208:211],a[116:119],v[136:139],v[208:211]
buffer_load_dwordx2 v[240:241],v242,s[8:11],s46 offen sc1
mm v[216:219],a[116:119],v[140:143],v[216:219]
bl v232,s[8:11],s41 offen offset:0 sc1 lds
mm v[216:219],a[120:123],v[132:135],v[216:219]
s_add_u32 s41,s71,73728
dr a[112:115],v235 offset:12288
s_add_u32 m0,s34,114688
mm v[208:211],a[120:123],v[128:131],v[208:211]
dr a[116:119],v235 offset:10240
mm v[208:211],a[124:127],v[160:163],v[208:211]
mm v[216:219],a[124:127],v[164:167],v[216:219]
bl v232,s[8:11],s41 offen offset:1024 sc1 lds
mm v[216:219],a[64:67],v[172:175],v[216:219]
s_add_u32 s41,s71,81920
dr a[120:123],v235 offset:8192
s_add_u32 m0,s34,122880
mm v[208:211],a[64:67],v[168:171],v[208:211]
dr a[124:127],v235 offset:16384
mm v[208:211],a[68:71],v[176:179],v[208:211]
mm v[216:219],a[68:71],v[180:183],v[216:219]
s_waitcnt lgkmcnt(3)
bl v232,s[8:11],s41 offen offset:0 sc1 lds
mm v[216:219],a[72:75],v[188:191],v[216:219]
s_add_u32 s41,s71,81920
dr a[64:67],v235 offset:18432
s_add_u32 m0,s34,122880
mm v[208:211],a[72:75],v[184:187],v[208:211]
dr a[68:71],v235 offset:20480
mm v[212:215],a[76:79],v[184:187],v[212:215]
mm v[220:223],a[76:79],v[188:191],v[220:223]
bl v232,s[8:11],s41 offen offset:1024 sc1 lds
mm v[220:223],a[80:83],v[180:183],v[220:223]
s_add_u32 s41,s71,90112
dr a[72:75],v235 offset:22528
s_add_u32 m0,s34,49152
)" R"(mm v[212:215],a[80:83],v[176:179],v[212:215]
dr a[76:79],v235 offset:23552
mm v[212:215],a[84:87],v[168:171],v[212:215]
mm v[220:223],a[84:87],v[172:175],v[220:223]
bl v232,s[8:11],s41 offen offset:0 sc1 lds
mm v[220:223],a[88:91],v[164:167],v[220:223]
s_add_u32 s41,s71,90112
dr a[80:83],v235 offset:21504
s_add_u32 m0,s34,49152
mm v[212:215],a[88:91],v[160:163],v[212:215]
dr a[84:87],v235 offset:19456
mm v[212:215],a[92:95],v[200:203],v[212:215]
mm v[220:223],a[92:95],v[204:207],v[220:223]
bl v232,s[8:11],s41 offen offset:1024 sc1 lds
mm v[220:223],a[96:99],v[92:95],v[220:223]
dr a[88:91],v235 offset:17408
mm v[212:215],a[96:99],v[84:87],v[212:215]
dr a[92:95],v235 offset:25600
mm v[212:215],a[100:103],v[100:103],v[212:215]
mm v[220:223],a[100:103],v[108:111],v[220:223]
dr a[96:99],v235 offset:27648
mm v[220:223],a[104:107],v[124:127],v[220:223]
dr a[100:103],v235 offset:29696
mm v[212:215],a[104:107],v[116:119],v[212:215]
mm v[208:211],a[108:111],v[116:119],v[208:211]
dr a[104:107],v235 offset:31744
mm v[216:219],a[108:111],v[124:127],v[216:219]
dr a[108:111],v235 offset:30720
mm v[216:219],a[112:115],v[108:111],v[216:219]
mm v[208:211],a[112:115],v[100:103],v[208:211]
s_waitcnt lgkmcnt(4)
dr a[112:115],v235 offset:28672
mm v[208:211],a[116:119],v[84:87],v[208:211]
bl v[36:39],v239,s[8:11],s66 offen offset:2048 sc1
mm v[216:219],a[116:119],v[92:95],v[216:219]
dr a[116:119],v235 offset:26624
mm v[216:219],a[120:123],v[204:207],v[216:219]
mm v[208:211],a[120:123],v[200:203],v[208:211]
dr a[120:123],v235 offset:24576
mm v[224:227],a[124:127],v[64:67],0
bl v[44:47],v239,s[8:11],s78 offen offset:2048 sc1
mm v[192:195],a[124:127],v[96:99],0
dr a[124:127],v232 offset:0
mm v[192:195],a[64:67],v[104:107],v[192:195]
mm v[224:227],a[64:67],v[72:75],v[224:227]
dr a[64:67],v232 offset:2048
cv v208,v208,v209
mm v[224:227],a[68:71],v[80:83],v[224:227]
cv v209,v210,v211
v_or_b32 v208,1,v208
mm v[192:195],a[68:71],v[112:115],v[192:195]
dr a[68:71],v232 offset:4096
cv v210,v212,v213
mm v[192:195],a[72:75],v[120:123],v[192:195]
cv v211,v214,v215
cv v216,v216,v217
bs v[208:211],v243,s[8:11],s64 offen offset:3072 sc1
cv v217,v218,v219
v_or_b32 v216,1,v216
cv v218,v220,v221
cv v219,v222,v223
mm v[224:227],a[72:75],v[88:91],v[224:227]
dr a[72:75],v232 offset:6144
mm v[228:231],a[76:79],v[88:91],0
)" R"(mm v[196:199],a[76:79],v[120:123],0
bs v[216:219],v243,s[8:11],s65 offen offset:3072 sc1
dr a[76:79],v232 offset:7168
mm v[196:199],a[80:83],v[112:115],v[196:199]
mm v[228:231],a[80:83],v[80:83],v[228:231]
dr a[80:83],v232 offset:5120
mm v[228:231],a[84:87],v[72:75],v[228:231]
mm v[196:199],a[84:87],v[104:107],v[196:199]
dr a[84:87],v232 offset:3072
mm v[196:199],a[88:91],v[96:99],v[196:199]
s_waitcnt vmcnt(10)
v_cmp_ne_u64 vcc,s[54:55],v[240:241]
s_cbranch_vccnz FP0_S
FP0_D:
mm v[228:231],a[88:91],v[64:67],v[228:231]
dr a[88:91],v232 offset:1024
mm v[228:231],a[92:95],v[128:131],v[228:231]
mm v[196:199],a[92:95],v[132:135],v[196:199]
s_waitcnt lgkmcnt(3)
bl a[32:35],v248,s[8:11],s79 offen offset:0 sc1
mm v[196:199],a[96:99],v[140:143],v[196:199]
dr a[92:95],v232 offset:9216
mm v[228:231],a[96:99],v[136:139],v[228:231]
dr a[96:99],v232 offset:11264
mm v[228:231],a[100:103],v[144:147],v[228:231]
mm v[196:199],a[100:103],v[148:151],v[196:199]
dr a[100:103],v232 offset:13312
mm v[196:199],a[104:107],v[156:159],v[196:199]
mm v[228:231],a[104:107],v[152:155],v[228:231]
dr a[104:107],v232 offset:15360
mm v[224:227],a[108:111],v[152:155],v[224:227]
mm v[192:195],a[108:111],v[156:159],v[192:195]
bl a[36:39],v248,s[8:11],s80 offen offset:0 sc1
mm v[192:195],a[112:115],v[148:151],v[192:195]
dr a[108:111],v232 offset:14336
mm v[224:227],a[112:115],v[144:147],v[224:227]
dr a[112:115],v232 offset:12288
mm v[224:227],a[116:119],v[136:139],v[224:227]
mm v[192:195],a[116:119],v[140:143],v[192:195]
mm v[192:195],a[120:123],v[132:135],v[192:195]
mm v[224:227],a[120:123],v[128:131],v[224:227]
mm v[224:227],a[124:127],v[160:163],v[224:227]
mm v[192:195],a[124:127],v[164:167],v[192:195]
bl a[0:3],v248,s[8:11],s66 offen offset:0 sc1
mm v[192:195],a[64:67],v[172:175],v[192:195]
s_barrier
dr a[116:119],v232 offset:10240
mm v[224:227],a[64:67],v[168:171],v[224:227]
dr a[120:123],v232 offset:8192
mm v[224:227],a[68:71],v[176:179],v[224:227]
mm v[192:195],a[68:71],v[180:183],v[192:195]
dr a[124:127],v232 offset:16384
mm v[192:195],a[72:75],v[188:191],v[192:195]
dr a[64:67],v232 offset:17408
mm v[224:227],a[72:75],v[184:187],v[224:227]
mm v[228:231],a[76:79],v[184:187],v[228:231]
dr a[68:71],v232 offset:18432
mm v[196:199],a[76:79],v[188:191],v[196:199]
s_waitcnt lgkmcnt(4)
bl a[4:7],v248,s[8:11],s78 offen offset:0 sc1
mm v[196:199],a[80:83],v[180:183],v[196:199]
dr a[72:75],v232 offset:19456
mm v[228:231],a[80:83],v[176:179],v[228:231]
dr a[76:79],v232 offset:24576
)" R"(mm v[228:231],a[84:87],v[168:171],v[228:231]
mm v[196:199],a[84:87],v[172:175],v[196:199]
dr a[80:83],v232 offset:29696
mm v[196:199],a[88:91],v[164:167],v[196:199]
dr a[84:87],v232 offset:34816
mm v[228:231],a[88:91],v[160:163],v[228:231]
mm v[228:231],a[92:95],v[200:203],v[228:231]
dr a[88:91],v232 offset:39936
mm v[196:199],a[92:95],v[204:207],v[196:199]
bl a[16:19],v248,s[8:11],s66 offen offset:2048 sc1
mm v[196:199],a[96:99],v[92:95],v[196:199]
dr a[92:95],v232 offset:40960
mm v[228:231],a[96:99],v[84:87],v[228:231]
dr a[96:99],v232 offset:35840
mm v[228:231],a[100:103],v[100:103],v[228:231]
mm v[196:199],a[100:103],v[108:111],v[196:199]
dr a[100:103],v232 offset:30720
mm v[196:199],a[104:107],v[124:127],v[196:199]
mm v[228:231],a[104:107],v[116:119],v[228:231]
dr a[104:107],v232 offset:25600
mm v[224:227],a[108:111],v[116:119],v[224:227]
mm v[192:195],a[108:111],v[124:127],v[192:195]
dr a[108:111],v232 offset:26624
mm v[192:195],a[112:115],v[108:111],v[192:195]
mm v[224:227],a[112:115],v[100:103],v[224:227]
dr a[112:115],v232 offset:31744
mm v[224:227],a[116:119],v[84:87],v[224:227]
bl v[52:55],v239,s[8:11],s66 offen offset:3072 sc1
mm v[192:195],a[116:119],v[92:95],v[192:195]
s_waitcnt lgkmcnt(3)
dr a[116:119],v232 offset:36864
mm v[192:195],a[120:123],v[204:207],v[192:195]
mm v[224:227],a[120:123],v[200:203],v[224:227]
dr a[120:123],v232 offset:41984
mm v[8:11],a[124:127],a[244:247],0
dr a[124:127],v232 offset:43008
mm v[16:19],a[64:67],a[244:247],0
dr a[64:67],v232 offset:37888
mm v[24:27],a[68:71],a[244:247],0
bl v[60:63],v239,s[8:11],s78 offen offset:3072 sc1
dr a[68:71],v232 offset:32768
mm v[32:35],a[72:75],a[244:247],0
dr a[72:75],v232 offset:27648
cv v224,v224,v225
mm v[0:3],a[76:79],a[128:131],0
dr a[76:79],v232 offset:28672
cv v225,v226,v227
mm v[0:3],a[80:83],a[132:135],v[0:3]
dr a[80:83],v232 offset:33792
v_or_b32 v224,1,v224
mm v[0:3],a[84:87],a[248:251],v[0:3]
dr a[84:87],v232 offset:38912
cv v226,v228,v229
cv v227,v230,v231
s_add_u32 s47,s64,4096
bs v[224:227],v243,s[8:11],s47 offen offset:0 sc1
cv v192,v192,v193
cv v193,v194,v195
v_or_b32 v192,1,v192
cv v194,v196,v197
cv v195,v198,v199
mm v[0:3],a[88:91],a[252:255],v[0:3]
dr a[88:91],v232 offset:44032
mm v[8:11],a[92:95],a[252:255],v[8:11]
)" R"(dr a[92:95],v234 offset:0
mm v[8:11],a[96:99],a[248:251],v[8:11]
dr a[96:99],v234 offset:5120
mm v[8:11],a[100:103],a[132:135],v[8:11]
s_waitcnt lgkmcnt(6)
dr a[100:103],v234 offset:10240
s_add_u32 s47,s65,4096
bs v[192:195],v243,s[8:11],s47 offen offset:0 sc1
mm v[8:11],a[104:107],a[128:131],v[8:11]
dr a[104:107],v234 offset:15360
mm v[16:19],a[108:111],a[128:131],v[16:19]
dr a[108:111],v234 offset:14336
mm v[16:19],a[112:115],a[132:135],v[16:19]
dr a[112:115],v234 offset:9216
mm v[16:19],a[116:119],a[248:251],v[16:19]
bl a[20:23],v248,s[8:11],s78 offen offset:2048 sc1
dr a[116:119],v234 offset:4096
mm v[16:19],a[120:123],a[252:255],v[16:19]
dr a[120:123],v232 offset:48128
mm v[24:27],a[124:127],a[252:255],v[24:27]
dr a[124:127],v232 offset:47104
mm v[24:27],a[64:67],a[248:251],v[24:27]
dr a[64:67],v234 offset:3072
mm v[24:27],a[68:71],a[132:135],v[24:27]
bl a[8:11],v248,s[8:11],s66 offen offset:1024 sc1
dr a[68:71],v234 offset:8192
mm v[24:27],a[72:75],a[128:131],v[24:27]
s_waitcnt lgkmcnt(6)
dr a[72:75],v234 offset:13312
mm v[32:35],a[76:79],a[128:131],v[32:35]
dr a[76:79],v234 offset:12288
mm v[32:35],a[80:83],a[132:135],v[32:35]
dr a[80:83],v234 offset:7168
mm v[32:35],a[84:87],a[248:251],v[32:35]
bl a[12:15],v248,s[8:11],s78 offen offset:1024 sc1
dr a[84:87],v234 offset:2048
mm v[32:35],a[88:91],a[252:255],v[32:35]
dr a[88:91],v232 offset:46080
mm v[32:35],a[92:95],a[180:183],v[32:35]
dr a[92:95],v232 offset:45056
mm v[32:35],a[96:99],a[184:187],v[32:35]
dr a[96:99],v234 offset:1024
mm v[32:35],a[100:103],a[188:191],v[32:35]
bl a[24:27],v248,s[8:11],s66 offen offset:3072 sc1
dr a[100:103],v234 offset:6144
mm v[32:35],a[104:107],a[192:195],v[32:35]
dr a[104:107],v234 offset:11264
mm v[24:27],a[108:111],a[192:195],v[24:27]
s_waitcnt lgkmcnt(6)
dr a[108:111],v234 offset:16384
mm v[24:27],a[112:115],a[188:191],v[24:27]
dr a[112:115],v234 offset:21504
mm v[24:27],a[116:119],a[184:187],v[24:27]
bl a[28:31],v248,s[8:11],s78 offen offset:3072 sc1
dr a[116:119],v234 offset:26624
mm v[24:27],a[120:123],a[180:183],v[24:27]
dr a[120:123],v234 offset:31744
mm v[16:19],a[124:127],a[180:183],v[16:19]
dr a[124:127],v234 offset:32768
mm v[16:19],a[64:67],a[184:187],v[16:19]
dr a[64:67],v234 offset:27648
mm v[16:19],a[68:71],a[188:191],v[16:19]
dr a[68:71],v234 offset:22528
mm v[16:19],a[72:75],a[192:195],v[16:19]
)" R"(dr a[72:75],v234 offset:17408
mm v[8:11],a[76:79],a[192:195],v[8:11]
dr a[76:79],v234 offset:18432
mm v[8:11],a[80:83],a[188:191],v[8:11]
s_waitcnt lgkmcnt(6)
dr a[80:83],v234 offset:23552
mm v[8:11],a[84:87],a[184:187],v[8:11]
dr a[84:87],v234 offset:28672
mm v[8:11],a[88:91],a[180:183],v[8:11]
dr a[88:91],v234 offset:33792
mm v[0:3],a[92:95],a[180:183],v[0:3]
dr a[92:95],v234 offset:34816
mm v[0:3],a[96:99],a[184:187],v[0:3]
dr a[96:99],v234 offset:29696
mm v[0:3],a[100:103],a[188:191],v[0:3]
dr a[100:103],v234 offset:24576
mm v[0:3],a[104:107],a[192:195],v[0:3]
dr a[104:107],v234 offset:19456
mm v[0:3],a[108:111],a[196:199],v[0:3]
dr a[108:111],v234 offset:20480
mm v[0:3],a[112:115],a[200:203],v[0:3]
dr a[112:115],v234 offset:25600
mm v[0:3],a[116:119],a[204:207],v[0:3]
s_waitcnt lgkmcnt(6)
dr a[116:119],v234 offset:30720
mm v[0:3],a[120:123],a[208:211],v[0:3]
dr a[120:123],v234 offset:35840
mm v[8:11],a[124:127],a[208:211],v[8:11]
dr a[124:127],v234 offset:40960
mm v[8:11],a[64:67],a[204:207],v[8:11]
dr a[64:67],v234 offset:46080
mm v[8:11],a[68:71],a[200:203],v[8:11]
dr a[68:71],v234 offset:45056
s_waitcnt vmcnt(18)
s_barrier
mm v[8:11],a[72:75],a[196:199],v[8:11]
mm v[16:19],a[76:79],a[196:199],v[16:19]
mm v[16:19],a[80:83],a[200:203],v[16:19]
mm v[16:19],a[84:87],a[204:207],v[16:19]
mm v[16:19],a[88:91],a[208:211],v[16:19]
s_waitcnt lgkmcnt(2)
mm v[24:27],a[92:95],a[208:211],v[24:27]
mm v[24:27],a[96:99],a[204:207],v[24:27]
mm v[24:27],a[100:103],a[200:203],v[24:27]
dr a[72:75],v234 offset:39936
mm v[24:27],a[104:107],a[196:199],v[24:27]
dr a[76:79],v234 offset:38912
mm v[32:35],a[108:111],a[196:199],v[32:35]
dr a[80:83],v234 offset:44032
mm v[32:35],a[112:115],a[200:203],v[32:35]
dr a[84:87],v234 offset:43008
mm v[32:35],a[116:119],a[204:207],v[32:35]
dr a[88:91],v234 offset:37888
mm v[32:35],a[120:123],a[208:211],v[32:35]
dr a[92:95],v234 offset:36864
mm v[32:35],a[124:127],a[216:219],v[32:35]
s_waitcnt lgkmcnt(6)
dr a[96:99],v234 offset:41984
mm v[32:35],a[64:67],a[220:223],v[32:35]
dr a[100:103],v234 offset:47104
mm v[24:27],a[68:71],a[220:223],v[24:27]
s_waitcnt lgkmcnt(6)
mm v[24:27],a[72:75],a[216:219],v[24:27]
mm v[16:19],a[76:79],a[216:219],v[16:19]
)" R"(s_waitcnt lgkmcnt(4)
dr a[104:107],v234 offset:52224
mm v[16:19],a[80:83],a[220:223],v[16:19]
dr a[108:111],v234 offset:57344
mm v[8:11],a[84:87],a[220:223],v[8:11]
s_waitcnt lgkmcnt(4)
dr a[112:115],v234 offset:62464
mm v[8:11],a[88:91],a[216:219],v[8:11]
dr a[116:119],v232 offset:51200
mm v[0:3],a[92:95],a[216:219],v[0:3]
s_waitcnt lgkmcnt(4)
dr a[120:123],v232 offset:56320
mm v[0:3],a[96:99],a[220:223],v[0:3]
dr a[124:127],v234 offset:48128
mm v[0:3],a[100:103],a[224:227],v[0:3]
s_waitcnt vmcnt(13)
dr a[64:67],v234 offset:53248
mm v[136:139],v[40:43],a[32:35],0
s_waitcnt vmcnt(12)
dr a[68:71],v234 offset:58368
mm v[136:139],v[40:43],a[36:39],v[136:139]
s_waitcnt vmcnt(11)
dr a[72:75],v234 offset:63488
mm v[128:131],v[40:43],a[0:3],0
s_waitcnt vmcnt(9)
dr a[76:79],v232 offset:52224
mm v[132:135],v[40:43],a[16:19],0
dr a[80:83],v234 offset:49152
mm v[128:131],v[40:43],a[4:7],v[128:131]
s_waitcnt vmcnt(4)
dr a[84:87],v234 offset:54272
mm v[132:135],v[40:43],a[20:23],v[132:135]
s_waitcnt vmcnt(3)
dr a[88:91],v234 offset:59392
mm v[128:131],v[48:51],a[8:11],v[128:131]
s_waitcnt vmcnt(1)
dr a[92:95],v234 offset:64512
mm v[132:135],v[48:51],a[24:27],v[132:135]
dr a[96:99],v232 offset:53248
mm v[128:131],v[48:51],a[12:15],v[128:131]
s_waitcnt vmcnt(0)
dr a[100:103],v234 offset:50176
mm v[132:135],v[48:51],a[28:31],v[132:135]
s_waitcnt lgkmcnt(6)
mm v[0:3],a[104:107],a[228:231],v[0:3]
cv v136,v136,v137
cv v137,v138,v139
ds_bpermute_b32 v136,v57,v136
mm v[0:3],a[108:111],a[232:235],v[0:3]
ds_bpermute_b32 v137,v57,v137
cv v128,v128,v129
mm v[0:3],a[112:115],a[236:239],v[0:3]
ds_bpermute_b32 v128,v57,v128
cv v129,v130,v131
mm v[0:3],a[116:119],a[240:243],v[0:3]
ds_bpermute_b32 v129,v57,v129
cv v132,v132,v133
mm v[0:3],a[120:123],a[244:247],v[0:3]
ds_bpermute_b32 v132,v57,v132
cv v133,v134,v135
mm v[8:11],a[124:127],a[224:227],v[8:11]
ds_bpermute_b32 v133,v57,v133
mm v[8:11],a[64:67],a[228:231],v[8:11]
dr a[104:107],v234 offset:55296
)" R"(mm v[8:11],a[68:71],a[232:235],v[8:11]
dr a[108:111],v234 offset:60416
cv v0,v0,v1
bl v[68:71],v239,s[8:11],s79 offen offset:0 sc1
mm v[8:11],a[72:75],a[236:239],v[8:11]
dr a[112:115],v232 offset:49152
cv v1,v2,v3
mm v[8:11],a[76:79],a[240:243],v[8:11]
s_waitcnt lgkmcnt(6)
dr a[116:119],v232 offset:54272
mm v[16:19],a[80:83],a[224:227],v[16:19]
dr a[120:123],v234 offset:51200
mm v[16:19],a[84:87],a[228:231],v[16:19]
dr a[124:127],v234 offset:56320
bl v[76:79],v239,s[8:11],s80 offen offset:0 sc1
mm v[16:19],a[88:91],a[232:235],v[16:19]
ds_bpermute_b32 v0,v58,v0
cv v8,v8,v9
mm v[16:19],a[92:95],a[236:239],v[16:19]
ds_bpermute_b32 v1,v58,v1
cv v9,v10,v11
mm v[16:19],a[96:99],a[240:243],v[16:19]
dr a[64:67],v234 offset:61440
mm v[24:27],a[100:103],a[224:227],v[24:27]
s_waitcnt lgkmcnt(6)
dr a[68:71],v232 offset:50176
mm v[24:27],a[104:107],a[228:231],v[24:27]
s_mul_i32 s73,s26,-655360
dr a[72:75],v232 offset:55296
s_add_u32 s73,s73,0x4b0000
buffer_store_dwordx2 v[136:137],v56,s[20:23],s69 offen offset:256
cv v16,v16,v17
cv v17,v18,v19
mm v[24:27],a[108:111],a[232:235],v[24:27]
buffer_store_dwordx2 v[128:129],v247,s[20:23],s69 offen offset:0
mm v[24:27],a[112:115],a[236:239],v[24:27]
s_waitcnt lgkmcnt(5)
buffer_store_dwordx2 v[132:133],v247,s[20:23],s69 offen offset:128
s_add_u32 s69,s69,s73
mm v[24:27],a[116:119],a[240:243],v[24:27]
mm v[32:35],a[120:123],a[224:227],v[32:35]
ds_bpermute_b32 v8,v58,v8
mm v[32:35],a[124:127],a[228:231],v[32:35]
s_waitcnt lgkmcnt(3)
ds_bpermute_b32 v9,v58,v9
mm v[32:35],a[64:67],a[232:235],v[32:35]
s_waitcnt lgkmcnt(3)
ds_bpermute_b32 v16,v58,v16
mm v[32:35],a[68:71],a[236:239],v[32:35]
s_waitcnt lgkmcnt(3)
ds_bpermute_b32 v17,v58,v17
mm v[32:35],a[72:75],a[240:243],v[32:35]
EE_1:
s_cbranch_execz EE_R
cv v24,v24,v25
cv v25,v26,v27
ds_bpermute_b32 v24,v58,v24
ds_bpermute_b32 v25,v58,v25
s_nop 3
cv v32,v32,v33
cv v33,v34,v35
ds_bpermute_b32 v32,v58,v32
ds_bpermute_b32 v33,v58,v33
buffer_store_dwordx2 v[0:1],v246,s[20:23],s72 offen offset:0
)" R"(s_waitcnt lgkmcnt(6)
buffer_store_dwordx2 v[8:9],v246,s[20:23],s72 offen offset:32
s_waitcnt lgkmcnt(4)
buffer_store_dwordx2 v[16:17],v246,s[20:23],s72 offen offset:64
s_waitcnt lgkmcnt(2)
buffer_store_dwordx2 v[24:25],v246,s[20:23],s72 offen offset:96
s_waitcnt lgkmcnt(0)
buffer_store_dwordx2 v[32:33],v246,s[20:23],s72 offen offset:128
v_bitop3_b32 v250,v4,v12,1 bitop3:0x80
v_bitop3_b32 v250,v36,v44,v250 bitop3:0x80
v_bitop3_b32 v250,v20,v28,v250 bitop3:0x80
v_bitop3_b32 v250,v52,v60,v250 bitop3:0x80
v_cmp_ne_u32 vcc,1,v250
s_cbranch_vccnz CH3
CH3_D:
mm v[140:143],v[40:43],v[4:7],0
mm v[144:147],v[40:43],v[36:39],0
mm v[140:143],v[40:43],v[12:15],v[140:143]
mm v[144:147],v[40:43],v[44:47],v[144:147]
mm v[140:143],v[48:51],v[20:23],v[140:143]
mm v[144:147],v[48:51],v[52:55],v[144:147]
mm v[140:143],v[48:51],v[28:31],v[140:143]
mm v[144:147],v[48:51],v[60:63],v[144:147]
s_nop 6
cv v140,v140,v141
cv v141,v142,v143
ds_bpermute_b32 v140,v57,v140
ds_bpermute_b32 v141,v57,v141
cv v144,v144,v145
cv v145,v146,v147
ds_bpermute_b32 v144,v57,v144
ds_bpermute_b32 v145,v57,v145
s_waitcnt lgkmcnt(2)
buffer_store_dwordx2 v[140:141],v247,s[20:23],s69 offen offset:0
s_waitcnt lgkmcnt(0)
buffer_store_dwordx2 v[144:145],v247,s[20:23],s69 offen offset:128
s_waitcnt vmcnt(10)
v_bitop3_b32 v250,v68,v76,1 bitop3:0x80
v_cmp_ne_u32 vcc,1,v250
s_cbranch_vccnz CH4
CH4_D:
mm v[148:151],v[40:43],v[68:71],0
mm v[148:151],v[40:43],v[76:79],v[148:151]
s_nop 7
cv v148,v148,v149
cv v149,v150,v151
ds_bpermute_b32 v148,v57,v148
ds_bpermute_b32 v149,v57,v149
s_waitcnt lgkmcnt(0)
buffer_store_dwordx2 v[148:149],v56,s[20:23],s69 offen offset:256
s_waitcnt vmcnt(0)
s_branch THEEND
UE:
CH1:
EE_3:
s_cbranch_execz EE_R
s_sub_u32 s100,s100,1
s_cmp_lt_i32 s100,0
s_cbranch_scc1 CH1_D
v_and_b32 v250,1,v128
v_cmp_eq_u32 vcc,0,v250
s_mov_b64 exec,vcc
bl v[128:131],v232,s[8:11],s75 offen offset:0 sc1
s_mov_b64 exec,-1
)" R"(v_and_b32 v250,1,v132
v_cmp_eq_u32 vcc,0,v250
s_mov_b64 exec,vcc
bl v[132:135],v233,s[8:11],s75 offen offset:0 sc1
s_mov_b64 exec,-1
v_and_b32 v250,1,v136
v_cmp_eq_u32 vcc,0,v250
s_mov_b64 exec,vcc
bl v[136:139],v232,s[8:11],s75 offen offset:1024 sc1
s_mov_b64 exec,-1
v_and_b32 v250,1,v140
v_cmp_eq_u32 vcc,0,v250
s_mov_b64 exec,vcc
bl v[140:143],v233,s[8:11],s75 offen offset:1024 sc1
s_mov_b64 exec,-1
v_and_b32 v250,1,v144
v_cmp_eq_u32 vcc,0,v250
s_mov_b64 exec,vcc
bl v[144:147],v232,s[8:11],s75 offen offset:2048 sc1
s_mov_b64 exec,-1
v_and_b32 v250,1,v148
v_cmp_eq_u32 vcc,0,v250
s_mov_b64 exec,vcc
bl v[148:151],v233,s[8:11],s75 offen offset:2048 sc1
s_mov_b64 exec,-1
v_and_b32 v250,1,v152
v_cmp_eq_u32 vcc,0,v250
s_mov_b64 exec,vcc
bl v[152:155],v232,s[8:11],s75 offen offset:3072 sc1
s_mov_b64 exec,-1
v_and_b32 v250,1,v156
v_cmp_eq_u32 vcc,0,v250
s_mov_b64 exec,vcc
bl v[156:159],v233,s[8:11],s75 offen offset:3072 sc1
s_mov_b64 exec,-1
v_and_b32 v250,1,v160
v_cmp_eq_u32 vcc,0,v250
s_mov_b64 exec,vcc
bl v[160:163],v232,s[8:11],s76 offen offset:0 sc1
s_mov_b64 exec,-1
v_and_b32 v250,1,v164
v_cmp_eq_u32 vcc,0,v250
s_mov_b64 exec,vcc
bl v[164:167],v233,s[8:11],s76 offen offset:0 sc1
s_mov_b64 exec,-1
v_and_b32 v250,1,v168
v_cmp_eq_u32 vcc,0,v250
s_mov_b64 exec,vcc
bl v[168:171],v232,s[8:11],s76 offen offset:1024 sc1
s_mov_b64 exec,-1
v_and_b32 v250,1,v172
v_cmp_eq_u32 vcc,0,v250
s_mov_b64 exec,vcc
bl v[172:175],v233,s[8:11],s76 offen offset:1024 sc1
s_mov_b64 exec,-1
v_and_b32 v250,1,v176
v_cmp_eq_u32 vcc,0,v250
s_mov_b64 exec,vcc
bl v[176:179],v232,s[8:11],s76 offen offset:2048 sc1
s_mov_b64 exec,-1
v_and_b32 v250,1,v180
v_cmp_eq_u32 vcc,0,v250
s_mov_b64 exec,vcc
bl v[180:183],v233,s[8:11],s76 offen offset:2048 sc1
)" R"(s_mov_b64 exec,-1
v_and_b32 v250,1,v184
v_cmp_eq_u32 vcc,0,v250
s_mov_b64 exec,vcc
bl v[184:187],v232,s[8:11],s76 offen offset:3072 sc1
s_mov_b64 exec,-1
v_and_b32 v250,1,v188
v_cmp_eq_u32 vcc,0,v250
s_mov_b64 exec,vcc
bl v[188:191],v233,s[8:11],s76 offen offset:3072 sc1
s_mov_b64 exec,-1
v_and_b32 v250,1,v200
v_cmp_eq_u32 vcc,0,v250
s_mov_b64 exec,vcc
bl v[200:203],v232,s[8:11],s77 offen offset:0 sc1
s_mov_b64 exec,-1
v_and_b32 v250,1,v204
v_cmp_eq_u32 vcc,0,v250
s_mov_b64 exec,vcc
bl v[204:207],v233,s[8:11],s77 offen offset:0 sc1
s_mov_b64 exec,-1
v_and_b32 v250,1,v84
v_cmp_eq_u32 vcc,0,v250
s_mov_b64 exec,vcc
bl v[84:87],v232,s[8:11],s77 offen offset:1024 sc1
s_mov_b64 exec,-1
v_and_b32 v250,1,v92
v_cmp_eq_u32 vcc,0,v250
s_mov_b64 exec,vcc
bl v[92:95],v233,s[8:11],s77 offen offset:1024 sc1
s_mov_b64 exec,-1
v_and_b32 v250,1,v100
v_cmp_eq_u32 vcc,0,v250
s_mov_b64 exec,vcc
bl v[100:103],v232,s[8:11],s77 offen offset:2048 sc1
s_mov_b64 exec,-1
v_and_b32 v250,1,v108
v_cmp_eq_u32 vcc,0,v250
s_mov_b64 exec,vcc
bl v[108:111],v233,s[8:11],s77 offen offset:2048 sc1
s_mov_b64 exec,-1
v_and_b32 v250,1,v116
v_cmp_eq_u32 vcc,0,v250
s_mov_b64 exec,vcc
bl v[116:119],v232,s[8:11],s77 offen offset:3072 sc1
s_mov_b64 exec,-1
v_and_b32 v250,1,v124
v_cmp_eq_u32 vcc,0,v250
s_mov_b64 exec,vcc
bl v[124:127],v233,s[8:11],s77 offen offset:3072 sc1
s_mov_b64 exec,-1
s_waitcnt vmcnt(0)
v_bitop3_b32 v250,v128,v132,1 bitop3:0x80
v_bitop3_b32 v250,v136,v140,v250 bitop3:0x80
v_bitop3_b32 v250,v144,v148,v250 bitop3:0x80
v_bitop3_b32 v250,v152,v156,v250 bitop3:0x80
v_bitop3_b32 v250,v160,v164,v250 bitop3:0x80
v_bitop3_b32 v250,v168,v172,v250 bitop3:0x80
v_bitop3_b32 v250,v176,v180,v250 bitop3:0x80
v_bitop3_b32 v250,v184,v188,v250 bitop3:0x80
v_bitop3_b32 v250,v200,v204,v250 bitop3:0x80
v_bitop3_b32 v250,v84,v92,v250 bitop3:0x80
v_bitop3_b32 v250,v100,v108,v250 bitop3:0x80
v_bitop3_b32 v250,v116,v124,v250 bitop3:0x80
)" R"(v_cmp_ne_u32 vcc,1,v250
s_cbranch_vccnz CH1
s_branch CH1_D
CH3:
EE_2:
s_cbranch_execz EE_R
s_sub_u32 s100,s100,1
s_cmp_lt_i32 s100,0
s_cbranch_scc1 CH3_D
s_waitcnt vmcnt(0)
v_and_b32 v250,1,v4
v_cmp_eq_u32 vcc,0,v250
s_mov_b64 exec,vcc
bl v[4:7],v239,s[8:11],s66 offen offset:0 sc1
s_mov_b64 exec,-1
v_and_b32 v250,1,v12
v_cmp_eq_u32 vcc,0,v250
s_mov_b64 exec,vcc
bl v[12:15],v239,s[8:11],s78 offen offset:0 sc1
s_mov_b64 exec,-1
v_and_b32 v250,1,v20
v_cmp_eq_u32 vcc,0,v250
s_mov_b64 exec,vcc
bl v[20:23],v239,s[8:11],s66 offen offset:1024 sc1
s_mov_b64 exec,-1
v_and_b32 v250,1,v28
v_cmp_eq_u32 vcc,0,v250
s_mov_b64 exec,vcc
bl v[28:31],v239,s[8:11],s78 offen offset:1024 sc1
s_mov_b64 exec,-1
v_and_b32 v250,1,v36
v_cmp_eq_u32 vcc,0,v250
s_mov_b64 exec,vcc
bl v[36:39],v239,s[8:11],s66 offen offset:2048 sc1
s_mov_b64 exec,-1
v_and_b32 v250,1,v44
v_cmp_eq_u32 vcc,0,v250
s_mov_b64 exec,vcc
bl v[44:47],v239,s[8:11],s78 offen offset:2048 sc1
s_mov_b64 exec,-1
v_and_b32 v250,1,v52
v_cmp_eq_u32 vcc,0,v250
s_mov_b64 exec,vcc
bl v[52:55],v239,s[8:11],s66 offen offset:3072 sc1
s_mov_b64 exec,-1
v_and_b32 v250,1,v60
v_cmp_eq_u32 vcc,0,v250
s_mov_b64 exec,vcc
bl v[60:63],v239,s[8:11],s78 offen offset:3072 sc1
s_mov_b64 exec,-1
v_and_b32 v250,1,v68
v_cmp_eq_u32 vcc,0,v250
s_mov_b64 exec,vcc
bl v[68:71],v239,s[8:11],s79 offen offset:0 sc1
s_mov_b64 exec,-1
v_and_b32 v250,1,v76
v_cmp_eq_u32 vcc,0,v250
s_mov_b64 exec,vcc
bl v[76:79],v239,s[8:11],s80 offen offset:0 sc1
s_mov_b64 exec,-1
s_waitcnt vmcnt(0)
v_bitop3_b32 v250,v4,v12,1 bitop3:0x80
v_bitop3_b32 v250,v36,v44,v250 bitop3:0x80
v_bitop3_b32 v250,v20,v28,v250 bitop3:0x80
)" R"(v_bitop3_b32 v250,v52,v60,v250 bitop3:0x80
v_cmp_ne_u32 vcc,1,v250
s_cbranch_vccnz CH3
s_branch CH3_D
CH4:
s_sub_u32 s100,s100,1
s_cmp_lt_i32 s100,0
s_cbranch_scc1 CH4_D
v_and_b32 v250,1,v68
v_cmp_eq_u32 vcc,0,v250
s_mov_b64 exec,vcc
bl v[68:71],v239,s[8:11],s79 offen offset:0 sc1
s_mov_b64 exec,-1
v_and_b32 v250,1,v76
v_cmp_eq_u32 vcc,0,v250
s_mov_b64 exec,vcc
bl v[76:79],v239,s[8:11],s80 offen offset:0 sc1
s_mov_b64 exec,-1
s_waitcnt vmcnt(0)
v_bitop3_b32 v250,v68,v76,1 bitop3:0x80
v_cmp_ne_u32 vcc,1,v250
s_cbranch_vccnz CH4
s_branch CH4_D
s_nop 0
s_nop 0
s_nop 0
s_nop 0
s_nop 0
s_nop 0
s_nop 0
s_nop 0
s_nop 0
s_nop 0
s_nop 0
s_nop 0
s_nop 0
s_nop 0
s_nop 0
s_nop 0
s_nop 0
s_nop 0
s_nop 0
s_nop 0
s_nop 0
s_nop 0
s_nop 0
s_nop 0
s_nop 0
s_nop 0
s_nop 0
s_nop 0
s_nop 0
s_nop 0
s_nop 0
s_nop 0
s_nop 0
s_nop 0
s_nop 0
s_nop 0
s_nop 0
s_nop 0
s_nop 0
s_nop 0
s_nop 0
)" R"(s_nop 0
s_nop 0
s_nop 0
s_nop 0
s_nop 0
s_nop 0
s_nop 0
s_nop 0
s_nop 0
s_nop 0
s_nop 0
s_nop 0
s_nop 0
s_nop 0
s_nop 0
s_nop 0
s_nop 0
s_nop 0
s_nop 0
s_nop 0
s_nop 0
s_nop 0
s_nop 0
s_nop 0
s_nop 0
s_nop 0
s_nop 0
s_nop 0
s_nop 0
s_nop 0
s_nop 0
s_nop 0
s_nop 0
s_nop 0
s_nop 0
s_nop 0
s_nop 0
s_nop 0
s_nop 0
s_nop 0
s_nop 0
s_nop 0
s_nop 0
s_nop 0
s_nop 0
s_nop 0
s_nop 0
s_nop 0
s_nop 0
s_nop 0
s_nop 0
s_nop 0
s_nop 0
s_nop 0
s_nop 0
s_nop 0
s_nop 0
s_nop 0
s_nop 0
s_nop 0
s_nop 0
s_nop 0
s_nop 0
s_nop 0
)" R"(s_nop 0
s_nop 0
s_nop 0
s_nop 0
s_nop 0
s_nop 0
s_nop 0
s_nop 0
s_nop 0
s_nop 0
s_nop 0
s_nop 0
s_nop 0
s_nop 0
s_nop 0
s_nop 0
s_nop 0
s_nop 0
s_nop 0
s_nop 0
s_nop 0
s_nop 0
s_nop 0
s_nop 0
s_nop 0
s_nop 0
s_nop 0
s_nop 0
s_nop 0
s_nop 0
s_nop 0
s_nop 0
s_nop 0
s_nop 0
s_nop 0
s_nop 0
s_nop 0
s_nop 0
s_nop 0
s_nop 0
s_nop 0
s_nop 0
s_nop 0
s_nop 0
s_nop 0
s_nop 0
s_nop 0
s_nop 0
s_nop 0
s_nop 0
s_nop 0
s_nop 0
s_nop 0
s_nop 0
s_nop 0
s_nop 0
s_nop 0
s_nop 0
s_nop 0
s_nop 0
s_nop 0
s_nop 0
s_nop 0
s_nop 0
)" R"(s_nop 0
s_nop 0
s_nop 0
s_nop 0
s_nop 0
s_nop 0
s_nop 0
s_nop 0
s_nop 0
s_nop 0
s_nop 0
s_nop 0
s_nop 0
s_nop 0
s_nop 0
s_nop 0
s_nop 0
s_nop 0
s_nop 0
s_nop 0
s_nop 0
s_nop 0
s_nop 0
s_nop 0
s_nop 0
s_nop 0
s_nop 0
s_nop 0
s_nop 0
s_nop 0
s_nop 0
s_nop 0
s_nop 0
s_nop 0
s_nop 0
s_nop 0
s_nop 0
s_nop 0
s_nop 0
s_nop 0
s_nop 0
s_nop 0
s_nop 0
s_nop 0
s_nop 0
s_nop 0
s_nop 0
s_nop 0
s_nop 0
s_nop 0
s_nop 0
s_nop 0
s_nop 0
s_nop 0
s_nop 0
s_nop 0
s_nop 0
s_nop 0
s_nop 0
s_nop 0
s_nop 0
s_nop 0
s_nop 0
s_nop 0
)" R"(s_nop 0
s_nop 0
s_nop 0
s_nop 0
s_nop 0
s_nop 0
s_nop 0
s_nop 0
s_nop 0
s_nop 0
s_nop 0
s_nop 0
s_nop 0
s_nop 0
s_nop 0
s_nop 0
s_nop 0
s_nop 0
s_nop 0
s_nop 0
s_nop 0
s_nop 0
s_nop 0
s_nop 0
s_nop 0
s_nop 0
s_nop 0
s_nop 0
s_nop 0
s_nop 0
s_nop 0
s_nop 0
s_nop 0
s_nop 0
s_nop 0
s_nop 0
s_nop 0
s_nop 0
s_nop 0
s_nop 0
s_nop 0
s_nop 0
s_nop 0
s_nop 0
s_nop 0
s_nop 0
s_nop 0
s_nop 0
s_nop 0
s_nop 0
s_nop 0
s_nop 0
s_nop 0
s_nop 0
s_nop 0
s_nop 0
s_nop 0
s_nop 0
s_nop 0
s_nop 0
s_nop 0
s_nop 0
s_nop 0
s_nop 0
)" R"(s_nop 0
s_nop 0
s_nop 0
s_nop 0
s_nop 0
s_nop 0
s_nop 0
s_nop 0
s_nop 0
s_nop 0
s_nop 0
s_nop 0
s_nop 0
s_nop 0
s_nop 0
s_nop 0
s_nop 0
s_nop 0
s_nop 0
s_nop 0
s_nop 0
s_nop 0
s_nop 0
s_nop 0
s_nop 0
s_nop 0
s_nop 0
s_nop 0
s_nop 0
s_nop 0
s_nop 0
s_nop 0
s_nop 0
s_nop 0
s_nop 0
s_nop 0
s_nop 0
s_nop 0
s_nop 0
s_nop 0
s_nop 0
s_nop 0
s_nop 0
s_nop 0
s_nop 0
s_nop 0
s_nop 0
s_nop 0
s_nop 0
s_nop 0
s_nop 0
s_nop 0
s_nop 0
s_nop 0
s_nop 0
s_nop 0
s_nop 0
s_nop 0
s_nop 0
s_nop 0
s_nop 0
s_nop 0
s_nop 0
s_nop 0
)" R"(s_nop 0
s_nop 0
s_nop 0
s_nop 0
s_nop 0
s_nop 0
s_nop 0
s_nop 0
s_nop 0
s_nop 0
s_nop 0
s_nop 0
s_nop 0
s_nop 0
s_nop 0
s_nop 0
s_nop 0
s_nop 0
s_nop 0
s_nop 0
s_nop 0
s_nop 0
s_nop 0
s_nop 0
s_nop 0
s_nop 0
s_nop 0
s_nop 0
s_nop 0
s_nop 0
s_nop 0
s_nop 0
s_nop 0
s_nop 0
s_nop 0
s_nop 0
s_nop 0
s_nop 0
s_nop 0
s_nop 0
s_nop 0
s_nop 0
s_nop 0
s_nop 0
s_nop 0
s_nop 0
s_nop 0
s_nop 0
s_nop 0
s_nop 0
s_nop 0
s_nop 0
s_nop 0
s_nop 0
s_nop 0
s_nop 0
s_nop 0
s_nop 0
s_nop 0
s_nop 0
s_nop 0
s_nop 0
s_nop 0
s_nop 0
)" R"(s_nop 0
s_nop 0
s_nop 0
s_nop 0
s_nop 0
s_nop 0
s_nop 0
s_nop 0
s_nop 0
s_nop 0
s_nop 0
s_nop 0
s_nop 0
s_nop 0
s_nop 0
s_nop 0
s_nop 0
s_nop 0
s_nop 0
s_nop 0
s_nop 0
s_nop 0
s_nop 0
s_nop 0
s_nop 0
s_nop 0
s_nop 0
s_nop 0
s_nop 0
s_nop 0
s_nop 0
s_nop 0
s_nop 0
s_nop 0
s_nop 0
s_nop 0
s_nop 0
s_nop 0
s_nop 0
s_nop 0
s_nop 0
s_nop 0
s_nop 0
s_nop 0
s_nop 0
s_nop 0
s_nop 0
s_nop 0
s_nop 0
s_nop 0
s_nop 0
s_nop 0
s_nop 0
s_nop 0
s_nop 0
s_nop 0
s_nop 0
s_nop 0
s_nop 0
s_nop 0
s_nop 0
s_nop 0
s_nop 0
s_nop 0
)" R"(s_nop 0
s_nop 0
s_nop 0
s_nop 0
s_nop 0
s_nop 0
s_nop 0
s_nop 0
s_nop 0
s_nop 0
s_nop 0
s_nop 0
s_nop 0
s_nop 0
s_nop 0
s_nop 0
s_nop 0
s_nop 0
s_nop 0
s_nop 0
s_nop 0
s_nop 0
s_nop 0
s_nop 0
s_nop 0
s_nop 0
s_nop 0
s_nop 0
s_nop 0
s_nop 0
s_nop 0
s_nop 0
s_nop 0
s_nop 0
s_nop 0
s_nop 0
s_nop 0
s_nop 0
s_nop 0
s_nop 0
s_nop 0
s_nop 0
s_nop 0
s_nop 0
s_nop 0
s_nop 0
s_nop 0
s_nop 0
s_nop 0
s_nop 0
s_nop 0
s_nop 0
s_nop 0
s_nop 0
s_nop 0
s_nop 0
s_nop 0
s_nop 0
s_nop 0
s_nop 0
s_nop 0
s_nop 0
s_nop 0
s_nop 0
)" R"(s_nop 0
s_nop 0
s_nop 0
s_nop 0
s_nop 0
s_nop 0
s_nop 0
s_nop 0
s_nop 0
s_nop 0
s_nop 0
s_nop 0
s_nop 0
s_nop 0
s_nop 0
s_nop 0
s_nop 0
s_nop 0
s_nop 0
s_nop 0
s_nop 0
s_nop 0
s_nop 0
s_nop 0
s_nop 0
s_nop 0
s_nop 0
s_nop 0
s_nop 0
s_nop 0
s_nop 0
s_nop 0
s_nop 0
s_nop 0
s_nop 0
s_nop 0
s_nop 0
s_nop 0
s_nop 0
s_nop 0
s_nop 0
s_nop 0
s_nop 0
s_nop 0
s_nop 0
s_nop 0
s_nop 0
s_nop 0
s_nop 0
s_nop 0
s_nop 0
s_nop 0
s_nop 0
s_nop 0
s_nop 0
s_nop 0
s_nop 0
s_nop 0
s_nop 0
s_nop 0
s_nop 0
s_nop 0
s_nop 0
s_nop 0
)" R"(s_nop 0
s_nop 0
s_nop 0
s_nop 0
s_nop 0
s_nop 0
s_nop 0
s_nop 0
s_nop 0
s_nop 0
s_nop 0
s_nop 0
s_nop 0
s_nop 0
s_nop 0
s_nop 0
s_nop 0
s_nop 0
s_nop 0
s_nop 0
s_nop 0
s_nop 0
s_nop 0
s_nop 0
s_nop 0
s_nop 0
s_nop 0
SB:
EA_SB:
s_cbranch_execz EA_R
s_bfe_i64 s[48:49],s[36:37],0x10000
s_bfe_i64 s[50:51],s[36:37],0x10004
s_and_b64 s[52:53],s[12:13],s[48:49]
s_or_b64 s[52:53],s[52:53],s[50:51]
v_cndmask_b32_e64 v208,v236,0,s[52:53]
s_and_b64 s[86:87],s[14:15],s[48:49]
s_or_b64 s[86:87],s[86:87],s[50:51]
v_cndmask_b32_e64 v209,v236,0,s[86:87]
s_and_b64 s[52:53],s[16:17],s[48:49]
s_or_b64 s[52:53],s[52:53],s[50:51]
v_cndmask_b32_e64 v210,v236,0,s[52:53]
s_and_b64 s[86:87],s[18:19],s[48:49]
s_or_b64 s[86:87],s[86:87],s[50:51]
v_cndmask_b32_e64 v211,v236,0,s[86:87]
s_waitcnt lgkmcnt(10)
s_nop 0
mm v[128:131],a[64:67],v[0:3],v[208:211]
s_bfe_i64 s[48:49],s[36:37],0x10001
buffer_store_dwordx2 v[252:253],v254,s[8:11],s74 offen sc1
s_bfe_i64 s[50:51],s[36:37],0x10005
s_and_b64 s[52:53],s[12:13],s[48:49]
s_or_b64 s[52:53],s[52:53],s[50:51]
mm v[144:147],a[64:67],v[32:35],0
s_and_b64 s[86:87],s[14:15],s[48:49]
v_cndmask_b32_e64 v212,v236,0,s[52:53]
s_or_b64 s[86:87],s[86:87],s[50:51]
mm v[144:147],a[68:71],v[40:43],v[144:147]
s_and_b64 s[52:53],s[16:17],s[48:49]
v_cndmask_b32_e64 v213,v236,0,s[86:87]
s_or_b64 s[52:53],s[52:53],s[50:51]
mm v[128:131],a[68:71],v[8:11],v[128:131]
s_and_b64 s[86:87],s[18:19],s[48:49]
v_cndmask_b32_e64 v214,v236,0,s[52:53]
s_or_b64 s[86:87],s[86:87],s[50:51]
)" R"(mm v[128:131],a[72:75],v[16:19],v[128:131]
s_add_u32 s31,s31,65536
v_cndmask_b32_e64 v215,v236,0,s[86:87]
mm v[144:147],a[72:75],v[48:51],v[144:147]
mm v[144:147],a[76:79],v[56:59],v[144:147]
s_waitcnt lgkmcnt(0)
s_barrier
mm v[128:131],a[76:79],v[24:27],v[128:131]
mm v[132:135],a[80:83],v[0:3],v[212:215]
mm v[148:151],a[80:83],v[32:35],0
mm v[148:151],a[84:87],v[40:43],v[148:151]
mm v[132:135],a[84:87],v[8:11],v[132:135]
mm v[132:135],a[88:91],v[16:19],v[132:135]
mm v[148:151],a[88:91],v[48:51],v[148:151]
mm v[148:151],a[92:95],v[56:59],v[148:151]
mm v[132:135],a[92:95],v[24:27],v[132:135]
s_bfe_i64 s[48:49],s[36:37],0x10002
v_max3_f32 v224,v128,v129,v130
s_bfe_i64 s[50:51],s[36:37],0x10006
mm v[168:171],a[64:67],v[64:67],0
s_and_b64 s[52:53],s[12:13],s[48:49]
s_or_b64 s[52:53],s[52:53],s[50:51]
s_and_b64 s[86:87],s[14:15],s[48:49]
mm v[184:187],a[64:67],v[96:99],0
s_or_b64 s[86:87],s[86:87],s[50:51]
dr a[64:67],v235 offset:0
v_max3_f32 v224,v224,v131,v132
mm v[184:187],a[68:71],v[104:107],v[184:187]
v_max3_f32 v224,v224,v133,v134
v_max_f32 v224,v224,v135
mm v[168:171],a[68:71],v[72:75],v[168:171]
dr a[68:71],v235 offset:1024
v_mov_b32 v226,v224
mm v[168:171],a[72:75],v[80:83],v[168:171]
v_max3_f32 v225,v144,v145,v146
v_max3_f32 v225,v225,v147,v148
mm v[184:187],a[72:75],v[112:115],v[184:187]
v_permlane32_swap_b32 v224,v226
mm v[184:187],a[76:79],v[120:123],v[184:187]
dr a[72:75],v235 offset:2048
v_max_f32 v224,v224,v226
mm v[168:171],a[76:79],v[88:91],v[168:171]
dr a[76:79],v235 offset:3072
v_mov_b32 v226,v224
mm v[172:175],a[80:83],v[64:67],0
v_max3_f32 v225,v225,v149,v150
v_max_f32 v225,v225,v151
mm v[188:191],a[80:83],v[96:99],0
v_permlane16_swap_b32 v224,v226
mm v[188:191],a[84:87],v[104:107],v[188:191]
dr a[80:83],v235 offset:4096
v_max_f32 v224,v224,v226
mm v[172:175],a[84:87],v[72:75],v[172:175]
dr a[84:87],v235 offset:5120
v_sub_f32 v192,0xc2400000,v224
mm v[172:175],a[88:91],v[80:83],v[172:175]
v_cndmask_b32_e64 v216,v236,v192,s[52:53]
v_cndmask_b32_e64 v217,v236,v192,s[86:87]
mm v[188:191],a[88:91],v[112:115],v[188:191]
s_and_b64 s[52:53],s[16:17],s[48:49]
dr a[88:91],v235 offset:6144
s_or_b64 s[52:53],s[52:53],s[50:51]
mm v[188:191],a[92:95],v[120:123],v[188:191]
s_and_b64 s[86:87],s[18:19],s[48:49]
)" R"(v_cndmask_b32_e64 v218,v236,v192,s[52:53]
s_or_b64 s[86:87],s[86:87],s[50:51]
v_cndmask_b32_e64 v219,v236,v192,s[86:87]
mm v[172:175],a[92:95],v[88:91],v[172:175]
s_bfe_i64 s[48:49],s[36:37],0x10003
dr a[92:95],v235 offset:7168
v_mov_b32 v227,v225
mm v[136:139],a[96:99],v[0:3],v[216:219]
s_bfe_i64 s[50:51],s[36:37],0x10007
v_permlane32_swap_b32 v225,v227
mm v[136:139],a[100:103],v[8:11],v[136:139]
s_and_b64 s[52:53],s[12:13],s[48:49]
v_max_f32 v225,v225,v227
v_mov_b32 v227,v225
s_nop 1
v_permlane16_swap_b32 v225,v227
mm v[136:139],a[104:107],v[16:19],v[136:139]
s_or_b64 s[52:53],s[52:53],s[50:51]
v_max_f32 v225,v225,v227
v_sub_f32 v196,0xc2400000,v225
v_sub_f32 v197,0xc2400000,v225
v_sub_f32 v198,0xc2400000,v225
v_sub_f32 v199,0xc2400000,v225
mm v[136:139],a[108:111],v[24:27],v[136:139]
s_and_b64 s[86:87],s[14:15],s[48:49]
v_cndmask_b32_e64 v220,v236,v192,s[52:53]
s_or_b64 s[86:87],s[86:87],s[50:51]
mm v[152:155],a[108:111],v[56:59],v[196:199]
s_and_b64 s[52:53],s[16:17],s[48:49]
v_cndmask_b32_e64 v221,v236,v192,s[86:87]
s_or_b64 s[52:53],s[52:53],s[50:51]
mm v[152:155],a[104:107],v[48:51],v[152:155]
s_and_b64 s[86:87],s[18:19],s[48:49]
ex2 v136,v136
mm v[152:155],a[100:103],v[40:43],v[152:155]
s_or_b64 s[86:87],s[86:87],s[50:51]
ex2 v137,v137
mm v[152:155],a[96:99],v[32:35],v[152:155]
s_bfe_i64 s[48:49],s[36:37],0x10008
dr a[96:99],v235 offset:8192
v_cndmask_b32_e64 v222,v236,v192,s[52:53]
mm v[156:159],a[112:115],v[32:35],v[196:199]
s_bfe_i64 s[50:51],s[36:37],0x1000c
dr a[100:103],v235 offset:9216
v_cndmask_b32_e64 v223,v236,v192,s[86:87]
mm v[156:159],a[116:119],v[40:43],v[156:159]
s_and_b64 s[52:53],s[12:13],s[48:49]
ex2 v152,v152
mm v[156:159],a[120:123],v[48:51],v[156:159]
s_or_b64 s[52:53],s[52:53],s[50:51]
dr a[104:107],v235 offset:10240
v_cndmask_b32_e64 v208,v236,v196,s[52:53]
mm v[156:159],a[124:127],v[56:59],v[156:159]
s_and_b64 s[86:87],s[14:15],s[48:49]
dr a[108:111],v235 offset:11264
s_or_b64 s[86:87],s[86:87],s[50:51]
mm v[140:143],a[124:127],v[24:27],v[220:223]
s_and_b64 s[52:53],s[16:17],s[48:49]
v_cndmask_b32_e64 v209,v236,v196,s[86:87]
s_or_b64 s[52:53],s[52:53],s[50:51]
mm v[140:143],a[120:123],v[16:19],v[140:143]
s_and_b64 s[86:87],s[18:19],s[48:49]
v_cndmask_b32_e64 v210,v236,v196,s[52:53]
s_or_b64 s[86:87],s[86:87],s[50:51]
)" R"(mm v[140:143],a[116:119],v[8:11],v[140:143]
s_bfe_i64 s[48:49],s[36:37],0x10009
v_cndmask_b32_e64 v211,v236,v196,s[86:87]
s_bfe_i64 s[50:51],s[36:37],0x1000d
mm v[140:143],a[112:115],v[0:3],v[140:143]
s_waitcnt lgkmcnt(2)
dr a[112:115],v235 offset:12288
s_and_b64 s[52:53],s[12:13],s[48:49]
dr a[116:119],v235 offset:13312
mm v[176:179],a[64:67],v[32:35],v[208:211]
s_or_b64 s[52:53],s[52:53],s[50:51]
dr a[120:123],v235 offset:14336
v_cndmask_b32_e64 v212,v236,v196,s[52:53]
mm v[176:179],a[68:71],v[40:43],v[176:179]
s_and_b64 s[86:87],s[14:15],s[48:49]
dr a[124:127],v235 offset:15360
s_or_b64 s[86:87],s[86:87],s[50:51]
mm v[176:179],a[72:75],v[48:51],v[176:179]
s_and_b64 s[52:53],s[16:17],s[48:49]
v_cndmask_b32_e64 v213,v236,v196,s[86:87]
s_or_b64 s[52:53],s[52:53],s[50:51]
v_cndmask_b32_e64 v214,v236,v196,s[52:53]
s_and_b64 s[86:87],s[18:19],s[48:49]
s_or_b64 s[86:87],s[86:87],s[50:51]
v_cndmask_b32_e64 v215,v236,v196,s[86:87]
mm v[176:179],a[76:79],v[56:59],v[176:179]
s_bfe_i64 s[48:49],s[36:37],0x1000a
ex2 v153,v153
mm v[180:183],a[80:83],v[32:35],v[212:215]
s_bfe_i64 s[50:51],s[36:37],0x1000e
s_and_b64 s[52:53],s[12:13],s[48:49]
s_or_b64 s[52:53],s[52:53],s[50:51]
mm v[180:183],a[84:87],v[40:43],v[180:183]
s_and_b64 s[86:87],s[14:15],s[48:49]
v_cndmask_b32_e64 v216,v236,v196,s[52:53]
s_or_b64 s[86:87],s[86:87],s[50:51]
mm v[180:183],a[88:91],v[48:51],v[180:183]
s_and_b64 s[52:53],s[16:17],s[48:49]
v_cndmask_b32_e64 v217,v236,v196,s[86:87]
s_or_b64 s[52:53],s[52:53],s[50:51]
v_cndmask_b32_e64 v218,v236,v196,s[52:53]
s_and_b64 s[86:87],s[18:19],s[48:49]
s_or_b64 s[86:87],s[86:87],s[50:51]
v_cndmask_b32_e64 v219,v236,v196,s[86:87]
s_waitcnt vmcnt(5) lgkmcnt(0)
s_barrier
mm v[180:183],a[92:95],v[56:59],v[180:183]
s_bfe_i64 s[48:49],s[36:37],0x1000b
dr a[64:67],v235 offset:16384
v_max3_f32 v229,v184,v185,v186
v_max3_f32 v229,v229,v187,v188
mm v[184:187],a[96:99],v[32:35],v[216:219]
s_bfe_i64 s[50:51],s[36:37],0x1000f
dr a[68:71],v235 offset:24576
s_and_b64 s[52:53],s[12:13],s[48:49]
mm v[184:187],a[100:103],v[40:43],v[184:187]
s_or_b64 s[52:53],s[52:53],s[50:51]
v_cndmask_b32_e64 v220,v236,v196,s[52:53]
s_and_b64 s[86:87],s[14:15],s[48:49]
mm v[184:187],a[104:107],v[48:51],v[184:187]
s_or_b64 s[86:87],s[86:87],s[50:51]
dr a[72:75],v235 offset:17408
v_cndmask_b32_e64 v221,v236,v196,s[86:87]
s_and_b64 s[52:53],s[16:17],s[48:49]
)" R"(s_or_b64 s[52:53],s[52:53],s[50:51]
v_cndmask_b32_e64 v222,v236,v196,s[52:53]
s_and_b64 s[86:87],s[18:19],s[48:49]
s_or_b64 s[86:87],s[86:87],s[50:51]
v_cndmask_b32_e64 v223,v236,v196,s[86:87]
mm v[184:187],a[108:111],v[56:59],v[184:187]
dr a[76:79],v235 offset:25600
v_max3_f32 v229,v229,v189,v190
v_max_f32 v229,v229,v191
mm v[188:191],a[112:115],v[32:35],v[220:223]
v_add_f32 v144,v196,v144
v_add_f32 v145,v196,v145
mm v[188:191],a[116:119],v[40:43],v[188:191]
dr a[80:83],v235 offset:18432
v_add_f32 v146,v196,v146
v_add_f32 v147,v196,v147
v_add_f32 v148,v196,v148
v_add_f32 v149,v196,v149
v_add_f32 v150,v196,v150
v_add_f32 v151,v196,v151
ex2 v150,v150
ex2 v151,v151
ex2 v148,v148
ex2 v149,v149
ex2 v144,v144
ex2 v145,v145
ex2 v146,v146
ex2 v147,v147
mm v[188:191],a[120:123],v[48:51],v[188:191]
dr a[84:87],v235 offset:26624
cv v151,v150,v151
cv v150,v148,v149
cv v148,v144,v145
cv v149,v146,v147
ex2 v154,v154
ex2 v155,v155
ex2 v156,v156
ex2 v157,v157
ex2 v158,v158
ex2 v159,v159
mm v[188:191],a[124:127],v[56:59],v[188:191]
s_waitcnt lgkmcnt(4)
dr a[88:91],v235 offset:19456
cv v152,v152,v153
cv v153,v154,v155
cv v154,v156,v157
cv v155,v158,v159
ex2 v138,v138
ex2 v139,v139
ex2 v140,v140
ex2 v141,v141
ex2 v142,v142
ex2 v143,v143
mm v[68:71],a[64:67],v[148:151],0
dr a[92:95],v235 offset:27648
v_add_f32 v128,v192,v128
v_add_f32 v129,v192,v129
v_add_f32 v130,v192,v130
v_add_f32 v131,v192,v131
v_add_f32 v132,v192,v132
v_add_f32 v133,v192,v133
v_add_f32 v134,v192,v134
v_add_f32 v135,v192,v135
ex2 v134,v134
)" R"(ex2 v135,v135
ex2 v132,v132
ex2 v133,v133
ex2 v128,v128
ex2 v129,v129
ex2 v130,v130
ex2 v131,v131
cv v136,v136,v137
cv v137,v138,v139
cv v138,v140,v141
cv v139,v142,v143
mm v[68:71],a[68:71],v[152:155],v[68:71]
dr a[96:99],v235 offset:20480
cv v135,v134,v135
cv v134,v132,v133
cv v132,v128,v129
cv v133,v130,v131
mm v[4:7],a[68:71],v[136:139],0
dr a[100:103],v235 offset:28672
aw a128,0
mm v[4:7],a[64:67],v[132:135],v[4:7]
s_waitcnt lgkmcnt(6)
dr a[104:107],v235 offset:21504
aw a129,0
mm v[12:15],a[72:75],v[132:135],0
dr a[108:111],v235 offset:29696
aw a132,0
mm v[12:15],a[76:79],v[136:139],v[12:15]
dr a[112:115],v235 offset:22528
aw a133,0
mm v[76:79],a[76:79],v[152:155],0
dr a[116:119],v235 offset:30720
v_max3_f32 v228,v168,v169,v170
mm v[76:79],a[72:75],v[148:151],v[76:79]
s_waitcnt lgkmcnt(6)
dr a[120:123],v235 offset:23552
v_max3_f32 v228,v228,v171,v172
mm v[84:87],a[80:83],v[148:151],0
dr a[124:127],v235 offset:31744
v_max3_f32 v228,v228,v173,v174
mm v[84:87],a[84:87],v[152:155],v[84:87]
ex2 v182,v182
mm v[20:23],a[84:87],v[136:139],0
ex2 v183,v183
mm v[20:23],a[80:83],v[132:135],v[20:23]
ex2 v180,v180
mm v[28:31],a[88:91],v[132:135],0
ex2 v181,v181
mm v[28:31],a[92:95],v[136:139],v[28:31]
ex2 v176,v176
mm v[92:95],a[92:95],v[152:155],0
s_waitcnt lgkmcnt(0)
s_barrier
mm v[92:95],a[88:91],v[148:151],v[92:95]
dr a[64:67],v232 offset:0
v_max_f32 v228,v228,v175
v_smfmac_f32_16x16x64_bf16 a[128:131],a[144:147],v[132:139],v246
dr a[68:71],v232 offset:1024
v_mov_b32 v230,v228
v_smfmac_f32_16x16x64_bf16 a[132:135],a[144:147],v[148:155],v246
ex2 v177,v177
mm v[100:103],a[96:99],v[148:151],0
dr a[72:75],v232 offset:2048
v_mov_b32 v231,v229
)" R"(mm v[100:103],a[100:103],v[152:155],v[100:103]
dr a[76:79],v232 offset:3072
v_sub_f32 v193,0xc2400000,v224
mm v[36:39],a[100:103],v[136:139],0
v_permlane32_swap_b32 v228,v230
mm v[36:39],a[96:99],v[132:135],v[36:39]
dr a[80:83],v232 offset:4096
v_max_f32 v228,v228,v230
mm v[44:47],a[104:107],v[132:135],0
dr a[84:87],v232 offset:5120
v_mov_b32 v230,v228
mm v[44:47],a[108:111],v[136:139],v[44:47]
v_permlane32_swap_b32 v229,v231
mm v[108:111],a[108:111],v[152:155],0
dr a[88:91],v232 offset:6144
v_max_f32 v229,v229,v231
mm v[108:111],a[104:107],v[148:151],v[108:111]
dr a[92:95],v232 offset:7168
v_mov_b32 v231,v229
mm v[116:119],a[112:115],v[148:151],0
v_permlane16_swap_b32 v228,v230
mm v[116:119],a[116:119],v[152:155],v[116:119]
dr a[96:99],v232 offset:8192
v_sub_f32 v194,0xc2400000,v224
mm v[52:55],a[116:119],v[136:139],0
dr a[100:103],v232 offset:9216
v_sub_f32 v195,0xc2400000,v224
mm v[52:55],a[112:115],v[132:135],v[52:55]
v_permlane16_swap_b32 v229,v231
mm v[60:63],a[120:123],v[132:135],0
ex2 v178,v178
mm v[60:63],a[124:127],v[136:139],v[60:63]
ex2 v179,v179
mm v[124:127],a[124:127],v[152:155],0
dr a[104:107],v232 offset:10240
v_max_f32 v228,v228,v230
mm v[124:127],a[120:123],v[148:151],v[124:127]
dr a[108:111],v232 offset:11264
v_max_f32 v229,v229,v231
v_sub_f32 v200,0xc2400000,v228
v_sub_f32 v201,0xc2400000,v228
v_sub_f32 v202,0xc2400000,v228
v_sub_f32 v203,0xc2400000,v228
v_sub_f32 v204,0xc2400000,v229
v_sub_f32 v205,0xc2400000,v229
v_sub_f32 v206,0xc2400000,v229
v_sub_f32 v207,0xc2400000,v229
cv v183,v182,v183
cv v182,v180,v181
cv v180,v176,v177
cv v181,v178,v179
s_branch T
SBE:
FX00:
s_waitcnt vmcnt(0) lgkmcnt(0)
s_nop 15
s_lshl_b32 s63,s26,7
s_lshr_b32 s60,s63,5
s_add_u32 s60,s60,2
s_lshl_b32 s61,s25,20
s_mov_b32 s62,s60
v_add_u32 v218,s63,v237
v_mov_b32 v216,v236
FX00_A:
)" R"(bl a[148:151],v232,s[8:11],s61 offen offset:0 sc1
bl a[152:155],v232,s[8:11],s61 offen offset:1024 sc1
bl a[156:159],v232,s[8:11],s61 offen offset:2048 sc1
bl a[160:163],v232,s[8:11],s61 offen offset:3072 sc1
bl a[164:167],v233,s[8:11],s61 offen offset:0 sc1
bl a[168:171],v233,s[8:11],s61 offen offset:1024 sc1
bl a[172:175],v233,s[8:11],s61 offen offset:2048 sc1
bl a[176:179],v233,s[8:11],s61 offen offset:3072 sc1
s_add_u32 s61,s61,8192
s_waitcnt vmcnt(7)
mm v[208:211],a[148:151],v[0:3],0
s_waitcnt vmcnt(6)
mm v[208:211],a[152:155],v[8:11],v[208:211]
s_waitcnt vmcnt(5)
mm v[208:211],a[156:159],v[16:19],v[208:211]
s_waitcnt vmcnt(4)
mm v[208:211],a[160:163],v[24:27],v[208:211]
s_waitcnt vmcnt(3)
mm v[212:215],a[164:167],v[0:3],0
s_waitcnt vmcnt(2)
mm v[212:215],a[168:171],v[8:11],v[212:215]
s_waitcnt vmcnt(1)
mm v[212:215],a[172:175],v[16:19],v[212:215]
s_waitcnt vmcnt(0)
mm v[212:215],a[176:179],v[24:27],v[212:215]
v_cmp_le_i32 vcc,0,v218
s_nop 1
v_cndmask_b32 v208,v236,v208,vcc
v_cmp_le_i32 vcc,1,v218
s_nop 1
v_cndmask_b32 v209,v236,v209,vcc
v_cmp_le_i32 vcc,2,v218
s_nop 1
v_cndmask_b32 v210,v236,v210,vcc
v_cmp_le_i32 vcc,3,v218
s_nop 1
v_cndmask_b32 v211,v236,v211,vcc
v_cmp_le_i32 vcc,16,v218
s_nop 1
v_cndmask_b32 v212,v236,v212,vcc
v_cmp_le_i32 vcc,17,v218
s_nop 1
v_cndmask_b32 v213,v236,v213,vcc
v_cmp_le_i32 vcc,18,v218
s_nop 1
v_cndmask_b32 v214,v236,v214,vcc
v_cmp_le_i32 vcc,19,v218
s_nop 1
v_cndmask_b32 v215,v236,v215,vcc
v_max3_f32 v216,v216,v208,v209
v_max3_f32 v216,v216,v210,v211
v_max3_f32 v216,v216,v212,v213
v_max3_f32 v216,v216,v214,v215
s_add_u32 s61,s61,8192
v_add_u32 v218,-32,v218
s_sub_u32 s62,s62,1
s_cmp_lg_u32 s62,0
s_cbranch_scc1 FX00_A
v_mov_b32 v217,v216
s_nop 1
v_permlane32_swap_b32 v216,v217
v_max_f32 v216,v216,v217
v_mov_b32 v217,v216
s_nop 1
)" R"(v_permlane16_swap_b32 v216,v217
v_max_f32 v216,v216,v217
v_sub_f32 v220,0,v216
v_sub_f32 v221,0,v216
v_sub_f32 v222,0,v216
v_sub_f32 v223,0,v216
v_mov_b32 v4,0
v_mov_b32 v5,0
v_mov_b32 v6,0
v_mov_b32 v7,0
v_mov_b32 v12,0
v_mov_b32 v13,0
v_mov_b32 v14,0
v_mov_b32 v15,0
v_mov_b32 v20,0
v_mov_b32 v21,0
v_mov_b32 v22,0
v_mov_b32 v23,0
v_mov_b32 v28,0
v_mov_b32 v29,0
v_mov_b32 v30,0
v_mov_b32 v31,0
v_mov_b32 v36,0
v_mov_b32 v37,0
v_mov_b32 v38,0
v_mov_b32 v39,0
v_mov_b32 v44,0
v_mov_b32 v45,0
v_mov_b32 v46,0
v_mov_b32 v47,0
v_mov_b32 v52,0
v_mov_b32 v53,0
v_mov_b32 v54,0
v_mov_b32 v55,0
v_mov_b32 v60,0
v_mov_b32 v61,0
v_mov_b32 v62,0
v_mov_b32 v63,0
aw a128,0
aw a129,0
aw a130,0
aw a131,0
s_lshl_b32 s61,s25,20
s_mov_b32 s62,s60
v_add_u32 v218,s63,v237
FX00_B:
bl a[148:151],v232,s[8:11],s61 offen offset:0 sc1
bl a[152:155],v232,s[8:11],s61 offen offset:1024 sc1
bl a[156:159],v232,s[8:11],s61 offen offset:2048 sc1
bl a[160:163],v232,s[8:11],s61 offen offset:3072 sc1
bl a[164:167],v233,s[8:11],s61 offen offset:0 sc1
bl a[168:171],v233,s[8:11],s61 offen offset:1024 sc1
bl a[172:175],v233,s[8:11],s61 offen offset:2048 sc1
bl a[176:179],v233,s[8:11],s61 offen offset:3072 sc1
s_add_u32 s61,s61,8192
s_waitcnt vmcnt(7)
mm v[208:211],a[148:151],v[0:3],v[220:223]
s_waitcnt vmcnt(6)
mm v[208:211],a[152:155],v[8:11],v[208:211]
s_waitcnt vmcnt(5)
mm v[208:211],a[156:159],v[16:19],v[208:211]
s_waitcnt vmcnt(4)
mm v[208:211],a[160:163],v[24:27],v[208:211]
s_waitcnt vmcnt(3)
)" R"(mm v[212:215],a[164:167],v[0:3],v[220:223]
s_waitcnt vmcnt(2)
mm v[212:215],a[168:171],v[8:11],v[212:215]
s_waitcnt vmcnt(1)
mm v[212:215],a[172:175],v[16:19],v[212:215]
s_waitcnt vmcnt(0)
mm v[212:215],a[176:179],v[24:27],v[212:215]
v_cmp_le_i32 vcc,0,v218
s_nop 1
v_cndmask_b32 v208,v236,v208,vcc
v_cmp_le_i32 vcc,1,v218
s_nop 1
v_cndmask_b32 v209,v236,v209,vcc
v_cmp_le_i32 vcc,2,v218
s_nop 1
v_cndmask_b32 v210,v236,v210,vcc
v_cmp_le_i32 vcc,3,v218
s_nop 1
v_cndmask_b32 v211,v236,v211,vcc
v_cmp_le_i32 vcc,16,v218
s_nop 1
v_cndmask_b32 v212,v236,v212,vcc
v_cmp_le_i32 vcc,17,v218
s_nop 1
v_cndmask_b32 v213,v236,v213,vcc
v_cmp_le_i32 vcc,18,v218
s_nop 1
v_cndmask_b32 v214,v236,v214,vcc
v_cmp_le_i32 vcc,19,v218
s_nop 1
v_cndmask_b32 v215,v236,v215,vcc
bl a[148:151],v232,s[8:11],s61 offen offset:0 sc1
bl a[152:155],v232,s[8:11],s61 offen offset:1024 sc1
bl a[156:159],v232,s[8:11],s61 offen offset:2048 sc1
bl a[160:163],v232,s[8:11],s61 offen offset:3072 sc1
bl a[164:167],v233,s[8:11],s61 offen offset:0 sc1
bl a[168:171],v233,s[8:11],s61 offen offset:1024 sc1
bl a[172:175],v233,s[8:11],s61 offen offset:2048 sc1
bl a[176:179],v233,s[8:11],s61 offen offset:3072 sc1
s_add_u32 s61,s61,8192
ex2 v208,v208
ex2 v209,v209
ex2 v210,v210
ex2 v211,v211
ex2 v212,v212
ex2 v213,v213
ex2 v214,v214
ex2 v215,v215
cv v208,v208,v209
cv v209,v210,v211
cv v210,v212,v213
cv v211,v214,v215
s_nop 1
mm a[128:131],a[144:147],v[208:211],a[128:131]
s_waitcnt vmcnt(7)
mm v[4:7],a[148:151],v[208:211],v[4:7]
s_waitcnt vmcnt(6)
mm v[12:15],a[152:155],v[208:211],v[12:15]
s_waitcnt vmcnt(5)
mm v[20:23],a[156:159],v[208:211],v[20:23]
s_waitcnt vmcnt(4)
mm v[28:31],a[160:163],v[208:211],v[28:31]
s_waitcnt vmcnt(3)
mm v[36:39],a[164:167],v[208:211],v[36:39]
)" R"(s_waitcnt vmcnt(2)
mm v[44:47],a[168:171],v[208:211],v[44:47]
s_waitcnt vmcnt(1)
mm v[52:55],a[172:175],v[208:211],v[52:55]
s_waitcnt vmcnt(0)
mm v[60:63],a[176:179],v[208:211],v[60:63]
s_nop 15
v_add_u32 v218,-32,v218
s_sub_u32 s62,s62,1
s_cmp_lg_u32 s62,0
s_cbranch_scc1 FX00_B
ard v244,a128
s_nop 1
s_branch FX00_D
FX01:
s_waitcnt vmcnt(0) lgkmcnt(0)
s_nop 15
s_lshl_b32 s63,s26,7
s_add_u32 s63,s63,0x40
s_lshr_b32 s60,s63,5
s_add_u32 s60,s60,2
s_lshl_b32 s61,s25,20
s_mov_b32 s62,s60
v_add_u32 v218,s63,v238
v_mov_b32 v216,v236
FX01_A:
bl a[148:151],v232,s[8:11],s61 offen offset:0 sc1
bl a[152:155],v232,s[8:11],s61 offen offset:1024 sc1
bl a[156:159],v232,s[8:11],s61 offen offset:2048 sc1
bl a[160:163],v232,s[8:11],s61 offen offset:3072 sc1
bl a[164:167],v233,s[8:11],s61 offen offset:0 sc1
bl a[168:171],v233,s[8:11],s61 offen offset:1024 sc1
bl a[172:175],v233,s[8:11],s61 offen offset:2048 sc1
bl a[176:179],v233,s[8:11],s61 offen offset:3072 sc1
s_add_u32 s61,s61,8192
s_waitcnt vmcnt(7)
mm v[208:211],a[148:151],v[32:35],0
s_waitcnt vmcnt(6)
mm v[208:211],a[152:155],v[40:43],v[208:211]
s_waitcnt vmcnt(5)
mm v[208:211],a[156:159],v[48:51],v[208:211]
s_waitcnt vmcnt(4)
mm v[208:211],a[160:163],v[56:59],v[208:211]
s_waitcnt vmcnt(3)
mm v[212:215],a[164:167],v[32:35],0
s_waitcnt vmcnt(2)
mm v[212:215],a[168:171],v[40:43],v[212:215]
s_waitcnt vmcnt(1)
mm v[212:215],a[172:175],v[48:51],v[212:215]
s_waitcnt vmcnt(0)
mm v[212:215],a[176:179],v[56:59],v[212:215]
v_cmp_le_i32 vcc,0,v218
s_nop 1
v_cndmask_b32 v208,v236,v208,vcc
v_cmp_le_i32 vcc,1,v218
s_nop 1
v_cndmask_b32 v209,v236,v209,vcc
v_cmp_le_i32 vcc,2,v218
s_nop 1
v_cndmask_b32 v210,v236,v210,vcc
v_cmp_le_i32 vcc,3,v218
s_nop 1
v_cndmask_b32 v211,v236,v211,vcc
v_cmp_le_i32 vcc,16,v218
)" R"(s_nop 1
v_cndmask_b32 v212,v236,v212,vcc
v_cmp_le_i32 vcc,17,v218
s_nop 1
v_cndmask_b32 v213,v236,v213,vcc
v_cmp_le_i32 vcc,18,v218
s_nop 1
v_cndmask_b32 v214,v236,v214,vcc
v_cmp_le_i32 vcc,19,v218
s_nop 1
v_cndmask_b32 v215,v236,v215,vcc
v_max3_f32 v216,v216,v208,v209
v_max3_f32 v216,v216,v210,v211
v_max3_f32 v216,v216,v212,v213
v_max3_f32 v216,v216,v214,v215
s_add_u32 s61,s61,8192
v_add_u32 v218,-32,v218
s_sub_u32 s62,s62,1
s_cmp_lg_u32 s62,0
s_cbranch_scc1 FX01_A
v_mov_b32 v217,v216
s_nop 1
v_permlane32_swap_b32 v216,v217
v_max_f32 v216,v216,v217
v_mov_b32 v217,v216
s_nop 1
v_permlane16_swap_b32 v216,v217
v_max_f32 v216,v216,v217
v_sub_f32 v220,0,v216
v_sub_f32 v221,0,v216
v_sub_f32 v222,0,v216
v_sub_f32 v223,0,v216
v_mov_b32 v68,0
v_mov_b32 v69,0
v_mov_b32 v70,0
v_mov_b32 v71,0
v_mov_b32 v76,0
v_mov_b32 v77,0
v_mov_b32 v78,0
v_mov_b32 v79,0
v_mov_b32 v84,0
v_mov_b32 v85,0
v_mov_b32 v86,0
v_mov_b32 v87,0
v_mov_b32 v92,0
v_mov_b32 v93,0
v_mov_b32 v94,0
v_mov_b32 v95,0
v_mov_b32 v100,0
v_mov_b32 v101,0
v_mov_b32 v102,0
v_mov_b32 v103,0
v_mov_b32 v108,0
v_mov_b32 v109,0
v_mov_b32 v110,0
v_mov_b32 v111,0
v_mov_b32 v116,0
v_mov_b32 v117,0
v_mov_b32 v118,0
v_mov_b32 v119,0
v_mov_b32 v124,0
v_mov_b32 v125,0
v_mov_b32 v126,0
v_mov_b32 v127,0
)" R"(aw a132,0
aw a133,0
aw a134,0
aw a135,0
s_lshl_b32 s61,s25,20
s_mov_b32 s62,s60
v_add_u32 v218,s63,v238
FX01_B:
bl a[148:151],v232,s[8:11],s61 offen offset:0 sc1
bl a[152:155],v232,s[8:11],s61 offen offset:1024 sc1
bl a[156:159],v232,s[8:11],s61 offen offset:2048 sc1
bl a[160:163],v232,s[8:11],s61 offen offset:3072 sc1
bl a[164:167],v233,s[8:11],s61 offen offset:0 sc1
bl a[168:171],v233,s[8:11],s61 offen offset:1024 sc1
bl a[172:175],v233,s[8:11],s61 offen offset:2048 sc1
bl a[176:179],v233,s[8:11],s61 offen offset:3072 sc1
s_add_u32 s61,s61,8192
s_waitcnt vmcnt(7)
mm v[208:211],a[148:151],v[32:35],v[220:223]
s_waitcnt vmcnt(6)
mm v[208:211],a[152:155],v[40:43],v[208:211]
s_waitcnt vmcnt(5)
mm v[208:211],a[156:159],v[48:51],v[208:211]
s_waitcnt vmcnt(4)
mm v[208:211],a[160:163],v[56:59],v[208:211]
s_waitcnt vmcnt(3)
mm v[212:215],a[164:167],v[32:35],v[220:223]
s_waitcnt vmcnt(2)
mm v[212:215],a[168:171],v[40:43],v[212:215]
s_waitcnt vmcnt(1)
mm v[212:215],a[172:175],v[48:51],v[212:215]
s_waitcnt vmcnt(0)
mm v[212:215],a[176:179],v[56:59],v[212:215]
v_cmp_le_i32 vcc,0,v218
s_nop 1
v_cndmask_b32 v208,v236,v208,vcc
v_cmp_le_i32 vcc,1,v218
s_nop 1
v_cndmask_b32 v209,v236,v209,vcc
v_cmp_le_i32 vcc,2,v218
s_nop 1
v_cndmask_b32 v210,v236,v210,vcc
v_cmp_le_i32 vcc,3,v218
s_nop 1
v_cndmask_b32 v211,v236,v211,vcc
v_cmp_le_i32 vcc,16,v218
s_nop 1
v_cndmask_b32 v212,v236,v212,vcc
v_cmp_le_i32 vcc,17,v218
s_nop 1
v_cndmask_b32 v213,v236,v213,vcc
v_cmp_le_i32 vcc,18,v218
s_nop 1
v_cndmask_b32 v214,v236,v214,vcc
v_cmp_le_i32 vcc,19,v218
s_nop 1
v_cndmask_b32 v215,v236,v215,vcc
bl a[148:151],v232,s[8:11],s61 offen offset:0 sc1
bl a[152:155],v232,s[8:11],s61 offen offset:1024 sc1
bl a[156:159],v232,s[8:11],s61 offen offset:2048 sc1
bl a[160:163],v232,s[8:11],s61 offen offset:3072 sc1
bl a[164:167],v233,s[8:11],s61 offen offset:0 sc1
bl a[168:171],v233,s[8:11],s61 offen offset:1024 sc1
bl a[172:175],v233,s[8:11],s61 offen offset:2048 sc1
)" R"(bl a[176:179],v233,s[8:11],s61 offen offset:3072 sc1
s_add_u32 s61,s61,8192
ex2 v208,v208
ex2 v209,v209
ex2 v210,v210
ex2 v211,v211
ex2 v212,v212
ex2 v213,v213
ex2 v214,v214
ex2 v215,v215
cv v208,v208,v209
cv v209,v210,v211
cv v210,v212,v213
cv v211,v214,v215
s_nop 1
mm a[132:135],a[144:147],v[208:211],a[132:135]
s_waitcnt vmcnt(7)
mm v[68:71],a[148:151],v[208:211],v[68:71]
s_waitcnt vmcnt(6)
mm v[76:79],a[152:155],v[208:211],v[76:79]
s_waitcnt vmcnt(5)
mm v[84:87],a[156:159],v[208:211],v[84:87]
s_waitcnt vmcnt(4)
mm v[92:95],a[160:163],v[208:211],v[92:95]
s_waitcnt vmcnt(3)
mm v[100:103],a[164:167],v[208:211],v[100:103]
s_waitcnt vmcnt(2)
mm v[108:111],a[168:171],v[208:211],v[108:111]
s_waitcnt vmcnt(1)
mm v[116:119],a[172:175],v[208:211],v[116:119]
s_waitcnt vmcnt(0)
mm v[124:127],a[176:179],v[208:211],v[124:127]
s_nop 15
v_add_u32 v218,-32,v218
s_sub_u32 s62,s62,1
s_cmp_lg_u32 s62,0
s_cbranch_scc1 FX01_B
ard v245,a132
s_nop 1
s_branch FX01_D
FX10:
s_waitcnt vmcnt(0) lgkmcnt(0)
s_nop 15
s_mul_i32 s63,s26,-128
s_add_u32 s63,s63,0x780
s_lshr_b32 s60,s63,5
s_add_u32 s60,s60,2
s_lshl_b32 s61,s25,20
s_mov_b32 s62,s60
v_add_u32 v218,s63,v237
v_mov_b32 v216,v236
FX10_A:
bl v[128:131],v232,s[8:11],s61 offen offset:0 sc1
bl v[132:135],v232,s[8:11],s61 offen offset:1024 sc1
bl v[136:139],v232,s[8:11],s61 offen offset:2048 sc1
bl v[140:143],v232,s[8:11],s61 offen offset:3072 sc1
bl v[144:147],v233,s[8:11],s61 offen offset:0 sc1
bl v[148:151],v233,s[8:11],s61 offen offset:1024 sc1
bl v[152:155],v233,s[8:11],s61 offen offset:2048 sc1
bl v[156:159],v233,s[8:11],s61 offen offset:3072 sc1
s_add_u32 s61,s61,8192
s_waitcnt vmcnt(7)
mm v[208:211],v[128:131],v[64:67],0
s_waitcnt vmcnt(6)
)" R"(mm v[208:211],v[132:135],v[72:75],v[208:211]
s_waitcnt vmcnt(5)
mm v[208:211],v[136:139],v[80:83],v[208:211]
s_waitcnt vmcnt(4)
mm v[208:211],v[140:143],v[88:91],v[208:211]
s_waitcnt vmcnt(3)
mm v[212:215],v[144:147],v[64:67],0
s_waitcnt vmcnt(2)
mm v[212:215],v[148:151],v[72:75],v[212:215]
s_waitcnt vmcnt(1)
mm v[212:215],v[152:155],v[80:83],v[212:215]
s_waitcnt vmcnt(0)
mm v[212:215],v[156:159],v[88:91],v[212:215]
v_cmp_le_i32 vcc,0,v218
s_nop 1
v_cndmask_b32 v208,v236,v208,vcc
v_cmp_le_i32 vcc,1,v218
s_nop 1
v_cndmask_b32 v209,v236,v209,vcc
v_cmp_le_i32 vcc,2,v218
s_nop 1
v_cndmask_b32 v210,v236,v210,vcc
v_cmp_le_i32 vcc,3,v218
s_nop 1
v_cndmask_b32 v211,v236,v211,vcc
v_cmp_le_i32 vcc,16,v218
s_nop 1
v_cndmask_b32 v212,v236,v212,vcc
v_cmp_le_i32 vcc,17,v218
s_nop 1
v_cndmask_b32 v213,v236,v213,vcc
v_cmp_le_i32 vcc,18,v218
s_nop 1
v_cndmask_b32 v214,v236,v214,vcc
v_cmp_le_i32 vcc,19,v218
s_nop 1
v_cndmask_b32 v215,v236,v215,vcc
v_max3_f32 v216,v216,v208,v209
v_max3_f32 v216,v216,v210,v211
v_max3_f32 v216,v216,v212,v213
v_max3_f32 v216,v216,v214,v215
s_add_u32 s61,s61,8192
v_add_u32 v218,-32,v218
s_sub_u32 s62,s62,1
s_cmp_lg_u32 s62,0
s_cbranch_scc1 FX10_A
v_mov_b32 v217,v216
s_nop 1
v_permlane32_swap_b32 v216,v217
v_max_f32 v216,v216,v217
v_mov_b32 v217,v216
s_nop 1
v_permlane16_swap_b32 v216,v217
v_max_f32 v216,v216,v217
v_sub_f32 v220,0,v216
v_sub_f32 v221,0,v216
v_sub_f32 v222,0,v216
v_sub_f32 v223,0,v216
v_mov_b32 v4,0
v_mov_b32 v5,0
v_mov_b32 v6,0
v_mov_b32 v7,0
v_mov_b32 v12,0
v_mov_b32 v13,0
)" R"(v_mov_b32 v14,0
v_mov_b32 v15,0
v_mov_b32 v20,0
v_mov_b32 v21,0
v_mov_b32 v22,0
v_mov_b32 v23,0
v_mov_b32 v28,0
v_mov_b32 v29,0
v_mov_b32 v30,0
v_mov_b32 v31,0
v_mov_b32 v36,0
v_mov_b32 v37,0
v_mov_b32 v38,0
v_mov_b32 v39,0
v_mov_b32 v44,0
v_mov_b32 v45,0
v_mov_b32 v46,0
v_mov_b32 v47,0
v_mov_b32 v52,0
v_mov_b32 v53,0
v_mov_b32 v54,0
v_mov_b32 v55,0
v_mov_b32 v60,0
v_mov_b32 v61,0
v_mov_b32 v62,0
v_mov_b32 v63,0
aw a136,0
aw a137,0
aw a138,0
aw a139,0
s_lshl_b32 s61,s25,20
s_mov_b32 s62,s60
v_add_u32 v218,s63,v237
FX10_B:
bl v[128:131],v232,s[8:11],s61 offen offset:0 sc1
bl v[132:135],v232,s[8:11],s61 offen offset:1024 sc1
bl v[136:139],v232,s[8:11],s61 offen offset:2048 sc1
bl v[140:143],v232,s[8:11],s61 offen offset:3072 sc1
bl v[144:147],v233,s[8:11],s61 offen offset:0 sc1
bl v[148:151],v233,s[8:11],s61 offen offset:1024 sc1
bl v[152:155],v233,s[8:11],s61 offen offset:2048 sc1
bl v[156:159],v233,s[8:11],s61 offen offset:3072 sc1
s_add_u32 s61,s61,8192
s_waitcnt vmcnt(7)
mm v[208:211],v[128:131],v[64:67],v[220:223]
s_waitcnt vmcnt(6)
mm v[208:211],v[132:135],v[72:75],v[208:211]
s_waitcnt vmcnt(5)
mm v[208:211],v[136:139],v[80:83],v[208:211]
s_waitcnt vmcnt(4)
mm v[208:211],v[140:143],v[88:91],v[208:211]
s_waitcnt vmcnt(3)
mm v[212:215],v[144:147],v[64:67],v[220:223]
s_waitcnt vmcnt(2)
mm v[212:215],v[148:151],v[72:75],v[212:215]
s_waitcnt vmcnt(1)
mm v[212:215],v[152:155],v[80:83],v[212:215]
s_waitcnt vmcnt(0)
mm v[212:215],v[156:159],v[88:91],v[212:215]
v_cmp_le_i32 vcc,0,v218
s_nop 1
v_cndmask_b32 v208,v236,v208,vcc
v_cmp_le_i32 vcc,1,v218
s_nop 1
)" R"(v_cndmask_b32 v209,v236,v209,vcc
v_cmp_le_i32 vcc,2,v218
s_nop 1
v_cndmask_b32 v210,v236,v210,vcc
v_cmp_le_i32 vcc,3,v218
s_nop 1
v_cndmask_b32 v211,v236,v211,vcc
v_cmp_le_i32 vcc,16,v218
s_nop 1
v_cndmask_b32 v212,v236,v212,vcc
v_cmp_le_i32 vcc,17,v218
s_nop 1
v_cndmask_b32 v213,v236,v213,vcc
v_cmp_le_i32 vcc,18,v218
s_nop 1
v_cndmask_b32 v214,v236,v214,vcc
v_cmp_le_i32 vcc,19,v218
s_nop 1
v_cndmask_b32 v215,v236,v215,vcc
bl v[128:131],v232,s[8:11],s61 offen offset:0 sc1
bl v[132:135],v232,s[8:11],s61 offen offset:1024 sc1
bl v[136:139],v232,s[8:11],s61 offen offset:2048 sc1
bl v[140:143],v232,s[8:11],s61 offen offset:3072 sc1
bl v[144:147],v233,s[8:11],s61 offen offset:0 sc1
bl v[148:151],v233,s[8:11],s61 offen offset:1024 sc1
bl v[152:155],v233,s[8:11],s61 offen offset:2048 sc1
bl v[156:159],v233,s[8:11],s61 offen offset:3072 sc1
s_add_u32 s61,s61,8192
ex2 v208,v208
ex2 v209,v209
ex2 v210,v210
ex2 v211,v211
ex2 v212,v212
ex2 v213,v213
ex2 v214,v214
ex2 v215,v215
cv v208,v208,v209
cv v209,v210,v211
cv v210,v212,v213
cv v211,v214,v215
s_nop 1
mm a[136:139],a[144:147],v[208:211],a[136:139]
s_waitcnt vmcnt(7)
mm v[4:7],v[128:131],v[208:211],v[4:7]
s_waitcnt vmcnt(6)
mm v[12:15],v[132:135],v[208:211],v[12:15]
s_waitcnt vmcnt(5)
mm v[20:23],v[136:139],v[208:211],v[20:23]
s_waitcnt vmcnt(4)
mm v[28:31],v[140:143],v[208:211],v[28:31]
s_waitcnt vmcnt(3)
mm v[36:39],v[144:147],v[208:211],v[36:39]
s_waitcnt vmcnt(2)
mm v[44:47],v[148:151],v[208:211],v[44:47]
s_waitcnt vmcnt(1)
mm v[52:55],v[152:155],v[208:211],v[52:55]
s_waitcnt vmcnt(0)
mm v[60:63],v[156:159],v[208:211],v[60:63]
s_nop 15
v_add_u32 v218,-32,v218
s_sub_u32 s62,s62,1
s_cmp_lg_u32 s62,0
s_cbranch_scc1 FX10_B
ard v244,a136
)" R"(s_nop 1
s_branch FX10_D
FX11:
s_waitcnt vmcnt(0) lgkmcnt(0)
s_nop 15
s_mul_i32 s63,s26,-128
s_add_u32 s63,s63,0x7c0
s_lshr_b32 s60,s63,5
s_add_u32 s60,s60,2
s_lshl_b32 s61,s25,20
s_mov_b32 s62,s60
v_add_u32 v218,s63,v238
v_mov_b32 v216,v236
FX11_A:
bl v[128:131],v232,s[8:11],s61 offen offset:0 sc1
bl v[132:135],v232,s[8:11],s61 offen offset:1024 sc1
bl v[136:139],v232,s[8:11],s61 offen offset:2048 sc1
bl v[140:143],v232,s[8:11],s61 offen offset:3072 sc1
bl v[144:147],v233,s[8:11],s61 offen offset:0 sc1
bl v[148:151],v233,s[8:11],s61 offen offset:1024 sc1
bl v[152:155],v233,s[8:11],s61 offen offset:2048 sc1
bl v[156:159],v233,s[8:11],s61 offen offset:3072 sc1
s_add_u32 s61,s61,8192
s_waitcnt vmcnt(7)
mm v[208:211],v[128:131],v[96:99],0
s_waitcnt vmcnt(6)
mm v[208:211],v[132:135],v[104:107],v[208:211]
s_waitcnt vmcnt(5)
mm v[208:211],v[136:139],v[112:115],v[208:211]
s_waitcnt vmcnt(4)
mm v[208:211],v[140:143],v[120:123],v[208:211]
s_waitcnt vmcnt(3)
mm v[212:215],v[144:147],v[96:99],0
s_waitcnt vmcnt(2)
mm v[212:215],v[148:151],v[104:107],v[212:215]
s_waitcnt vmcnt(1)
mm v[212:215],v[152:155],v[112:115],v[212:215]
s_waitcnt vmcnt(0)
mm v[212:215],v[156:159],v[120:123],v[212:215]
v_cmp_le_i32 vcc,0,v218
s_nop 1
v_cndmask_b32 v208,v236,v208,vcc
v_cmp_le_i32 vcc,1,v218
s_nop 1
v_cndmask_b32 v209,v236,v209,vcc
v_cmp_le_i32 vcc,2,v218
s_nop 1
v_cndmask_b32 v210,v236,v210,vcc
v_cmp_le_i32 vcc,3,v218
s_nop 1
v_cndmask_b32 v211,v236,v211,vcc
v_cmp_le_i32 vcc,16,v218
s_nop 1
v_cndmask_b32 v212,v236,v212,vcc
v_cmp_le_i32 vcc,17,v218
s_nop 1
v_cndmask_b32 v213,v236,v213,vcc
v_cmp_le_i32 vcc,18,v218
s_nop 1
v_cndmask_b32 v214,v236,v214,vcc
v_cmp_le_i32 vcc,19,v218
s_nop 1
v_cndmask_b32 v215,v236,v215,vcc
v_max3_f32 v216,v216,v208,v209
)" R"(v_max3_f32 v216,v216,v210,v211
v_max3_f32 v216,v216,v212,v213
v_max3_f32 v216,v216,v214,v215
s_add_u32 s61,s61,8192
v_add_u32 v218,-32,v218
s_sub_u32 s62,s62,1
s_cmp_lg_u32 s62,0
s_cbranch_scc1 FX11_A
v_mov_b32 v217,v216
s_nop 1
v_permlane32_swap_b32 v216,v217
v_max_f32 v216,v216,v217
v_mov_b32 v217,v216
s_nop 1
v_permlane16_swap_b32 v216,v217
v_max_f32 v216,v216,v217
v_sub_f32 v220,0,v216
v_sub_f32 v221,0,v216
v_sub_f32 v222,0,v216
v_sub_f32 v223,0,v216
v_mov_b32 v68,0
v_mov_b32 v69,0
v_mov_b32 v70,0
v_mov_b32 v71,0
v_mov_b32 v76,0
v_mov_b32 v77,0
v_mov_b32 v78,0
v_mov_b32 v79,0
v_mov_b32 v84,0
v_mov_b32 v85,0
v_mov_b32 v86,0
v_mov_b32 v87,0
v_mov_b32 v92,0
v_mov_b32 v93,0
v_mov_b32 v94,0
v_mov_b32 v95,0
v_mov_b32 v100,0
v_mov_b32 v101,0
v_mov_b32 v102,0
v_mov_b32 v103,0
v_mov_b32 v108,0
v_mov_b32 v109,0
v_mov_b32 v110,0
v_mov_b32 v111,0
v_mov_b32 v116,0
v_mov_b32 v117,0
v_mov_b32 v118,0
v_mov_b32 v119,0
v_mov_b32 v124,0
v_mov_b32 v125,0
v_mov_b32 v126,0
v_mov_b32 v127,0
aw a140,0
aw a141,0
aw a142,0
aw a143,0
s_lshl_b32 s61,s25,20
s_mov_b32 s62,s60
v_add_u32 v218,s63,v238
FX11_B:
bl v[128:131],v232,s[8:11],s61 offen offset:0 sc1
bl v[132:135],v232,s[8:11],s61 offen offset:1024 sc1
bl v[136:139],v232,s[8:11],s61 offen offset:2048 sc1
bl v[140:143],v232,s[8:11],s61 offen offset:3072 sc1
)" R"(bl v[144:147],v233,s[8:11],s61 offen offset:0 sc1
bl v[148:151],v233,s[8:11],s61 offen offset:1024 sc1
bl v[152:155],v233,s[8:11],s61 offen offset:2048 sc1
bl v[156:159],v233,s[8:11],s61 offen offset:3072 sc1
s_add_u32 s61,s61,8192
s_waitcnt vmcnt(7)
mm v[208:211],v[128:131],v[96:99],v[220:223]
s_waitcnt vmcnt(6)
mm v[208:211],v[132:135],v[104:107],v[208:211]
s_waitcnt vmcnt(5)
mm v[208:211],v[136:139],v[112:115],v[208:211]
s_waitcnt vmcnt(4)
mm v[208:211],v[140:143],v[120:123],v[208:211]
s_waitcnt vmcnt(3)
mm v[212:215],v[144:147],v[96:99],v[220:223]
s_waitcnt vmcnt(2)
mm v[212:215],v[148:151],v[104:107],v[212:215]
s_waitcnt vmcnt(1)
mm v[212:215],v[152:155],v[112:115],v[212:215]
s_waitcnt vmcnt(0)
mm v[212:215],v[156:159],v[120:123],v[212:215]
v_cmp_le_i32 vcc,0,v218
s_nop 1
v_cndmask_b32 v208,v236,v208,vcc
v_cmp_le_i32 vcc,1,v218
s_nop 1
v_cndmask_b32 v209,v236,v209,vcc
v_cmp_le_i32 vcc,2,v218
s_nop 1
v_cndmask_b32 v210,v236,v210,vcc
v_cmp_le_i32 vcc,3,v218
s_nop 1
v_cndmask_b32 v211,v236,v211,vcc
v_cmp_le_i32 vcc,16,v218
s_nop 1
v_cndmask_b32 v212,v236,v212,vcc
v_cmp_le_i32 vcc,17,v218
s_nop 1
v_cndmask_b32 v213,v236,v213,vcc
v_cmp_le_i32 vcc,18,v218
s_nop 1
v_cndmask_b32 v214,v236,v214,vcc
v_cmp_le_i32 vcc,19,v218
s_nop 1
v_cndmask_b32 v215,v236,v215,vcc
bl v[128:131],v232,s[8:11],s61 offen offset:0 sc1
bl v[132:135],v232,s[8:11],s61 offen offset:1024 sc1
bl v[136:139],v232,s[8:11],s61 offen offset:2048 sc1
bl v[140:143],v232,s[8:11],s61 offen offset:3072 sc1
bl v[144:147],v233,s[8:11],s61 offen offset:0 sc1
bl v[148:151],v233,s[8:11],s61 offen offset:1024 sc1
bl v[152:155],v233,s[8:11],s61 offen offset:2048 sc1
bl v[156:159],v233,s[8:11],s61 offen offset:3072 sc1
s_add_u32 s61,s61,8192
ex2 v208,v208
ex2 v209,v209
ex2 v210,v210
ex2 v211,v211
ex2 v212,v212
ex2 v213,v213
ex2 v214,v214
ex2 v215,v215
cv v208,v208,v209
cv v209,v210,v211
)" R"(cv v210,v212,v213
cv v211,v214,v215
s_nop 1
mm a[140:143],a[144:147],v[208:211],a[140:143]
s_waitcnt vmcnt(7)
mm v[68:71],v[128:131],v[208:211],v[68:71]
s_waitcnt vmcnt(6)
mm v[76:79],v[132:135],v[208:211],v[76:79]
s_waitcnt vmcnt(5)
mm v[84:87],v[136:139],v[208:211],v[84:87]
s_waitcnt vmcnt(4)
mm v[92:95],v[140:143],v[208:211],v[92:95]
s_waitcnt vmcnt(3)
mm v[100:103],v[144:147],v[208:211],v[100:103]
s_waitcnt vmcnt(2)
mm v[108:111],v[148:151],v[208:211],v[108:111]
s_waitcnt vmcnt(1)
mm v[116:119],v[152:155],v[208:211],v[116:119]
s_waitcnt vmcnt(0)
mm v[124:127],v[156:159],v[208:211],v[124:127]
s_nop 15
v_add_u32 v218,-32,v218
s_sub_u32 s62,s62,1
s_cmp_lg_u32 s62,0
s_cbranch_scc1 FX11_B
ard v245,a140
s_nop 1
s_branch FX11_D
FW_S:
s_sleep 2
s_sub_u32 s100,s100,1
s_cmp_lt_i32 s100,0
s_cbranch_scc1 FW_D
buffer_load_dwordx2 v[248:249],v219,s[8:11],s63 offen sc1
s_waitcnt vmcnt(0)
v_cmp_ne_u64 vcc,s[54:55],v[248:249]
s_cbranch_vccnz FW_S
s_branch FW_D
FWO_S:
s_sleep 2
s_sub_u32 s100,s100,1
s_cmp_lt_i32 s100,0
s_cbranch_scc1 FWO_D
buffer_load_dwordx2 v[228:229],v226,s[8:11],s71 offen sc1
s_waitcnt vmcnt(0)
v_cmp_ne_u64 vcc,s[54:55],v[228:229]
s_cbranch_vccnz FWO_S
s_branch FWO_D
RED_S:
s_sleep 2
s_sub_u32 s100,s100,1
s_cmp_lt_i32 s100,0
s_cbranch_scc1 RED_D
buffer_load_dwordx2 v[128:129],v146,s[8:11],s69 offen sc1
s_waitcnt vmcnt(0)
v_cmp_ne_u64 vcc,s[54:55],v[128:129]
s_cbranch_vccnz RED_S
s_branch RED_D
FKV_S:
s_sleep 2
s_sub_u32 s100,s100,1
s_cmp_lt_i32 s100,0
s_cbranch_scc1 FKV_D
buffer_load_dwordx2 v[228:229],v227,s[8:11],s71 offen sc1
)" R"(s_waitcnt vmcnt(0)
v_cmp_ne_u64 vcc,s[54:55],v[228:229]
s_cbranch_vccnz FKV_S
s_branch FKV_D
FA20_S:
s_sleep 2
s_sub_u32 s100,s100,1
s_cmp_lt_i32 s100,0
s_cbranch_scc1 FA20_D
buffer_load_dwordx2 v[240:241],v242,s[8:11],s46 offen sc1
s_waitcnt vmcnt(0)
v_cmp_ne_u64 vcc,s[54:55],v[240:241]
s_cbranch_vccnz FA20_S
s_branch FA20_D
FA21_S:
s_sleep 2
s_sub_u32 s100,s100,1
s_cmp_lt_i32 s100,0
s_cbranch_scc1 FA21_D
buffer_load_dwordx2 v[240:241],v242,s[8:11],s46 offen sc1
s_waitcnt vmcnt(0)
v_cmp_ne_u64 vcc,s[54:55],v[240:241]
s_cbranch_vccnz FA21_S
s_branch FA21_D
FK2_S:
s_sleep 2
s_sub_u32 s100,s100,1
s_cmp_lt_i32 s100,0
s_cbranch_scc1 FK2_D
buffer_load_dwordx2 v[240:241],v242,s[8:11],s46 offen sc1
s_waitcnt vmcnt(0)
v_cmp_ne_u64 vcc,s[54:55],v[240:241]
s_cbranch_vccnz FK2_S
s_branch FK2_D
FS_S:
s_sleep 2
s_sub_u32 s100,s100,1
s_cmp_lt_i32 s100,0
s_cbranch_scc1 FS_D
buffer_load_dwordx2 v[240:241],v242,s[8:11],s46 offen sc1
s_waitcnt vmcnt(0)
v_cmp_ne_u64 vcc,s[54:55],v[240:241]
s_cbranch_vccnz FS_S
s_branch FS_D
FK3_S:
s_sleep 2
s_sub_u32 s100,s100,1
s_cmp_lt_i32 s100,0
s_cbranch_scc1 FK3_D
buffer_load_dwordx2 v[240:241],v242,s[8:11],s46 offen sc1
s_waitcnt vmcnt(0)
v_cmp_ne_u64 vcc,s[54:55],v[240:241]
s_cbranch_vccnz FK3_S
s_branch FK3_D
FO_S:
s_sleep 2
s_sub_u32 s100,s100,1
s_cmp_lt_i32 s100,0
s_cbranch_scc1 FO_D
buffer_load_dwordx2 v[240:241],v242,s[8:11],s46 offen sc1
s_waitcnt vmcnt(0)
v_cmp_ne_u64 vcc,s[54:55],v[240:241]
s_cbranch_vccnz FO_S
s_branch FO_D
)" R"(FT_S:
s_sleep 2
s_sub_u32 s100,s100,1
s_cmp_lt_i32 s100,0
s_cbranch_scc1 FT_D
buffer_load_dwordx2 v[240:241],v242,s[8:11],s46 offen sc1
s_waitcnt vmcnt(0)
v_cmp_ne_u64 vcc,s[54:55],v[240:241]
s_cbranch_vccnz FT_S
s_branch FT_D
FR_S:
s_sleep 2
s_sub_u32 s100,s100,1
s_cmp_lt_i32 s100,0
s_cbranch_scc1 FR_D
buffer_load_dwordx2 v[240:241],v242,s[8:11],s46 offen sc1
s_waitcnt vmcnt(0)
v_cmp_ne_u64 vcc,s[54:55],v[240:241]
s_cbranch_vccnz FR_S
s_branch FR_D
FP0_S:
s_sleep 2
s_sub_u32 s100,s100,1
s_cmp_lt_i32 s100,0
s_cbranch_scc1 FP0_D
buffer_load_dwordx2 v[240:241],v242,s[8:11],s46 offen sc1
s_waitcnt vmcnt(0)
v_cmp_ne_u64 vcc,s[54:55],v[240:241]
s_cbranch_vccnz FP0_S
s_branch FP0_D
WTAB:
s_mov_b32 s0,((EA_9-EA_G)/4)|(((EB_R10-EB_G)/4)<<16)
s_mov_b32 s0,((EA_SB-EA_G)/4)|(((EB_17-EB_G)/4)<<16)
s_mov_b32 s0,((EA_T-EA_G)/4)|(((EB_18-EB_G)/4)<<16)
s_mov_b32 s0,((EA_15-EA_G)/4)|(((EB_19-EB_G)/4)<<16)
s_mov_b32 s0,((EA_9-EA_G)/4)|(((EB_R11-EB_G)/4)<<16)
s_mov_b32 s0,((EA_10-EA_G)/4)|(((EB_17-EB_G)/4)<<16)
s_mov_b32 s0,((EA_D-EA_G)/4)|(((EB_18-EB_G)/4)<<16)
s_mov_b32 s0,((EA_15-EA_G)/4)|(((EB_19-EB_G)/4)<<16)
s_mov_b32 s0,((EA_9-EA_G)/4)|(((EB_15-EB_G)/4)<<16)
s_mov_b32 s0,((EA_10-EA_G)/4)|(((EB_R12-EB_G)/4)<<16)
s_mov_b32 s0,((EA_11-EA_G)/4)|(((EB_18-EB_G)/4)<<16)
s_mov_b32 s0,((EA_D-EA_G)/4)|(((EB_19-EB_G)/4)<<16)
s_mov_b32 s0,((EA_9-EA_G)/4)|(((EB_15-EB_G)/4)<<16)
s_mov_b32 s0,((EA_10-EA_G)/4)|(((EB_R13-EB_G)/4)<<16)
s_mov_b32 s0,((EA_11-EA_G)/4)|(((EB_18-EB_G)/4)<<16)
s_mov_b32 s0,((EA_D-EA_G)/4)|(((EB_19-EB_G)/4)<<16)
s_mov_b32 s0,((EA_9-EA_G)/4)|(((EB_D-EB_G)/4)<<16)
s_mov_b32 s0,((EA_10-EA_G)/4)|(((EB_15-EB_G)/4)<<16)
s_mov_b32 s0,((EA_11-EA_G)/4)|(((EB_R14-EB_G)/4)<<16)
s_mov_b32 s0,((EA_12-EA_G)/4)|(((EB_19-EB_G)/4)<<16)
s_mov_b32 s0,((EA_9-EA_G)/4)|(((EB_13-EB_G)/4)<<16)
s_mov_b32 s0,((EA_10-EA_G)/4)|(((EB_D-EB_G)/4)<<16)
s_mov_b32 s0,((EA_11-EA_G)/4)|(((EB_15-EB_G)/4)<<16)
s_mov_b32 s0,((EA_12-EA_G)/4)|(((EB_R15-EB_G)/4)<<16)
s_mov_b32 s0,((EA_9-EA_G)/4)|(((EB_13-EB_G)/4)<<16)
s_mov_b32 s0,((EA_10-EA_G)/4)|(((EB_D-EB_G)/4)<<16)
s_mov_b32 s0,((EA_11-EA_G)/4)|(((EB_15-EB_G)/4)<<16)
s_mov_b32 s0,((EA_12-EA_G)/4)|(((EB_R16-EB_G)/4)<<16)
s_mov_b32 s0,((EA_9-EA_G)/4)|(((EB_13-EB_G)/4)<<16)
s_mov_b32 s0,((EA_10-EA_G)/4)|(((EB_14-EB_G)/4)<<16)
s_mov_b32 s0,((EA_11-EA_G)/4)|(((EB_15-EB_G)/4)<<16)
s_mov_b32 s0,((EA_12-EA_G)/4)|(((EB_U-EB_G)/4)<<16)
THEEND:
)" R"(
)"
  :: "s"(__builtin_amdgcn_kernarg_segment_ptr()), "s"(blockIdx.x), "s"(wid), "s"(lds)
  : "memory", "vcc", "scc", "v0", "v1", "v2", "v3", "v4", "v5", "v6", "v7", "v8", "v9", "v10", "v11", "v12", "v13", "v14", "v15", "v16", "v17", "v18", "v19", "v20", "v21", "v22", "v23", "v24", "v25", "v26", "v27", "v28", "v29", "v30", "v31", "v32", "v33", "v34", "v35", "v36", "v37", "v38", "v39", "v40", "v41", "v42", "v43", "v44", "v45", "v46", "v47", "v48", "v49", "v50", "v51", "v52", "v53", "v54", "v55", "v56", "v57", "v58", "v59", "v60", "v61", "v62", "v63", "v64", "v65", "v66", "v67", "v68", "v69", "v70", "v71", "v72", "v73", "v74", "v75", "v76", "v77", "v78", "v79", "v80", "v81", "v82", "v83", "v84", "v85", "v86", "v87", "v88", "v89", "v90", "v91", "v92", "v93", "v94", "v95", "v96", "v97", "v98", "v99", "v100", "v101", "v102", "v103", "v104", "v105", "v106", "v107", "v108", "v109", "v110", "v111", "v112", "v113", "v114", "v115", "v116", "v117", "v118", "v119", "v120", "v121", "v122", "v123", "v124", "v125", "v126", "v127", "v128", "v129", "v130", "v131", "v132", "v133", "v134", "v135", "v136", "v137", "v138", "v139", "v140", "v141", "v142", "v143", "v144", "v145", "v146", "v147", "v148", "v149", "v150", "v151", "v152", "v153", "v154", "v155", "v156", "v157", "v158", "v159", "v160", "v161", "v162", "v163", "v164", "v165", "v166", "v167", "v168", "v169", "v170", "v171", "v172", "v173", "v174", "v175", "v176", "v177", "v178", "v179", "v180", "v181", "v182", "v183", "v184", "v185", "v186", "v187", "v188", "v189", "v190", "v191", "v192", "v193", "v194", "v195", "v196", "v197", "v198", "v199", "v200", "v201", "v202", "v203", "v204", "v205", "v206", "v207", "v208", "v209", "v210", "v211", "v212", "v213", "v214", "v215", "v216", "v217", "v218", "v219", "v220", "v221", "v222", "v223", "v224", "v225", "v226", "v227", "v228", "v229", "v230", "v231", "v232", "v233", "v234", "v235", "v236", "v237", "v238", "v239", "v240", "v241", "v242", "v243", "v244", "v245", "v246", "v247", "v248", "v249", "v250", "v251", "v252", "v253", "v254", "v255", "a0", "a1", "a2", "a3", "a4", "a5", "a6", "a7", "a8", "a9", "a10", "a11", "a12", "a13", "a14", "a15", "a16", "a17", "a18", "a19", "a20", "a21", "a22", "a23", "a24", "a25", "a26", "a27", "a28", "a29", "a30", "a31", "a32", "a33", "a34", "a35", "a36", "a37", "a38", "a39", "a40", "a41", "a42", "a43", "a44", "a45", "a46", "a47", "a48", "a49", "a50", "a51", "a52", "a53", "a54", "a55", "a56", "a57", "a58", "a59", "a60", "a61", "a62", "a63", "a64", "a65", "a66", "a67", "a68", "a69", "a70", "a71", "a72", "a73", "a74", "a75", "a76", "a77", "a78", "a79", "a80", "a81", "a82", "a83", "a84", "a85", "a86", "a87", "a88", "a89", "a90", "a91", "a92", "a93", "a94", "a95", "a96", "a97", "a98", "a99", "a100", "a101", "a102", "a103", "a104", "a105", "a106", "a107", "a108", "a109", "a110", "a111", "a112", "a113", "a114", "a115", "a116", "a117", "a118", "a119", "a120", "a121", "a122", "a123", "a124", "a125", "a126", "a127", "a128", "a129", "a130", "a131", "a132", "a133", "a134", "a135", "a136", "a137", "a138", "a139", "a140", "a141", "a142", "a143", "a144", "a145", "a146", "a147", "a148", "a149", "a150", "a151", "a152", "a153", "a154", "a155", "a156", "a157", "a158", "a159", "a160", "a161", "a162", "a163", "a164", "a165", "a166", "a167", "a168", "a169", "a170", "a171", "a172", "a173", "a174", "a175", "a176", "a177", "a178", "a179", "a180", "a181", "a182", "a183", "a184", "a185", "a186", "a187", "a188", "a189", "a190", "a191", "a192", "a193", "a194", "a195", "a196", "a197", "a198", "a199", "a200", "a201", "a202", "a203", "a204", "a205", "a206", "a207", "a208", "a209", "a210", "a211", "a212", "a213", "a214", "a215", "a216", "a217", "a218", "a219", "a220", "a221", "a222", "a223", "a224", "a225", "a226", "a227", "a228", "a229", "a230", "a231", "a232", "a233", "a234", "a235", "a236", "a237", "a238", "a239", "a240", "a241", "a242", "a243", "a244", "a245", "a246", "a247", "a248", "a249", "a250", "a251", "a252", "a253", "a254", "a255", "s8", "s9", "s10", "s11", "s12", "s13", "s14", "s15", "s16", "s17", "s18", "s19", "s20", "s21", "s22", "s23", "s24", "s25", "s26", "s27", "s28", "s29", "s30", "s31", "s32", "s33", "s34", "s35", "s36", "s37", "s38", "s39", "s40", "s41", "s42", "s43", "s44", "s45", "s46", "s47", "s48", "s49", "s50", "s51", "s52", "s53", "s54", "s55", "s56", "s57", "s58", "s59", "s60", "s61", "s62", "s63", "s64", "s65", "s66", "s67", "s68", "s69", "s70", "s71", "s72", "s73", "s74", "s75", "s76", "s77", "s78", "s79", "s80", "s81", "s82", "s83", "s84", "s85", "s86", "s87", "s88", "s89", "s90", "s91", "s92", "s93", "s94", "s95", "s96", "s97", "s98", "s99", "s100", "s101");
}

extern "C" void kernel_launch(void* const* d_in, const int* in_sizes, int n_in, void* d_out, int out_size, void* d_ws, size_t ws_size, hipStream_t stream) {
  Params p;
  for (int i = 0; i < 10; i++) p.in[i] = d_in[i];
  p.out = d_out; p.ws = (char*)d_ws + 0u;
  k_main<<<256, 256, 0, stream>>>(p);

}
```
